```python
import torch

def _softmax(x, axis=-1):
    return torch.softmax(x, dim=axis)
def _gelu(x):
    return torch.nn.functional.gelu(x, approximate="tanh")
def _silu(x):
    return torch.nn.functional.silu(x)
def _sigmoid(x):
    return torch.sigmoid(x)
def _layernorm(x, axis=-1, eps=1e-5):
    m = x.mean(dim=axis, keepdim=True)
    v = x.var(dim=axis, keepdim=True, unbiased=False)
    return (x - m) / torch.sqrt(v + eps)
def _scatter_add(tgt, idx, src, axis=0):
    return tgt.clone().index_add_(axis, idx, src)
def _segment_sum(src, seg, n):
    out = torch.zeros((n,) + src.shape[1:], dtype=src.dtype)
    return out.index_add_(0, seg, src)
def _pad(x, pw):
    flat = tuple(v for pair in reversed(pw) for v in pair)
    return torch.nn.functional.pad(x, flat)

kernel_name = 'interleave_layers__sliding_local_dilated__dense_activation_gelu__prompt'

def setup_inputs():

    _w = (lambda a: (lambda t: t.to(torch.bfloat16) if t.is_floating_point() else t)(torch.as_tensor(a)))
    def _A_setup():
        B, S, D = 1, 2048, 1024
        return {
            "x": _w((lambda *s: torch.randn(*s, dtype=torch.bfloat16) * 0.05)(B, S, D)),
            "wqkv": _w((lambda *s: torch.randn(*s, dtype=torch.bfloat16) * 0.05)(D, 3 * D)),
            "wo": _w((lambda *s: torch.randn(*s, dtype=torch.bfloat16) * 0.05)(D, D)),
        }
    _A_in = _A_setup()

    def _B_setup():
        B, S, D, F = 1, 2048, 1024, 3584
        return {
            "x": _w((lambda *s: torch.randn(*s, dtype=torch.bfloat16) * 0.05)(B, S, D)),
            "w1": _w((lambda *s: torch.randn(*s, dtype=torch.bfloat16) * 1.6)(D, F) * (D ** -0.5)),
            "b1": _w((lambda *s: torch.randn(*s, dtype=torch.bfloat16) * 1.6)(F) * 0.01),
            "w2": _w((lambda *s: torch.randn(*s, dtype=torch.bfloat16) * 1.6)(F, D) * (F ** -0.5)),
            "b2": _w((lambda *s: torch.randn(*s, dtype=torch.bfloat16) * 1.6)(D) * 0.01),
        }
    _B_in = _B_setup()

    return {
        "A_x": _A_in["x"],
        "A_wqkv": _A_in["wqkv"],
        "A_wo": _A_in["wo"],
        "B_x": _B_in["x"],
        "B_w1": _B_in["w1"],
        "B_b1": _B_in["b1"],
        "B_w2": _B_in["w2"],
        "B_b2": _B_in["b2"],
    }

def reference(A_x, A_wqkv, A_wo, B_x, B_w1, B_b1, B_w2, B_b2):
    def _A_ref(x, wqkv, wo):
        B, S, D = x.shape
        H = 32; Dh = D // H
        W, dil = 8, 2
        qkv = (lambda x, *d: x.permute(*d))((x @ wqkv).reshape(B, S, 3, H, Dh), 2, 0, 3, 1, 4)
        q, k, v = qkv[0], qkv[1], qkv[2]
        scale = Dh ** -0.5
        pos = torch.arange(S)
        off = torch.arange(W) * dil
        idx = pos[:, None] - off[None, :]
        mask = idx >= 0
        idx_c = torch.where(mask, idx, 0).reshape(-1)
        kg = k[:, :, idx_c, :].reshape(B, H, S, W, Dh)
        vg = v[:, :, idx_c, :].reshape(B, H, S, W, Dh)
        logits = torch.einsum("bhsd,bhswd->bhsw", q, kg) * scale
        logits = torch.where(mask[None, None, :, :], logits, -1e9)
        attn = _softmax(logits, axis=-1)
        o = torch.einsum("bhsw,bhswd->bhsd", attn, vg)
        return (lambda x, *d: x.permute(*d))(o, 0, 2, 1, 3).reshape(B, S, D) @ wo

    def _B_ref(x, w1, b1, w2, b2):
        h = torch.einsum("bsd,df->bsf", x, w1) + b1
        h = _gelu(h)
        return torch.einsum("bsf,fd->bsd", h, w2) + b2

    x = A_x
    x = x + _A_ref(_layernorm(x, axis=-1), A_wqkv, A_wo)
    x = x + _B_ref(_layernorm(x, axis=-1), B_w1, B_b1, B_w2, B_b2)
    x = x + _A_ref(_layernorm(x, axis=-1), A_wqkv, A_wo)
    x = x + _B_ref(_layernorm(x, axis=-1), B_w1, B_b1, B_w2, B_b2)
    return x
```

```cpp
#include <hip/hip_runtime.h>
#include <stdint.h>
#include <type_traits>
#include <utility>

typedef unsigned short u16;
typedef unsigned int u32;
typedef __bf16 bf16x8 __attribute__((ext_vector_type(8)));
typedef __bf16 bf16x2 __attribute__((ext_vector_type(2)));
typedef short s16x4 __attribute__((ext_vector_type(4)));
typedef float f32x4 __attribute__((ext_vector_type(4)));
typedef float f32x2 __attribute__((ext_vector_type(2)));
typedef u32 u32x4 __attribute__((ext_vector_type(4)));
typedef u32 u32x2 __attribute__((ext_vector_type(2)));
typedef int i32x4 __attribute__((ext_vector_type(4)));
typedef int i32x2 __attribute__((ext_vector_type(2)));
#define LDS_AS __attribute__((address_space(3)))
#define GLB_AS __attribute__((address_space(1)))

constexpr int SEQ = 2048, DM = 1024, FF = 3584;
constexpr float LN_EPS = 1e-5f;

constexpr u32 MB = 1u << 20;
constexpr u32 OFF_WQ = 0;
constexpr u32 OFF_WO = 6 * MB;
constexpr u32 OFF_W1 = 8 * MB;
constexpr u32 OFF_W2 = 15 * MB;
constexpr u32 OFF_X = 22 * MB;
constexpr u32 OFF_O = 38 * MB;
constexpr u32 OFF_H = 46 * MB;
constexpr u32 OFF_ST = 74 * MB;
constexpr u32 OFF_CSQ = 75 * MB;
constexpr u32 OFF_CS1 = OFF_CSQ + 65536;
constexpr u32 OFF_FL = OFF_CS1 + 65536;
constexpr u32 OFF_DBG = OFF_FL + 7 * 32768;
constexpr u32 WS_USED = 76 * MB;

constexpr int SLOT = 24576, NRING = 6, RING = SLOT * NRING;
constexpr int L_INV = RING, L_MNV = L_INV + 576, L_CS = L_MNV + 576, L_BS = L_CS + 896, L_CNT = L_BS + 896;
constexpr int L_XC = RING + 3072;
constexpr int LDS_BYTES = L_XC + 6144;

__device__ __forceinline__ float bf_lo(u32 w) { return __uint_as_float(w << 16); }
__device__ __forceinline__ float bf_hi(u32 w) { return __uint_as_float(w & 0xffff0000u); }
__device__ __forceinline__ u32 pack2(float lo, float hi) {
  f32x2 v = {lo, hi};
  bf16x2 r = __builtin_convertvector(v, bf16x2);
  return __builtin_bit_cast(u32, r);
}
__device__ __forceinline__ float bfr(float x) { return bf_lo(pack2(x, 0.f) & 0xffffu); }
__device__ __forceinline__ u32 gelu_tanh2(f32x2 x) {
  const float c0 = -2.3022081983f, c1 = -0.1029432396f;
  const f32x2 p = x * x * c1 + c0;
  const f32x2 a = x * p;
  const f32x2 d = (f32x2){__builtin_amdgcn_exp2f(a[0]), __builtin_amdgcn_exp2f(a[1])} + 1.f;
  const f32x2 g = x * (f32x2){__builtin_amdgcn_rcpf(d[0]), __builtin_amdgcn_rcpf(d[1])};
  return pack2(g[0], g[1]);
}
template <int N_> __device__ __forceinline__ void wait_vm() { asm volatile("s_waitcnt vmcnt(%0)" ::"n"(N_) : "memory"); }
__device__ __forceinline__ void wait_lgkm0() { asm volatile("s_waitcnt lgkmcnt(0)" ::: "memory"); }
__device__ __forceinline__ void bar() {
  __builtin_amdgcn_sched_barrier(0);
  asm volatile("s_barrier" ::: "memory");
  __builtin_amdgcn_sched_barrier(0);
}
template <int V> using IC = std::integral_constant<int, V>;
template <typename F, int... I>
__device__ __forceinline__ void for_seq(F&& f, std::integer_sequence<int, I...>) { (f(IC<I>{}), ...); }
template <int N, typename F> __device__ __forceinline__ void for_n(F&& f) { for_seq(f, std::make_integer_sequence<int, N>{}); }

__device__ __forceinline__ float xsum(float x) {
#if defined(__HIP_DEVICE_COMPILE__)
  auto a = __builtin_amdgcn_permlane16_swap(__float_as_uint(x), __float_as_uint(x), false, false);
  x = __uint_as_float(a[0]) + __uint_as_float(a[1]);
  auto b = __builtin_amdgcn_permlane32_swap(__float_as_uint(x), __float_as_uint(x), false, false);
  x = __uint_as_float(b[0]) + __uint_as_float(b[1]);
#endif
  return x;
}
__device__ __forceinline__ float xmax(float x) {
#if defined(__HIP_DEVICE_COMPILE__)
  auto a = __builtin_amdgcn_permlane16_swap(__float_as_uint(x), __float_as_uint(x), false, false);
  x = fmaxf(__uint_as_float(a[0]), __uint_as_float(a[1]));
  auto b = __builtin_amdgcn_permlane32_swap(__float_as_uint(x), __float_as_uint(x), false, false);
  x = fmaxf(__uint_as_float(b[0]), __uint_as_float(b[1]));
#endif
  return x;
}
__device__ __forceinline__ f32x2 row_part(const u32x4& c0, const u32x4& c1) {
  float v[16];
#pragma unroll
  for (int k = 0; k < 4; ++k) {
    v[2 * k] = bf_lo(c0[k]); v[2 * k + 1] = bf_hi(c0[k]);
    v[8 + 2 * k] = bf_lo(c1[k]); v[8 + 2 * k + 1] = bf_hi(c1[k]);
  }
  float s = 0.f;
#pragma unroll
  for (int k = 0; k < 16; ++k) s += v[k];
  const float m = xsum(s) * (1.f / 64.f);
  float q = 0.f;
#pragma unroll
  for (int k = 0; k < 16; ++k) { const float d = v[k] - m; q += d * d; }
  return (f32x2){m, xsum(q)};
}

constexpr int PB_Q = 192, PB_X = 256, PB_O = 64, PB_1 = 224, PB_2 = 224;
__global__ __launch_bounds__(256) void prep_k(const u16* __restrict__ x, const u16* __restrict__ wqkv,
                                              const u16* __restrict__ wo, const u16* __restrict__ w1,
                                              const u16* __restrict__ w2, char* __restrict__ ws) {
#if defined(__HIP_DEVICE_COMPILE__)
  __shared__ __attribute__((aligned(16))) char img[4 * 8192];
  __shared__ float red[4][64];
  const int tid = threadIdx.x, lane = tid & 63, wid = __builtin_amdgcn_readfirstlane(tid >> 6);
  const int r = lane & 15, g = lane >> 4;
  int b = blockIdx.x;
  if (b >= PB_Q && b < PB_Q + PB_X) {
    b -= PB_Q;
    const int tm = b >> 4, tn = b & 15;
    if (tid < 224) ((u32*)(ws + OFF_FL))[b * 224 + tid] = 0u;
    u32x4 c[2][2];
#pragma unroll
    for (int i = 0; i < 2; ++i)
#pragma unroll
      for (int gl = 0; gl < 2; ++gl)
        c[i][gl] = *(const u32x4*)(x + (size_t)(tm * 128 + (2 * wid + i) * 16 + r) * DM + tn * 64 + gl * 32 + g * 8);
#pragma unroll
    for (int i = 0; i < 2; ++i)
#pragma unroll
      for (int gl = 0; gl < 2; ++gl)
        *(u32x4*)(ws + OFF_X + ((tm * 32 + 2 * tn + gl) * 8 + 2 * wid + i) * 1024 + lane * 16) = c[i][gl];
    const f32x2 s0 = row_part(c[0][0], c[0][1]), s1 = row_part(c[1][0], c[1][1]);
    if (lane < 32) *(f32x2*)(ws + OFF_ST + ((tm * 16 + tn) * 128 + wid * 32 + lane) * 8) = g ? s1 : s0;
    return;
  }
  int which, N, cb, kq;
  const u16* W;
  if (b < PB_Q) { which = 0; W = wqkv; N = 3 * DM; cb = b % 48; kq = b / 48; }
  else {
    b -= PB_Q + PB_X;
    if (b < PB_O) { which = 1; W = wo; N = DM; cb = b & 15; kq = b >> 4; }
    else if (b < PB_O + PB_1) { b -= PB_O; which = 2; W = w1; N = FF; cb = b % 56; kq = b / 56; }
    else { b -= PB_O + PB_1; which = 3; W = w2; N = DM; cb = b & 15; kq = b >> 4; }
  }
  const int kt = kq * 4 + wid;
  char* im = img + wid * 8192;
  {
    u32x4 v[8];
#pragma unroll
    for (int e = 0; e < 8; ++e)
      v[e] = *(const u32x4*)(W + (size_t)(kt * 64 + e * 8 + (lane >> 3)) * N + cb * 64 + (lane & 7) * 8);
#pragma unroll
    for (int e = 0; e < 8; ++e) *(u32x4*)(im + e * 1024 + lane * 16) = v[e];
  }
  const int q = r >> 2, pp = r & 3;
#pragma unroll
  for (int gh = 0; gh < 2; ++gh) {
    const int gam = cb * 2 + gh;
    bool split = false;
    u32 dst, step, tstep = 1;
    if (which == 0) {
      const int part = gam >> 5, head = gam & 31;
      dst = OFF_WQ / 1024 + (head >> 1) * 32 * 12 + (head & 1) * 6 + part * 2; step = 12;
    } else if (which == 1) {
      dst = OFF_WO / 1024 + (gam >> 1) * 32 * 4 + (gam & 1) * 2; step = 4;
    } else if (which == 2) {
      const int tn = gam / 7, gl = gam % 7;
      split = gl == 3;
      dst = OFF_W1 / 1024 + tn * 32 * 14 + (split ? 6 : (gl < 3 ? gl * 2 : 7 + (gl - 4) * 2)); step = 14;
      if (split) tstep = 7;
    } else {
      dst = OFF_W2 / 1024 + (gam >> 1) * 112 * 4 + (gam & 1) * 2; step = 4;
    }
#pragma unroll
    for (int t = 0; t < 2; ++t) {
      const int cbyte = gh * 64 + (split ? t * 32 + pp * 8 : pp * 16 + t * 8);
      float sum = 0.f;
#pragma unroll
      for (int s = 0; s < 2; ++s) {
        const char* p = im + (32 * s + 8 * g + q) * 128 + cbyte;
        const s16x4 lo = __builtin_amdgcn_ds_read_tr16_b64_v4i16((LDS_AS s16x4*)p);
        const s16x4 hi = __builtin_amdgcn_ds_read_tr16_b64_v4i16((LDS_AS s16x4*)(p + 512));
        const u32x2 l2 = __builtin_bit_cast(u32x2, lo), h2 = __builtin_bit_cast(u32x2, hi);
        const u32x4 f = {l2[0], l2[1], h2[0], h2[1]};
        *(u32x4*)(ws + (size_t)(dst + (2 * kt + s) * step + t * tstep) * 1024 + lane * 16) = f;
#pragma unroll
        for (int k = 0; k < 4; ++k) sum += bf_lo(f[k]) + bf_hi(f[k]);
      }
      sum = xsum(sum);
      if (g == 0) red[wid][gh * 32 + (split ? 16 * t + r : 8 * q + 4 * t + pp)] = sum;
    }
  }
  if (which == 0 || which == 2) {
    __syncthreads();
    if (tid < 64)
      ((float*)(ws + (which == 0 ? OFF_CSQ : OFF_CS1)))[kq * N + cb * 64 + tid] =
          (red[0][tid] + red[1][tid]) + (red[2][tid] + red[3][tid]);
  }
#endif
}

__device__ __forceinline__ int fresh_lane(int tid) { int l = tid & 63; asm volatile("" : "+v"(l)); return l; }
__device__ __forceinline__ f32x4 zero4() {
  f32x4 z = {0.f, 0.f, 0.f, 0.f};
  asm volatile("" : "+v"(z));
  return z;
}
__device__ __forceinline__ void next_slot(int& so) { so += SLOT; if (so == RING) so = 0; }

#define SB1
#define DMA __builtin_amdgcn_raw_ptr_buffer_load_lds
template <int NSL, int NG, class RD, class MM>
__device__ __forceinline__ void kloop(int& so, RD&& rd_, MM&& mm_) {
  auto& mm = mm_;
  auto& rd = rd_;
  bar();
  for_n<NG>([&](auto G) { rd(IC<0>{}, G, so); });
  next_slot(so);
  wait_lgkm0();
#pragma nounroll
  for (int u = 1; u < NSL - 1; u += 2) {
    bar();
    for_n<NG>([&](auto G) { mm(IC<0>{}, G); SB1; rd(IC<1>{}, G, so); __builtin_amdgcn_sched_barrier(0); });
    next_slot(so);
    wait_lgkm0();
    bar();
    for_n<NG>([&](auto G) { mm(IC<1>{}, G); SB1; rd(IC<0>{}, G, so); __builtin_amdgcn_sched_barrier(0); });
    next_slot(so);
    wait_lgkm0();
  }
  for_n<NG>([&](auto G) { mm(IC<0>{}, G); });
  bar();
  for_n<NG>([&](auto G) { rd(IC<1>{}, G, so); });
  next_slot(so);
  for_n<NG>([&](auto G) { mm(IC<1>{}, G); });
}

#define STAMP(w, i)

__global__ __launch_bounds__(512, 2) void mega_k(char* __restrict__ ws, const u16* __restrict__ b1,
                                                 const u16* __restrict__ b2, u16* __restrict__ out) {
#if defined(__HIP_DEVICE_COMPILE__)
  extern __shared__ __attribute__((aligned(16))) char smem[];
  const int tid = threadIdx.x;
  const int wid = __builtin_amdgcn_readfirstlane(tid >> 6);
#define FRESH_LANE() const int lane = fresh_lane(tid), r = lane & 15, g = lane >> 4; (void)r; (void)g
  const int xcd = blockIdx.x & 7, idx = blockIdx.x >> 3;
  const int tmA = (xcd >> 2) * 8 + (idx >> 2), tnA = (xcd & 3) * 4 + (idx & 3);
  const int tmB = (xcd >> 1) * 4 + (idx >> 3), tnB = (xcd & 1) * 8 + (idx & 7);
  const __amdgpu_buffer_rsrc_t rs = __builtin_amdgcn_make_buffer_rsrc((void*)ws, 0, WS_USED, 0x00020000);
  if (tid == 0) *(LDS_AS u32*)(smem + L_CNT) = 0u;
  wait_lgkm0();
  int so = 0;

  if (wid < 4) {
#define st16(v, off) __builtin_amdgcn_raw_buffer_store_b128(__builtin_bit_cast(i32x4, v), rs, lane * 16 + (off), 0, 16)
    auto publish = [&](int ph, int tm, int tn) {
      FRESH_LANE();
      wait_vm<0>();
      u32 old = 0;
      if (lane == 0) old = __hip_atomic_fetch_add((LDS_AS u32*)(smem + L_CNT), 1u, __ATOMIC_RELAXED, __HIP_MEMORY_SCOPE_WORKGROUP);
      old = __builtin_amdgcn_readfirstlane(old);
      if ((old & 3) == 3 && lane == 0)
        __hip_atomic_store((GLB_AS u32*)(ws + OFF_FL + ((ph * 16 + tm) * 16 + tn) * 128), 1u, __ATOMIC_RELAXED,
                           __HIP_MEMORY_SCOPE_AGENT);
    };
    const float* t_inv = (const float*)(smem + L_INV);
    const float* t_mnv = (const float*)(smem + L_MNV);
    const float* t_cs = (const float*)(smem + L_CS);
    const float* t_bs = (const float*)(smem + L_BS);
    const int wmi = wid >> 1, wni = wid & 1;

    auto res_phase = [&](auto NSL_, u32 roff, int xo  , bool bias, int ph) {
      constexpr int NSL = decltype(NSL_)::value;
      FRESH_LANE();
      u32x4 res[2][2], bb[2];
#pragma unroll
      for (int i = 0; i < 2; ++i)
#pragma unroll
        for (int gl = 0; gl < 2; ++gl)
          res[i][gl] = __builtin_bit_cast(u32x4, __builtin_amdgcn_raw_buffer_load_b128(
              rs, lane * 16, roff + ((tmB * 32 + 2 * tnB + gl) * 8 + 2 * wid + i) * 1024, 16));
#pragma unroll
      for (int gl = 0; gl < 2; ++gl) {
        bb[gl] = *(const u32x4*)(b2 + tnB * 64 + gl * 32 + g * 8);
        if (!bias) bb[gl] = (u32x4){0u, 0u, 0u, 0u};
      }
      f32x4 acc[2][4];
#pragma unroll
      for (int i = 0; i < 2; ++i)
#pragma unroll
        for (int j = 0; j < 4; ++j) acc[i][j] = zero4();
      bf16x8 fA[2][2][2], fB[2][2][4];
      const char* pA = smem + lane * 16 + wid * 2048;
      const char* pB = smem + lane * 16 + 16384;
      kloop<NSL, 8>(so,
        [&](auto SET, auto G, int s_) {
          constexpr int set = decltype(SET)::value, gi = decltype(G)::value;
          if constexpr (gi < 6) {
            constexpr int s = gi / 3, m = gi % 3;
            if constexpr (m == 0) {
              fA[set][s][0] = *(const bf16x8*)(pA + s_ + s * 8192);
              fA[set][s][1] = *(const bf16x8*)(pA + s_ + s * 8192 + 1024);
            } else {
              fB[set][s][2 * m - 2] = *(const bf16x8*)(pB + s_ + s * 4096 + (2 * m - 2) * 1024);
              fB[set][s][2 * m - 1] = *(const bf16x8*)(pB + s_ + s * 4096 + (2 * m - 1) * 1024);
            }
          }
        },
        [&](auto SET, auto G) {
          constexpr int set = decltype(SET)::value, gi = decltype(G)::value, s = gi >> 2, j = gi & 3;
#pragma unroll
          for (int i = 0; i < 2; ++i)
            acc[i][j] = __builtin_amdgcn_mfma_f32_16x16x32_bf16(fB[set][s][j], fA[set][s][i], acc[i][j], 0, 0, 0);
        });
      STAMP(0, 4 * ph + 2);
      u32x4 c[2][2];
#pragma unroll
      for (int i = 0; i < 2; ++i)
#pragma unroll
        for (int gl = 0; gl < 2; ++gl) {
#pragma unroll
          for (int k = 0; k < 4; ++k) {
            const f32x4& a = acc[i][2 * gl + (k >> 1)];
            const float y0 = a[2 * (k & 1)] + bf_lo(bb[gl][k]), y1 = a[2 * (k & 1) + 1] + bf_hi(bb[gl][k]);
            c[i][gl][k] = pack2(bf_lo(res[i][gl][k]) + y0, bf_hi(res[i][gl][k]) + y1);
          }
          if (xo < 4) st16(c[i][gl], OFF_X + xo * 4 * MB + ((tmB * 32 + 2 * tnB + gl) * 8 + 2 * wid + i) * 1024);
          else *(u32x4*)(out + (size_t)(tmB * 128 + (2 * wid + i) * 16 + r) * DM + tnB * 64 + gl * 32 + g * 8) = c[i][gl];
        }
      if (xo < 4) {
        const f32x2 s0 = row_part(c[0][0], c[0][1]), s1 = row_part(c[1][0], c[1][1]);
        if (lane < 32)
          __builtin_amdgcn_raw_buffer_store_b64(__builtin_bit_cast(i32x2, g ? s1 : s0), rs, lane * 8,
                                                OFF_ST + xo * 262144 + ((tmB * 16 + tnB) * 128 + wid * 32) * 8, 16);
        publish(ph, tmB, tnB);
      }
      STAMP(0, 4 * ph + 3);
    };

#pragma nounroll
    for (int pair = 0; pair < 2; ++pair) {
      {
        f32x4 acc[4][6], acch[2];
        {
        FRESH_LANE();
        STAMP(0, 16 * pair);
#pragma unroll
        for (int i = 0; i < 4; ++i)
#pragma unroll
          for (int j = 0; j < 6; ++j) acc[i][j] = zero4();
        acch[0] = zero4(); acch[1] = zero4();
        bf16x8 fA[2][4], fH[2], fB[2][6];
        const char* pA = smem + lane * 16 + wmi * 4096;
        const char* pB = smem + lane * 16 + 9216 + wni * 6144;
        kloop<32, 6>(so,
          [&](auto SET, auto G, int s_) {
            constexpr int set = decltype(SET)::value, gi = decltype(G)::value;
            if constexpr (gi < 2) {
              fA[set][2 * gi] = *(const bf16x8*)(pA + s_ + 2 * gi * 1024);
              fA[set][2 * gi + 1] = *(const bf16x8*)(pA + s_ + (2 * gi + 1) * 1024);
              fB[set][gi] = *(const bf16x8*)(pB + s_ + gi * 1024);
            } else if constexpr (gi == 2) {
              fH[set] = *(const bf16x8*)(smem + lane * 16 + 8192 + s_);
              fB[set][2] = *(const bf16x8*)(pB + s_ + 2 * 1024);
              fB[set][3] = *(const bf16x8*)(pB + s_ + 3 * 1024);
            } else if constexpr (gi == 3) {
              fB[set][4] = *(const bf16x8*)(pB + s_ + 4 * 1024);
              fB[set][5] = *(const bf16x8*)(pB + s_ + 5 * 1024);
            }
          },
          [&](auto SET, auto G) {
            constexpr int set = decltype(SET)::value, j = decltype(G)::value;
#pragma unroll
            for (int i = 0; i < 4; ++i) {
              if constexpr (j < 4)
                acc[i][j] = __builtin_amdgcn_mfma_f32_16x16x32_bf16(fB[set][j], fA[set][i], acc[i][j], 0, 0, 0);
              else
                acc[i][j] = __builtin_amdgcn_mfma_f32_16x16x32_bf16(fA[set][i], fB[set][j], acc[i][j], 0, 0, 0);
            }
            if constexpr (j == 2 || j == 3) {
              if (wmi == 0) acch[j - 2] = __builtin_amdgcn_mfma_f32_16x16x32_bf16(fB[set][j], fH[set], acch[j - 2], 0, 0, 0);
            }
            if constexpr (j >= 4) {
              if (wmi == 1) acch[j - 4] = __builtin_amdgcn_mfma_f32_16x16x32_bf16(fH[set], fB[set][j], acch[j - 4], 0, 0, 0);
            }
          });
        }
        FRESH_LANE();
        STAMP(0, 16 * pair + 2);
        const float* csw = t_cs + wni * 96;
        auto fix_qk = [&](const f32x4& a0, const f32x4& a1, int trow, int f) {
          const float iv = t_inv[trow + r], mv = t_mnv[trow + r];
          const f32x4 c0 = *(const f32x4*)(csw + f * 16 + 4 * g), c1 = *(const f32x4*)(csw + f * 16 + 16 + 4 * g);
          const f32x4 y0 = a0 * iv - c0 * mv, y1 = a1 * iv - c1 * mv;
          return (u32x4){pack2(y0[0], y0[1]), pack2(y0[2], y0[3]), pack2(y1[0], y1[1]), pack2(y1[2], y1[3])};
        };
        auto fix_v = [&](const f32x4& a0, const f32x4& a1, int trow) {
          const f32x4 iv = *(const f32x4*)(t_inv + trow + 4 * g), mv = *(const f32x4*)(t_mnv + trow + 4 * g);
          const float c0 = csw[64 + r], c1 = csw[80 + r];
          const f32x4 y0 = a0 * iv - mv * c0, y1 = a1 * iv - mv * c1;
          return (u32x4){pack2(y0[0], y0[1]), pack2(y0[2], y0[3]), pack2(y1[0], y1[1]), pack2(y1[2], y1[3])};
        };
        u32x4 Qf[4], Kf[5], Vf[5];
        char* xc = smem + L_XC + wni * 3072 + lane * 16;
        const int row0 = 16 + wmi * 64;
        Kf[4] = fix_qk(acc[3][2], acc[3][3], row0 + 48, 2);
        Vf[4] = fix_v(acc[3][4], acc[3][5], row0 + 48);
        if (wmi == 0) {
          *(u32x4*)xc = Kf[4];
          *(u32x4*)(xc + 1024) = Vf[4];
          Kf[0] = fix_qk(acch[0], acch[1], 0, 2);
        } else {
          *(u32x4*)(xc + 2048) = fix_v(acch[0], acch[1], 0);
        }
#pragma unroll
        for (int i = 0; i < 3; ++i) {
          Kf[i + 1] = fix_qk(acc[i][2], acc[i][3], row0 + 16 * i, 2);
          Vf[i + 1] = fix_v(acc[i][4], acc[i][5], row0 + 16 * i);
        }
        wait_lgkm0();
        bar();
        if (wmi == 1) Kf[0] = *(const u32x4*)xc;
        Vf[0] = *(const u32x4*)(xc + (wmi == 0 ? 2048 : 1024));
#pragma unroll
        for (int i = 0; i < 4; ++i) Qf[i] = fix_qk(acc[i][0], acc[i][1], row0 + 16 * i, 0);
        const bool par = r & 1;
        const float m0 = par ? 0.f : 1.f, m1 = par ? 1.f : 0.f;
        const bool own0 = 4 * g + (r & 1) <= r, own1 = 4 * g + (r & 1) + 2 <= r;
        const bool first = (tmA == 0) && (wmi == 0);
#pragma unroll
        for (int ii = 0; ii < 4; ++ii) {
          const int i = 3 - ii;
          const f32x4 z = {0.f, 0.f, 0.f, 0.f};
          const f32x4 S0 = __builtin_amdgcn_mfma_f32_16x16x32_bf16(__builtin_bit_cast(bf16x8, Kf[i]), __builtin_bit_cast(bf16x8, Qf[i]), z, 0, 0, 0);
          const f32x4 S1 = __builtin_amdgcn_mfma_f32_16x16x32_bf16(__builtin_bit_cast(bf16x8, Kf[i + 1]), __builtin_bit_cast(bf16x8, Qf[i]), z, 0, 0, 0);
          float l0 = own0 ? (par ? S1[1] : S1[0]) : (par ? S0[1] : S0[0]);
          float l1 = own1 ? (par ? S1[3] : S1[2]) : (par ? S0[3] : S0[2]);
          const float c = 0.17677669529663687f * 1.4426950408889634f;
          l0 *= c; l1 *= c;
          if (i == 0 && first) {
            if (!own0) l0 = -1.4426950e9f;
            if (!own1) l1 = -1.4426950e9f;
          }
          const float mx = xmax(fmaxf(l0, l1));
          const float p0 = __builtin_amdgcn_exp2f(l0 - mx), p1 = __builtin_amdgcn_exp2f(l1 - mx);
          const float rsum = __builtin_amdgcn_rcpf(xsum(p0 + p1));
          const float q0 = p0 * rsum, q1 = p1 * rsum;
          const u32 w0 = pack2(q0 * m0, q0 * m1), w1 = pack2(q1 * m0, q1 * m1);
          const u32x4 P = {own0 ? 0u : w0, own1 ? 0u : w1, own0 ? w0 : 0u, own1 ? w1 : 0u};
          const u32x4 V0 = {Vf[i][0], Vf[i][1], Vf[i + 1][0], Vf[i + 1][1]};
          const u32x4 V1 = {Vf[i][2], Vf[i][3], Vf[i + 1][2], Vf[i + 1][3]};
          const f32x4 O0 = __builtin_amdgcn_mfma_f32_16x16x32_bf16(__builtin_bit_cast(bf16x8, V0), __builtin_bit_cast(bf16x8, P), z, 0, 0, 0);
          const f32x4 O1 = __builtin_amdgcn_mfma_f32_16x16x32_bf16(__builtin_bit_cast(bf16x8, V1), __builtin_bit_cast(bf16x8, P), z, 0, 0, 0);
          const u32x4 o = {pack2(O0[0], O0[1]), pack2(O0[2], O0[3]), pack2(O1[0], O1[1]), pack2(O1[2], O1[3])};
          st16(o, OFF_O + pair * 4 * MB + ((tmA * 32 + 2 * tnA + wni) * 8 + wmi * 4 + i) * 1024);
        }
        publish(4 * pair, tmA, tnA);
        STAMP(0, 16 * pair + 3);
      }
      res_phase(IC<16>{}, OFF_X + (2 * pair) * 4 * MB, 2 * pair + 1, false, 4 * pair + 1);
      {
        f32x4 acc[4][7];
        {
        FRESH_LANE();
#pragma unroll
        for (int i = 0; i < 4; ++i)
#pragma unroll
          for (int j = 0; j < 7; ++j) acc[i][j] = zero4();
        bf16x8 fA[2][4], fB[2][7];
        const char* pA = smem + lane * 16 + wmi * 4096;
        const char* pB = smem + lane * 16 + 8192 + wni * 7168;
        kloop<32, 7>(so,
          [&](auto SET, auto G, int s_) {
            constexpr int set = decltype(SET)::value, gi = decltype(G)::value;
            if constexpr (gi < 2) {
              fA[set][2 * gi] = *(const bf16x8*)(pA + s_ + 2 * gi * 1024);
              fA[set][2 * gi + 1] = *(const bf16x8*)(pA + s_ + (2 * gi + 1) * 1024);
              fB[set][gi] = *(const bf16x8*)(pB + s_ + gi * 1024);
            } else if constexpr (gi < 4) {
              fB[set][2 * gi - 2] = *(const bf16x8*)(pB + s_ + (2 * gi - 2) * 1024);
              fB[set][2 * gi - 1] = *(const bf16x8*)(pB + s_ + (2 * gi - 1) * 1024);
            } else if constexpr (gi == 4) {
              fB[set][6] = *(const bf16x8*)(pB + s_ + 6 * 1024);
            }
          },
          [&](auto SET, auto G) {
            constexpr int set = decltype(SET)::value, j = decltype(G)::value;
#pragma unroll
            for (int i = 0; i < 4; ++i)
              acc[i][j] = __builtin_amdgcn_mfma_f32_16x16x32_bf16(fB[set][j], fA[set][i], acc[i][j], 0, 0, 0);
          });
        }
        FRESH_LANE();
        STAMP(0, 16 * pair + 10);
        float iv[4], mv[4];
#pragma unroll
        for (int i = 0; i < 4; ++i) { iv[i] = t_inv[16 + wmi * 64 + 16 * i + r]; mv[i] = t_mnv[16 + wmi * 64 + 16 * i + r]; }
        const u32 hbase = OFF_H + pair * 14 * MB + ((tmA * 112 + 7 * tnA) * 8 + wmi * 4) * 1024;
        auto act = [&](const f32x4& a, const f32x4& cs, const f32x4& bs, int i) {
          const f32x4 y = a * iv[i] + (bs - cs * mv[i]);
          return (u32x2){gelu_tanh2((f32x2){y[0], y[1]}), gelu_tanh2((f32x2){y[2], y[3]})};
        };
#pragma unroll
        for (int jp = 0; jp < 3; ++jp) {
          const int t0 = (wni * 7 + 2 * jp) * 16 + 4 * g;
          const f32x4 cs0 = *(const f32x4*)(t_cs + t0), cs1 = *(const f32x4*)(t_cs + t0 + 16);
          const f32x4 bs0 = *(const f32x4*)(t_bs + t0), bs1 = *(const f32x4*)(t_bs + t0 + 16);
#pragma unroll
          for (int i = 0; i < 4; ++i) {
            const u32x2 a = act(acc[i][2 * jp], cs0, bs0, i), b = act(acc[i][2 * jp + 1], cs1, bs1, i);
            const u32x4 v = {a[0], a[1], b[0], b[1]};
            st16(v, hbase + ((wni * 4 + jp) * 8 + i) * 1024);
          }
        }
        {
          const int t0 = (wni * 7 + 6) * 16 + 4 * g;
          const f32x4 cs0 = *(const f32x4*)(t_cs + t0), bs0 = *(const f32x4*)(t_bs + t0);
#pragma unroll
          for (int i = 0; i < 4; i += 2) {
            const u32x2 a = act(acc[i][6], cs0, bs0, i), b = act(acc[i + 1][6], cs0, bs0, i + 1);
            const auto s0 = __builtin_amdgcn_permlane16_swap(a[0], b[0], false, false);
            const auto s1 = __builtin_amdgcn_permlane16_swap(a[1], b[1], false, false);
            const u32x4 v = {s0[0], s1[0], s0[1], s1[1]};
            __builtin_amdgcn_raw_buffer_store_b128(__builtin_bit_cast(i32x4, v), rs,
                                                   (g & 1) * 1024 + ((2 * wni + (g >> 1)) * 16 + r) * 16 +
                                                   hbase + (3 * 8 + i) * 1024, 0, 16);
          }
        }
        publish(4 * pair + 2, tmA, tnA);
        STAMP(0, 16 * pair + 11);
      }
      res_phase(IC<56>{}, OFF_X + (2 * pair + 1) * 4 * MB, pair == 0 ? 2 : 4, true, 4 * pair + 3);
    }
  } else if (wid == 4) {
    FRESH_LANE();
    auto phase = [&](auto NSL_, auto PA_, auto HALO_, u32 src, u32 hsrc, int ph  ,
                     int tm, int tmh) {
      constexpr int NSL = decltype(NSL_)::value, PA = decltype(PA_)::value, HALO = decltype(HALO_)::value;
      constexpr int PT = PA + HALO, D = PA == 16 ? 4 : 5;
      if (ph >= 0) {
        const GLB_AS u32* f = (const GLB_AS u32*)(ws + OFF_FL + ((ph * 16 + ((lane & 16) ? tmh : tm)) * 16 + r) * 128);
#pragma nounroll
        for (int spin = 0; spin < (1 << 22); ++spin) {
          const u32 v = __hip_atomic_load(f, __ATOMIC_RELAXED, __HIP_MEMORY_SCOPE_AGENT);
          if (__all(v != 0u)) break;
          __builtin_amdgcn_s_sleep(1);
        }
      }
      STAMP(1, 4 * (ph + 1));
      int si = so;
      auto issue = [&](int u) {
        for_n<PA / 4>([&](auto Q) {
          constexpr int q4 = decltype(Q)::value;
          for_n<4>([&](auto E) {
            DMA(rs, (LDS_AS void*)(smem + si + q4 * 4096), 16, lane * 16,
                                                     src + u * (PA * 1024) + q4 * 4096, decltype(E)::value * 1024, 16);
          });
        });
        if constexpr (HALO)
          DMA(rs, (LDS_AS void*)(smem + si + 8192), 16, lane * 16, hsrc + u * 8192, 0, 16);
        next_slot(si);
      };
#pragma nounroll
      for (int u = 0; u < D; ++u) { if constexpr (PA == 16) wait_vm<47>(); issue(u); }
#pragma nounroll
      for (int u = 0; u < NSL - D; ++u) {
        wait_vm<(D - 1) * PT>();
        bar();
        if constexpr (PA == 16) wait_vm<47>();
        issue(u + D);
      }
      for_n<D>([&](auto M) { wait_vm<(D - 1 - decltype(M)::value) * PT>(); bar(); });
      so = si;
    };
#pragma nounroll
    for (int pair = 0; pair < 2; ++pair) {
      const int tmh = tmA > 0 ? tmA - 1 : 0;
      const u32 xq = OFF_X + 2 * pair * 4 * MB;
      phase(IC<32>{}, IC<8>{}, IC<1>{}, xq + tmA * 262144, xq + tmh * 262144 + 7168, 4 * pair - 1, tmA, tmh);
      bar();
      phase(IC<16>{}, IC<16>{}, IC<0>{}, OFF_O + pair * 4 * MB + tmB * 262144, 0, 4 * pair, tmB, tmB);
      phase(IC<32>{}, IC<8>{}, IC<0>{}, xq + 4 * MB + tmA * 262144, 0, 4 * pair + 1, tmA, tmA);
      phase(IC<56>{}, IC<16>{}, IC<0>{}, OFF_H + pair * 14 * MB + tmB * (112 * 8192), 0, 4 * pair + 2, tmB, tmB);
    }
  } else if (wid == 5) {
    FRESH_LANE();
    auto issue = [&](auto PB_, auto BOFF_, u32 src, int u) {
      constexpr int PB = decltype(PB_)::value, BOFF = decltype(BOFF_)::value;
      wait_vm<49>();
      for_n<PB>([&](auto I) {
        constexpr int i = decltype(I)::value;
        DMA(rs, (LDS_AS void*)(smem + so + BOFF + (i >> 2) * 4096), 16, lane * 16,
                                                 src + u * (PB * 1024) + (i >> 2) * 4096, (i & 3) * 1024, 0);
      });
      next_slot(so);
    };
    auto phase = [&](auto NSL_, auto PB_, auto BOFF_, u32 src, auto PN_, auto BOFFN_, u32 srcn) {
      constexpr int NSL = decltype(NSL_)::value, PB = decltype(PB_)::value, PN = decltype(PN_)::value;
#pragma nounroll
      for (int u = 0; u < NSL - 5; ++u) {
        wait_vm<4 * PB>();
        bar();
        issue(PB_, BOFF_, src, u + 5);
      }
      for_n<5>([&](auto M) {
        constexpr int m = decltype(M)::value;
        wait_vm<(4 - m) * PB + m * PN>();
        bar();
        if constexpr (PN > 0) issue(PN_, BOFFN_, srcn, m);
      });
    };
    const u32 sq = OFF_WQ + tnA * (32 * 12 * 1024), so_ = OFF_WO + tnB * (16 * 8 * 1024);
    const u32 s1 = OFF_W1 + tnA * (32 * 14 * 1024), s2 = OFF_W2 + tnB * (56 * 8 * 1024);
#pragma nounroll
    for (int u = 0; u < 5; ++u) issue(IC<12>{}, IC<9216>{}, sq, u);
#pragma nounroll
    for (int pair = 0; pair < 2; ++pair) {
      phase(IC<32>{}, IC<12>{}, IC<9216>{}, sq, IC<8>{}, IC<16384>{}, so_);
      bar();
      phase(IC<16>{}, IC<8>{}, IC<16384>{}, so_, IC<14>{}, IC<8192>{}, s1);
      phase(IC<32>{}, IC<14>{}, IC<8192>{}, s1, IC<8>{}, IC<16384>{}, s2);
      if (pair == 0) phase(IC<56>{}, IC<8>{}, IC<16384>{}, s2, IC<12>{}, IC<9216>{}, sq);
      else phase(IC<56>{}, IC<8>{}, IC<16384>{}, s2, IC<0>{}, IC<0>{}, 0u);
    }
  } else {
    FRESH_LANE();
    const int l128 = (wid - 6) * 64 + lane;
    auto ln_phase = [&](bool qkv, int xi) {
      bar();
      f32x2 sv[2][16];
      const int tmh = tmA > 0 ? tmA - 1 : 0;
#pragma unroll
      for (int p = 0; p < 16; ++p) {
        sv[0][p] = __builtin_bit_cast(f32x2, __builtin_amdgcn_raw_buffer_load_b64(
            rs, l128 * 8, OFF_ST + xi * 262144 + (tmA * 16 + p) * 1024, 16));
        sv[1][p] = __builtin_bit_cast(f32x2, __builtin_amdgcn_raw_buffer_load_b64(
            rs, (112 + r) * 8, OFF_ST + xi * 262144 + ((qkv ? tmh : tmA) * 16 + p) * 1024, 16));
      }
      float cv[2][4], bv[2];
#pragma unroll
      for (int h = 0; h < 2; ++h) {
        int t = l128 + 128 * h;
        const int nt = qkv ? 192 : 224;
        t = t < nt ? t : nt - 1;
        const int f = t >> 4, rho = t & 15;
        int col;
        if (qkv) {
          const int hh = f / 6, fj = f % 6;
          col = (fj >> 1) * DM + (2 * tnA + hh) * 32 + 8 * (rho >> 2) + 4 * (fj & 1) + (rho & 3);
        } else {
          const int wn = f / 7, j = f % 7;
          col = tnA * 224 + (j == 6 ? 96 + 16 * wn + rho : (4 * wn + (j >> 1)) * 32 + 8 * (rho >> 2) + 4 * (j & 1) + (rho & 3));
        }
        const float* csp = (const float*)(ws + (qkv ? OFF_CSQ : OFF_CS1));
#pragma unroll
        for (int p = 0; p < 4; ++p) cv[h][p] = csp[p * (qkv ? 3 * DM : FF) + col];
        bv[h] = bf_lo(b1[qkv ? 0 : col]);
      }
      bar(); bar(); bar(); bar();
#pragma unroll
      for (int h = 0; h < 2; ++h) {
        float mean = 0.f;
#pragma unroll
        for (int p = 0; p < 16; ++p) mean += sv[h][p][0];
        mean *= (1.f / 16.f);
        float m2 = 0.f;
#pragma unroll
        for (int p = 0; p < 16; ++p) { const float d = sv[h][p][0] - mean; m2 += sv[h][p][1] + 64.f * d * d; }
        const float mb = bfr(mean), vb = bfr(m2 * (1.f / DM));
        const float iv = 1.f / bfr(sqrtf(bfr(vb + LN_EPS)));
        const int t = h == 0 ? 16 + l128 : r;
        if (h == 0 || l128 < 16) {
          ((float*)(smem + L_INV))[t] = iv;
          ((float*)(smem + L_MNV))[t] = mb * iv;
        }
        const int tc = l128 + 128 * h;
        if (tc < 224) {
          ((float*)(smem + L_CS))[tc] = (cv[h][0] + cv[h][1]) + (cv[h][2] + cv[h][3]);
          ((float*)(smem + L_BS))[tc] = bv[h];
        }
      }
      wait_lgkm0();
#pragma nounroll
      for (int u = 5; u < 32; ++u) bar();
    };
#pragma nounroll
    for (int pair = 0; pair < 2; ++pair) {
      ln_phase(true, 2 * pair);
      bar();
#pragma nounroll
      for (int u = 0; u < 16; ++u) bar();
      ln_phase(false, 2 * pair + 1);
#pragma nounroll
      for (int u = 0; u < 56; ++u) bar();
    }
  }
#endif
}

extern "C" void kernel_launch(void* const* d_in, const int* in_sizes, int n_in, void* d_out, int out_size,
                              void* d_ws, size_t ws_size, hipStream_t stream) {
  const u16* x0 = (const u16*)d_in[0];
  const u16* wqkv = (const u16*)d_in[1];
  const u16* wo = (const u16*)d_in[2];
  const u16* w1 = (const u16*)d_in[4];
  const u16* b1 = (const u16*)d_in[5];
  const u16* w2 = (const u16*)d_in[6];
  const u16* b2 = (const u16*)d_in[7];
  prep_k<<<PB_Q + PB_X + PB_O + PB_1 + PB_2, 256, 0, stream>>>(x0, wqkv, wo, w1, w2, (char*)d_ws);
  (void)hipFuncSetAttribute((const void*)mega_k, hipFuncAttributeMaxDynamicSharedMemorySize, LDS_BYTES);
  mega_k<<<256, 512, LDS_BYTES, stream>>>((char*)d_ws, b1, b2, (u16*)d_out);
}
```

```cpp
#include <hip/hip_runtime.h>
#include <stdint.h>
#include <type_traits>
#include <utility>

typedef unsigned short u16;
typedef unsigned int u32;
typedef __bf16 bf16x8 __attribute__((ext_vector_type(8)));
typedef __bf16 bf16x2 __attribute__((ext_vector_type(2)));
typedef short s16x4 __attribute__((ext_vector_type(4)));
typedef float f32x4 __attribute__((ext_vector_type(4)));
typedef float f32x2 __attribute__((ext_vector_type(2)));
typedef u32 u32x4 __attribute__((ext_vector_type(4)));
typedef u32 u32x2 __attribute__((ext_vector_type(2)));
typedef int i32x4 __attribute__((ext_vector_type(4)));
typedef int i32x2 __attribute__((ext_vector_type(2)));
#define LDS_AS __attribute__((address_space(3)))
#define GLB_AS __attribute__((address_space(1)))

constexpr int SEQ = 2048, DM = 1024, FF = 3584;
constexpr float LN_EPS = 1e-5f;

#define SKIP 1
constexpr u32 MB = 1u << 20;
constexpr u32 QS = SKIP ? 5120 : 4096;
constexpr u32 XS = 5 * MB, HS = 18 * MB;
constexpr u32 OFF_WQ = 0;
constexpr u32 OFF_WO = 8 * MB;
constexpr u32 OFF_W1 = 11 * MB;
constexpr u32 OFF_W2 = 21 * MB;
constexpr u32 OFF_X = 30 * MB;
constexpr u32 OFF_O = OFF_X + 4 * XS;
constexpr u32 OFF_H = OFF_O + 2 * XS;
constexpr u32 OFF_ST = OFF_H + 2 * HS;
constexpr u32 OFF_CSQ = OFF_ST + MB;
constexpr u32 OFF_CS1 = OFF_CSQ + 65536;
constexpr u32 OFF_FL = OFF_CS1 + 65536;
constexpr u32 OFF_DBG = OFF_FL + 7 * 32768;
constexpr u32 WS_USED = OFF_ST + 2 * MB;
__device__ __forceinline__ u32 qoff(u32 e, u32 slot) {
  const u32 n = 8 * e + (slot >> 3);
  return ((n + (SKIP ? (((n + 4) * 37) >> 8) : 0)) << 7) + (slot & 7) * 16;
}
__device__ __forceinline__ u32 foff(u32 f, u32 slot) { return (f >> 2) * QS + qoff(f & 3, slot); }

constexpr int SLOT = 24576, NRING = 6, RING = SLOT * NRING;
constexpr int L_INV = RING, L_MNV = L_INV + 576, L_CS = L_MNV + 576, L_BS = L_CS + 896, L_CNT = L_BS + 896;
constexpr int L_XC = RING + 3072;
constexpr int LDS_BYTES = L_XC + 6144;

__device__ __forceinline__ float bf_lo(u32 w) { return __uint_as_float(w << 16); }
__device__ __forceinline__ float bf_hi(u32 w) { return __uint_as_float(w & 0xffff0000u); }
__device__ __forceinline__ u32 pack2(float lo, float hi) {
  f32x2 v = {lo, hi};
  bf16x2 r = __builtin_convertvector(v, bf16x2);
  return __builtin_bit_cast(u32, r);
}
__device__ __forceinline__ float bfr(float x) { return bf_lo(pack2(x, 0.f) & 0xffffu); }
__device__ __forceinline__ u32 gelu_tanh2(f32x2 x) {
  const float c0 = -2.3022081983f, c1 = -0.1029432396f;
  const f32x2 p = x * x * c1 + c0;
  const f32x2 a = x * p;
  const f32x2 d = (f32x2){__builtin_amdgcn_exp2f(a[0]), __builtin_amdgcn_exp2f(a[1])} + 1.f;
  const f32x2 g = x * (f32x2){__builtin_amdgcn_rcpf(d[0]), __builtin_amdgcn_rcpf(d[1])};
  return pack2(g[0], g[1]);
}
template <int N_> __device__ __forceinline__ void wait_vm() { asm volatile("s_waitcnt vmcnt(%0)" ::"n"(N_) : "memory"); }
__device__ __forceinline__ void wait_lgkm0() { asm volatile("s_waitcnt lgkmcnt(0)" ::: "memory"); }
__device__ __forceinline__ void bar() {
  __builtin_amdgcn_sched_barrier(0);
  asm volatile("s_barrier" ::: "memory");
  __builtin_amdgcn_sched_barrier(0);
}
template <int V> using IC = std::integral_constant<int, V>;
template <typename F, int... I>
__device__ __forceinline__ void for_seq(F&& f, std::integer_sequence<int, I...>) { (f(IC<I>{}), ...); }
template <int N, typename F> __device__ __forceinline__ void for_n(F&& f) { for_seq(f, std::make_integer_sequence<int, N>{}); }

__device__ __forceinline__ float xsum(float x) {
#if defined(__HIP_DEVICE_COMPILE__)
  auto a = __builtin_amdgcn_permlane16_swap(__float_as_uint(x), __float_as_uint(x), false, false);
  x = __uint_as_float(a[0]) + __uint_as_float(a[1]);
  auto b = __builtin_amdgcn_permlane32_swap(__float_as_uint(x), __float_as_uint(x), false, false);
  x = __uint_as_float(b[0]) + __uint_as_float(b[1]);
#endif
  return x;
}
__device__ __forceinline__ float xmax(float x) {
#if defined(__HIP_DEVICE_COMPILE__)
  auto a = __builtin_amdgcn_permlane16_swap(__float_as_uint(x), __float_as_uint(x), false, false);
  x = fmaxf(__uint_as_float(a[0]), __uint_as_float(a[1]));
  auto b = __builtin_amdgcn_permlane32_swap(__float_as_uint(x), __float_as_uint(x), false, false);
  x = fmaxf(__uint_as_float(b[0]), __uint_as_float(b[1]));
#endif
  return x;
}
__device__ __forceinline__ f32x2 row_part(const u32x4& c0, const u32x4& c1) {
  float v[16];
#pragma unroll
  for (int k = 0; k < 4; ++k) {
    v[2 * k] = bf_lo(c0[k]); v[2 * k + 1] = bf_hi(c0[k]);
    v[8 + 2 * k] = bf_lo(c1[k]); v[8 + 2 * k + 1] = bf_hi(c1[k]);
  }
  float s = 0.f;
#pragma unroll
  for (int k = 0; k < 16; ++k) s += v[k];
  const float m = xsum(s) * (1.f / 64.f);
  float q = 0.f;
#pragma unroll
  for (int k = 0; k < 16; ++k) { const float d = v[k] - m; q += d * d; }
  return (f32x2){m, xsum(q)};
}

constexpr int PB_Q = 192, PB_X = 256, PB_O = 64, PB_1 = 224, PB_2 = 224;
__global__ __launch_bounds__(256) void prep_k(const u16* __restrict__ x, const u16* __restrict__ wqkv,
                                              const u16* __restrict__ wo, const u16* __restrict__ w1,
                                              const u16* __restrict__ w2, char* __restrict__ ws) {
#if defined(__HIP_DEVICE_COMPILE__)
  __shared__ __attribute__((aligned(16))) char img[4 * 8192];
  __shared__ float red[4][64];
  const int tid = threadIdx.x, lane = tid & 63, wid = __builtin_amdgcn_readfirstlane(tid >> 6);
  const int r = lane & 15, g = lane >> 4;
  int b = blockIdx.x;
#define PSTAMP
  if (b >= PB_Q && b < PB_Q + PB_X) {
    b -= PB_Q;
    const int tm = b >> 4, tn = b & 15;
    if (tid < 224) ((u32*)(ws + OFF_FL))[b * 224 + tid] = 0u;
    u32x4 c[2][2];
#pragma unroll
    for (int i = 0; i < 2; ++i)
#pragma unroll
      for (int gl = 0; gl < 2; ++gl)
        c[i][gl] = *(const u32x4*)(x + (size_t)(tm * 128 + (2 * wid + i) * 16 + r) * DM + tn * 64 + gl * 32 + g * 8);
#pragma unroll
    for (int i = 0; i < 2; ++i)
#pragma unroll
      for (int gl = 0; gl < 2; ++gl)
        *(u32x4*)(ws + OFF_X + foff((tm * 32 + 2 * tn + gl) * 8 + 2 * wid + i, lane)) = c[i][gl];
    const f32x2 s0 = row_part(c[0][0], c[0][1]), s1 = row_part(c[1][0], c[1][1]);
    if (lane < 32) *(f32x2*)(ws + OFF_ST + ((tm * 16 + tn) * 128 + wid * 32 + lane) * 8) = g ? s1 : s0;
    PSTAMP;
    return;
  }
  int which, N, cb, kq;
  const u16* W;
  if (b < PB_Q) { which = 0; W = wqkv; N = 3 * DM; cb = b % 48; kq = b / 48; }
  else {
    b -= PB_Q + PB_X;
    if (b < PB_O) { which = 1; W = wo; N = DM; cb = b & 15; kq = b >> 4; }
    else if (b < PB_O + PB_1) { b -= PB_O; which = 2; W = w1; N = FF; cb = b % 56; kq = b / 56; }
    else { b -= PB_O + PB_1; which = 3; W = w2; N = DM; cb = b & 15; kq = b >> 4; }
  }
  const int kt = kq * 4 + wid;
  char* im = img + wid * 8192;
  {
    u32x4 v[8];
#pragma unroll
    for (int e = 0; e < 8; ++e)
      v[e] = *(const u32x4*)(W + (size_t)(kt * 64 + e * 8 + (lane >> 3)) * N + cb * 64 + (lane & 7) * 8);
#pragma unroll
    for (int e = 0; e < 8; ++e) *(u32x4*)(im + e * 1024 + lane * 16) = v[e];
  }
  const int q = r >> 2, pp = r & 3;
#pragma unroll
  for (int gh = 0; gh < 2; ++gh) {
    const int gam = cb * 2 + gh;
    bool split = false;
    u32 dst, step, tstep = 1, reg;
    if (which == 0) {
      const int part = gam >> 5, head = gam & 31;
      reg = OFF_WQ; dst = (head >> 1) * 32 * 12 + (head & 1) * 6 + part * 2; step = 12;
    } else if (which == 1) {
      reg = OFF_WO; dst = (gam >> 1) * 32 * 4 + (gam & 1) * 2; step = 4;
    } else if (which == 2) {
      const int tn = gam / 7, gl = gam % 7;
      split = gl == 3;
      reg = OFF_W1; dst = tn * 32 * 16 + (split ? 6 : (gl < 3 ? gl * 2 : 7 + (gl - 4) * 2)); step = 16;
      if (split) tstep = 7;
    } else {
      reg = OFF_W2; dst = (gam >> 1) * 112 * 4 + (gam & 1) * 2; step = 4;
    }
#pragma unroll
    for (int t = 0; t < 2; ++t) {
      const int cbyte = gh * 64 + (split ? t * 32 + pp * 8 : pp * 16 + t * 8);
      float sum = 0.f;
#pragma unroll
      for (int s = 0; s < 2; ++s) {
        const char* p = im + (32 * s + 8 * g + q) * 128 + cbyte;
        const s16x4 lo = __builtin_amdgcn_ds_read_tr16_b64_v4i16((LDS_AS s16x4*)p);
        const s16x4 hi = __builtin_amdgcn_ds_read_tr16_b64_v4i16((LDS_AS s16x4*)(p + 512));
        const u32x2 l2 = __builtin_bit_cast(u32x2, lo), h2 = __builtin_bit_cast(u32x2, hi);
        const u32x4 f = {l2[0], l2[1], h2[0], h2[1]};
        *(u32x4*)(ws + reg + foff(dst + (2 * kt + s) * step + t * tstep, lane)) = f;
#pragma unroll
        for (int k = 0; k < 4; ++k) sum += bf_lo(f[k]) + bf_hi(f[k]);
      }
      sum = xsum(sum);
      if (g == 0) red[wid][gh * 32 + (split ? 16 * t + r : 8 * q + 4 * t + pp)] = sum;
    }
  }
  if (which == 0 || which == 2) {
    __syncthreads();
    if (tid < 64)
      ((float*)(ws + (which == 0 ? OFF_CSQ : OFF_CS1)))[kq * N + cb * 64 + tid] =
          (red[0][tid] + red[1][tid]) + (red[2][tid] + red[3][tid]);
  }
  PSTAMP;
#endif
}

__device__ __forceinline__ int fresh_lane(int tid) { int l = tid & 63; asm volatile("" : "+v"(l)); return l; }
__device__ __forceinline__ f32x4 zero4() {
  f32x4 z = {0.f, 0.f, 0.f, 0.f};
  asm volatile("" : "+v"(z));
  return z;
}
__device__ __forceinline__ void next_slot(int& so) { so += SLOT; if (so == RING) so = 0; }

#define DMA __builtin_amdgcn_raw_ptr_buffer_load_lds
#define SB() __builtin_amdgcn_sched_barrier(0)
__device__ __forceinline__ void mfma(f32x4& c, const bf16x8& a, const bf16x8& b) {
  asm volatile("v_mfma_f32_16x16x32_bf16 %0, %1, %2, %0" : "+v"(c) : "v"(a), "v"(b));
}
template <int OFF> __device__ __forceinline__ void dsrd(bf16x8& d, u32 addr) {
  asm volatile("ds_read_b128 %0, %1 offset:%2" : "=v"(d) : "v"(addr), "n"(OFF));
}
__device__ __forceinline__ void pin2(u32& a, u32& b) { asm volatile("" : "+v"(a), "+v"(b)); }
template <int NSL, int NM, int NR, class AD, class RD, class MM>
__device__ __forceinline__ void kloop(int& so, AD&& ad, RD&& rd, MM&& mm) {
  ad(so);
  bar();
  for_n<NR>([&](auto Q) { rd(IC<0>{}, Q); });
  next_slot(so);
  ad(so);
  auto body = [&](auto S) {
    constexpr int s = decltype(S)::value;
    wait_lgkm0();
    bar();
    for_n<NM>([&](auto M) {
      constexpr int m = decltype(M)::value;
      mm(S, M);
      if constexpr (m < NR) rd(IC<1 - s>{}, IC<m>{});
      if constexpr (m == NR) { next_slot(so); asm volatile("" : "+s"(so)); }
      if constexpr (m == NR + 1) ad(so);
      SB();
    });
  };
#pragma nounroll
  for (int u = NSL / 2 - 1; u > 0; --u) {
    body(IC<0>{});
    body(IC<1>{});
  }
  body(IC<0>{});
  wait_lgkm0();
  SB();
  for_n<NM>([&](auto M) { mm(IC<1>{}, M); });
  asm volatile("s_nop 15");
  SB();
}

#define STAMP(w, i)

__global__ __launch_bounds__(512, 2) void mega_k(char* __restrict__ ws, const u16* __restrict__ b1,
                                                 const u16* __restrict__ b2, u16* __restrict__ out) {
#if defined(__HIP_DEVICE_COMPILE__)
  extern __shared__ __attribute__((aligned(16))) char smem[];
  const int tid = threadIdx.x;
  const int wid = __builtin_amdgcn_readfirstlane(tid >> 6);
#define FRESH_LANE() const int lane = fresh_lane(tid), r = lane & 15, g = lane >> 4; (void)r; (void)g
  const int xcd = blockIdx.x & 7, idx = blockIdx.x >> 3;
#define STAGMODE 0
#if STAGMODE == 2
  const int tmA = (xcd >> 2) * 4 + ((idx >> 2) & 3) + 8 * (idx >> 4), tnA = (xcd & 3) * 4 + (idx & 3);
  const int tmB = (xcd >> 1) * 2 + ((idx >> 3) & 1) + 8 * (idx >> 4), tnB = (xcd & 1) * 8 + (idx & 7);
#else
  const int tmA = (xcd >> 2) * 8 + (idx >> 2), tnA = (xcd & 3) * 4 + (idx & 3);
  const int tmB = (xcd >> 1) * 4 + (idx >> 3), tnB = (xcd & 1) * 8 + (idx & 7);
#endif
  const __amdgpu_buffer_rsrc_t rs = __builtin_amdgcn_make_buffer_rsrc((void*)ws, 0, WS_USED, 0x00020000);
  if (tid == 0) *(LDS_AS u32*)(smem + L_CNT) = 0u;
  wait_lgkm0();
  int so = 0;

  auto a_phase = [&](auto NSL_, auto QPS_, auto HALO_, u32 src, u32 hsrc, int ph  ,
                     int tm, int tmh) {
    constexpr int NSL = decltype(NSL_)::value, QPS = decltype(QPS_)::value, HALO = decltype(HALO_)::value;
    constexpr int PT = 8 + HALO, D = 5;
    FRESH_LANE();
    u32 vo[4];
#pragma unroll
    for (int e = 0; e < 4; ++e) vo[e] = qoff(e, lane) - e * 1024;
    if (ph >= 0) {
      const GLB_AS u32* f = (const GLB_AS u32*)(ws + OFF_FL + ((ph * 16 + ((lane & 16) ? tmh : tm)) * 16 + r) * 128);
#pragma nounroll
      for (int spin = 0; spin < (1 << 22); ++spin) {
        const u32 v = __hip_atomic_load(f, __ATOMIC_RELAXED, __HIP_MEMORY_SCOPE_AGENT);
        if (__all(v != 0u)) break;
        __builtin_amdgcn_s_sleep(1);
      }
    }
    if (wid == 4) STAMP(1, 4 * (ph + 1));
    const int half = wid == 4 ? 0 : 8192;
    auto issue = [&](int u) {
      for_n<2>([&](auto Q) {
        constexpr int q4 = decltype(Q)::value;
        for_n<4>([&](auto E) {
          DMA(rs, (LDS_AS void*)(smem + so + half + q4 * 4096), 16, vo[decltype(E)::value],
              src + u * (QPS * QS) + q4 * QS, decltype(E)::value * 1024, 16);
        });
      });
      if constexpr (HALO) DMA(rs, (LDS_AS void*)(smem + so + 8192), 16, vo[3], hsrc + u * (2 * QS), 0, 16);
      next_slot(so);
    };
#pragma nounroll
    for (int u = 0; u < D; ++u) issue(u);
#pragma nounroll
    for (int u = 0; u < NSL - D; ++u) {
      wait_vm<(D - 1) * PT>();
      bar();
      issue(u + D);
    }
    for_n<D>([&](auto M) { wait_vm<(D - 1 - decltype(M)::value) * PT>(); bar(); });
  };

  if (wid < 4) {
#define st16(v, off) __builtin_amdgcn_raw_buffer_store_b128(__builtin_bit_cast(i32x4, v), rs, (off), 0, 16)
    auto publish = [&](int ph, int tm, int tn) {
      FRESH_LANE();
      wait_vm<0>();
      u32 old = 0;
      if (lane == 0) old = __hip_atomic_fetch_add((LDS_AS u32*)(smem + L_CNT), 1u, __ATOMIC_RELAXED, __HIP_MEMORY_SCOPE_WORKGROUP);
      old = __builtin_amdgcn_readfirstlane(old);
      if ((old & 3) == 3 && lane == 0)
        __hip_atomic_store((GLB_AS u32*)(ws + OFF_FL + ((ph * 16 + tm) * 16 + tn) * 128), 1u, __ATOMIC_RELAXED,
                           __HIP_MEMORY_SCOPE_AGENT);
    };
    const float* t_inv = (const float*)(smem + L_INV);
    const float* t_mnv = (const float*)(smem + L_MNV);
    const float* t_cs = (const float*)(smem + L_CS);
    const float* t_bs = (const float*)(smem + L_BS);
    const int wmi = wid >> 1, wni = wid & 1;

    auto res_phase = [&](auto NSL_, u32 roff, int xo  , bool bias, int ph) {
      constexpr int NSL = decltype(NSL_)::value;
      FRESH_LANE();
      u32x4 res[2][2], bb[2];
#pragma unroll
      for (int i = 0; i < 2; ++i)
#pragma unroll
        for (int gl = 0; gl < 2; ++gl)
          res[i][gl] = __builtin_bit_cast(u32x4, __builtin_amdgcn_raw_buffer_load_b128(
              rs, qoff((wid & 1) * 2 + i, lane), roff + ((tmB * 32 + 2 * tnB + gl) * 2 + (wid >> 1)) * QS, 16));
#pragma unroll
      for (int gl = 0; gl < 2; ++gl) {
        bb[gl] = *(const u32x4*)(b2 + tnB * 64 + gl * 32 + g * 8);
        if (!bias) bb[gl] = (u32x4){0u, 0u, 0u, 0u};
      }
      f32x4 acc[2][4];
#pragma unroll
      for (int i = 0; i < 2; ++i)
#pragma unroll
        for (int j = 0; j < 4; ++j) acc[i][j] = zero4();
      bf16x8 fA[2][2][2], fB[2][2][4];
      u32 pA, pB;
      kloop<NSL, 16, 12>(so,
        [&](int s_) { pA = lane * 16 + wid * 2048 + s_; pB = lane * 16 + s_; pin2(pA, pB); },
        [&](auto SET, auto Q) {
          constexpr int set = decltype(SET)::value, q = decltype(Q)::value, s = q / 6, m = q % 6;
          if constexpr (m < 2) dsrd<s * 8192 + m * 1024>(fA[set][s][m], pA);
          else dsrd<16384 + s * 4096 + (m - 2) * 1024>(fB[set][s][m - 2], pB);
        },
        [&](auto SET, auto M) {
          constexpr int set = decltype(SET)::value, m = decltype(M)::value, s = m >> 3, j = (m >> 1) & 3, i = m & 1;
          mfma(acc[i][j], fB[set][s][j], fA[set][s][i]);
        });
      STAMP(0, 4 * ph + 2);
      u32x4 c[2][2];
#pragma unroll
      for (int i = 0; i < 2; ++i)
#pragma unroll
        for (int gl = 0; gl < 2; ++gl) {
#pragma unroll
          for (int k = 0; k < 4; ++k) {
            const f32x4& a = acc[i][2 * gl + (k >> 1)];
            const float y0 = a[2 * (k & 1)] + bf_lo(bb[gl][k]), y1 = a[2 * (k & 1) + 1] + bf_hi(bb[gl][k]);
            c[i][gl][k] = pack2(bf_lo(res[i][gl][k]) + y0, bf_hi(res[i][gl][k]) + y1);
          }
          if (xo < 4) st16(c[i][gl], OFF_X + xo * XS + ((tmB * 32 + 2 * tnB + gl) * 2 + (wid >> 1)) * QS + qoff((wid & 1) * 2 + i, lane));
          else *(u32x4*)(out + (size_t)(tmB * 128 + (2 * wid + i) * 16 + r) * DM + tnB * 64 + gl * 32 + g * 8) = c[i][gl];
        }
      if (xo < 4) {
        const f32x2 s0 = row_part(c[0][0], c[0][1]), s1 = row_part(c[1][0], c[1][1]);
        if (lane < 32)
          __builtin_amdgcn_raw_buffer_store_b64(__builtin_bit_cast(i32x2, g ? s1 : s0), rs, lane * 8,
                                                OFF_ST + xo * 262144 + ((tmB * 16 + tnB) * 128 + wid * 32) * 8, 16);
        publish(ph, tmB, tnB);
      }
      STAMP(0, 4 * ph + 3);
    };

#pragma nounroll
    for (int pair = 0; pair < 2; ++pair) {
      {
        f32x4 acc[4][6], acch[2];
        {
        FRESH_LANE();
        STAMP(0, 16 * pair);
        u32 pA, pB;
        auto loop = [&](auto WM) {
          constexpr int wm = decltype(WM)::value;
#pragma unroll
          for (int i = 0; i < 4; ++i)
#pragma unroll
            for (int j = 0; j < 6; ++j) acc[i][j] = zero4();
          acch[0] = zero4(); acch[1] = zero4();
          bf16x8 fA[2][4], fH[2], fB[2][6];
          kloop<32, 26, 11>(so,
            [&](int s_) { pA = lane * 16 + s_; pB = lane * 16 + wni * 6144 + s_; pin2(pA, pB); },
            [&](auto SET, auto Q) {
              constexpr int set = decltype(SET)::value, q = decltype(Q)::value;
              if constexpr (q < 4) dsrd<wm * 4096 + q * 1024>(fA[set][q], pA);
              else if constexpr (q < 10) dsrd<9216 + (q - 4) * 1024>(fB[set][q - 4], pB);
              else dsrd<8192>(fH[set], pA);
            },
            [&](auto SET, auto M) {
              constexpr int set = decltype(SET)::value, m = decltype(M)::value, j = m >> 2, i = m & 3;
              if constexpr (m < 16) mfma(acc[i][j], fB[set][j], fA[set][i]);
              else if constexpr (m < 24) mfma(acc[i][j], fA[set][i], fB[set][j]);
              else if constexpr (wm == 0) mfma(acch[i], fB[set][2 + i], fH[set]);
              else mfma(acch[i], fH[set], fB[set][4 + i]);
            });
        };
        if (wmi == 0) loop(IC<0>{}); else loop(IC<1>{});
        }
        FRESH_LANE();
        STAMP(0, 16 * pair + 2);
        const float* csw = t_cs + wni * 96;
        auto fix_qk = [&](const f32x4& a0, const f32x4& a1, int trow, int f) {
          const float iv = t_inv[trow + r], mv = t_mnv[trow + r];
          const f32x4 c0 = *(const f32x4*)(csw + f * 16 + 4 * g), c1 = *(const f32x4*)(csw + f * 16 + 16 + 4 * g);
          const f32x4 y0 = a0 * iv - c0 * mv, y1 = a1 * iv - c1 * mv;
          return (u32x4){pack2(y0[0], y0[1]), pack2(y0[2], y0[3]), pack2(y1[0], y1[1]), pack2(y1[2], y1[3])};
        };
        auto fix_v = [&](const f32x4& a0, const f32x4& a1, int trow) {
          const f32x4 iv = *(const f32x4*)(t_inv + trow + 4 * g), mv = *(const f32x4*)(t_mnv + trow + 4 * g);
          const float c0 = csw[64 + r], c1 = csw[80 + r];
          const f32x4 y0 = a0 * iv - mv * c0, y1 = a1 * iv - mv * c1;
          return (u32x4){pack2(y0[0], y0[1]), pack2(y0[2], y0[3]), pack2(y1[0], y1[1]), pack2(y1[2], y1[3])};
        };
        u32x4 Qf[4], Kf[5], Vf[5];
        char* xc = smem + L_XC + wni * 3072 + lane * 16;
        const int row0 = 16 + wmi * 64;
        Kf[4] = fix_qk(acc[3][2], acc[3][3], row0 + 48, 2);
        Vf[4] = fix_v(acc[3][4], acc[3][5], row0 + 48);
        if (wmi == 0) {
          *(u32x4*)xc = Kf[4];
          *(u32x4*)(xc + 1024) = Vf[4];
          Kf[0] = fix_qk(acch[0], acch[1], 0, 2);
        } else {
          *(u32x4*)(xc + 2048) = fix_v(acch[0], acch[1], 0);
        }
#pragma unroll
        for (int i = 0; i < 3; ++i) {
          Kf[i + 1] = fix_qk(acc[i][2], acc[i][3], row0 + 16 * i, 2);
          Vf[i + 1] = fix_v(acc[i][4], acc[i][5], row0 + 16 * i);
        }
        wait_lgkm0();
        bar();
        if (wmi == 1) Kf[0] = *(const u32x4*)xc;
        Vf[0] = *(const u32x4*)(xc + (wmi == 0 ? 2048 : 1024));
#pragma unroll
        for (int i = 0; i < 4; ++i) Qf[i] = fix_qk(acc[i][0], acc[i][1], row0 + 16 * i, 0);
        const bool par = r & 1;
        const float m0 = par ? 0.f : 1.f, m1 = par ? 1.f : 0.f;
        const bool own0 = 4 * g + (r & 1) <= r, own1 = 4 * g + (r & 1) + 2 <= r;
        const bool first = (tmA == 0) && (wmi == 0);
#pragma unroll
        for (int ii = 0; ii < 4; ++ii) {
          const int i = 3 - ii;
          const f32x4 z = {0.f, 0.f, 0.f, 0.f};
          const f32x4 S0 = __builtin_amdgcn_mfma_f32_16x16x32_bf16(__builtin_bit_cast(bf16x8, Kf[i]), __builtin_bit_cast(bf16x8, Qf[i]), z, 0, 0, 0);
          const f32x4 S1 = __builtin_amdgcn_mfma_f32_16x16x32_bf16(__builtin_bit_cast(bf16x8, Kf[i + 1]), __builtin_bit_cast(bf16x8, Qf[i]), z, 0, 0, 0);
          float l0 = own0 ? (par ? S1[1] : S1[0]) : (par ? S0[1] : S0[0]);
          float l1 = own1 ? (par ? S1[3] : S1[2]) : (par ? S0[3] : S0[2]);
          const float c = 0.17677669529663687f * 1.4426950408889634f;
          l0 *= c; l1 *= c;
          if (i == 0 && first) {
            if (!own0) l0 = -1.4426950e9f;
            if (!own1) l1 = -1.4426950e9f;
          }
          const float mx = xmax(fmaxf(l0, l1));
          const float p0 = __builtin_amdgcn_exp2f(l0 - mx), p1 = __builtin_amdgcn_exp2f(l1 - mx);
          const float rsum = __builtin_amdgcn_rcpf(xsum(p0 + p1));
          const float q0 = p0 * rsum, q1 = p1 * rsum;
          const u32 w0 = pack2(q0 * m0, q0 * m1), w1 = pack2(q1 * m0, q1 * m1);
          const u32x4 P = {own0 ? 0u : w0, own1 ? 0u : w1, own0 ? w0 : 0u, own1 ? w1 : 0u};
          const u32x4 V0 = {Vf[i][0], Vf[i][1], Vf[i + 1][0], Vf[i + 1][1]};
          const u32x4 V1 = {Vf[i][2], Vf[i][3], Vf[i + 1][2], Vf[i + 1][3]};
          const f32x4 O0 = __builtin_amdgcn_mfma_f32_16x16x32_bf16(__builtin_bit_cast(bf16x8, V0), __builtin_bit_cast(bf16x8, P), z, 0, 0, 0);
          const f32x4 O1 = __builtin_amdgcn_mfma_f32_16x16x32_bf16(__builtin_bit_cast(bf16x8, V1), __builtin_bit_cast(bf16x8, P), z, 0, 0, 0);
          const u32x4 o = {pack2(O0[0], O0[1]), pack2(O0[2], O0[3]), pack2(O1[0], O1[1]), pack2(O1[2], O1[3])};
          st16(o, OFF_O + pair * XS + ((tmA * 32 + 2 * tnA + wni) * 2 + wmi) * QS + qoff(i, lane));
        }
        publish(4 * pair, tmA, tnA);
        STAMP(0, 16 * pair + 3);
      }
      res_phase(IC<16>{}, OFF_X + (2 * pair) * XS, 2 * pair + 1, false, 4 * pair + 1);
      {
        f32x4 acc[4][7];
        {
        FRESH_LANE();
#pragma unroll
        for (int i = 0; i < 4; ++i)
#pragma unroll
          for (int j = 0; j < 7; ++j) acc[i][j] = zero4();
        bf16x8 fA[2][4], fB[2][7];
        u32 pA, pB;
        kloop<32, 28, 11>(so,
          [&](int s_) { pA = lane * 16 + wmi * 4096 + s_; pB = lane * 16 + wni * 7168 + s_; pin2(pA, pB); },
          [&](auto SET, auto Q) {
            constexpr int set = decltype(SET)::value, q = decltype(Q)::value;
            if constexpr (q < 4) dsrd<q * 1024>(fA[set][q], pA);
            else dsrd<8192 + (q - 4) * 1024>(fB[set][q - 4], pB);
          },
          [&](auto SET, auto M) {
            constexpr int set = decltype(SET)::value, m = decltype(M)::value, j = m >> 2, i = m & 3;
            mfma(acc[i][j], fB[set][j], fA[set][i]);
          });
        }
        FRESH_LANE();
        STAMP(0, 16 * pair + 10);
        float iv[4], mv[4];
#pragma unroll
        for (int i = 0; i < 4; ++i) { iv[i] = t_inv[16 + wmi * 64 + 16 * i + r]; mv[i] = t_mnv[16 + wmi * 64 + 16 * i + r]; }
        const u32 hbase = OFF_H + pair * HS + ((tmA * 112 + 7 * tnA) * 2 + wmi) * QS;
        auto act = [&](const f32x4& a, const f32x4& cs, const f32x4& bs, int i) {
          const f32x4 y = a * iv[i] + (bs - cs * mv[i]);
          return (u32x2){gelu_tanh2((f32x2){y[0], y[1]}), gelu_tanh2((f32x2){y[2], y[3]})};
        };
#pragma unroll
        for (int jp = 0; jp < 3; ++jp) {
          const int t0 = (wni * 7 + 2 * jp) * 16 + 4 * g;
          const f32x4 cs0 = *(const f32x4*)(t_cs + t0), cs1 = *(const f32x4*)(t_cs + t0 + 16);
          const f32x4 bs0 = *(const f32x4*)(t_bs + t0), bs1 = *(const f32x4*)(t_bs + t0 + 16);
#pragma unroll
          for (int i = 0; i < 4; ++i) {
            const u32x2 a = act(acc[i][2 * jp], cs0, bs0, i), b = act(acc[i][2 * jp + 1], cs1, bs1, i);
            const u32x4 v = {a[0], a[1], b[0], b[1]};
            st16(v, hbase + (wni * 4 + jp) * 2 * QS + qoff(i, lane));
          }
        }
        {
          const int t0 = (wni * 7 + 6) * 16 + 4 * g;
          const f32x4 cs0 = *(const f32x4*)(t_cs + t0), bs0 = *(const f32x4*)(t_bs + t0);
#pragma unroll
          for (int i = 0; i < 4; i += 2) {
            const u32x2 a = act(acc[i][6], cs0, bs0, i), b = act(acc[i + 1][6], cs0, bs0, i + 1);
            const auto s0 = __builtin_amdgcn_permlane16_swap(a[0], b[0], false, false);
            const auto s1 = __builtin_amdgcn_permlane16_swap(a[1], b[1], false, false);
            const u32x4 v = {s0[0], s1[0], s0[1], s1[1]};
            st16(v, hbase + 3 * 2 * QS + qoff(i + (g & 1), (2 * wni + (g >> 1)) * 16 + r));
          }
        }
        publish(4 * pair + 2, tmA, tnA);
        STAMP(0, 16 * pair + 11);
      }
      res_phase(IC<56>{}, OFF_X + (2 * pair + 1) * XS, pair == 0 ? 2 : 4, true, 4 * pair + 3);
    }
  } else if (wid == 4) {
#pragma nounroll
    for (int pair = 0; pair < 2; ++pair) {
      const int tmh = tmA > 0 ? tmA - 1 : 0;
      const u32 xq = OFF_X + 2 * pair * XS;
      a_phase(IC<32>{}, IC<2>{}, IC<1>{}, xq + tmA * (64 * QS), xq + tmh * (64 * QS) + QS + 3072, 4 * pair - 1, tmA, tmh);
      bar();
      a_phase(IC<16>{}, IC<4>{}, IC<0>{}, OFF_O + pair * XS + tmB * (64 * QS), 0, 4 * pair, tmB, tmB);
      a_phase(IC<32>{}, IC<2>{}, IC<0>{}, xq + XS + tmA * (64 * QS), 0, 4 * pair + 1, tmA, tmA);
      a_phase(IC<56>{}, IC<4>{}, IC<0>{}, OFF_H + pair * HS + tmB * (224 * QS), 0, 4 * pair + 2, tmB, tmB);
    }
  } else if (wid == 5) {
    FRESH_LANE();
    u32 vo[4];
#pragma unroll
    for (int e = 0; e < 4; ++e) vo[e] = qoff(e, lane) - e * 1024;
    auto issue = [&](auto PB_, auto BOFF_, u32 src, int u) {
      constexpr int PB = decltype(PB_)::value, BOFF = decltype(BOFF_)::value;
      wait_vm<49>();
      for_n<PB>([&](auto I) {
        constexpr int i = decltype(I)::value;
        DMA(rs, (LDS_AS void*)(smem + so + BOFF + (i >> 2) * 4096), 16, vo[i & 3],
                                                 src + u * ((PB + 3) / 4 * QS) + (i >> 2) * QS, (i & 3) * 1024, 0);
      });
      next_slot(so);
    };
    auto phase = [&](auto NSL_, auto PB_, auto BOFF_, u32 src, auto PN_, auto BOFFN_, u32 srcn) {
      constexpr int NSL = decltype(NSL_)::value, PB = decltype(PB_)::value, PN = decltype(PN_)::value;
#pragma nounroll
      for (int u = 0; u < NSL - 5; ++u) {
        wait_vm<4 * PB>();
        bar();
        issue(PB_, BOFF_, src, u + 5);
      }
      for_n<5>([&](auto M) {
        constexpr int m = decltype(M)::value;
        wait_vm<(4 - m) * PB + m * PN>();
        bar();
        if constexpr (PN > 0) issue(PN_, BOFFN_, srcn, m);
      });
    };
    const u32 sq = OFF_WQ + tnA * (32 * 3 * QS), so_ = OFF_WO + tnB * (32 * QS);
    const u32 s1 = OFF_W1 + tnA * (32 * 4 * QS), s2 = OFF_W2 + tnB * (112 * QS);
#pragma nounroll
    for (int u = 0; u < 5; ++u) issue(IC<12>{}, IC<9216>{}, sq, u);
#pragma nounroll
    for (int pair = 0; pair < 2; ++pair) {
      phase(IC<32>{}, IC<12>{}, IC<9216>{}, sq, IC<8>{}, IC<16384>{}, so_);
      bar();
      phase(IC<16>{}, IC<8>{}, IC<16384>{}, so_, IC<14>{}, IC<8192>{}, s1);
      phase(IC<32>{}, IC<14>{}, IC<8192>{}, s1, IC<8>{}, IC<16384>{}, s2);
      if (pair == 0) phase(IC<56>{}, IC<8>{}, IC<16384>{}, s2, IC<12>{}, IC<9216>{}, sq);
      else phase(IC<56>{}, IC<8>{}, IC<16384>{}, s2, IC<0>{}, IC<0>{}, 0u);
    }
  } else {
    FRESH_LANE();
    const int l128 = (wid - 6) * 64 + lane;
    auto ln_phase = [&](bool qkv, int xi) {
#pragma unroll
      for (int h = 0; h < 2; ++h) {
        int t = l128 + 128 * h;
        const int nt = qkv ? 192 : 224;
        t = t < nt ? t : nt - 1;
        const int f = t >> 4, rho = t & 15;
        int col;
        if (qkv) {
          const int hh = f / 6, fj = f % 6;
          col = (fj >> 1) * DM + (2 * tnA + hh) * 32 + 8 * (rho >> 2) + 4 * (fj & 1) + (rho & 3);
        } else {
          const int wn = f / 7, j = f % 7;
          col = tnA * 224 + (j == 6 ? 96 + 16 * wn + rho : (4 * wn + (j >> 1)) * 32 + 8 * (rho >> 2) + 4 * (j & 1) + (rho & 3));
        }
        const float* csp = (const float*)(ws + (qkv ? OFF_CSQ : OFF_CS1));
        float cv[4];
#pragma unroll
        for (int p = 0; p < 4; ++p) cv[p] = csp[p * (qkv ? 3 * DM : FF) + col];
        const float bv = bf_lo(b1[qkv ? 0 : col]);
        const int tc = l128 + 128 * h;
        if (tc < 224) {
          ((float*)(smem + L_CS))[tc] = (cv[0] + cv[1]) + (cv[2] + cv[3]);
          ((float*)(smem + L_BS))[tc] = bv;
        }
      }
      f32x2 sv[2][16];
      const int tmh = tmA > 0 ? tmA - 1 : 0;
      const u32 so0 = OFF_ST + xi * 262144 + tmA * 16384, so1 = OFF_ST + xi * 262144 + (qkv ? tmh : tmA) * 16384;
      for_n<8>([&](auto C) {
        constexpr int c = decltype(C)::value;
        bar();
#pragma unroll
        for (int p = 4 * (c & 3); p < 4 * (c & 3) + 4; ++p)
          sv[c >> 2][p] = __builtin_bit_cast(f32x2, __builtin_amdgcn_raw_buffer_load_b64(
              rs, (c < 4 ? l128 : 112 + r) * 8, (c < 4 ? so0 : so1) + p * 1024, 16));
      });
      bar(); bar(); bar(); bar();
#pragma unroll
      for (int h = 0; h < 2; ++h) {
        bar();
        float mean = 0.f;
#pragma unroll
        for (int p = 0; p < 16; ++p) mean += sv[h][p][0];
        mean *= (1.f / 16.f);
        asm volatile("" : "+v"(mean));
        float m2 = 0.f;
#pragma unroll
        for (int p = 0; p < 16; ++p) {
          if (p % 8 == 0) { asm volatile("" : "+v"(m2)); bar(); asm volatile("" : "+v"(mean)); }
          const float d = sv[h][p][0] - mean; m2 += sv[h][p][1] + 64.f * d * d;
        }
        asm volatile("" : "+v"(m2));
        bar();
        asm volatile("" : "+v"(m2));
        const float mb = bfr(mean), vb = bfr(m2 * (1.f / DM));
        float sd = bfr(sqrtf(bfr(vb + LN_EPS)));
        asm volatile("" : "+v"(sd));
        bar();
        asm volatile("" : "+v"(sd));
        const float iv = 1.f / sd;
        const int t = h == 0 ? 16 + l128 : r;
        if (h == 0 || l128 < 16) {
          ((float*)(smem + L_INV))[t] = iv;
          ((float*)(smem + L_MNV))[t] = mb * iv;
        }
      }
      wait_lgkm0();
#pragma nounroll
      for (int u = 22; u < 32; ++u) bar();
    };
#pragma nounroll
    for (int pair = 0; pair < 2; ++pair) {
      ln_phase(true, 2 * pair);
      bar();
      if (wid == 6) {
        so = pair == 0 ? 2 * SLOT : 0;
        a_phase(IC<16>{}, IC<4>{}, IC<0>{}, OFF_O + pair * XS + tmB * (64 * QS) + 2 * QS, 0, 4 * pair, tmB, tmB);
      } else {
#pragma nounroll
        for (int u = 0; u < 16; ++u) bar();
      }
      ln_phase(false, 2 * pair + 1);
      if (wid == 6) {
        so = pair == 0 ? 2 * SLOT : 0;
        a_phase(IC<56>{}, IC<4>{}, IC<0>{}, OFF_H + pair * HS + tmB * (224 * QS) + 2 * QS, 0, 4 * pair + 2, tmB, tmB);
      } else {
#pragma nounroll
        for (int u = 0; u < 56; ++u) bar();
      }
    }
  }
#endif
}

extern "C" void kernel_launch(void* const* d_in, const int* in_sizes, int n_in, void* d_out, int out_size,
                              void* d_ws, size_t ws_size, hipStream_t stream) {
  const u16* x0 = (const u16*)d_in[0];
  const u16* wqkv = (const u16*)d_in[1];
  const u16* wo = (const u16*)d_in[2];
  const u16* w1 = (const u16*)d_in[4];
  const u16* b1 = (const u16*)d_in[5];
  const u16* w2 = (const u16*)d_in[6];
  const u16* b2 = (const u16*)d_in[7];
  prep_k<<<PB_Q + PB_X + PB_O + PB_1 + PB_2, 256, 0, stream>>>(x0, wqkv, wo, w1, w2, (char*)d_ws);
  (void)hipFuncSetAttribute((const void*)mega_k, hipFuncAttributeMaxDynamicSharedMemorySize, LDS_BYTES);
  mega_k<<<256, 512, LDS_BYTES, stream>>>((char*)d_ws, b1, b2, (u16*)d_out);
}
```

```cpp
#include <hip/hip_runtime.h>
#include <stdint.h>
#include <type_traits>
#include <utility>

typedef unsigned short u16;
typedef unsigned int u32;
typedef unsigned long long u64;
typedef __bf16 bf16x8 __attribute__((ext_vector_type(8)));
typedef __bf16 bf16x2 __attribute__((ext_vector_type(2)));
typedef short s16x4 __attribute__((ext_vector_type(4)));
typedef float f32x4 __attribute__((ext_vector_type(4)));
typedef float f32x2 __attribute__((ext_vector_type(2)));
typedef u32 u32x4 __attribute__((ext_vector_type(4)));
typedef u32 u32x2 __attribute__((ext_vector_type(2)));
typedef int i32x4 __attribute__((ext_vector_type(4)));
typedef int i32x2 __attribute__((ext_vector_type(2)));
#define LDS_AS __attribute__((address_space(3)))
#define GLB_AS __attribute__((address_space(1)))

constexpr int SEQ = 2048, DM = 1024, FF = 3584;
constexpr float LN_EPS = 1e-5f;

#define SKIP 1
#define DN 4
#define DW 5
#define UAUX 2
#define PFW0 30
#define PF0 4
#define PFS 2
#define PFJ 2
#define PFM 3
#define CQP 7
#define HL 16
#define FLD 3
#define XW 30
#define XL 0
#define TOKB 3
#define XP 7
#define POLB 18
#define G0 27
#define G1 12
#define H2 4
#define PRE 1
constexpr u32 MB = 1u << 20;
constexpr u32 QS = SKIP ? 5120 : 4096;
constexpr u32 XS = 5 * MB, HS = 18 * MB;
constexpr u32 OFF_WO = 8 * MB;
constexpr u32 OFF_W1 = 11 * MB;
constexpr u32 OFF_W2 = 21 * MB;
constexpr u32 OFF_X = 30 * MB;
constexpr u32 OFF_O = OFF_X + 4 * XS;
constexpr u32 OFF_H = OFF_O + 2 * XS;
constexpr u32 OFF_ST = OFF_H + 2 * HS;
constexpr u32 OFF_CSQ = OFF_ST + MB;
constexpr u32 OFF_CS1 = OFF_CSQ + 262144;
constexpr u32 OFF_FL = OFF_CS1 + 524288;
constexpr u32 OFF_EP = OFF_FL + 12 * 32768;
constexpr u32 OFF_DBG = OFF_ST + 3 * MB;
constexpr u32 WS_USED = OFF_ST + 4 * MB;
constexpr int F_ST = 0, F_CQ = 8, F_WO = 9, F_W1 = 10, F_W2 = 11;
__device__ __forceinline__ u32 qoff(u32 e, u32 slot) {
  const u32 n = 8 * e + (slot >> 3);
  return ((n + (SKIP ? (((n + 4) * 37) >> 8) : 0)) << 7) + (slot & 7) * 16;
}
__device__ __forceinline__ u32 foff(u32 f, u32 slot) { return (f >> 2) * QS + qoff(f & 3, slot); }

constexpr int SLOT = 24576, NRING = 6, RING = SLOT * NRING;
constexpr int L_INV = RING, L_MNV = L_INV + 576, L_CNT = L_MNV + 576, L_TOK = L_CNT + 8;
constexpr int L_CSQ = RING + 1280, L_CS1 = L_CSQ + 1024, L_BS = L_CS1 + 1024;
constexpr int L_XC = L_BS + 1024;
constexpr int L_IM = L_XC + 6144;
constexpr int L_PF = L_IM + 4096;
constexpr int LDS_BYTES = L_PF + 256;

__device__ __forceinline__ float bf_lo(u32 w) { return __uint_as_float(w << 16); }
__device__ __forceinline__ float bf_hi(u32 w) { return __uint_as_float(w & 0xffff0000u); }
__device__ __forceinline__ u32 pack2(float lo, float hi) {
  f32x2 v = {lo, hi};
  bf16x2 r = __builtin_convertvector(v, bf16x2);
  return __builtin_bit_cast(u32, r);
}
__device__ __forceinline__ float bfr(float x) { return bf_lo(pack2(x, 0.f) & 0xffffu); }
__device__ __forceinline__ u32 gelu_tanh2(f32x2 x) {
  const float c0 = -2.3022081983f, c1 = -0.1029432396f;
  const f32x2 p = x * x * c1 + c0;
  const f32x2 a = x * p;
  const f32x2 d = (f32x2){__builtin_amdgcn_exp2f(a[0]), __builtin_amdgcn_exp2f(a[1])} + 1.f;
  const f32x2 g = x * (f32x2){__builtin_amdgcn_rcpf(d[0]), __builtin_amdgcn_rcpf(d[1])};
  return pack2(g[0], g[1]);
}
template <int N_> __device__ __forceinline__ void wait_vm() { asm volatile("s_waitcnt vmcnt(%0)" ::"n"(N_) : "memory"); }
__device__ __forceinline__ void wait_lgkm0() { asm volatile("s_waitcnt lgkmcnt(0)" ::: "memory"); }
__device__ __forceinline__ void bar() {
  __builtin_amdgcn_sched_barrier(0);
  asm volatile("s_barrier" ::: "memory");
  __builtin_amdgcn_sched_barrier(0);
}
template <int V> using IC = std::integral_constant<int, V>;
template <typename F, int... I>
__device__ __forceinline__ void for_seq(F&& f, std::integer_sequence<int, I...>) { (f(IC<I>{}), ...); }
template <int N, typename F> __device__ __forceinline__ void for_n(F&& f) { for_seq(f, std::make_integer_sequence<int, N>{}); }

__device__ __forceinline__ float xsum(float x) {
#if defined(__HIP_DEVICE_COMPILE__)
  auto a = __builtin_amdgcn_permlane16_swap(__float_as_uint(x), __float_as_uint(x), false, false);
  x = __uint_as_float(a[0]) + __uint_as_float(a[1]);
  auto b = __builtin_amdgcn_permlane32_swap(__float_as_uint(x), __float_as_uint(x), false, false);
  x = __uint_as_float(b[0]) + __uint_as_float(b[1]);
#endif
  return x;
}
__device__ __forceinline__ float xmax(float x) {
#if defined(__HIP_DEVICE_COMPILE__)
  auto a = __builtin_amdgcn_permlane16_swap(__float_as_uint(x), __float_as_uint(x), false, false);
  x = fmaxf(__uint_as_float(a[0]), __uint_as_float(a[1]));
  auto b = __builtin_amdgcn_permlane32_swap(__float_as_uint(x), __float_as_uint(x), false, false);
  x = fmaxf(__uint_as_float(b[0]), __uint_as_float(b[1]));
#endif
  return x;
}
__device__ __forceinline__ f32x2 row_part(const u32x4& c0, const u32x4& c1) {
  float v[16];
#pragma unroll
  for (int k = 0; k < 4; ++k) {
    v[2 * k] = bf_lo(c0[k]); v[2 * k + 1] = bf_hi(c0[k]);
    v[8 + 2 * k] = bf_lo(c1[k]); v[8 + 2 * k + 1] = bf_hi(c1[k]);
  }
  float s = 0.f;
#pragma unroll
  for (int k = 0; k < 16; ++k) s += v[k];
  const float m = xsum(s) * (1.f / 64.f);
  float q = 0.f;
#pragma unroll
  for (int k = 0; k < 16; ++k) { const float d = v[k] - m; q += d * d; }
  return (f32x2){m, xsum(q)};
}

#define st16(v, off) __builtin_amdgcn_raw_buffer_store_b128(__builtin_bit_cast(i32x4, v), rs, (off), 0, 16)
__device__ __forceinline__ float sum8(const u32x4& f) {
  float s = bf_lo(f[0]) + bf_hi(f[0]);
#pragma unroll
  for (int k = 1; k < 4; ++k) { s += bf_lo(f[k]); s += bf_hi(f[k]); }
  return s;
}
__device__ __forceinline__ float fold32(float a, float b) {
#if defined(__HIP_DEVICE_COMPILE__)
  const auto x = __builtin_amdgcn_permlane32_swap(__float_as_uint(a), __float_as_uint(b), false, false);
  a = __uint_as_float(x[0]) + __uint_as_float(x[1]);
#endif
  return a;
}
__device__ __forceinline__ float fold16(float a, float b) {
#if defined(__HIP_DEVICE_COMPILE__)
  const auto x = __builtin_amdgcn_permlane16_swap(__float_as_uint(a), __float_as_uint(b), false, false);
  a = __uint_as_float(x[0]) + __uint_as_float(x[1]);
#endif
  return a;
}

__device__ __forceinline__ int fresh_lane(int tid) { int l = tid & 63; asm volatile("" : "+v"(l)); return l; }
__device__ __forceinline__ f32x4 zero4() {
  f32x4 z = {0.f, 0.f, 0.f, 0.f};
  asm volatile("" : "+v"(z));
  return z;
}
__device__ __forceinline__ void next_slot(int& so) { so += SLOT; if (so == RING) so = 0; }

#define DMA __builtin_amdgcn_raw_ptr_buffer_load_lds
#define SB() __builtin_amdgcn_sched_barrier(0)
__device__ __forceinline__ void mfma(f32x4& c, const bf16x8& a, const bf16x8& b) {
  asm volatile("v_mfma_f32_16x16x32_bf16 %0, %1, %2, %0" : "+v"(c) : "v"(a), "v"(b));
}
template <int OFF> __device__ __forceinline__ void dsrd(bf16x8& d, u32 addr) {
  asm volatile("ds_read_b128 %0, %1 offset:%2" : "=v"(d) : "v"(addr), "n"(OFF));
}
template <int OFF> __device__ __forceinline__ void dstr(bf16x8& d, u32 addr) {
  u32x2 lo, hi;
  asm volatile("ds_read_b64_tr_b16 %0, %2 offset:%3\n\tds_read_b64_tr_b16 %1, %2 offset:%4"
               : "=&v"(lo), "=&v"(hi) : "v"(addr), "n"(OFF), "n"(OFF + 512));
  const u32x4 v = {lo[0], lo[1], hi[0], hi[1]};
  d = __builtin_bit_cast(bf16x8, v);
}
__device__ __forceinline__ void pin1(u32& a) { asm volatile("" : "+v"(a)); }
__device__ __forceinline__ void pin2(u32& a, u32& b) { asm volatile("" : "+v"(a), "+v"(b)); }
template <int NSL, int NM, int NR, class AD, class RD, class MM>
__device__ __forceinline__ void kloop(int& so, AD&& ad, RD&& rd, MM&& mm) {
  ad(so, IC<0>{});
  bar();
  for_n<NR>([&](auto Q) { rd(IC<0>{}, Q); });
  next_slot(so);
  ad(so, IC<1>{});
  auto body = [&](auto S) {
    constexpr int s = decltype(S)::value;
    wait_lgkm0();
    bar();
    for_n<NM>([&](auto M) {
      constexpr int m = decltype(M)::value;
      mm(S, M);
      if constexpr (m < NR) rd(IC<1 - s>{}, IC<m>{});
      if constexpr (m == NR) { next_slot(so); asm volatile("" : "+s"(so)); }
      if constexpr (m == NR + 1) ad(so, S);
      SB();
    });
  };
#pragma nounroll
  for (int u = NSL / 2 - 1; u > 0; --u) {
    body(IC<0>{});
    body(IC<1>{});
  }
  body(IC<0>{});
  wait_lgkm0();
  SB();
  for_n<NM>([&](auto M) { mm(IC<1>{}, M); });
  asm volatile("s_nop 15");
  SB();
}

#define STAMP(w, i)

constexpr int w1_frag(int i, int e) {
  const int gam = 2 * (i - 2) + (e >> 1), gl = gam % 7;
  return (gam / 7) * 16 + (gl == 3 ? 6 + 7 * (e & 1) : (gl < 3 ? gl * 2 : 7 + (gl - 4) * 2) + (e & 1));
}
constexpr int between(int s0, int s1) {
  int n = 0;
  for (int s = s0 + 1; s < s1; ++s) {
    if (s >= PF0 && (s - PF0) % PFS == 0 && (s - PF0) / PFS < 8 - PFJ) n += (PFM & 1) + (PFM >> 1);
    if (s >= CQP && s < CQP + 6 && (s - CQP) % 2 == 1) n += 1;
    if (s >= HL && s < HL + 4 && (s - HL) % 2 == 1) n += 4;
  }
  return n < 63 ? n : 63;
}

__global__ __launch_bounds__(512, 2) void mega_k(char* __restrict__ ws, const u16* __restrict__ x,
                                                 const u16* __restrict__ wqkv, const u16* __restrict__ wo,
                                                 const u16* __restrict__ w1, const u16* __restrict__ b1,
                                                 const u16* __restrict__ w2, const u16* __restrict__ b2,
                                                 u16* __restrict__ out) {
#if defined(__HIP_DEVICE_COMPILE__)
  extern __shared__ __attribute__((aligned(16))) char smem[];
  const int tid = threadIdx.x;
  const int wid = __builtin_amdgcn_readfirstlane(tid >> 6);
#define FRESH_LANE() const int lane = fresh_lane(tid), r = lane & 15, g = lane >> 4; (void)r; (void)g
  const int xcd = blockIdx.x & 7, idx = blockIdx.x >> 3;
  const int tmA = (xcd >> 2) * 8 + (idx >> 2), tnA = (xcd & 3) * 4 + (idx & 3);
  const int tmB = (xcd >> 1) * 4 + (idx >> 3), tnB = (xcd & 1) * 8 + (idx & 7);
  const __amdgpu_buffer_rsrc_t rs = __builtin_amdgcn_make_buffer_rsrc((void*)ws, 0, WS_USED, 0x00020000);
  auto flag = [&](int set, int tm, int tn) { return (GLB_AS u64*)(ws + OFF_FL + ((set * 16 + tm) * 16 + tn) * 128); };
  auto rsrc = [&](const u16* p, u32 bytes) { return __builtin_amdgcn_make_buffer_rsrc((void*)p, 0, bytes, 0x00020000); };
  auto token = [&]() {
    const u32x2 tk = *(const volatile LDS_AS u32x2*)(smem + L_TOK);
    return (u64)(u32)__builtin_amdgcn_readfirstlane(tk[0]) | ((u64)(u32)__builtin_amdgcn_readfirstlane(tk[1]) << 32);
  };
  auto poll = [&](const GLB_AS u64* f) {
    const u64 tok = token();
#pragma nounroll
    for (int spin = 0; spin < (1 << 22); ++spin) {
      const u64 v = __hip_atomic_load(f, __ATOMIC_RELAXED, __HIP_MEMORY_SCOPE_AGENT);
      if (__all(v == tok)) break;
      __builtin_amdgcn_s_sleep(1);
    }
  };
  auto raise = [&](int set, int tm, int tn) {
    __hip_atomic_store(flag(set, tm, tn), token(), __ATOMIC_RELAXED, __HIP_MEMORY_SCOPE_AGENT);
  };
  int so = 0;

  if (wid < 4) {
    auto publish = [&](int ph, int tm, int tn) {
      FRESH_LANE();
      wait_vm<0>();
      u32 old = 0;
      if (lane == 0) old = __hip_atomic_fetch_add((LDS_AS u32*)(smem + L_CNT), 1u, __ATOMIC_RELAXED, __HIP_MEMORY_SCOPE_WORKGROUP);
      old = __builtin_amdgcn_readfirstlane(old);
      if ((old & 3) == 3 && lane == 0) raise(ph + 1, tm, tn);
    };
    const float* t_inv = (const float*)(smem + L_INV);
    const float* t_mnv = (const float*)(smem + L_MNV);
    const float* t_cs = (const float*)(smem + L_CS1);
    const float* t_bs = (const float*)(smem + L_BS);
    const int wmi = wid >> 1, wni = wid & 1;

    auto res_phase = [&](auto NSL_, u32 roff, int xo  , bool bias, int ph) {
      constexpr int NSL = decltype(NSL_)::value;
      FRESH_LANE();
      u32x4 res[2][2], bb[2];
#pragma unroll
      for (int i = 0; i < 2; ++i)
#pragma unroll
        for (int gl = 0; gl < 2; ++gl)
          res[i][gl] = __builtin_bit_cast(u32x4, __builtin_amdgcn_raw_buffer_load_b128(
              ph == 1 ? rsrc(x, SEQ * DM * 2) : rs, ph == 1 ? (16 * i + r) * 2048 + g * 16 : qoff((wid & 1) * 2 + i, lane),
              ph == 1 ? (tmB * 128 + wid * 32) * 2048 + tnB * 128 + gl * 64
                      : roff + ((tmB * 32 + 2 * tnB + gl) * 2 + (wid >> 1)) * QS, 16));
#pragma unroll
      for (int gl = 0; gl < 2; ++gl) {
        bb[gl] = *(const u32x4*)(b2 + tnB * 64 + gl * 32 + g * 8);
        if (!bias) bb[gl] = (u32x4){0u, 0u, 0u, 0u};
      }
      f32x4 acc[2][4];
#pragma unroll
      for (int i = 0; i < 2; ++i)
#pragma unroll
        for (int j = 0; j < 4; ++j) acc[i][j] = zero4();
      bf16x8 fA[2][2][2], fB[2][2][4];
      u32 pA, pB;
      kloop<NSL, 16, 12>(so,
        [&](int s_, auto) { pA = lane * 16 + wid * 2048 + s_; pB = lane * 16 + s_; pin2(pA, pB); },
        [&](auto SET, auto Q) {
          constexpr int set = decltype(SET)::value, q = decltype(Q)::value, s = q / 6, m = q % 6;
          if constexpr (m < 2) dsrd<s * 8192 + m * 1024>(fA[set][s][m], pA);
          else dsrd<16384 + s * 4096 + (m - 2) * 1024>(fB[set][s][m - 2], pB);
        },
        [&](auto SET, auto M) {
          constexpr int set = decltype(SET)::value, m = decltype(M)::value, s = m >> 3, j = (m >> 1) & 3, i = m & 1;
          mfma(acc[i][j], fB[set][s][j], fA[set][s][i]);
        });
      STAMP(0, 4 * ph + 2);
      u32x4 c[2][2];
#pragma unroll
      for (int i = 0; i < 2; ++i)
#pragma unroll
        for (int gl = 0; gl < 2; ++gl) {
#pragma unroll
          for (int k = 0; k < 4; ++k) {
            const f32x4& a = acc[i][2 * gl + (k >> 1)];
            const float y0 = a[2 * (k & 1)] + bf_lo(bb[gl][k]), y1 = a[2 * (k & 1) + 1] + bf_hi(bb[gl][k]);
            c[i][gl][k] = pack2(bf_lo(res[i][gl][k]) + y0, bf_hi(res[i][gl][k]) + y1);
          }
          if (xo < 4) st16(c[i][gl], OFF_X + xo * XS + ((tmB * 32 + 2 * tnB + gl) * 2 + (wid >> 1)) * QS + qoff((wid & 1) * 2 + i, lane));
          else *(u32x4*)(out + (size_t)(tmB * 128 + (2 * wid + i) * 16 + r) * DM + tnB * 64 + gl * 32 + g * 8) = c[i][gl];
        }
      if (xo < 4) {
        const f32x2 s0 = row_part(c[0][0], c[0][1]), s1 = row_part(c[1][0], c[1][1]);
        if (lane < 32)
          __builtin_amdgcn_raw_buffer_store_b64(__builtin_bit_cast(i32x2, g ? s1 : s0), rs, lane * 8,
                                                OFF_ST + xo * 262144 + ((tmB * 16 + tnB) * 128 + wid * 32) * 8, 16);
        publish(ph, tmB, tnB);
      }
      STAMP(0, 4 * ph + 3);
    };

#pragma nounroll
    for (int pair = 0; pair < 2; ++pair) {
      {
        f32x4 acc[4][6], acch[2];
        {
        FRESH_LANE();
        STAMP(0, 16 * pair);
        u32 pA, pB0, pB1;
        const u32 lB = 9216 + (8 * g + (r >> 2)) * 128 + (((2 * wni) ^ ((r >> 3) & 1) ^ ((g & 1) << 1)) * 32) + (r & 3) * 8;
        const u32 lA = r * 128 + ((g ^ (r >> 1)) * 16);
        auto loop = [&](auto WM, auto RAW) {
          constexpr int wm = decltype(WM)::value, raw = decltype(RAW)::value;
#pragma unroll
          for (int i = 0; i < 4; ++i)
#pragma unroll
            for (int j = 0; j < 6; ++j) acc[i][j] = zero4();
          acch[0] = zero4(); acch[1] = zero4();
          bf16x8 fA[2][4], fH[2], fB[2][6];
          u32 lA0 = raw ? lA : lane * 16, lA1 = raw ? (lA ^ 64) - SLOT : lane * 16, lB1 = lB ^ 32;
          pin2(lA0, lA1); pin1(lB1);
          kloop<32, 26, 11>(so,
            [&](int s_, auto PAR) {
              pA = (decltype(PAR)::value ? lA1 : lA0) + s_; pB0 = lB + s_; pB1 = lB1 + s_;
              pin2(pA, pB0); pin1(pB1);
            },
            [&](auto SET, auto Q) {
              constexpr int set = decltype(SET)::value, q = decltype(Q)::value;
              if constexpr (q < 4) dsrd<raw ? wm * SLOT + q * 2048 : wm * 4096 + q * 1024>(fA[set][q], pA);
              else if constexpr (q < 10) dstr<((q - 4) >> 1) * 4096>(fB[set][q - 4], (q & 1) ? pB1 : pB0);
              else dsrd<raw ? SLOT + 21504 : 8192>(fH[set], pA);
            },
            [&](auto SET, auto M) {
              constexpr int set = decltype(SET)::value, m = decltype(M)::value, j = m >> 2, i = m & 3;
              if constexpr (m < 16) mfma(acc[i][j], fB[set][j], fA[set][i]);
              else if constexpr (m < 24) mfma(acc[i][j], fA[set][i], fB[set][j]);
              else if constexpr (wm == 0) mfma(acch[i], fB[set][2 + i], fH[set]);
              else mfma(acch[i], fH[set], fB[set][4 + i]);
            });
        };
        if (pair == 0) { if (wmi == 0) loop(IC<0>{}, IC<1>{}); else loop(IC<1>{}, IC<1>{}); }
        else { if (wmi == 0) loop(IC<0>{}, IC<0>{}); else loop(IC<1>{}, IC<0>{}); }
        }
        FRESH_LANE();
        STAMP(0, 16 * pair + 2);
        const float* csw = (const float*)(smem + L_CSQ) + wni * 96;
        auto fix_qk = [&](const f32x4& a0, const f32x4& a1, int trow, int f) {
          const float iv = t_inv[trow + r], mv = t_mnv[trow + r];
          const f32x4 c0 = *(const f32x4*)(csw + f * 16 + 4 * g), c1 = *(const f32x4*)(csw + f * 16 + 16 + 4 * g);
          const f32x4 y0 = a0 * iv - c0 * mv, y1 = a1 * iv - c1 * mv;
          return (u32x4){pack2(y0[0], y0[1]), pack2(y0[2], y0[3]), pack2(y1[0], y1[1]), pack2(y1[2], y1[3])};
        };
        auto fix_v = [&](const f32x4& a0, const f32x4& a1, int trow) {
          const f32x4 iv = *(const f32x4*)(t_inv + trow + 4 * g), mv = *(const f32x4*)(t_mnv + trow + 4 * g);
          const float c0 = csw[64 + r], c1 = csw[80 + r];
          const f32x4 y0 = a0 * iv - mv * c0, y1 = a1 * iv - mv * c1;
          return (u32x4){pack2(y0[0], y0[1]), pack2(y0[2], y0[3]), pack2(y1[0], y1[1]), pack2(y1[2], y1[3])};
        };
        u32x4 Qf[4], Kf[5], Vf[5];
        char* xc = smem + L_XC + wni * 3072 + lane * 16;
        const int row0 = 16 + wmi * 64;
        Kf[4] = fix_qk(acc[3][2], acc[3][3], row0 + 48, 2);
        Vf[4] = fix_v(acc[3][4], acc[3][5], row0 + 48);
        if (wmi == 0) {
          *(u32x4*)xc = Kf[4];
          *(u32x4*)(xc + 1024) = Vf[4];
          Kf[0] = fix_qk(acch[0], acch[1], 0, 2);
        } else {
          *(u32x4*)(xc + 2048) = fix_v(acch[0], acch[1], 0);
        }
#pragma unroll
        for (int i = 0; i < 3; ++i) {
          Kf[i + 1] = fix_qk(acc[i][2], acc[i][3], row0 + 16 * i, 2);
          Vf[i + 1] = fix_v(acc[i][4], acc[i][5], row0 + 16 * i);
        }
        wait_lgkm0();
        bar();
        if (wmi == 1) Kf[0] = *(const u32x4*)xc;
        Vf[0] = *(const u32x4*)(xc + (wmi == 0 ? 2048 : 1024));
#pragma unroll
        for (int i = 0; i < 4; ++i) Qf[i] = fix_qk(acc[i][0], acc[i][1], row0 + 16 * i, 0);
        const bool par = r & 1;
        const float m0 = par ? 0.f : 1.f, m1 = par ? 1.f : 0.f;
        const bool own0 = 4 * g + (r & 1) <= r, own1 = 4 * g + (r & 1) + 2 <= r;
        const bool first = (tmA == 0) && (wmi == 0);
#pragma unroll
        for (int ii = 0; ii < 4; ++ii) {
          const int i = 3 - ii;
          const f32x4 z = {0.f, 0.f, 0.f, 0.f};
          const f32x4 S0 = __builtin_amdgcn_mfma_f32_16x16x32_bf16(__builtin_bit_cast(bf16x8, Kf[i]), __builtin_bit_cast(bf16x8, Qf[i]), z, 0, 0, 0);
          const f32x4 S1 = __builtin_amdgcn_mfma_f32_16x16x32_bf16(__builtin_bit_cast(bf16x8, Kf[i + 1]), __builtin_bit_cast(bf16x8, Qf[i]), z, 0, 0, 0);
          float l0 = own0 ? (par ? S1[1] : S1[0]) : (par ? S0[1] : S0[0]);
          float l1 = own1 ? (par ? S1[3] : S1[2]) : (par ? S0[3] : S0[2]);
          const float c = 0.17677669529663687f * 1.4426950408889634f;
          l0 *= c; l1 *= c;
          if (i == 0 && first) {
            if (!own0) l0 = -1.4426950e9f;
            if (!own1) l1 = -1.4426950e9f;
          }
          const float mx = xmax(fmaxf(l0, l1));
          const float p0 = __builtin_amdgcn_exp2f(l0 - mx), p1 = __builtin_amdgcn_exp2f(l1 - mx);
          const float rsum = __builtin_amdgcn_rcpf(xsum(p0 + p1));
          const float q0 = p0 * rsum, q1 = p1 * rsum;
          const u32 w0 = pack2(q0 * m0, q0 * m1), w1 = pack2(q1 * m0, q1 * m1);
          const u32x4 P = {own0 ? 0u : w0, own1 ? 0u : w1, own0 ? w0 : 0u, own1 ? w1 : 0u};
          const u32x4 V0 = {Vf[i][0], Vf[i][1], Vf[i + 1][0], Vf[i + 1][1]};
          const u32x4 V1 = {Vf[i][2], Vf[i][3], Vf[i + 1][2], Vf[i + 1][3]};
          const f32x4 O0 = __builtin_amdgcn_mfma_f32_16x16x32_bf16(__builtin_bit_cast(bf16x8, V0), __builtin_bit_cast(bf16x8, P), z, 0, 0, 0);
          const f32x4 O1 = __builtin_amdgcn_mfma_f32_16x16x32_bf16(__builtin_bit_cast(bf16x8, V1), __builtin_bit_cast(bf16x8, P), z, 0, 0, 0);
          const u32x4 o = {pack2(O0[0], O0[1]), pack2(O0[2], O0[3]), pack2(O1[0], O1[1]), pack2(O1[2], O1[3])};
          st16(o, OFF_O + pair * XS + ((tmA * 32 + 2 * tnA + wni) * 2 + wmi) * QS + qoff(i, lane));
        }
        publish(4 * pair, tmA, tnA);
        STAMP(0, 16 * pair + 3);
      }
      res_phase(IC<16>{}, OFF_X + (2 * pair) * XS, 2 * pair + 1, false, 4 * pair + 1);
      {
        f32x4 acc[4][7];
        {
        FRESH_LANE();
#pragma unroll
        for (int i = 0; i < 4; ++i)
#pragma unroll
          for (int j = 0; j < 7; ++j) acc[i][j] = zero4();
        bf16x8 fA[2][4], fB[2][7];
        u32 pA, pB;
        kloop<32, 28, 11>(so,
          [&](int s_, auto) { pA = lane * 16 + wmi * 4096 + s_; pB = lane * 16 + wni * 7168 + s_; pin2(pA, pB); },
          [&](auto SET, auto Q) {
            constexpr int set = decltype(SET)::value, q = decltype(Q)::value;
            if constexpr (q < 4) dsrd<q * 1024>(fA[set][q], pA);
            else dsrd<8192 + (q - 4) * 1024>(fB[set][q - 4], pB);
          },
          [&](auto SET, auto M) {
            constexpr int set = decltype(SET)::value, m = decltype(M)::value, j = m >> 2, i = m & 3;
            mfma(acc[i][j], fB[set][j], fA[set][i]);
          });
        }
        FRESH_LANE();
        STAMP(0, 16 * pair + 10);
        float iv[4], mv[4];
#pragma unroll
        for (int i = 0; i < 4; ++i) { iv[i] = t_inv[16 + wmi * 64 + 16 * i + r]; mv[i] = t_mnv[16 + wmi * 64 + 16 * i + r]; }
        const u32 hbase = OFF_H + pair * HS + ((tmA * 112 + 7 * tnA) * 2 + wmi) * QS;
        auto act = [&](const f32x4& a, const f32x4& cs, const f32x4& bs, int i) {
          const f32x4 y = a * iv[i] + (bs - cs * mv[i]);
          return (u32x2){gelu_tanh2((f32x2){y[0], y[1]}), gelu_tanh2((f32x2){y[2], y[3]})};
        };
#pragma unroll
        for (int jp = 0; jp < 3; ++jp) {
          const int t0 = (wni * 7 + 2 * jp) * 16 + 4 * g;
          const f32x4 cs0 = *(const f32x4*)(t_cs + t0), cs1 = *(const f32x4*)(t_cs + t0 + 16);
          const f32x4 bs0 = *(const f32x4*)(t_bs + t0), bs1 = *(const f32x4*)(t_bs + t0 + 16);
#pragma unroll
          for (int i = 0; i < 4; ++i) {
            const u32x2 a = act(acc[i][2 * jp], cs0, bs0, i), b = act(acc[i][2 * jp + 1], cs1, bs1, i);
            const u32x4 v = {a[0], a[1], b[0], b[1]};
            st16(v, hbase + (wni * 4 + jp) * 2 * QS + qoff(i, lane));
          }
        }
        {
          const int t0 = (wni * 7 + 6) * 16 + 4 * g;
          const f32x4 cs0 = *(const f32x4*)(t_cs + t0), bs0 = *(const f32x4*)(t_bs + t0);
#pragma unroll
          for (int i = 0; i < 4; i += 2) {
            const u32x2 a = act(acc[i][6], cs0, bs0, i), b = act(acc[i + 1][6], cs0, bs0, i + 1);
            const auto s0 = __builtin_amdgcn_permlane16_swap(a[0], b[0], false, false);
            const auto s1 = __builtin_amdgcn_permlane16_swap(a[1], b[1], false, false);
            const u32x4 v = {s0[0], s1[0], s0[1], s1[1]};
            st16(v, hbase + 3 * 2 * QS + qoff(i + (g & 1), (2 * wni + (g >> 1)) * 16 + r));
          }
        }
        publish(4 * pair + 2, tmA, tnA);
        STAMP(0, 16 * pair + 11);
      }
      res_phase(IC<56>{}, OFF_X + (2 * pair + 1) * XS, pair == 0 ? 2 : 4, true, 4 * pair + 3);
    }
  } else if (wid == 4) {
    {
      FRESH_LANE();
      STAMP(1, 0);
      const __amdgpu_buffer_rsrc_t rx = rsrc(x, SEQ * DM * 2);
      u32 vo[4];
#pragma unroll
      for (int e = 0; e < 4; ++e)
        vo[e] = (8 * e + (lane >> 3)) * 2048 + (((lane & 7) ^ (4 * (e & 1) + (lane >> 4))) * 16) - e * 1024;
      const u32 sx = tmA * 128 * 2048, sh = (tmA > 0 ? tmA * 128 - 16 : 0) * 2048;
      auto half = [&](auto H, int U) {
        constexpr int h = decltype(H)::value;
        for_n<8>([&](auto E) {
          constexpr int e = decltype(E)::value;
          DMA(rx, (LDS_AS void*)(smem + so + h * SLOT + (e >> 2) * 4096), 16, vo[e & 3],
              sx + (h * 64 + (e >> 2) * 32) * 2048 + U * 128, (e & 3) * 1024, 0);
        });
        DMA(rx, (LDS_AS void*)(smem + so + SLOT + 21504), 16, vo[h], sh + U * 128, h * 1024, 0);
        if constexpr (h == 1) { so += 2 * SLOT; if (so == RING) so = 0; }
      };
#pragma nounroll
      for (int U = 0; U < 2 + PRE; ++U) { half(IC<0>{}, U); half(IC<1>{}, U); }
      if (PRE) { wait_vm<36>(); bar(); bar(); }
#pragma nounroll
      for (int U = 2 + PRE; U < 16; ++U) {
        wait_vm<18>();
        bar();
        half(IC<0>{}, U);
        bar();
        half(IC<1>{}, U);
      }
      wait_vm<18>(); bar(); bar();
      wait_vm<0>(); bar(); bar();
    }
    auto phase = [&](auto NSL_, auto PA_, auto HALO_, u32 src, u32 hsrc, int ph  , int tm, int tmh) {
      constexpr int NSL = decltype(NSL_)::value, PA = decltype(PA_)::value, HALO = decltype(HALO_)::value;
      constexpr int PT = PA + HALO, D = PA == 16 ? DN : DW;
      FRESH_LANE();
      u32 vo[4];
#pragma unroll
      for (int e = 0; e < 4; ++e) vo[e] = qoff(e, lane) - e * 1024;
      poll(flag(ph + 1, (lane & 16) ? tmh : tm, r));
      if (ph == 2 && blockIdx.x == 0 && lane == 0)
        __hip_atomic_store((GLB_AS u64*)(ws + OFF_EP), token(), __ATOMIC_RELAXED, __HIP_MEMORY_SCOPE_AGENT);
      STAMP(1, 4 * (ph + 1));
      auto issue = [&](int u) {
        if constexpr (PA == 16) wait_vm<47>();
        for_n<PA>([&](auto E) {
          constexpr int e = decltype(E)::value;
          DMA(rs, (LDS_AS void*)(smem + so + (e >> 2) * 4096), 16, vo[e & 3], src + u * (PA / 4 * QS) + (e >> 2) * QS,
              (e & 3) * 1024, 16);
        });
        if constexpr (HALO) DMA(rs, (LDS_AS void*)(smem + so + 8192), 16, vo[3], hsrc + u * (2 * QS), 0, 16);
        next_slot(so);
      };
#pragma nounroll
      for (int u = 0; u < D; ++u) issue(u);
#pragma nounroll
      for (int u = 0; u < NSL - D; ++u) {
        wait_vm<(D - 1) * PT>();
        bar();
        issue(u + D);
      }
      for_n<D>([&](auto M) { wait_vm<(D - 1 - decltype(M)::value) * PT>(); bar(); });
    };
#pragma nounroll
    for (int pair = 0; pair < 2; ++pair) {
      const int tmh = tmA > 0 ? tmA - 1 : 0;
      const u32 xq = OFF_X + 2 * pair * XS;
      if (pair) phase(IC<32>{}, IC<8>{}, IC<1>{}, xq + tmA * (64 * QS), xq + tmh * (64 * QS) + QS + 3072, 3, tmA, tmh);
      bar();
      phase(IC<16>{}, IC<16>{}, IC<0>{}, OFF_O + pair * XS + tmB * (64 * QS), 0, 4 * pair, tmB, tmB);
      phase(IC<32>{}, IC<8>{}, IC<0>{}, xq + XS + tmA * (64 * QS), 0, 4 * pair + 1, tmA, tmA);
      phase(IC<56>{}, IC<16>{}, IC<0>{}, OFF_H + pair * HS + tmB * (224 * QS), 0, 4 * pair + 2, tmB, tmB);
    }
  } else if (wid == 5) {
    FRESH_LANE();
    auto phase = [&](auto NSL_, auto PB_, auto&& piece) {
      constexpr int NSL = decltype(NSL_)::value, PB = decltype(PB_)::value;
      auto issue = [&](int u) {
        wait_vm<63 - PB>();
        for_n<PB>([&](auto I) { piece(I, u); });
        next_slot(so);
      };
      constexpr int D = PB == 8 ? (DN < 4 ? DN : 5) : DW;
#pragma nounroll
      for (int u = 0; u < D; ++u) issue(u);
#pragma nounroll
      for (int u = 0; u < NSL - D; ++u) {
        wait_vm<(D - 1) * PB>();
        bar();
        issue(u + D);
      }
      for_n<D>([&](auto M) { wait_vm<(D - 1 - decltype(M)::value) * PB>(); bar(); });
    };
    auto tiles = [&](auto NSL_, auto PB_, auto BOFF_, u32 src) {
      constexpr int PB = decltype(PB_)::value, BOFF = decltype(BOFF_)::value;
      u32 vo[4];
#pragma unroll
      for (int e = 0; e < 4; ++e) vo[e] = qoff(e, lane) - e * 1024;
      phase(NSL_, PB_, [&](auto I, int u) {
        constexpr int i = decltype(I)::value;
        DMA(rs, (LDS_AS void*)(smem + so + BOFF + (i >> 2) * 4096), 16, vo[i & 3],
            src + u * ((PB + 3) / 4 * QS) + (i >> 2) * QS, (i & 3) * 1024, 16);
      });
    };
#pragma nounroll
    for (int pair = 0; pair < 2; ++pair) {
      {
        const __amdgpu_buffer_rsrc_t rq = rsrc(wqkv, DM * 3 * DM * 2);
        u32 vo[4];
#pragma unroll
        for (int e = 0; e < 4; ++e)
          vo[e] = (8 * e + (lane >> 3)) * 6144 + (((lane & 7) ^ (2 * ((lane >> 4) & 1) + 4 * (e & 1))) * 16) - e * 1024;
        phase(IC<32>{}, IC<12>{}, [&](auto I, int u) {
          constexpr int i = decltype(I)::value;
          DMA(rq, (LDS_AS void*)(smem + so + 9216 + (i >> 2) * 4096), 16, vo[i & 3],
              u * (32 * 6144) + (i >> 2) * 2048 + tnA * 128, (i & 3) * 1024, 0);
        });
      }
      bar();
      if (pair == 0) poll(flag(F_WO, r, tnB));
      tiles(IC<16>{}, IC<8>{}, IC<16384>{}, OFF_WO + tnB * (32 * QS));
      if (pair == 0) poll(flag(F_W1, r, (tnA & 14) + (g & 1)));
      tiles(IC<32>{}, IC<14>{}, IC<8192>{}, OFF_W1 + tnA * (32 * 4 * QS));
      if (pair == 0) poll(flag(F_W2, r, tnB));
      tiles(IC<56>{}, IC<8>{}, IC<16384>{}, OFF_W2 + tnB * (112 * QS));
    }
  } else if (wid == 6) {
    FRESH_LANE();
    const int rr = lane >> 3, c16 = (lane & 7) * 16;
    const __amdgpu_buffer_rsrc_t rx = rsrc(x, SEQ * DM * 2), rq = rsrc(wqkv, DM * 3 * DM * 2);
    auto warm = [&](int j) {
      const int n = idx * 64 + lane, m = idx * 48 + (lane < 48 ? lane : 47), k = (m * 5462) >> 16, p = m - 12 * k;
      if (PFM & 1) DMA(rx, (LDS_AS void*)(smem + L_PF), 4, (n & 1023) * 2048 + (n >> 10) * 128, (xcd >> 2) * (1024 * 2048) + j * 256, 0, 0);
      if (PFM & 2) DMA(rq, (LDS_AS void*)(smem + L_PF), 4, k * 6144 + (p >> 2) * 2048 + (p & 3) * 128, (xcd & 3) * 512 + j * (128 * 6144), 0, 0);
    };
    u32x4 U[7][4], F[4];
    auto u_load = [&](auto I) {
      constexpr int i = decltype(I)::value;
#pragma unroll
      for (int e = 0; e < 4; ++e) {
        if constexpr (i < 2)
          U[(i + 5) % 7][e] = __builtin_bit_cast(u32x4, __builtin_amdgcn_raw_buffer_load_b128(rsrc(wo, DM * DM * 2),
              ((rr & 3) + 16 * (rr >> 2)) * 2048 + c16, ((2 * tmA + i) * 32 + 4 * e) * 2048 + tnA * 128, UAUX));
        else if constexpr (i < 9)
          U[(i + 5) % 7][e] = __builtin_bit_cast(u32x4, __builtin_amdgcn_raw_buffer_load_b128(rsrc(w1, DM * FF * 2),
              rr * 7168 + c16, ((2 * tmA + (tnA & 1)) * 32 + 8 * e) * 7168 + (7 * (tnA >> 1) + i - 2) * 128, UAUX));
        else
          U[(i + 5) % 7][e] = __builtin_bit_cast(u32x4, __builtin_amdgcn_raw_buffer_load_b128(rsrc(w2, DM * FF * 2),
              rr * 2048 + c16, ((7 * tmA + i - 9) * 32 + 8 * e) * 2048 + tnA * 128, UAUX));
      }
    };
    auto u_write = [&](auto I) {
      constexpr int i = decltype(I)::value;
#pragma unroll
      for (int e = 0; e < 4; ++e) {
        const u32x4 v = U[(i + 5) % 7][e];
        *(u32x4*)(smem + L_IM + e * 1024 + rr * 128 + (c16 ^ ((rr & 2) << 5))) = (e & 1) ? (u32x4){v[2], v[3], v[0], v[1]} : v;
      }
    };
    auto u_store = [&](auto I) {
      constexpr int i = decltype(I)::value;
      const u32 sw = ((r & 8) << 3) | ((g & 1) << 3);
      const char* im = smem + L_IM + (8 * g + (r >> 2)) * 128;
      for_n<4>([&](auto E) {
        constexpr int e = decltype(E)::value;
        constexpr bool split = i >= 2 && i < 9 && (2 * (i - 2) + (e >> 1)) % 7 == 3;
        const char* p = im + (((e >> 1) * 64 + (split ? (e & 1) * 32 + (r & 3) * 8 : (r & 3) * 16 + (e & 1) * 8)) ^ sw);
        const u32x2 lo = __builtin_bit_cast(u32x2, __builtin_amdgcn_ds_read_tr16_b64_v4i16((LDS_AS s16x4*)p));
        const u32x2 hi = __builtin_bit_cast(u32x2, __builtin_amdgcn_ds_read_tr16_b64_v4i16((LDS_AS s16x4*)(p + 512)));
        F[e] = (u32x4){lo[0], lo[1], hi[0], hi[1]};
      });
      for_n<4>([&](auto E) {
        constexpr int e = decltype(E)::value;
        if constexpr (i < 2) st16(F[e], OFF_WO + (tnA * 32 + 2 * tmA + i) * QS + qoff(e, lane));
        else if constexpr (i < 9) {
          constexpr int f = w1_frag(i, e);
          st16(F[e], OFF_W1 + ((((tnA & 14) + (f >> 4)) * 32 + 2 * tmA + (tnA & 1)) * 4 + ((f & 15) >> 2)) * QS + qoff(f & 3, lane));
        } else st16(F[e], OFF_W2 + (tnA * 112 + 7 * tmA + i - 9) * QS + qoff(e, lane));
      });
    };
    auto u_sums = [&](auto I) {
      constexpr int i = decltype(I)::value;
      constexpr u32 K = w1_frag(i, 0) | (w1_frag(i, 1) << 8) | (w1_frag(i, 2) << 16) | (w1_frag(i, 3) << 24);
      const float cs = fold16(fold32(sum8(F[0]), sum8(F[2])), fold32(sum8(F[1]), sum8(F[3])));
      __builtin_amdgcn_raw_buffer_store_b32(__float_as_uint(cs), rs,
          OFF_CS1 + ((2 * tmA + (tnA & 1)) * 4096 + (tnA & 14) * 256 + ((K >> (8 * g)) & 255) * 16 + r) * 4, 0, 16);
    };
    auto up = [&](int set) { if (lane == 0) raise(set, tmA, tnA); };
    u32x4 Q[24];
    float qs[8];
    __builtin_amdgcn_s_sleep(PFW0);
#pragma unroll
    for (int e = 0; e < 24; ++e)
      Q[e] = __builtin_bit_cast(u32x4, __builtin_amdgcn_raw_buffer_load_b128(rq, rr * 6144 + c16,
          (tmA * 64 + 8 * (e & 7)) * 6144 + (e >> 3) * 2048 + tnA * 128, 0));
    u_load(IC<0>{});
    u_load(IC<1>{});
    for_n<33>([&](auto S) {
      constexpr int s = decltype(S)::value;
      bar();
      if constexpr (s == CQP + 5 + FLD) { wait_vm<between(CQP + 5, s)>(); up(F_CQ); }
      if constexpr (s == 26) { wait_vm<0>(); up(F_WO); }
      if constexpr (s >= PF0 && (s - PF0) % PFS == 0 && (s - PF0) / PFS < 8 - PFJ) warm(PFJ + (s - PF0) / PFS);
      if constexpr (s >= CQP && s < CQP + 6) {
        constexpr int h = s - CQP;
#pragma unroll
        for (int e = 0; e < 4; ++e) {
          u32x4 v = Q[4 * h + e];
          asm volatile("" : "+v"(v));
#pragma unroll
          for (int k = 0; k < 4; ++k) {
            if (h % 2 == 0 && e == 0) { qs[2 * k] = bf_lo(v[k]); qs[2 * k + 1] = bf_hi(v[k]); }
            else { qs[2 * k] += bf_lo(v[k]); qs[2 * k + 1] += bf_hi(v[k]); }
          }
        }
        if constexpr (h % 2 == 0) {
#pragma unroll
          for (int k = 0; k < 8; ++k) asm volatile("" : "+v"(qs[k]));
        } else {
          float c0 = fold16(fold32(qs[0], qs[4]), fold32(qs[2], qs[6])), c1 = fold16(fold32(qs[1], qs[5]), fold32(qs[3], qs[7]));
          c0 += __uint_as_float(__builtin_amdgcn_mov_dpp(__float_as_uint(c0), 0x128, 0xf, 0xf, false));
          c1 += __uint_as_float(__builtin_amdgcn_mov_dpp(__float_as_uint(c1), 0x128, 0xf, 0xf, false));
          const int col = 8 * (lane & 7) + rr;
          __builtin_amdgcn_raw_buffer_store_b32(__float_as_uint((rr & 1) ? c1 : c0), rs,
              OFF_CSQ + (tmA * 4096 + tnA * 256 + (col >> 5) * 96 + (h >> 1) * 32 + (col & 31)) * 4, 0, 16);
        }
      }
      if constexpr (s >= HL && s < HL + 4) {
        if constexpr ((s - HL) % 2 == 0) u_write(IC<(s - HL) / 2>{}); else u_store(IC<(s - HL) / 2>{});
      }
      if constexpr (s == G0) for_n<7>([&](auto I) { u_load(IC<2 + decltype(I)::value>{}); });
    });
    static_assert(HL >= CQP + 6 && HL + 4 < 26 && CQP + 5 + FLD < 26 && G0 >= 26, "schedule");
    for_n<7>([&](auto I) {
      constexpr int i = 2 + decltype(I)::value;
      u_write(IC<i>{});
      u_store(IC<i>{});
      u_sums(IC<i>{});
    });
    bar(); bar(); bar();
    wait_vm<0>();
    up(F_W1);
#pragma nounroll
    for (int u = 3; u <= G1; ++u) bar();
    for_n<7>([&](auto I) { u_load(IC<9 + decltype(I)::value>{}); });
#pragma nounroll
    for (int u = G1 + 1; u < 16 + H2; ++u) bar();
    for_n<14>([&](auto S) {
      constexpr int s = decltype(S)::value, i = 9 + s / 2;
      bar();
      if constexpr (s % 2 == 0) u_write(IC<i>{}); else u_store(IC<i>{});
    });
    bar(); bar(); bar(); bar();
    wait_vm<0>();
    up(F_W2);
  } else {
    FRESH_LANE();
    const int tmh = tmA > 0 ? tmA - 1 : 0;
    auto ln_phase = [&](bool qkv, int xi) {
      f32x4 sv[16], cv[16];
      f32x2 hv[4];
      const u32 so0 = OFF_ST + xi * 262144 + tmA * 16384, so1 = OFF_ST + xi * 262144 + (qkv ? tmh : tmA) * 16384;
      const u32 sc = (qkv ? OFF_CSQ : OFF_CS1) + tnA * 1024;
      auto ldcs = [&](int p0) {
#pragma unroll
        for (int p = 0; p < 16; ++p)
          cv[p] = __builtin_bit_cast(f32x4, __builtin_amdgcn_raw_buffer_load_b128(rs, lane * 16, sc + (p0 + p) * 16384, 16));
      };
#pragma unroll
      for (int p = 0; p < 16; ++p)
        sv[p] = __builtin_bit_cast(f32x4, __builtin_amdgcn_raw_buffer_load_b128(rs, lane * 16, so0 + p * 1024, 16));
#pragma unroll
      for (int p = 0; p < 4; ++p)
        hv[p] = __builtin_bit_cast(f32x2, __builtin_amdgcn_raw_buffer_load_b64(rs, (112 + r) * 8 + g * 4096, so1 + p * 1024, 16));
      bar();
      if (xi < 2) ldcs(0);
      bar(); bar();
      float mean[3] = {0.f, 0.f, 0.f}, m2[3] = {0.f, 0.f, 0.f}, dd[3] = {0.f, 0.f, 0.f};
#pragma unroll
      for (int p = 0; p < 16; ++p) { mean[0] += sv[p][0]; mean[1] += sv[p][2]; }
      mean[2] = xsum(hv[0][0] + hv[1][0] + hv[2][0] + hv[3][0]);
#pragma unroll
      for (int h = 0; h < 3; ++h) mean[h] *= (1.f / 16.f);
#pragma unroll
      for (int p = 0; p < 4; ++p) { const float d = hv[p][0] - mean[2]; m2[2] += hv[p][1]; dd[2] += d * d; }
      auto cut = [&]() {
        asm volatile("" : "+v"(mean[0]), "+v"(mean[1]), "+v"(mean[2]), "+v"(m2[0]), "+v"(m2[1]), "+v"(m2[2]),
                          "+v"(dd[0]), "+v"(dd[1]), "+v"(dd[2]));
        bar();
        asm volatile("" : "+v"(mean[0]), "+v"(mean[1]), "+v"(mean[2]), "+v"(m2[0]), "+v"(m2[1]), "+v"(m2[2]),
                          "+v"(dd[0]), "+v"(dd[1]), "+v"(dd[2]));
      };
#pragma unroll
      for (int p = 0; p < 16; ++p) {
        if (p % 8 == 0) cut();
        const float d0 = sv[p][0] - mean[0], d1 = sv[p][2] - mean[1];
        m2[0] += sv[p][1]; dd[0] += d0 * d0;
        m2[1] += sv[p][3]; dd[1] += d1 * d1;
      }
      m2[0] += 64.f * dd[0]; m2[1] += 64.f * dd[1];
      m2[2] = xsum(m2[2] + 64.f * dd[2]);
      cut();
#pragma unroll
      for (int h = 0; h < 3; ++h) { mean[h] = bfr(mean[h]); m2[h] = bfr(sqrtf(bfr(bfr(m2[h] * (1.f / DM)) + LN_EPS))); }
      cut();
#pragma unroll
      for (int h = 0; h < 3; ++h) m2[h] = 1.f / m2[h];
      *(f32x2*)(smem + L_INV + 64 + lane * 8) = (f32x2){m2[0], m2[1]};
      *(f32x2*)(smem + L_MNV + 64 + lane * 8) = (f32x2){mean[0] * m2[0], mean[1] * m2[1]};
      *(float*)(smem + L_INV + r * 4) = m2[2];
      *(float*)(smem + L_MNV + r * 4) = mean[2] * m2[2];
      bar();
      f32x4 ca = {0.f, 0.f, 0.f, 0.f};
      if (xi < 2) {
#pragma unroll
        for (int p = 0; p < 16; ++p) ca += cv[p];
        if (qkv) *(f32x4*)(smem + L_CSQ + lane * 16) = ca;
      }
      if (!qkv) {
        asm volatile("" : "+v"(ca));
        if (xi < 2) ldcs(16);
        bar(); bar(); bar(); bar();
        if (xi < 2) {
#pragma unroll
          for (int p = 0; p < 16; ++p) ca += cv[p];
          *(f32x4*)(smem + L_CS1 + lane * 16) = ca;
        }
      }
      wait_lgkm0();
    };
    constexpr int NLN = 8;
    auto bars = [&](int n) {
#pragma nounroll
      for (int u = 0; u < n; ++u) bar();
    };
    {
      const u64 ep = __hip_atomic_load((const GLB_AS u64*)(ws + OFF_EP), __ATOMIC_RELAXED, __HIP_MEMORY_SCOPE_AGENT);
      u16 bv[4];
#pragma unroll
      for (int h = 0; h < 4; ++h) {
        const int t = min(lane + 64 * h, 223), f = t >> 4, rho = t & 15, wn = f / 7, j = f % 7;
        bv[h] = b1[tnA * 224 + (j == 6 ? 96 + 16 * wn + rho : (4 * wn + (j >> 1)) * 32 + 8 * (rho >> 2) + 4 * (j & 1) + (rho & 3))];
      }
      __builtin_amdgcn_s_sleep(XW);
      bars(XL);
      const __amdgpu_buffer_rsrc_t rx = rsrc(x, SEQ * DM * 2);
      bf16x8 xf[8][2];
#pragma unroll
      for (int i = 0; i < 8; ++i)
#pragma unroll
        for (int gl = 0; gl < 2; ++gl)
          xf[i][gl] = __builtin_bit_cast(bf16x8, __builtin_amdgcn_raw_buffer_load_b128(rx, r * 2048 + g * 16,
              (tmA * 128 + 16 * i) * 2048 + tnA * 128 + gl * 64, 0));
      bars(TOKB - XL);
#pragma unroll
      for (int h = 0; h < 4; ++h) ((float*)(smem + L_BS))[lane + 64 * h] = bf_lo(bv[h]);
      if (lane == 0) {
        *(LDS_AS u32*)(smem + L_CNT) = 0u;
        *(LDS_AS u64*)(smem + L_TOK) = ep * 6364136223846793005ull + 1442695040888963407ull;
      }
      wait_lgkm0();
      bars(XP - TOKB);
      const u32x4 one4 = {0x3f803f80u, 0x3f803f80u, 0x3f803f80u, 0x3f803f80u};
      const bf16x8 ones = __builtin_bit_cast(bf16x8, one4);
      const u32 svo = g == (r >> 2) ? OFF_ST + (tmA * 16 + tnA) * 1024 + r * 8 : 0x80000000u;
#pragma unroll
      for (int i = 0; i < 8; ++i) {
        if (i == 4) bar();
        const f32x4 z = {0.f, 0.f, 0.f, 0.f};
        f32x4 s = __builtin_amdgcn_mfma_f32_16x16x32_bf16(ones, xf[i][0], z, 0, 0, 0);
        f32x4 q = __builtin_amdgcn_mfma_f32_16x16x32_bf16(xf[i][0], xf[i][0], z, 0, 0, 0);
        s = __builtin_amdgcn_mfma_f32_16x16x32_bf16(ones, xf[i][1], s, 0, 0, 0);
        q = __builtin_amdgcn_mfma_f32_16x16x32_bf16(xf[i][1], xf[i][1], q, 0, 0, 0);
        const float qq = (r & 2) ? ((r & 1) ? q[3] : q[2]) : ((r & 1) ? q[1] : q[0]);
        const float m = s[0] * (1.f / 64.f);
        const f32x2 o = {m, fmaxf(__builtin_fmaf(-s[0], m, qq), 0.f)};
        __builtin_amdgcn_raw_buffer_store_b64(__builtin_bit_cast(i32x2, o), rs, svo + i * 128, 0, 16);
      }
      bars(FLD);
      wait_vm<0>();
      if (lane == 0) raise(F_ST, tmA, tnA);
      bars(POLB - (XP + 1 + FLD));
      const GLB_AS u64* fp = g < 2 ? flag(F_ST, g ? tmh : tmA, r) : flag(F_CQ, r, tnA);
      const u64 v = __hip_atomic_load(fp, __ATOMIC_RELAXED, __HIP_MEMORY_SCOPE_AGENT);
      bar(); bar();
      if (!__all(v == token())) poll(fp);
      static_assert(TOKB >= XL && XP >= TOKB && POLB >= XP + 1 + FLD && POLB + 2 + NLN <= 31 && POLB + 2 > CQP + 5 + FLD, "schedule");
    }
#pragma nounroll
    for (int pair = 0; pair < 2; ++pair) {
      if (pair) {
        const __amdgpu_buffer_rsrc_t rq = rsrc(wqkv, DM * 3 * DM * 2);
        const int m = idx * 48 + (lane < 48 ? lane : 47), k = (m * 5462) >> 16, p = m - 12 * k;
#pragma nounroll
        for (int j = 0; j < 8; ++j) {
          if (j >= 3) { bar(); bar(); }
          DMA(rq, (LDS_AS void*)(smem + L_PF), 4, k * 6144 + (p >> 2) * 2048 + (p & 3) * 128, (xcd & 3) * 512 + j * (128 * 6144), 0, 0);
        }
        bars(POLB + 2 - 10);
      }
      ln_phase(true, 2 * pair);
      bars(33 - (POLB + 2 + NLN) + 16 + 1);
      ln_phase(false, 2 * pair + 1);
      bars(32 - (1 + NLN + 4) + 56);
    }
  }
#endif
}

extern "C" void kernel_launch(void* const* d_in, const int* in_sizes, int n_in, void* d_out, int out_size,
                              void* d_ws, size_t ws_size, hipStream_t stream) {
  const u16* x0 = (const u16*)d_in[0];
  const u16* wqkv = (const u16*)d_in[1];
  const u16* wo = (const u16*)d_in[2];
  const u16* w1 = (const u16*)d_in[4];
  const u16* b1 = (const u16*)d_in[5];
  const u16* w2 = (const u16*)d_in[6];
  const u16* b2 = (const u16*)d_in[7];
  char* ws = (char*)(((uintptr_t)d_ws + 1023) & ~(uintptr_t)1023);
  (void)hipFuncSetAttribute((const void*)mega_k, hipFuncAttributeMaxDynamicSharedMemorySize, LDS_BYTES);
  mega_k<<<256, 512, LDS_BYTES, stream>>>(ws, x0, wqkv, wo, w1, b1, w2, b2, (u16*)d_out);
}
```

```cpp
#include <hip/hip_runtime.h>
#include <stdint.h>
#include <type_traits>
#include <utility>

typedef unsigned short u16;
typedef unsigned int u32;
typedef unsigned long long u64;
typedef __bf16 bf16x8 __attribute__((ext_vector_type(8)));
typedef __bf16 bf16x2 __attribute__((ext_vector_type(2)));
typedef short s16x4 __attribute__((ext_vector_type(4)));
typedef float f32x4 __attribute__((ext_vector_type(4)));
typedef float f32x2 __attribute__((ext_vector_type(2)));
typedef u32 u32x4 __attribute__((ext_vector_type(4)));
typedef u32 u32x2 __attribute__((ext_vector_type(2)));
typedef int i32x4 __attribute__((ext_vector_type(4)));
typedef int i32x2 __attribute__((ext_vector_type(2)));
#define LDS_AS __attribute__((address_space(3)))
#define GLB_AS __attribute__((address_space(1)))

constexpr int SEQ = 2048, DM = 1024, FF = 3584;
constexpr float LN_EPS = 1e-5f;

#define SKIP 1
#define DN 4
#define DW 5
#define UAUX 2
#define PFW0 30
#define PF0 4
#define PFS 2
#define PFJ 2
#define PFM 3
#define CQP 7
#define HL 16
#define FLD 3
#define XW 30
#define XL 0
#define TOKB 3
#define XP 7
#define POLB 18
#define G0 27
#define G1 12
#define H2 4
#define PRE 1
#define PUBJ 2
#define HKT 0
#define ROT 1
#define CB 4
#define KW1 11
constexpr u32 MB = 1u << 20;
constexpr u32 QS = SKIP ? 5120 : 4096;
constexpr u32 XS = 5 * MB, HS = 18 * MB;
constexpr u32 OFF_WO = 8 * MB;
constexpr u32 OFF_W1 = 11 * MB;
constexpr u32 OFF_W2 = 21 * MB;
constexpr u32 OFF_X = 30 * MB;
constexpr u32 OFF_O = OFF_X + 4 * XS;
constexpr u32 OFF_H = OFF_O + 2 * XS;
constexpr u32 OFF_ST = OFF_H + 2 * HS;
constexpr u32 OFF_CSQ = OFF_ST + MB;
constexpr u32 OFF_CS1 = OFF_CSQ + 262144;
constexpr u32 OFF_FL = OFF_CS1 + 524288;
constexpr u32 OFF_EP = OFF_FL + 18 * 32768;
constexpr u32 OFF_DBG = OFF_ST + 3 * MB;
constexpr u32 WS_USED = OFF_ST + 4 * MB;
constexpr int F_ST = 0, F_CQ = 8, F_WO = 9, F_W1 = 10, F_W2 = 11, F_2ND = 10;
__device__ __forceinline__ u32 qoff(u32 e, u32 slot) {
  const u32 n = 8 * e + (slot >> 3);
  return ((n + (SKIP ? (((n + 4) * 37) >> 8) : 0)) << 7) + (slot & 7) * 16;
}
__device__ __forceinline__ u32 hpos(u32 t, u32 j) { return (j & 3) == 0 ? 2 * t + (j >> 2) : 32 + 5 * t + j - 1 - (j >> 2); }

constexpr int SLOT = 24576, NRING = 6, RING = SLOT * NRING;
constexpr int L_INV = RING, L_MNV = L_INV + 576, L_CNT = L_MNV + 576, L_TOK = L_CNT + 8;
constexpr int L_CSQ = RING + 1280, L_CS1 = L_CSQ + 1024, L_BS = L_CS1 + 1024;
constexpr int L_XC = L_BS + 1024;
constexpr int L_IM = L_XC + 6144;
constexpr int L_PF = L_IM + 4096;
constexpr int LDS_BYTES = L_PF + 256;

__device__ __forceinline__ float bf_lo(u32 w) { return __uint_as_float(w << 16); }
__device__ __forceinline__ float bf_hi(u32 w) { return __uint_as_float(w & 0xffff0000u); }
__device__ __forceinline__ u32 pack2(float lo, float hi) {
  f32x2 v = {lo, hi};
  bf16x2 r = __builtin_convertvector(v, bf16x2);
  return __builtin_bit_cast(u32, r);
}
__device__ __forceinline__ float bfr(float x) { return bf_lo(pack2(x, 0.f) & 0xffffu); }
__device__ __forceinline__ u32 gelu_tanh2(f32x2 x) {
  const float c0 = -2.3022081983f, c1 = -0.1029432396f;
  const f32x2 p = x * x * c1 + c0;
  const f32x2 a = x * p;
  const f32x2 d = (f32x2){__builtin_amdgcn_exp2f(a[0]), __builtin_amdgcn_exp2f(a[1])} + 1.f;
  const f32x2 g = x * (f32x2){__builtin_amdgcn_rcpf(d[0]), __builtin_amdgcn_rcpf(d[1])};
  return pack2(g[0], g[1]);
}
template <int N_> __device__ __forceinline__ void wait_vm() { asm volatile("s_waitcnt vmcnt(%0)" ::"n"(N_) : "memory"); }
__device__ __forceinline__ void wait_lgkm0() { asm volatile("s_waitcnt lgkmcnt(0)" ::: "memory"); }
__device__ __forceinline__ void bar() {
  __builtin_amdgcn_sched_barrier(0);
  asm volatile("s_barrier" ::: "memory");
  __builtin_amdgcn_sched_barrier(0);
}
template <int V> using IC = std::integral_constant<int, V>;
template <typename F, int... I>
__device__ __forceinline__ void for_seq(F&& f, std::integer_sequence<int, I...>) { (f(IC<I>{}), ...); }
template <int N, typename F> __device__ __forceinline__ void for_n(F&& f) { for_seq(f, std::make_integer_sequence<int, N>{}); }

__device__ __forceinline__ float xsum(float x) {
#if defined(__HIP_DEVICE_COMPILE__)
  auto a = __builtin_amdgcn_permlane16_swap(__float_as_uint(x), __float_as_uint(x), false, false);
  x = __uint_as_float(a[0]) + __uint_as_float(a[1]);
  auto b = __builtin_amdgcn_permlane32_swap(__float_as_uint(x), __float_as_uint(x), false, false);
  x = __uint_as_float(b[0]) + __uint_as_float(b[1]);
#endif
  return x;
}
__device__ __forceinline__ float xmax(float x) {
#if defined(__HIP_DEVICE_COMPILE__)
  auto a = __builtin_amdgcn_permlane16_swap(__float_as_uint(x), __float_as_uint(x), false, false);
  x = fmaxf(__uint_as_float(a[0]), __uint_as_float(a[1]));
  auto b = __builtin_amdgcn_permlane32_swap(__float_as_uint(x), __float_as_uint(x), false, false);
  x = fmaxf(__uint_as_float(b[0]), __uint_as_float(b[1]));
#endif
  return x;
}
__device__ __forceinline__ f32x2 row_part(const u32x4& c0, const u32x4& c1) {
  float v[16];
#pragma unroll
  for (int k = 0; k < 4; ++k) {
    v[2 * k] = bf_lo(c0[k]); v[2 * k + 1] = bf_hi(c0[k]);
    v[8 + 2 * k] = bf_lo(c1[k]); v[8 + 2 * k + 1] = bf_hi(c1[k]);
  }
  float s = 0.f;
#pragma unroll
  for (int k = 0; k < 16; ++k) s += v[k];
  const float m = xsum(s) * (1.f / 64.f);
  float q = 0.f;
#pragma unroll
  for (int k = 0; k < 16; ++k) { const float d = v[k] - m; q += d * d; }
  return (f32x2){m, xsum(q)};
}

#define st16(v, off) __builtin_amdgcn_raw_buffer_store_b128(__builtin_bit_cast(i32x4, v), rs, (off), 0, 16)
__device__ __forceinline__ float sum8(const u32x4& f) {
  float s = bf_lo(f[0]) + bf_hi(f[0]);
#pragma unroll
  for (int k = 1; k < 4; ++k) { s += bf_lo(f[k]); s += bf_hi(f[k]); }
  return s;
}
__device__ __forceinline__ float fold32(float a, float b) {
#if defined(__HIP_DEVICE_COMPILE__)
  const auto x = __builtin_amdgcn_permlane32_swap(__float_as_uint(a), __float_as_uint(b), false, false);
  a = __uint_as_float(x[0]) + __uint_as_float(x[1]);
#endif
  return a;
}
__device__ __forceinline__ float fold16(float a, float b) {
#if defined(__HIP_DEVICE_COMPILE__)
  const auto x = __builtin_amdgcn_permlane16_swap(__float_as_uint(a), __float_as_uint(b), false, false);
  a = __uint_as_float(x[0]) + __uint_as_float(x[1]);
#endif
  return a;
}

__device__ __forceinline__ int fresh_lane(int tid) { int l = tid & 63; asm volatile("" : "+v"(l)); return l; }
__device__ __forceinline__ f32x4 zero4() {
  f32x4 z = {0.f, 0.f, 0.f, 0.f};
  asm volatile("" : "+v"(z));
  return z;
}
__device__ __forceinline__ void next_slot(int& so) { so += SLOT; if (so == RING) so = 0; }

#define DMA __builtin_amdgcn_raw_ptr_buffer_load_lds
#define SB() __builtin_amdgcn_sched_barrier(0)
__device__ __forceinline__ void mfma(f32x4& c, const bf16x8& a, const bf16x8& b) {
  asm volatile("v_mfma_f32_16x16x32_bf16 %0, %1, %2, %0" : "+v"(c) : "v"(a), "v"(b));
}
template <int OFF> __device__ __forceinline__ void dsrd(bf16x8& d, u32 addr) {
  asm volatile("ds_read_b128 %0, %1 offset:%2" : "=v"(d) : "v"(addr), "n"(OFF));
}
template <int OFF> __device__ __forceinline__ void dstr(bf16x8& d, u32 addr) {
  u32x2 lo, hi;
  asm volatile("ds_read_b64_tr_b16 %0, %2 offset:%3\n\tds_read_b64_tr_b16 %1, %2 offset:%4"
               : "=&v"(lo), "=&v"(hi) : "v"(addr), "n"(OFF), "n"(OFF + 512));
  const u32x4 v = {lo[0], lo[1], hi[0], hi[1]};
  d = __builtin_bit_cast(bf16x8, v);
}
__device__ __forceinline__ void pin1(u32& a) { asm volatile("" : "+v"(a)); }
__device__ __forceinline__ void pin2(u32& a, u32& b) { asm volatile("" : "+v"(a), "+v"(b)); }
template <int NSL, int NM, int NR, class AD, class RD, class MM, class HK>
__device__ __forceinline__ void kloop(int& so, AD&& ad, RD&& rd, MM&& mm, HK&& hook) {
  ad(so, IC<0>{});
  bar();
  for_n<NR>([&](auto Q) { rd(IC<0>{}, Q); });
  next_slot(so);
  ad(so, IC<1>{});
  auto body = [&](auto S) {
    constexpr int s = decltype(S)::value;
    wait_lgkm0();
    bar();
    for_n<NM>([&](auto M) {
      constexpr int m = decltype(M)::value;
      mm(S, M);
      if constexpr (m < NR) rd(IC<1 - s>{}, IC<m>{});
      if constexpr (m == NR) { next_slot(so); asm volatile("" : "+s"(so)); }
      if constexpr (m == NR + 1) ad(so, S);
      SB();
    });
  };
#pragma nounroll
  for (int u = NSL / 2 - 1; u > 0; --u) {
    body(IC<0>{});
    body(IC<1>{});
    hook(NSL / 2 - 1 - u);
  }
  body(IC<0>{});
  wait_lgkm0();
  SB();
  for_n<NM>([&](auto M) { mm(IC<1>{}, M); });
  asm volatile("s_nop 15");
  SB();
}

#define STAMP(w, i)

constexpr int w1_frag(int i, int e) {
  const int gam = 2 * (i - 2) + (e >> 1), gl = gam % 7;
  return (gam / 7) * 16 + (gl == 3 ? 6 + 7 * (e & 1) : (gl < 3 ? gl * 2 : 7 + (gl - 4) * 2) + (e & 1));
}
constexpr int between(int s0, int s1) {
  int n = 0;
  for (int s = s0 + 1; s < s1; ++s) {
    if (s >= PF0 && (s - PF0) % PFS == 0 && (s - PF0) / PFS < 8 - PFJ) n += (PFM & 1) + (PFM >> 1);
    if (s >= CQP && s < CQP + 6 && (s - CQP) % 2 == 1) n += 1;
    if (s >= HL && s < HL + 4 && (s - HL) % 2 == 1) n += 4;
  }
  return n < 63 ? n : 63;
}

__global__ __launch_bounds__(512, 2) void mega_k(char* __restrict__ ws, const u16* __restrict__ x,
                                                 const u16* __restrict__ wqkv, const u16* __restrict__ wo,
                                                 const u16* __restrict__ w1, const u16* __restrict__ b1,
                                                 const u16* __restrict__ w2, const u16* __restrict__ b2,
                                                 u16* __restrict__ out) {
#if defined(__HIP_DEVICE_COMPILE__)
  extern __shared__ __attribute__((aligned(16))) char smem[];
  const int tid = threadIdx.x;
  const int wid = __builtin_amdgcn_readfirstlane(tid >> 6);
#define FRESH_LANE() const int lane = fresh_lane(tid), r = lane & 15, g = lane >> 4; (void)r; (void)g
  const int xcd = blockIdx.x & 7, idx = blockIdx.x >> 3;
  const int tmA = (xcd >> 2) * 8 + (idx >> 2), tnA = (xcd & 3) * 4 + (idx & 3);
  const int tmB = (xcd >> 1) * 4 + (idx >> 3), tnB = (xcd & 1) * 8 + (idx & 7);
  const int rotA = ROT ? 8 - 8 * (xcd & 1) : 0, rotB = ROT ? 4 * ((xcd + 1) & 3) : 0;
  const __amdgpu_buffer_rsrc_t rs = __builtin_amdgcn_make_buffer_rsrc((void*)ws, 0, WS_USED, 0x00020000);
  auto flag = [&](int set, int tm, int tn) { return (GLB_AS u64*)(ws + OFF_FL + ((set * 16 + tm) * 16 + tn) * 128); };
  auto rsrc = [&](const u16* p, u32 bytes) { return __builtin_amdgcn_make_buffer_rsrc((void*)p, 0, bytes, 0x00020000); };
  auto token = [&]() {
    const u32x2 tk = *(const volatile LDS_AS u32x2*)(smem + L_TOK);
    return (u64)(u32)__builtin_amdgcn_readfirstlane(tk[0]) | ((u64)(u32)__builtin_amdgcn_readfirstlane(tk[1]) << 32);
  };
  auto ldf = [&](const GLB_AS u64* f) { return __hip_atomic_load(f, __ATOMIC_RELAXED, __HIP_MEMORY_SCOPE_AGENT); };
  auto poll = [&](const GLB_AS u64* f) {
    const u64 tok = token();
#pragma nounroll
    for (int spin = 0; spin < (1 << 22); ++spin) {
      if (__all(ldf(f) == tok)) break;
      __builtin_amdgcn_s_sleep(1);
    }
  };
  auto raise = [&](int set, int tm, int tn) {
    __hip_atomic_store(flag(set, tm, tn), token(), __ATOMIC_RELAXED, __HIP_MEMORY_SCOPE_AGENT);
  };
  int so = 0;

  if (wid < 4) {
    auto publish = [&](auto N, int ph, int tm, int tn, int st) {
      FRESH_LANE();
      SB();
      wait_vm<decltype(N)::value>();
      u32 old = 0;
      if (lane == 0) old = __hip_atomic_fetch_add((LDS_AS u32*)(smem + L_CNT) + st, 1u, __ATOMIC_RELAXED, __HIP_MEMORY_SCOPE_WORKGROUP);
      old = __builtin_amdgcn_readfirstlane(old);
      if ((old & 3) == 3 && lane == 0) raise(ph + 1 + F_2ND * st, tm, tn);
      SB();
    };
    const float* t_inv = (const float*)(smem + L_INV);
    const float* t_mnv = (const float*)(smem + L_MNV);
    const float* t_cs = (const float*)(smem + L_CS1);
    const float* t_bs = (const float*)(smem + L_BS);
    const int wmi = wid >> 1, wni = wid & 1;

    auto res_phase = [&](auto NSL_, u32 roff, int xo  , bool bias, int ph) {
      constexpr int NSL = decltype(NSL_)::value;
      FRESH_LANE();
      u32x4 res[2][2], bb[2];
#pragma unroll
      for (int i = 0; i < 2; ++i)
#pragma unroll
        for (int gl = 0; gl < 2; ++gl)
          res[i][gl] = __builtin_bit_cast(u32x4, __builtin_amdgcn_raw_buffer_load_b128(
              ph == 1 ? rsrc(x, SEQ * DM * 2) : rs, ph == 1 ? (16 * i + r) * 2048 + g * 16 : qoff((wid & 1) * 2 + i, lane),
              ph == 1 ? (tmB * 128 + wid * 32) * 2048 + tnB * 128 + gl * 64
                      : roff + ((tmB * 32 + 2 * tnB + gl) * 2 + (wid >> 1)) * QS, 16));
#pragma unroll
      for (int gl = 0; gl < 2; ++gl) {
        bb[gl] = *(const u32x4*)(b2 + tnB * 64 + gl * 32 + g * 8);
        if (!bias) bb[gl] = (u32x4){0u, 0u, 0u, 0u};
      }
      f32x4 acc[2][4];
#pragma unroll
      for (int i = 0; i < 2; ++i)
#pragma unroll
        for (int j = 0; j < 4; ++j) acc[i][j] = zero4();
      bf16x8 fA[2][2][2], fB[2][2][4];
      u32 pA, pB;
      kloop<NSL, 16, 12>(so,
        [&](int s_, auto) { pA = lane * 16 + wid * 2048 + s_; pB = lane * 16 + s_; pin2(pA, pB); },
        [&](auto SET, auto Q) {
          constexpr int set = decltype(SET)::value, q = decltype(Q)::value, s = q / 6, m = q % 6;
          if constexpr (m < 2) dsrd<s * 8192 + m * 1024>(fA[set][s][m], pA);
          else dsrd<16384 + s * 4096 + (m - 2) * 1024>(fB[set][s][m - 2], pB);
        },
        [&](auto SET, auto M) {
          constexpr int set = decltype(SET)::value, m = decltype(M)::value, s = m >> 3, j = (m >> 1) & 3, i = m & 1;
          mfma(acc[i][j], fB[set][s][j], fA[set][s][i]);
        },
        [&](int t) { if (NSL == 56 && t == HKT) publish(IC<0>{}, ph - 1, tmA, tnA, 1); });
      STAMP(0, 4 * ph + 2);
      u32x4 c[2][2];
#pragma unroll
      for (int i = 0; i < 2; ++i)
#pragma unroll
        for (int gl = 0; gl < 2; ++gl) {
#pragma unroll
          for (int k = 0; k < 4; ++k) {
            const f32x4& a = acc[i][2 * gl + (k >> 1)];
            const float y0 = a[2 * (k & 1)] + bf_lo(bb[gl][k]), y1 = a[2 * (k & 1) + 1] + bf_hi(bb[gl][k]);
            c[i][gl][k] = pack2(bf_lo(res[i][gl][k]) + y0, bf_hi(res[i][gl][k]) + y1);
          }
          if (xo < 4) st16(c[i][gl], OFF_X + xo * XS + ((tmB * 32 + 2 * tnB + gl) * 2 + (wid >> 1)) * QS + qoff((wid & 1) * 2 + i, lane));
          else __builtin_amdgcn_raw_buffer_store_b128(__builtin_bit_cast(i32x4, c[i][gl]), rsrc(out, SEQ * DM * 2),
                   (tmB * 128 + (2 * wid + i) * 16 + r) * 2048 + tnB * 128 + gl * 64 + g * 16, 0, 16);
        }
      if (xo < 4) {
        SB();
        const f32x2 s0 = row_part(c[0][0], c[0][1]), s1 = row_part(c[1][0], c[1][1]);
        __builtin_amdgcn_raw_buffer_store_b64(__builtin_bit_cast(i32x2, (g & 1) ? s1 : s0), rs,
            lane < 32 ? OFF_ST + xo * 262144 + ((tmB * 16 + tnB) * 128 + wid * 32 + lane) * 8 : 0x80000000u, 0, 16);
        publish(IC<1>{}, ph, tmB, tnB, 0);
        publish(IC<0>{}, ph, tmB, tnB, 1);
      }
      STAMP(0, 4 * ph + 3);
    };

#pragma nounroll
    for (int pair = 0; pair < 2; ++pair) {
      {
        f32x4 acc[4][6], acch[2];
        {
        FRESH_LANE();
        STAMP(0, 16 * pair);
        u32 pA, pB0, pB1;
        const u32 lB = 9216 + (8 * g + (r >> 2)) * 128 + (((2 * wni) ^ ((r >> 3) & 1) ^ ((g & 1) << 1)) * 32) + (r & 3) * 8;
        const u32 lA = r * 128 + ((g ^ (r >> 1)) * 16);
        auto loop = [&](auto WM, auto RAW) {
          constexpr int wm = decltype(WM)::value, raw = decltype(RAW)::value;
#pragma unroll
          for (int i = 0; i < 4; ++i)
#pragma unroll
            for (int j = 0; j < 6; ++j) acc[i][j] = zero4();
          acch[0] = zero4(); acch[1] = zero4();
          bf16x8 fA[2][4], fH[2], fB[2][6];
          u32 lA0 = raw ? lA : lane * 16, lA1 = raw ? (lA ^ 64) - SLOT : lane * 16, lB1 = lB ^ 32;
          pin2(lA0, lA1); pin1(lB1);
          kloop<32, 26, 11>(so,
            [&](int s_, auto PAR) {
              pA = (decltype(PAR)::value ? lA1 : lA0) + s_; pB0 = lB + s_; pB1 = lB1 + s_;
              pin2(pA, pB0); pin1(pB1);
            },
            [&](auto SET, auto Q) {
              constexpr int set = decltype(SET)::value, q = decltype(Q)::value;
              if constexpr (q < 4) dsrd<raw ? wm * SLOT + q * 2048 : wm * 4096 + q * 1024>(fA[set][q], pA);
              else if constexpr (q < 10) dstr<((q - 4) >> 1) * 4096>(fB[set][q - 4], (q & 1) ? pB1 : pB0);
              else dsrd<raw ? SLOT + 21504 : 8192>(fH[set], pA);
            },
            [&](auto SET, auto M) {
              constexpr int set = decltype(SET)::value, m = decltype(M)::value, j = m >> 2, i = m & 3;
              if constexpr (m < 16) mfma(acc[i][j], fB[set][j], fA[set][i]);
              else if constexpr (m < 24) mfma(acc[i][j], fA[set][i], fB[set][j]);
              else if constexpr (wm == 0) mfma(acch[i], fB[set][2 + i], fH[set]);
              else mfma(acch[i], fH[set], fB[set][4 + i]);
            }, [](int) {});
        };
        if (pair == 0) { if (wmi == 0) loop(IC<0>{}, IC<1>{}); else loop(IC<1>{}, IC<1>{}); }
        else { if (wmi == 0) loop(IC<0>{}, IC<0>{}); else loop(IC<1>{}, IC<0>{}); }
        }
        FRESH_LANE();
        STAMP(0, 16 * pair + 2);
        const float* csw = (const float*)(smem + L_CSQ) + wni * 96;
        auto fix_qk = [&](const f32x4& a0, const f32x4& a1, int trow, int f) {
          const float iv = t_inv[trow + r], mv = t_mnv[trow + r];
          const f32x4 c0 = *(const f32x4*)(csw + f * 16 + 4 * g), c1 = *(const f32x4*)(csw + f * 16 + 16 + 4 * g);
          const f32x4 y0 = a0 * iv - c0 * mv, y1 = a1 * iv - c1 * mv;
          return (u32x4){pack2(y0[0], y0[1]), pack2(y0[2], y0[3]), pack2(y1[0], y1[1]), pack2(y1[2], y1[3])};
        };
        auto fix_v = [&](const f32x4& a0, const f32x4& a1, int trow) {
          const f32x4 iv = *(const f32x4*)(t_inv + trow + 4 * g), mv = *(const f32x4*)(t_mnv + trow + 4 * g);
          const float c0 = csw[64 + r], c1 = csw[80 + r];
          const f32x4 y0 = a0 * iv - mv * c0, y1 = a1 * iv - mv * c1;
          return (u32x4){pack2(y0[0], y0[1]), pack2(y0[2], y0[3]), pack2(y1[0], y1[1]), pack2(y1[2], y1[3])};
        };
        u32x4 Qf[4], Kf[5], Vf[5];
        char* xc = smem + L_XC + wni * 3072 + lane * 16;
        const int row0 = 16 + wmi * 64;
        Kf[4] = fix_qk(acc[3][2], acc[3][3], row0 + 48, 2);
        Vf[4] = fix_v(acc[3][4], acc[3][5], row0 + 48);
        if (wmi == 0) {
          *(u32x4*)xc = Kf[4];
          *(u32x4*)(xc + 1024) = Vf[4];
          Kf[0] = fix_qk(acch[0], acch[1], 0, 2);
        } else {
          *(u32x4*)(xc + 2048) = fix_v(acch[0], acch[1], 0);
        }
#pragma unroll
        for (int i = 0; i < 3; ++i) {
          Kf[i + 1] = fix_qk(acc[i][2], acc[i][3], row0 + 16 * i, 2);
          Vf[i + 1] = fix_v(acc[i][4], acc[i][5], row0 + 16 * i);
        }
        wait_lgkm0();
        bar();
        if (wmi == 1) Kf[0] = *(const u32x4*)xc;
        Vf[0] = *(const u32x4*)(xc + (wmi == 0 ? 2048 : 1024));
#pragma unroll
        for (int i = 0; i < 4; ++i) Qf[i] = fix_qk(acc[i][0], acc[i][1], row0 + 16 * i, 0);
        const bool par = r & 1;
        const float m0 = par ? 0.f : 1.f, m1 = par ? 1.f : 0.f;
        const bool own0 = 4 * g + (r & 1) <= r, own1 = 4 * g + (r & 1) + 2 <= r;
        const bool first = (tmA == 0) && (wmi == 0);
#pragma unroll
        for (int ii = 0; ii < 4; ++ii) {
          const int i = 3 - ii;
          const f32x4 z = {0.f, 0.f, 0.f, 0.f};
          const f32x4 S0 = __builtin_amdgcn_mfma_f32_16x16x32_bf16(__builtin_bit_cast(bf16x8, Kf[i]), __builtin_bit_cast(bf16x8, Qf[i]), z, 0, 0, 0);
          const f32x4 S1 = __builtin_amdgcn_mfma_f32_16x16x32_bf16(__builtin_bit_cast(bf16x8, Kf[i + 1]), __builtin_bit_cast(bf16x8, Qf[i]), z, 0, 0, 0);
          float l0 = own0 ? (par ? S1[1] : S1[0]) : (par ? S0[1] : S0[0]);
          float l1 = own1 ? (par ? S1[3] : S1[2]) : (par ? S0[3] : S0[2]);
          const float c = 0.17677669529663687f * 1.4426950408889634f;
          l0 *= c; l1 *= c;
          if (i == 0 && first) {
            if (!own0) l0 = -1.4426950e9f;
            if (!own1) l1 = -1.4426950e9f;
          }
          const float mx = xmax(fmaxf(l0, l1));
          const float p0 = __builtin_amdgcn_exp2f(l0 - mx), p1 = __builtin_amdgcn_exp2f(l1 - mx);
          const float rsum = __builtin_amdgcn_rcpf(xsum(p0 + p1));
          const float q0 = p0 * rsum, q1 = p1 * rsum;
          const u32 w0 = pack2(q0 * m0, q0 * m1), w1 = pack2(q1 * m0, q1 * m1);
          const u32x4 P = {own0 ? 0u : w0, own1 ? 0u : w1, own0 ? w0 : 0u, own1 ? w1 : 0u};
          const u32x4 V0 = {Vf[i][0], Vf[i][1], Vf[i + 1][0], Vf[i + 1][1]};
          const u32x4 V1 = {Vf[i][2], Vf[i][3], Vf[i + 1][2], Vf[i + 1][3]};
          const f32x4 O0 = __builtin_amdgcn_mfma_f32_16x16x32_bf16(__builtin_bit_cast(bf16x8, V0), __builtin_bit_cast(bf16x8, P), z, 0, 0, 0);
          const f32x4 O1 = __builtin_amdgcn_mfma_f32_16x16x32_bf16(__builtin_bit_cast(bf16x8, V1), __builtin_bit_cast(bf16x8, P), z, 0, 0, 0);
          const u32x4 o = {pack2(O0[0], O0[1]), pack2(O0[2], O0[3]), pack2(O1[0], O1[1]), pack2(O1[2], O1[3])};
          st16(o, OFF_O + pair * XS + ((tmA * 32 + 2 * tnA + wni) * 2 + wmi) * QS + qoff(i, lane));
        }
        publish(IC<0>{}, 4 * pair, tmA, tnA, 0);
        STAMP(0, 16 * pair + 3);
      }
      res_phase(IC<16>{}, OFF_X + (2 * pair) * XS, 2 * pair + 1, false, 4 * pair + 1);
      {
        f32x4 acc[4][7];
        {
        FRESH_LANE();
#pragma unroll
        for (int i = 0; i < 4; ++i)
#pragma unroll
          for (int j = 0; j < 7; ++j) acc[i][j] = zero4();
        bf16x8 fA[2][4], fB[2][7];
        u32 pA, pB;
        kloop<32, 28, 11>(so,
          [&](int s_, auto) { pA = lane * 16 + wmi * 4096 + s_; pB = lane * 16 + wni * 7168 + s_; pin2(pA, pB); },
          [&](auto SET, auto Q) {
            constexpr int set = decltype(SET)::value, q = decltype(Q)::value;
            if constexpr (q < 4) dsrd<q * 1024>(fA[set][q], pA);
            else dsrd<8192 + (q - 4) * 1024>(fB[set][q - 4], pB);
          },
          [&](auto SET, auto M) {
            constexpr int set = decltype(SET)::value, m = decltype(M)::value, j = m >> 2, i = m & 3;
            mfma(acc[i][j], fB[set][j], fA[set][i]);
          }, [](int) {});
        }
        FRESH_LANE();
        STAMP(0, 16 * pair + 10);
        float iv[4], mv[4];
#pragma unroll
        for (int i = 0; i < 4; ++i) { iv[i] = t_inv[16 + wmi * 64 + 16 * i + r]; mv[i] = t_mnv[16 + wmi * 64 + 16 * i + r]; }
        const u32 hbase = OFF_H + pair * HS + (tmA * 224 + wmi) * QS;
        auto act = [&](const f32x4& a, const f32x4& cs, const f32x4& bs, int i) {
          const f32x4 y = a * iv[i] + (bs - cs * mv[i]);
          return (u32x2){gelu_tanh2((f32x2){y[0], y[1]}), gelu_tanh2((f32x2){y[2], y[3]})};
        };
#pragma unroll
        for (int jp = 0; jp < 3; ++jp) {
          const int t0 = (wni * 7 + 2 * jp) * 16 + 4 * g;
          const f32x4 cs0 = *(const f32x4*)(t_cs + t0), cs1 = *(const f32x4*)(t_cs + t0 + 16);
          const f32x4 bs0 = *(const f32x4*)(t_bs + t0), bs1 = *(const f32x4*)(t_bs + t0 + 16);
#pragma unroll
          for (int i = 0; i < 4; ++i) {
            const u32x2 a = act(acc[i][2 * jp], cs0, bs0, i), b = act(acc[i][2 * jp + 1], cs1, bs1, i);
            const u32x4 v = {a[0], a[1], b[0], b[1]};
            st16(v, hbase + hpos(tnA, wni * 4 + jp) * 2 * QS + qoff(i, lane));
            if (4 * jp + i == 3 + PUBJ) publish(IC<PUBJ>{}, 4 * pair + 2, tmA, tnA, 0);
          }
          if (jp == 0) SB();
        }
        {
          const int t0 = (wni * 7 + 6) * 16 + 4 * g;
          const f32x4 cs0 = *(const f32x4*)(t_cs + t0), bs0 = *(const f32x4*)(t_bs + t0);
#pragma unroll
          for (int i = 0; i < 4; i += 2) {
            const u32x2 a = act(acc[i][6], cs0, bs0, i), b = act(acc[i + 1][6], cs0, bs0, i + 1);
            const auto s0 = __builtin_amdgcn_permlane16_swap(a[0], b[0], false, false);
            const auto s1 = __builtin_amdgcn_permlane16_swap(a[1], b[1], false, false);
            const u32x4 v = {s0[0], s1[0], s0[1], s1[1]};
            st16(v, hbase + hpos(tnA, 3) * 2 * QS + qoff(i + (g & 1), (2 * wni + (g >> 1)) * 16 + r));
          }
        }
        if (HKT < 0) publish(IC<0>{}, 4 * pair + 2, tmA, tnA, 1);
        STAMP(0, 16 * pair + 11);
      }
      res_phase(IC<56>{}, OFF_X + (2 * pair + 1) * XS, pair == 0 ? 2 : 4, true, 4 * pair + 3);
    }
  } else if (wid == 4) {
    {
      FRESH_LANE();
      STAMP(1, 0);
      const __amdgpu_buffer_rsrc_t rx = rsrc(x, SEQ * DM * 2);
      u32 vo[4];
#pragma unroll
      for (int e = 0; e < 4; ++e)
        vo[e] = (8 * e + (lane >> 3)) * 2048 + (((lane & 7) ^ (4 * (e & 1) + (lane >> 4))) * 16) - e * 1024;
      const u32 sx = tmA * 128 * 2048, sh = (tmA > 0 ? tmA * 128 - 16 : 0) * 2048;
      auto half = [&](auto H, int U) {
        constexpr int h = decltype(H)::value;
        for_n<8>([&](auto E) {
          constexpr int e = decltype(E)::value;
          DMA(rx, (LDS_AS void*)(smem + so + h * SLOT + (e >> 2) * 4096), 16, vo[e & 3],
              sx + (h * 64 + (e >> 2) * 32) * 2048 + U * 128, (e & 3) * 1024, 0);
        });
        DMA(rx, (LDS_AS void*)(smem + so + SLOT + 21504), 16, vo[h], sh + U * 128, h * 1024, 0);
        if constexpr (h == 1) { so += 2 * SLOT; if (so == RING) so = 0; }
      };
#pragma nounroll
      for (int U = 0; U < 2 + PRE; ++U) { half(IC<0>{}, U); half(IC<1>{}, U); }
      if (PRE) { wait_vm<36>(); bar(); bar(); }
#pragma nounroll
      for (int U = 2 + PRE; U < 16; ++U) {
        wait_vm<18>();
        bar();
        half(IC<0>{}, U);
        bar();
        half(IC<1>{}, U);
      }
      wait_vm<18>(); bar(); bar();
      wait_vm<0>(); bar(); bar();
    }
    auto phase = [&](auto NSL_, auto PA_, auto HALO_, u32 src, u32 hsrc, int ph  , int tm, int tmh) {
      constexpr int NSL = decltype(NSL_)::value, PA = decltype(PA_)::value, HALO = decltype(HALO_)::value;
      constexpr int PT = PA + HALO, D = PA == 16 ? DN : DW;
      FRESH_LANE();
      u32 vo[4];
#pragma unroll
      for (int e = 0; e < 4; ++e) vo[e] = qoff(e, lane) - e * 1024;
      poll(flag(ph + 1, (lane & 16) ? tmh : tm, PA == 16 ? (rotB + (r < DN ? r : 0)) & 15 : (rotA + (2 * r < DW ? r : 0)) & 15));
      STAMP(1, 4 * (ph + 1));
      auto issue = [&](int u) {
        u = PA == 16 ? (u < 16 ? (u + rotB) & 15 : u) : (u + 2 * rotA) & 31;
        if constexpr (PA == 16) wait_vm<47>();
        for_n<PA>([&](auto E) {
          constexpr int e = decltype(E)::value;
          DMA(rs, (LDS_AS void*)(smem + so + (e >> 2) * 4096), 16, vo[e & 3], src + u * (PA / 4 * QS) + (e >> 2) * QS,
              (e & 3) * 1024, 16);
        });
        if constexpr (HALO) DMA(rs, (LDS_AS void*)(smem + so + 8192), 16, vo[3], hsrc + u * (2 * QS), 0, 16);
        next_slot(so);
      };
#pragma nounroll
      for (int u = 0; u < D; ++u) issue(u);
#pragma nounroll
      for (int u = 0; u < NSL - D; ++u) {
        wait_vm<(D - 1) * PT>();
        bar();
        issue(u + D);
      }
      for_n<D>([&](auto M) { wait_vm<(D - 1 - decltype(M)::value) * PT>(); bar(); });
    };
#pragma nounroll
    for (int pair = 0; pair < 2; ++pair) {
      const int tmh = tmA > 0 ? tmA - 1 : 0;
      const u32 xq = OFF_X + 2 * pair * XS;
      if (pair) phase(IC<32>{}, IC<8>{}, IC<1>{}, xq + tmA * (64 * QS), xq + tmh * (64 * QS) + QS + 3072, 3, tmA, tmh);
      bar();
      phase(IC<16>{}, IC<16>{}, IC<0>{}, OFF_O + pair * XS + tmB * (64 * QS), 0, 4 * pair, tmB, tmB);
      phase(IC<32>{}, IC<8>{}, IC<0>{}, xq + XS + tmA * (64 * QS), 0, 4 * pair + 1, tmA, tmA);
      phase(IC<56>{}, IC<16>{}, IC<0>{}, OFF_H + pair * HS + tmB * (224 * QS), 0, 4 * pair + 2, tmB, tmB);
    }
  } else if (wid == 5) {
    FRESH_LANE();
    auto phase = [&](auto NSL_, auto PB_, int rot, auto&& piece) {
      constexpr int NSL = decltype(NSL_)::value, PB = decltype(PB_)::value;
      auto issue = [&](int u) {
        u = PB == 8 ? (u < 16 ? (u + rot) & 15 : u) : (u + rot) & 31;
        wait_vm<63 - PB>();
        for_n<PB>([&](auto I) { piece(I, u); });
        next_slot(so);
      };
      constexpr int D = PB == 8 ? (DN < 4 ? DN : 5) : DW;
#pragma nounroll
      for (int u = 0; u < D; ++u) issue(u);
#pragma nounroll
      for (int u = 0; u < NSL - D; ++u) {
        wait_vm<(D - 1) * PB>();
        bar();
        issue(u + D);
      }
      for_n<D>([&](auto M) { wait_vm<(D - 1 - decltype(M)::value) * PB>(); bar(); });
    };
    auto tiles = [&](auto NSL_, auto PB_, auto BOFF_, u32 src) {
      constexpr int PB = decltype(PB_)::value, BOFF = decltype(BOFF_)::value;
      u32 vo[4];
#pragma unroll
      for (int e = 0; e < 4; ++e) vo[e] = qoff(e, lane) - e * 1024;
      phase(NSL_, PB_, PB == 8 ? rotB : 2 * rotA, [&](auto I, int u) {
        constexpr int i = decltype(I)::value;
        DMA(rs, (LDS_AS void*)(smem + so + BOFF + (i >> 2) * 4096), 16, vo[i & 3],
            src + u * ((PB + 3) / 4 * QS) + (i >> 2) * QS, (i & 3) * 1024, 16);
      });
    };
#pragma nounroll
    for (int pair = 0; pair < 2; ++pair) {
      {
        const __amdgpu_buffer_rsrc_t rq = rsrc(wqkv, DM * 3 * DM * 2);
        u32 vo[4];
#pragma unroll
        for (int e = 0; e < 4; ++e)
          vo[e] = (8 * e + (lane >> 3)) * 6144 + (((lane & 7) ^ (2 * ((lane >> 4) & 1) + 4 * (e & 1))) * 16) - e * 1024;
        phase(IC<32>{}, IC<12>{}, pair ? 2 * rotA : 0, [&](auto I, int u) {
          constexpr int i = decltype(I)::value;
          DMA(rq, (LDS_AS void*)(smem + so + 9216 + (i >> 2) * 4096), 16, vo[i & 3],
              u * (32 * 6144) + (i >> 2) * 2048 + tnA * 128, (i & 3) * 1024, 0);
        });
      }
      bar();
      if (pair == 0) poll(flag(F_WO, r, tnB));
      tiles(IC<16>{}, IC<8>{}, IC<16384>{}, OFF_WO + tnB * (32 * QS));
      if (pair == 0) poll(flag(F_W1, r, (tnA & 14) + (g & 1)));
      tiles(IC<32>{}, IC<14>{}, IC<8192>{}, OFF_W1 + tnA * (32 * 4 * QS));
      if (pair == 0) poll(flag(F_W2, r, tnB));
      tiles(IC<56>{}, IC<8>{}, IC<16384>{}, OFF_W2 + tnB * (112 * QS));
    }
  } else if (wid == 6) {
    FRESH_LANE();
    const int rr = lane >> 3, c16 = (lane & 7) * 16;
    const __amdgpu_buffer_rsrc_t rx = rsrc(x, SEQ * DM * 2), rq = rsrc(wqkv, DM * 3 * DM * 2);
    auto warm = [&](int j) {
      const int n = idx * 64 + lane, m = idx * 48 + (lane < 48 ? lane : 47), k = (m * 5462) >> 16, p = m - 12 * k;
      if (PFM & 1) DMA(rx, (LDS_AS void*)(smem + L_PF), 4, (n & 1023) * 2048 + (n >> 10) * 128, (xcd >> 2) * (1024 * 2048) + j * 256, 0, 0);
      if (PFM & 2) DMA(rq, (LDS_AS void*)(smem + L_PF), 4, k * 6144 + (p >> 2) * 2048 + (p & 3) * 128, (xcd & 3) * 512 + j * (128 * 6144), 0, 0);
    };
    u32x4 U[7][4], F[4];
    auto u_load = [&](auto I) {
      constexpr int i = decltype(I)::value;
#pragma unroll
      for (int e = 0; e < 4; ++e) {
        if constexpr (i < 2)
          U[(i + 5) % 7][e] = __builtin_bit_cast(u32x4, __builtin_amdgcn_raw_buffer_load_b128(rsrc(wo, DM * DM * 2),
              ((rr & 3) + 16 * (rr >> 2)) * 2048 + c16, ((2 * tmA + i) * 32 + 4 * e) * 2048 + tnA * 128, UAUX));
        else if constexpr (i < 9)
          U[(i + 5) % 7][e] = __builtin_bit_cast(u32x4, __builtin_amdgcn_raw_buffer_load_b128(rsrc(w1, DM * FF * 2),
              rr * 7168 + c16, ((2 * tmA + (tnA & 1)) * 32 + 8 * e) * 7168 + (7 * (tnA >> 1) + i - 2) * 128, UAUX));
        else
          U[(i + 5) % 7][e] = __builtin_bit_cast(u32x4, __builtin_amdgcn_raw_buffer_load_b128(rsrc(w2, DM * FF * 2),
              rr * 2048 + c16, ((7 * tmA + i - 9) * 32 + 8 * e) * 2048 + tnA * 128, UAUX));
      }
    };
    auto u_write = [&](auto I) {
      constexpr int i = decltype(I)::value;
#pragma unroll
      for (int e = 0; e < 4; ++e) {
        const u32x4 v = U[(i + 5) % 7][e];
        *(u32x4*)(smem + L_IM + e * 1024 + rr * 128 + (c16 ^ ((rr & 2) << 5))) = (e & 1) ? (u32x4){v[2], v[3], v[0], v[1]} : v;
      }
    };
    auto u_store = [&](auto I) {
      constexpr int i = decltype(I)::value;
      const u32 sw = ((r & 8) << 3) | ((g & 1) << 3);
      const char* im = smem + L_IM + (8 * g + (r >> 2)) * 128;
      for_n<4>([&](auto E) {
        constexpr int e = decltype(E)::value;
        constexpr bool split = i >= 2 && i < 9 && (2 * (i - 2) + (e >> 1)) % 7 == 3;
        const char* p = im + (((e >> 1) * 64 + (split ? (e & 1) * 32 + (r & 3) * 8 : (r & 3) * 16 + (e & 1) * 8)) ^ sw);
        const u32x2 lo = __builtin_bit_cast(u32x2, __builtin_amdgcn_ds_read_tr16_b64_v4i16((LDS_AS s16x4*)p));
        const u32x2 hi = __builtin_bit_cast(u32x2, __builtin_amdgcn_ds_read_tr16_b64_v4i16((LDS_AS s16x4*)(p + 512)));
        F[e] = (u32x4){lo[0], lo[1], hi[0], hi[1]};
      });
      for_n<4>([&](auto E) {
        constexpr int e = decltype(E)::value;
        if constexpr (i < 2) st16(F[e], OFF_WO + (tnA * 32 + 2 * tmA + i) * QS + qoff(e, lane));
        else if constexpr (i < 9) {
          constexpr int f = w1_frag(i, e);
          st16(F[e], OFF_W1 + ((((tnA & 14) + (f >> 4)) * 32 + 2 * tmA + (tnA & 1)) * 4 + ((f & 15) >> 2)) * QS + qoff(f & 3, lane));
        } else st16(F[e], OFF_W2 + (tnA * 112 + hpos(tmA, i - 9)) * QS + qoff(e, lane));
      });
    };
    auto u_sums = [&](auto I) {
      constexpr int i = decltype(I)::value;
      constexpr u32 K = w1_frag(i, 0) | (w1_frag(i, 1) << 8) | (w1_frag(i, 2) << 16) | (w1_frag(i, 3) << 24);
      const float cs = fold16(fold32(sum8(F[0]), sum8(F[2])), fold32(sum8(F[1]), sum8(F[3])));
      __builtin_amdgcn_raw_buffer_store_b32(__float_as_uint(cs), rs,
          OFF_CS1 + ((2 * tmA + (tnA & 1)) * 4096 + (tnA & 14) * 256 + ((K >> (8 * g)) & 255) * 16 + r) * 4, 0, 16);
    };
    auto up = [&](int set) { if (lane == 0) raise(set, tmA, tnA); };
    u32x4 Q[24];
    float qs[8];
    __builtin_amdgcn_s_sleep(PFW0);
#pragma unroll
    for (int e = 0; e < 24; ++e)
      Q[e] = __builtin_bit_cast(u32x4, __builtin_amdgcn_raw_buffer_load_b128(rq, rr * 6144 + c16,
          (tmA * 64 + 8 * (e & 7)) * 6144 + (e >> 3) * 2048 + tnA * 128, 0));
    u_load(IC<0>{});
    u_load(IC<1>{});
    for_n<33>([&](auto S) {
      constexpr int s = decltype(S)::value;
      bar();
      if constexpr (s == CQP + 5 + FLD) { wait_vm<between(CQP + 5, s)>(); up(F_CQ); }
      if constexpr (s == 26) { wait_vm<0>(); up(F_WO); }
      if constexpr (s >= PF0 && (s - PF0) % PFS == 0 && (s - PF0) / PFS < 8 - PFJ) warm(PFJ + (s - PF0) / PFS);
      if constexpr (s >= CQP && s < CQP + 6) {
        constexpr int h = s - CQP;
#pragma unroll
        for (int e = 0; e < 4; ++e) {
          u32x4 v = Q[4 * h + e];
          asm volatile("" : "+v"(v));
#pragma unroll
          for (int k = 0; k < 4; ++k) {
            if (h % 2 == 0 && e == 0) { qs[2 * k] = bf_lo(v[k]); qs[2 * k + 1] = bf_hi(v[k]); }
            else { qs[2 * k] += bf_lo(v[k]); qs[2 * k + 1] += bf_hi(v[k]); }
          }
        }
        if constexpr (h % 2 == 0) {
#pragma unroll
          for (int k = 0; k < 8; ++k) asm volatile("" : "+v"(qs[k]));
        } else {
          float c0 = fold16(fold32(qs[0], qs[4]), fold32(qs[2], qs[6])), c1 = fold16(fold32(qs[1], qs[5]), fold32(qs[3], qs[7]));
          c0 += __uint_as_float(__builtin_amdgcn_mov_dpp(__float_as_uint(c0), 0x128, 0xf, 0xf, false));
          c1 += __uint_as_float(__builtin_amdgcn_mov_dpp(__float_as_uint(c1), 0x128, 0xf, 0xf, false));
          const int col = 8 * (lane & 7) + rr;
          __builtin_amdgcn_raw_buffer_store_b32(__float_as_uint((rr & 1) ? c1 : c0), rs,
              OFF_CSQ + (tmA * 4096 + tnA * 256 + (col >> 5) * 96 + (h >> 1) * 32 + (col & 31)) * 4, 0, 16);
        }
      }
      if constexpr (s >= HL && s < HL + 4) {
        if constexpr ((s - HL) % 2 == 0) u_write(IC<(s - HL) / 2>{}); else u_store(IC<(s - HL) / 2>{});
      }
      if constexpr (s == G0) for_n<7>([&](auto I) { u_load(IC<2 + decltype(I)::value>{}); });
    });
    static_assert(HL >= CQP + 6 && HL + 4 < 26 && CQP + 5 + FLD < 26 && G0 >= 26, "schedule");
    for_n<7>([&](auto I) {
      constexpr int i = 2 + decltype(I)::value;
      u_write(IC<i>{});
      u_store(IC<i>{});
      u_sums(IC<i>{});
    });
    bar(); bar(); bar();
    wait_vm<0>();
    up(F_W1);
#pragma nounroll
    for (int u = 3; u <= G1; ++u) bar();
    for_n<7>([&](auto I) { u_load(IC<9 + decltype(I)::value>{}); });
#pragma nounroll
    for (int u = G1 + 1; u < 16 + H2; ++u) bar();
    for_n<14>([&](auto S) {
      constexpr int s = decltype(S)::value, i = 9 + s / 2;
      bar();
      if constexpr (s % 2 == 0) u_write(IC<i>{}); else u_store(IC<i>{});
    });
    bar(); bar(); bar(); bar();
    wait_vm<0>();
    up(F_W2);
  } else {
    FRESH_LANE();
    const int tmh = tmA > 0 ? tmA - 1 : 0;
    auto ln_phase = [&](bool qkv, int xi) {
      f32x4 sv[16], cv[16];
      f32x2 hv[4];
      const u32 so0 = OFF_ST + xi * 262144 + tmA * 16384, so1 = OFF_ST + xi * 262144 + (qkv ? tmh : tmA) * 16384;
      const u32 sc = (qkv ? OFF_CSQ : OFF_CS1) + tnA * 1024;
      auto ldcs = [&](int p0) {
#pragma unroll
        for (int p = 0; p < 16; ++p)
          cv[p] = __builtin_bit_cast(f32x4, __builtin_amdgcn_raw_buffer_load_b128(rs, lane * 16, sc + (p0 + p) * 16384, 16));
      };
#pragma unroll
      for (int p = 0; p < 16; ++p)
        sv[p] = __builtin_bit_cast(f32x4, __builtin_amdgcn_raw_buffer_load_b128(rs, lane * 16, so0 + p * 1024, 16));
#pragma unroll
      for (int p = 0; p < 4; ++p)
        hv[p] = __builtin_bit_cast(f32x2, __builtin_amdgcn_raw_buffer_load_b64(rs, (112 + r) * 8 + g * 4096, so1 + p * 1024, 16));
      bar();
      if (xi < 2) ldcs(0);
      bar(); bar();
      float mean[3] = {0.f, 0.f, 0.f}, m2[3] = {0.f, 0.f, 0.f}, dd[3] = {0.f, 0.f, 0.f};
#pragma unroll
      for (int p = 0; p < 16; ++p) { mean[0] += sv[p][0]; mean[1] += sv[p][2]; }
      mean[2] = xsum(hv[0][0] + hv[1][0] + hv[2][0] + hv[3][0]);
#pragma unroll
      for (int h = 0; h < 3; ++h) mean[h] *= (1.f / 16.f);
#pragma unroll
      for (int p = 0; p < 4; ++p) { const float d = hv[p][0] - mean[2]; m2[2] += hv[p][1]; dd[2] += d * d; }
      auto cut = [&]() {
        asm volatile("" : "+v"(mean[0]), "+v"(mean[1]), "+v"(mean[2]), "+v"(m2[0]), "+v"(m2[1]), "+v"(m2[2]),
                          "+v"(dd[0]), "+v"(dd[1]), "+v"(dd[2]));
        bar();
        asm volatile("" : "+v"(mean[0]), "+v"(mean[1]), "+v"(mean[2]), "+v"(m2[0]), "+v"(m2[1]), "+v"(m2[2]),
                          "+v"(dd[0]), "+v"(dd[1]), "+v"(dd[2]));
      };
#pragma unroll
      for (int p = 0; p < 16; ++p) {
        if (p % 8 == 0) cut();
        const float d0 = sv[p][0] - mean[0], d1 = sv[p][2] - mean[1];
        m2[0] += sv[p][1]; dd[0] += d0 * d0;
        m2[1] += sv[p][3]; dd[1] += d1 * d1;
      }
      m2[0] += 64.f * dd[0]; m2[1] += 64.f * dd[1];
      m2[2] = xsum(m2[2] + 64.f * dd[2]);
      cut();
#pragma unroll
      for (int h = 0; h < 3; ++h) { mean[h] = bfr(mean[h]); m2[h] = bfr(sqrtf(bfr(bfr(m2[h] * (1.f / DM)) + LN_EPS))); }
      cut();
#pragma unroll
      for (int h = 0; h < 3; ++h) m2[h] = 1.f / m2[h];
      *(f32x2*)(smem + L_INV + 64 + lane * 8) = (f32x2){m2[0], m2[1]};
      *(f32x2*)(smem + L_MNV + 64 + lane * 8) = (f32x2){mean[0] * m2[0], mean[1] * m2[1]};
      *(float*)(smem + L_INV + r * 4) = m2[2];
      *(float*)(smem + L_MNV + r * 4) = mean[2] * m2[2];
      bar();
      f32x4 ca = {0.f, 0.f, 0.f, 0.f};
      if (xi < 2) {
#pragma unroll
        for (int p = 0; p < 16; ++p) ca += cv[p];
        if (qkv) *(f32x4*)(smem + L_CSQ + lane * 16) = ca;
      }
      if (!qkv) {
        asm volatile("" : "+v"(ca));
        if (xi < 2) ldcs(16);
        bar(); bar(); bar(); bar();
        if (xi < 2) {
#pragma unroll
          for (int p = 0; p < 16; ++p) ca += cv[p];
          *(f32x4*)(smem + L_CS1 + lane * 16) = ca;
        }
      }
      wait_lgkm0();
    };
    constexpr int NLN = 8;
    auto bars = [&](int n) {
#pragma nounroll
      for (int u = 0; u < n; ++u) bar();
    };
    auto check = [&](const GLB_AS u64* f, int n) {
      const u64 v = ldf(f);
      bars(n);
      if (!__all(v == token())) poll(f);
    };
    auto guard = [&](const GLB_AS u64* f, int spp, int D, int K, auto&& step) {
      const u64 tok = token();
      int b = 0, lim = 0;
#pragma nounroll
      for (int spin = 0; spin < (1 << 22); ++spin) {
        const u64 v = ldf(f);
        if (b < lim) { bar(); step(b++); } else __builtin_amdgcn_s_sleep(1);
        if (b < lim) { bar(); step(b++); }
        const u32 m = (u32)__ballot(v == tok);
        const int n = __builtin_ctz(~(m & (m >> 16)));
        if (n >= 16) break;
        lim = min(K, n * spp - D);
      }
#pragma nounroll
      while (b < K) { bar(); step(b++); }
    };
    {
      const u64 ep = __hip_atomic_load((const GLB_AS u64*)(ws + OFF_EP), __ATOMIC_RELAXED, __HIP_MEMORY_SCOPE_AGENT);
      u16 bv[4];
#pragma unroll
      for (int h = 0; h < 4; ++h) {
        const int t = min(lane + 64 * h, 223), f = t >> 4, rho = t & 15, wn = f / 7, j = f % 7;
        bv[h] = b1[tnA * 224 + (j == 6 ? 96 + 16 * wn + rho : (4 * wn + (j >> 1)) * 32 + 8 * (rho >> 2) + 4 * (j & 1) + (rho & 3))];
      }
      __builtin_amdgcn_s_sleep(XW);
      bars(XL);
      const __amdgpu_buffer_rsrc_t rx = rsrc(x, SEQ * DM * 2);
      bf16x8 xf[8][2];
#pragma unroll
      for (int i = 0; i < 8; ++i)
#pragma unroll
        for (int gl = 0; gl < 2; ++gl)
          xf[i][gl] = __builtin_bit_cast(bf16x8, __builtin_amdgcn_raw_buffer_load_b128(rx, r * 2048 + g * 16,
              (tmA * 128 + 16 * i) * 2048 + tnA * 128 + gl * 64, 0));
      bars(TOKB - XL);
#pragma unroll
      for (int h = 0; h < 4; ++h) ((float*)(smem + L_BS))[lane + 64 * h] = bf_lo(bv[h]);
      if (lane == 0) {
        *(LDS_AS u64*)(smem + L_CNT) = 0ull;
        *(LDS_AS u64*)(smem + L_TOK) = ep * 6364136223846793005ull + 1442695040888963407ull;
      }
      wait_lgkm0();
      bars(XP - TOKB);
      const u32x4 one4 = {0x3f803f80u, 0x3f803f80u, 0x3f803f80u, 0x3f803f80u};
      const bf16x8 ones = __builtin_bit_cast(bf16x8, one4);
      const u32 svo = g == (r >> 2) ? OFF_ST + (tmA * 16 + tnA) * 1024 + r * 8 : 0x80000000u;
#pragma unroll
      for (int i = 0; i < 8; ++i) {
        if (i == 4) bar();
        const f32x4 z = {0.f, 0.f, 0.f, 0.f};
        f32x4 s = __builtin_amdgcn_mfma_f32_16x16x32_bf16(ones, xf[i][0], z, 0, 0, 0);
        f32x4 q = __builtin_amdgcn_mfma_f32_16x16x32_bf16(xf[i][0], xf[i][0], z, 0, 0, 0);
        s = __builtin_amdgcn_mfma_f32_16x16x32_bf16(ones, xf[i][1], s, 0, 0, 0);
        q = __builtin_amdgcn_mfma_f32_16x16x32_bf16(xf[i][1], xf[i][1], q, 0, 0, 0);
        const float qq = (r & 2) ? ((r & 1) ? q[3] : q[2]) : ((r & 1) ? q[1] : q[0]);
        const float m = s[0] * (1.f / 64.f);
        const f32x2 o = {m, fmaxf(__builtin_fmaf(-s[0], m, qq), 0.f)};
        __builtin_amdgcn_raw_buffer_store_b64(__builtin_bit_cast(i32x2, o), rs, svo + i * 128, 0, 16);
      }
      bars(FLD);
      wait_vm<0>();
      if (lane == 0) raise(F_ST, tmA, tnA);
      bars(POLB - (XP + 1 + FLD));
      static_assert(TOKB >= XL && XP >= TOKB && POLB >= XP + 1 + FLD && POLB + 2 + NLN <= 31 && POLB + 2 > CQP + 5 + FLD, "schedule");
    }
#pragma nounroll
    for (int pair = 0; pair < 2; ++pair) {
      if (pair) {
        const __amdgpu_buffer_rsrc_t rq = rsrc(wqkv, DM * 3 * DM * 2);
        const int m = idx * 48 + (lane < 48 ? lane : 47), k = (m * 5462) >> 16, p = m - 12 * k;
        auto warm = [&](int j) {
          DMA(rq, (LDS_AS void*)(smem + L_PF), 4, k * 6144 + (p >> 2) * 2048 + (p & 3) * 128,
              (xcd & 3) * 512 + ((j + (rotA >> 1)) & 7) * (128 * 6144), 0, 0);
        };
        warm(0); warm(1); warm(2);
        guard(flag(4, (g & 1) ? tmh : tmA, (rotA + r) & 15), 2, DW, POLB, [&](int b) { if ((b & 1) && b < 10) warm(3 + (b >> 1)); });
      }
      check(pair == 0 && g >= 2 ? flag(F_CQ, r, tnA) : flag(pair ? 4 + F_2ND : F_ST, (g & 1) ? tmh : tmA, r), 2);
      ln_phase(true, 2 * pair);
      bars(33 - (POLB + 2 + NLN));
      guard(flag(4 * pair + 1, tmB, (rotB + r) & 15), 1, DN, 16, [](int) {});
      guard(flag(4 * pair + 2, tmA, (rotA + r) & 15), 2, DW, KW1, [](int) {});
      check(flag(4 * pair + 2 + F_2ND, tmA, r), 2);
      ln_phase(false, 2 * pair + 1);
      bars(32 - (KW1 + 2 + NLN + 4));
      guard(flag(4 * pair + 3, tmB, (rotB + r) & 15), 1, DN, 16 - DN - CB, [](int) {});
      if (pair == 0 && blockIdx.x == 0 && lane == 0)
        __hip_atomic_store((GLB_AS u64*)(ws + OFF_EP), token(), __ATOMIC_RELAXED, __HIP_MEMORY_SCOPE_AGENT);
      check(flag(4 * pair + 3 + F_2ND, tmB, r), CB);
      bars(56 - (16 - DN));
    }
  }
#endif
}

extern "C" void kernel_launch(void* const* d_in, const int* in_sizes, int n_in, void* d_out, int out_size,
                              void* d_ws, size_t ws_size, hipStream_t stream) {
  const u16* x0 = (const u16*)d_in[0];
  const u16* wqkv = (const u16*)d_in[1];
  const u16* wo = (const u16*)d_in[2];
  const u16* w1 = (const u16*)d_in[4];
  const u16* b1 = (const u16*)d_in[5];
  const u16* w2 = (const u16*)d_in[6];
  const u16* b2 = (const u16*)d_in[7];
  char* ws = (char*)(((uintptr_t)d_ws + 1023) & ~(uintptr_t)1023);
  (void)hipFuncSetAttribute((const void*)mega_k, hipFuncAttributeMaxDynamicSharedMemorySize, LDS_BYTES);
  mega_k<<<256, 512, LDS_BYTES, stream>>>(ws, x0, wqkv, wo, w1, b1, w2, b2, (u16*)d_out);
}
```

```cpp
#include <hip/hip_runtime.h>
#include <stdint.h>
#include <type_traits>
#include <utility>

typedef unsigned short u16;
typedef unsigned int u32;
typedef unsigned long long u64;
typedef __bf16 bf16x8 __attribute__((ext_vector_type(8)));
typedef __bf16 bf16x2 __attribute__((ext_vector_type(2)));
typedef short s16x4 __attribute__((ext_vector_type(4)));
typedef float f32x4 __attribute__((ext_vector_type(4)));
typedef float f32x2 __attribute__((ext_vector_type(2)));
typedef u32 u32x4 __attribute__((ext_vector_type(4)));
typedef u32 u32x2 __attribute__((ext_vector_type(2)));
typedef int i32x4 __attribute__((ext_vector_type(4)));
typedef int i32x2 __attribute__((ext_vector_type(2)));
#define LDS_AS __attribute__((address_space(3)))
#define GLB_AS __attribute__((address_space(1)))

constexpr int SEQ = 2048, DM = 1024, FF = 3584;
constexpr float LN_EPS = 1e-5f;

#define SKIP 1
#define DN 4
#define DW 5
#define UAUX 2
#define PFW0 30
#define PF0 4
#define PFS 2
#define PFJ 2
#define PFM 3
#define CQP 7
#define HL 16
#define FLD 3
#define XW 30
#define XL 0
#define TOKB 3
#define XP 7
#define POLB 18
#define G0 27
#define G1 12
#define H2 4
#define PRE 1
#define PUBJ 2
#define HKT -2
#define ROT 1
#define CB 1
#define KW1 11
#define ROT0 1
#define AXM 0
#define AX(b) ((AXM >> (b)) & 1 ? 2 : 0)
#define R0 5
#define RA 3
#define J0 PFJ
#define QB -1
#define CLB 1
#define MT 0
#define MTS 12
#define MTJ 1
#define WOB (QB + 3)
#define CW 0
#define SND 12
#define RST 9
#define PFX 29
constexpr u32 MB = 1u << 20;
constexpr u32 QS = SKIP ? 5120 : 4096;
constexpr u32 XS = 5 * MB, HS = 18 * MB;
constexpr u32 OFF_WO = 8 * MB;
constexpr u32 OFF_W1 = 11 * MB;
constexpr u32 OFF_W2 = 21 * MB;
constexpr u32 OFF_X = 30 * MB;
constexpr u32 OFF_O = OFF_X + 4 * XS;
constexpr u32 OFF_H = OFF_O + 2 * XS;
constexpr u32 OFF_ST = OFF_H + 2 * HS;
constexpr u32 OFF_CSQ = OFF_ST + MB;
constexpr u32 OFF_CS1 = OFF_CSQ + 262144;
constexpr u32 OFF_FL = OFF_CS1 + 524288;
constexpr u32 OFF_EP = OFF_FL + 26 * 32768;
constexpr u32 OFF_DBG = OFF_ST + 3 * MB;
constexpr u32 OFF_XC = OFF_ST + 4 * MB;
constexpr u32 WS_USED = OFF_XC + 16 * MB;
constexpr int F_ST = 0, F_CQ = 8, F_WO = 9, F_W1 = 10, F_W2 = 11, F_2ND = 10, F_XC = 18;
__device__ __forceinline__ u32 qoff(u32 e, u32 slot) {
  const u32 n = 8 * e + (slot >> 3);
  return ((n + (SKIP ? (((n + 4) * 37) >> 8) : 0)) << 7) + (slot & 7) * 16;
}
__device__ __forceinline__ u32 hpos(u32 t, u32 j) {
  return (t >> 3) * 56 + ((j & 3) == 0 ? 2 * (t & 7) + (j >> 2) : 16 + 5 * (t & 7) + j - 1 - (j >> 2));
}

constexpr int SLOT = 24576, NRING = 6, RING = SLOT * NRING;
constexpr int L_INV = RING, L_MNV = L_INV + 576, L_CNT = L_MNV + 576, L_TOK = L_CNT + 8;
constexpr int L_CSQ = RING + 1280, L_CS1 = L_CSQ + 1024, L_BS = L_CS1 + 1024;
constexpr int L_XC = L_BS + 1024;
constexpr int L_IM = L_XC + 6144;
constexpr int L_PF = L_IM + 4096;
constexpr int LDS_BYTES = L_PF + 256;

__device__ __forceinline__ float bf_lo(u32 w) { return __uint_as_float(w << 16); }
__device__ __forceinline__ float bf_hi(u32 w) { return __uint_as_float(w & 0xffff0000u); }
__device__ __forceinline__ u32 pack2(float lo, float hi) {
  f32x2 v = {lo, hi};
  bf16x2 r = __builtin_convertvector(v, bf16x2);
  return __builtin_bit_cast(u32, r);
}
__device__ __forceinline__ float bfr(float x) { return bf_lo(pack2(x, 0.f) & 0xffffu); }
__device__ __forceinline__ u32 gelu_tanh2(f32x2 x) {
  const float c0 = -2.3022081983f, c1 = -0.1029432396f;
  const f32x2 p = x * x * c1 + c0;
  const f32x2 a = x * p;
  const f32x2 d = (f32x2){__builtin_amdgcn_exp2f(a[0]), __builtin_amdgcn_exp2f(a[1])} + 1.f;
  const f32x2 g = x * (f32x2){__builtin_amdgcn_rcpf(d[0]), __builtin_amdgcn_rcpf(d[1])};
  return pack2(g[0], g[1]);
}
template <int N_> __device__ __forceinline__ void wait_vm() { asm volatile("s_waitcnt vmcnt(%0)" ::"n"(N_) : "memory"); }
__device__ __forceinline__ void wait_lgkm0() { asm volatile("s_waitcnt lgkmcnt(0)" ::: "memory"); }
__device__ __forceinline__ void bar() {
  __builtin_amdgcn_sched_barrier(0);
  asm volatile("s_barrier" ::: "memory");
  __builtin_amdgcn_sched_barrier(0);
}
template <int V> using IC = std::integral_constant<int, V>;
template <typename F, int... I>
__device__ __forceinline__ void for_seq(F&& f, std::integer_sequence<int, I...>) { (f(IC<I>{}), ...); }
template <int N, typename F> __device__ __forceinline__ void for_n(F&& f) { for_seq(f, std::make_integer_sequence<int, N>{}); }

__device__ __forceinline__ float xsum(float x) {
#if defined(__HIP_DEVICE_COMPILE__)
  auto a = __builtin_amdgcn_permlane16_swap(__float_as_uint(x), __float_as_uint(x), false, false);
  x = __uint_as_float(a[0]) + __uint_as_float(a[1]);
  auto b = __builtin_amdgcn_permlane32_swap(__float_as_uint(x), __float_as_uint(x), false, false);
  x = __uint_as_float(b[0]) + __uint_as_float(b[1]);
#endif
  return x;
}
__device__ __forceinline__ float xmax(float x) {
#if defined(__HIP_DEVICE_COMPILE__)
  auto a = __builtin_amdgcn_permlane16_swap(__float_as_uint(x), __float_as_uint(x), false, false);
  x = fmaxf(__uint_as_float(a[0]), __uint_as_float(a[1]));
  auto b = __builtin_amdgcn_permlane32_swap(__float_as_uint(x), __float_as_uint(x), false, false);
  x = fmaxf(__uint_as_float(b[0]), __uint_as_float(b[1]));
#endif
  return x;
}
__device__ __forceinline__ f32x2 row_part(const u32x4& c0, const u32x4& c1) {
  float v[16];
#pragma unroll
  for (int k = 0; k < 4; ++k) {
    v[2 * k] = bf_lo(c0[k]); v[2 * k + 1] = bf_hi(c0[k]);
    v[8 + 2 * k] = bf_lo(c1[k]); v[8 + 2 * k + 1] = bf_hi(c1[k]);
  }
  float s = 0.f;
#pragma unroll
  for (int k = 0; k < 16; ++k) s += v[k];
  const float m = xsum(s) * (1.f / 64.f);
  float q = 0.f;
#pragma unroll
  for (int k = 0; k < 16; ++k) { const float d = v[k] - m; q += d * d; }
  return (f32x2){m, xsum(q)};
}

#define st16(v, off) __builtin_amdgcn_raw_buffer_store_b128(__builtin_bit_cast(i32x4, v), rs, (off), 0, 16)
__device__ __forceinline__ float sum8(const u32x4& f) {
  float s = bf_lo(f[0]) + bf_hi(f[0]);
#pragma unroll
  for (int k = 1; k < 4; ++k) { s += bf_lo(f[k]); s += bf_hi(f[k]); }
  return s;
}
__device__ __forceinline__ float fold32(float a, float b) {
#if defined(__HIP_DEVICE_COMPILE__)
  const auto x = __builtin_amdgcn_permlane32_swap(__float_as_uint(a), __float_as_uint(b), false, false);
  a = __uint_as_float(x[0]) + __uint_as_float(x[1]);
#endif
  return a;
}
__device__ __forceinline__ float fold16(float a, float b) {
#if defined(__HIP_DEVICE_COMPILE__)
  const auto x = __builtin_amdgcn_permlane16_swap(__float_as_uint(a), __float_as_uint(b), false, false);
  a = __uint_as_float(x[0]) + __uint_as_float(x[1]);
#endif
  return a;
}

__device__ __forceinline__ int fresh_lane(int tid) { int l = tid & 63; asm volatile("" : "+v"(l)); return l; }
__device__ __forceinline__ f32x4 zero4() {
  f32x4 z = {0.f, 0.f, 0.f, 0.f};
  asm volatile("" : "+v"(z));
  return z;
}
template <int BIG = 0> __device__ __forceinline__ void next_slot(int& so) {
  if constexpr (BIG) so = (so + 32768) & 131071; else { so += SLOT; if (so == RING) so = 0; }
}

#define DMA __builtin_amdgcn_raw_ptr_buffer_load_lds
#define SB() __builtin_amdgcn_sched_barrier(0)
__device__ __forceinline__ void mfma(f32x4& c, const bf16x8& a, const bf16x8& b) {
  asm volatile("v_mfma_f32_16x16x32_bf16 %0, %1, %2, %0" : "+v"(c) : "v"(a), "v"(b));
}
template <int OFF> __device__ __forceinline__ void dsrd(bf16x8& d, u32 addr) {
  asm volatile("ds_read_b128 %0, %1 offset:%2" : "=v"(d) : "v"(addr), "n"(OFF));
}
template <int OFF> __device__ __forceinline__ void dstr(bf16x8& d, u32 addr) {
  u32x2 lo, hi;
  asm volatile("ds_read_b64_tr_b16 %0, %2 offset:%3\n\tds_read_b64_tr_b16 %1, %2 offset:%4"
               : "=&v"(lo), "=&v"(hi) : "v"(addr), "n"(OFF), "n"(OFF + 512));
  const u32x4 v = {lo[0], lo[1], hi[0], hi[1]};
  d = __builtin_bit_cast(bf16x8, v);
}
__device__ __forceinline__ void pin1(u32& a) { asm volatile("" : "+v"(a)); }
__device__ __forceinline__ void pin2(u32& a, u32& b) { asm volatile("" : "+v"(a), "+v"(b)); }
template <int NSL, int NM, int NR, int ENDB, class AD, class RD, class MM, class HK, class TL>
__device__ __forceinline__ void kloop(int& so, AD&& ad, RD&& rd, MM&& mm, HK&& hook, TL&& tail) {
  constexpr int BIG = NM == 32;
  ad(so, IC<0>{});
  bar();
  for_n<NR>([&](auto Q) { rd(IC<0>{}, Q); });
  next_slot<BIG>(so);
  ad(so, IC<1>{});
  hook(-1);
  auto body = [&](auto S) {
    constexpr int s = decltype(S)::value;
    wait_lgkm0();
    bar();
    for_n<NM>([&](auto M) {
      constexpr int m = decltype(M)::value;
      mm(S, M);
      if constexpr (m < NR) rd(IC<1 - s>{}, IC<m>{});
      if constexpr (m == NR) { next_slot<BIG>(so); asm volatile("" : "+s"(so)); }
      if constexpr (m == NR + 1) ad(so, S);
      SB();
    });
  };
#pragma nounroll
  for (int u = NSL / 2 - 1; u > 0; --u) {
    body(IC<0>{});
    body(IC<1>{});
    hook(NSL / 2 - 1 - u);
  }
  body(IC<0>{});
  wait_lgkm0();
  if constexpr (ENDB) { bar(); so = 0; }
  SB();
  for_n<NM>([&](auto M) { mm(IC<1>{}, M); SB(); tail(M); SB(); });
  asm volatile("s_nop 15");
  SB();
}

#define STAMP(w, i)

constexpr int w1_frag(int i, int e) {
  const int gam = 2 * (i - 2) + (e >> 1), gl = gam % 7;
  return (gam / 7) * 16 + (gl == 3 ? 6 + 7 * (e & 1) : (gl < 3 ? gl * 2 : 7 + (gl - 4) * 2) + (e & 1));
}
constexpr int between(int s0, int s1) {
  int n = 0;
  for (int s = s0 + 1; s < s1; ++s) {
    if (s >= PF0 && (s - PF0) % PFS == 0 && (s - PF0) / PFS < 8 - PFJ) n += (PFM & 1) + (PFM >> 1);
    if (s >= CQP && s < CQP + 6 && (s - CQP) % 2 == 1) n += 1;
    if (s >= HL && s < HL + 4 && (s - HL) % 2 == 1) n += 4;
  }
  return n < 63 ? n : 63;
}

__global__ __launch_bounds__(512, 2) void mega_k(char* __restrict__ ws, const u16* __restrict__ x,
                                                 const u16* __restrict__ wqkv, const u16* __restrict__ wo,
                                                 const u16* __restrict__ w1, const u16* __restrict__ b1,
                                                 const u16* __restrict__ w2, const u16* __restrict__ b2,
                                                 u16* __restrict__ out) {
#if defined(__HIP_DEVICE_COMPILE__)
  extern __shared__ __attribute__((aligned(16))) char smem[];
  const int tid = threadIdx.x;
  const int wid = __builtin_amdgcn_readfirstlane(tid >> 6);
#define FRESH_LANE() const int lane = fresh_lane(tid), r = lane & 15, g = lane >> 4; (void)r; (void)g
  const int xcd = blockIdx.x & 7, idx = blockIdx.x >> 3;
#define GRP 1
#define STG 0
  const int tmA = GRP ? (idx >> 4) * 8 + (xcd >> 2) * 4 + ((idx >> 2) & 3) : (xcd >> 2) * 8 + (idx >> 2), tnA = (xcd & 3) * 4 + (idx & 3);
#define XMAP 1
  const int tmB = GRP ? (idx >> 4) * 8 + (xcd >> 1) * 2 + ((idx >> 3) & 1) : (xcd >> 1) * 4 + (idx >> 3), tnB = XMAP ? (idx & 7) * 2 + (xcd & 1) : (xcd & 1) * 8 + (idx & 7);
  constexpr int PX = XMAP ? 1 : 8;
  const int rotA = ROT ? 8 - 8 * (xcd & 1) : 0, rotB = ROT ? 4 * ((xcd + 1) & 3) : 0;
  const int rot0 = ROT0 == 1 ? xcd : ROT0 == 2 ? (xcd & 3) * 2 : ROT0 == 3 ? (xcd >> 2) * 4 : 0;
  const __amdgpu_buffer_rsrc_t rs = __builtin_amdgcn_make_buffer_rsrc((void*)ws, 0, WS_USED, 0x00020000);
  auto flag = [&](int set, int tm, int tn) { return (GLB_AS u64*)(ws + OFF_FL + ((set * 16 + tm) * 16 + tn) * 128); };
  auto rsrc = [&](const u16* p, u32 bytes) { return __builtin_amdgcn_make_buffer_rsrc((void*)p, 0, bytes, 0x00020000); };
  auto token = [&]() {
    const u32x2 tk = *(const volatile LDS_AS u32x2*)(smem + L_TOK);
    return (u64)(u32)__builtin_amdgcn_readfirstlane(tk[0]) | ((u64)(u32)__builtin_amdgcn_readfirstlane(tk[1]) << 32);
  };
  auto ldf = [&](const GLB_AS u64* f) { return __hip_atomic_load(f, __ATOMIC_RELAXED, __HIP_MEMORY_SCOPE_AGENT); };
  auto poll = [&](const GLB_AS u64* f) {
    const u64 tok = token();
#pragma nounroll
    for (int spin = 0; spin < (1 << 22); ++spin) {
      if (__all(ldf(f) == tok)) break;
      __builtin_amdgcn_s_sleep(1);
    }
  };
  auto raise = [&](int set, int tm, int tn) {
    __hip_atomic_store(flag(set, tm, tn), token(), __ATOMIC_RELAXED, __HIP_MEMORY_SCOPE_AGENT);
  };
  int so = 0;

  if (STG && idx >= 16 && (wid == 4 || wid == 5))
    for (int i = 0; i < STG; ++i) __builtin_amdgcn_s_sleep(16);
  if (wid < 4) {
    if (CW && wid == 0) {
      u64 pc;
      u32 pos, len;
      asm volatile("s_getpc_b64 %0\n.Lmega_pc:\n\ts_mov_b32 %1, .Lmega_pc - _Z6mega_kPcPKtS1_S1_S1_S1_S1_S1_Pt\n\t"
                   "s_mov_b32 %2, .Lmega_end - _Z6mega_kPcPKtS1_S1_S1_S1_S1_S1_Pt" : "=s"(pc), "=s"(pos), "=s"(len));
      __builtin_amdgcn_s_sleep(CW ? CW - 1 : 0);
      DMA(rsrc((const u16*)(pc - pos), len), (LDS_AS void*)(smem + L_PF), 4, ((tid & 63) * 32 + idx) * 128, 0, 0, 0);
    }
    auto publish = [&](auto N, int ph, int tm, int tn, int st) {
      FRESH_LANE();
      SB();
      wait_vm<decltype(N)::value>();
      u32 old = 0;
      if (lane == 0) old = __hip_atomic_fetch_add((LDS_AS u32*)(smem + L_CNT) + st, 1u, __ATOMIC_RELAXED, __HIP_MEMORY_SCOPE_WORKGROUP);
      old = __builtin_amdgcn_readfirstlane(old);
      if ((old & 3) == 3 && lane == 0) raise(ph + 1 + F_2ND * st, tm, tn);
      SB();
    };
    const float* t_inv = (const float*)(smem + L_INV);
    const float* t_mnv = (const float*)(smem + L_MNV);
    const float* t_cs = (const float*)(smem + L_CS1);
    const float* t_bs = (const float*)(smem + L_BS);
    const int wmi = wid >> 1, wni = wid & 1;

    auto finish = [&](const f32x4 (&acc)[4][4], const u32x4 (&res)[2][2], const u32x4 (&bb)[2], int w, int tn,
                      int xo  , int ph) {
      FRESH_LANE();
      u32x4 c[2][2];
#pragma unroll
      for (int i = 0; i < 2; ++i)
#pragma unroll
        for (int gl = 0; gl < 2; ++gl) {
#pragma unroll
          for (int k = 0; k < 4; ++k) {
            const f32x4& a = acc[i][2 * gl + (k >> 1)];
            const float y0 = a[2 * (k & 1)] + bf_lo(bb[gl][k]), y1 = a[2 * (k & 1) + 1] + bf_hi(bb[gl][k]);
            c[i][gl][k] = pack2(bf_lo(res[i][gl][k]) + y0, bf_hi(res[i][gl][k]) + y1);
          }
          if (xo < 4) st16(c[i][gl], OFF_X + xo * XS + ((tmB * 32 + 2 * tn + gl) * 2 + (w >> 1)) * QS + qoff((w & 1) * 2 + i, lane));
          else __builtin_amdgcn_raw_buffer_store_b128(__builtin_bit_cast(i32x4, c[i][gl]), rsrc(out, SEQ * DM * 2),
                   (tmB * 128 + (2 * w + i) * 16 + r) * 2048 + tn * 128 + gl * 64 + g * 16, 0, 16);
        }
      if (xo < 4) {
        SB();
        const f32x2 s0 = row_part(c[0][0], c[0][1]), s1 = row_part(c[1][0], c[1][1]);
        __builtin_amdgcn_raw_buffer_store_b64(__builtin_bit_cast(i32x2, (g & 1) ? s1 : s0), rs,
            lane < 32 ? OFF_ST + xo * 262144 + ((tmB * 16 + tn) * 128 + w * 32 + lane) * 8 : 0x80000000u, 0, 16);
        publish(IC<1>{}, ph, tmB, tnB, 0);
        publish(IC<0>{}, ph, tmB, tnB, 1);
      }
      STAMP(0, 4 * ph + 3);
    };

#pragma nounroll
    for (int pair = 0; pair < 2; ++pair) {
      {
        f32x4 acc[4][6], acch[2];
        {
        FRESH_LANE();
        STAMP(0, 16 * pair);
        u32 pA, pB0, pB1;
        const u32 lB = 9216 + (8 * g + (r >> 2)) * 128 + (((2 * wni) ^ ((r >> 3) & 1) ^ ((g & 1) << 1)) * 32) + (r & 3) * 8;
        const u32 lA = r * 128 + ((g ^ (r >> 1)) * 16);
        auto loop = [&](auto WM, auto RAW) {
          constexpr int wm = decltype(WM)::value, raw = decltype(RAW)::value;
#pragma unroll
          for (int i = 0; i < 4; ++i)
#pragma unroll
            for (int j = 0; j < 6; ++j) acc[i][j] = zero4();
          acch[0] = zero4(); acch[1] = zero4();
          bf16x8 fA[2][4], fH[2], fB[2][6];
          u32 lA0 = raw ? lA : lane * 16, lA1 = raw ? (lA ^ 64) - SLOT : lane * 16, lB1 = lB ^ 32;
          pin2(lA0, lA1); pin1(lB1);
          kloop<32, 26, 11, 0>(so,
            [&](int s_, auto PAR) {
              pA = (decltype(PAR)::value ? lA1 : lA0) + s_; pB0 = lB + s_; pB1 = lB1 + s_;
              pin2(pA, pB0); pin1(pB1);
            },
            [&](auto SET, auto Q) {
              constexpr int set = decltype(SET)::value, q = decltype(Q)::value;
              if constexpr (q < 4) dsrd<raw ? wm * SLOT + q * 2048 : wm * 4096 + q * 1024>(fA[set][q], pA);
              else if constexpr (q < 10) dstr<((q - 4) >> 1) * 4096>(fB[set][q - 4], (q & 1) ? pB1 : pB0);
              else dsrd<raw ? SLOT + 21504 : 8192>(fH[set], pA);
            },
            [&](auto SET, auto M) {
              constexpr int set = decltype(SET)::value, m = decltype(M)::value, j = m >> 2, i = m & 3;
              if constexpr (m < 16) mfma(acc[i][j], fB[set][j], fA[set][i]);
              else if constexpr (m < 24) mfma(acc[i][j], fA[set][i], fB[set][j]);
              else if constexpr (wm == 0) mfma(acch[i], fB[set][2 + i], fH[set]);
              else mfma(acch[i], fH[set], fB[set][4 + i]);
            }, [](int) {}, [](auto) {});
        };
        if (pair == 0) { if (wmi == 0) loop(IC<0>{}, IC<1>{}); else loop(IC<1>{}, IC<1>{}); }
        else { if (wmi == 0) loop(IC<0>{}, IC<0>{}); else loop(IC<1>{}, IC<0>{}); }
        }
        FRESH_LANE();
        STAMP(0, 16 * pair + 2);
        const float* csw = (const float*)(smem + L_CSQ) + wni * 96;
        auto fix_qk = [&](const f32x4& a0, const f32x4& a1, int trow, int f) {
          const float iv = t_inv[trow + r], mv = t_mnv[trow + r];
          const f32x4 c0 = *(const f32x4*)(csw + f * 16 + 4 * g), c1 = *(const f32x4*)(csw + f * 16 + 16 + 4 * g);
          const f32x4 y0 = a0 * iv - c0 * mv, y1 = a1 * iv - c1 * mv;
          return (u32x4){pack2(y0[0], y0[1]), pack2(y0[2], y0[3]), pack2(y1[0], y1[1]), pack2(y1[2], y1[3])};
        };
        auto fix_v = [&](const f32x4& a0, const f32x4& a1, int trow) {
          const f32x4 iv = *(const f32x4*)(t_inv + trow + 4 * g), mv = *(const f32x4*)(t_mnv + trow + 4 * g);
          const float c0 = csw[64 + r], c1 = csw[80 + r];
          const f32x4 y0 = a0 * iv - mv * c0, y1 = a1 * iv - mv * c1;
          return (u32x4){pack2(y0[0], y0[1]), pack2(y0[2], y0[3]), pack2(y1[0], y1[1]), pack2(y1[2], y1[3])};
        };
        u32x4 Qf[4], Kf[5], Vf[5];
        char* xc = smem + L_XC + wni * 3072 + lane * 16;
        const int row0 = 16 + wmi * 64;
        Kf[4] = fix_qk(acc[3][2], acc[3][3], row0 + 48, 2);
        Vf[4] = fix_v(acc[3][4], acc[3][5], row0 + 48);
        if (wmi == 0) {
          *(u32x4*)xc = Kf[4];
          *(u32x4*)(xc + 1024) = Vf[4];
          Kf[0] = fix_qk(acch[0], acch[1], 0, 2);
        } else {
          *(u32x4*)(xc + 2048) = fix_v(acch[0], acch[1], 0);
        }
#pragma unroll
        for (int i = 0; i < 3; ++i) {
          Kf[i + 1] = fix_qk(acc[i][2], acc[i][3], row0 + 16 * i, 2);
          Vf[i + 1] = fix_v(acc[i][4], acc[i][5], row0 + 16 * i);
        }
        wait_lgkm0();
        bar();
        if (wmi == 1) Kf[0] = *(const u32x4*)xc;
        Vf[0] = *(const u32x4*)(xc + (wmi == 0 ? 2048 : 1024));
#pragma unroll
        for (int i = 0; i < 4; ++i) Qf[i] = fix_qk(acc[i][0], acc[i][1], row0 + 16 * i, 0);
        const bool par = r & 1;
        const float m0 = par ? 0.f : 1.f, m1 = par ? 1.f : 0.f;
        const bool own0 = 4 * g + (r & 1) <= r, own1 = 4 * g + (r & 1) + 2 <= r;
        const bool first = (tmA == 0) && (wmi == 0);
#pragma unroll
        for (int ii = 0; ii < 4; ++ii) {
          const int i = 3 - ii;
          const f32x4 z = {0.f, 0.f, 0.f, 0.f};
          const f32x4 S0 = __builtin_amdgcn_mfma_f32_16x16x32_bf16(__builtin_bit_cast(bf16x8, Kf[i]), __builtin_bit_cast(bf16x8, Qf[i]), z, 0, 0, 0);
          const f32x4 S1 = __builtin_amdgcn_mfma_f32_16x16x32_bf16(__builtin_bit_cast(bf16x8, Kf[i + 1]), __builtin_bit_cast(bf16x8, Qf[i]), z, 0, 0, 0);
          float l0 = own0 ? (par ? S1[1] : S1[0]) : (par ? S0[1] : S0[0]);
          float l1 = own1 ? (par ? S1[3] : S1[2]) : (par ? S0[3] : S0[2]);
          const float c = 0.17677669529663687f * 1.4426950408889634f;
          l0 *= c; l1 *= c;
          if (i == 0 && first) {
            if (!own0) l0 = -1.4426950e9f;
            if (!own1) l1 = -1.4426950e9f;
          }
          const float mx = xmax(fmaxf(l0, l1));
          const float p0 = __builtin_amdgcn_exp2f(l0 - mx), p1 = __builtin_amdgcn_exp2f(l1 - mx);
          const float rsum = __builtin_amdgcn_rcpf(xsum(p0 + p1));
          const float q0 = p0 * rsum, q1 = p1 * rsum;
          const u32 w0 = pack2(q0 * m0, q0 * m1), w1 = pack2(q1 * m0, q1 * m1);
          const u32x4 P = {own0 ? 0u : w0, own1 ? 0u : w1, own0 ? w0 : 0u, own1 ? w1 : 0u};
          const u32x4 V0 = {Vf[i][0], Vf[i][1], Vf[i + 1][0], Vf[i + 1][1]};
          const u32x4 V1 = {Vf[i][2], Vf[i][3], Vf[i + 1][2], Vf[i + 1][3]};
          const f32x4 O0 = __builtin_amdgcn_mfma_f32_16x16x32_bf16(__builtin_bit_cast(bf16x8, V0), __builtin_bit_cast(bf16x8, P), z, 0, 0, 0);
          const f32x4 O1 = __builtin_amdgcn_mfma_f32_16x16x32_bf16(__builtin_bit_cast(bf16x8, V1), __builtin_bit_cast(bf16x8, P), z, 0, 0, 0);
          const u32x4 o = {pack2(O0[0], O0[1]), pack2(O0[2], O0[3]), pack2(O1[0], O1[1]), pack2(O1[2], O1[3])};
          st16(o, OFF_O + pair * XS + ((tmA * 32 + 2 * tnA + wni) * 2 + wmi) * QS + qoff(i, lane));
        }
        publish(IC<0>{}, 4 * pair, tmA, tnA, 0);
        STAMP(0, 16 * pair + 3);
      }
      {
        FRESH_LANE();
        u32x4 res[2][2];
        const u32x4 bb[2] = {{0u, 0u, 0u, 0u}, {0u, 0u, 0u, 0u}};
        f32x4 acc[4][4];
#pragma unroll
        for (int i = 0; i < 2; ++i)
#pragma unroll
          for (int j = 0; j < 4; ++j) acc[i][j] = zero4();
        bf16x8 fA[2][2][2], fB[2][2][4];
        u32 pA, pB;
        auto ldres = [&]() {
#pragma unroll
          for (int i = 0; i < 2; ++i)
#pragma unroll
            for (int gl = 0; gl < 2; ++gl)
              res[i][gl] = __builtin_bit_cast(u32x4, __builtin_amdgcn_raw_buffer_load_b128(
                  pair ? rs : rsrc(x, SEQ * DM * 2), pair ? qoff((wid & 1) * 2 + i, lane) : (16 * i + r) * 2048 + g * 16,
                  pair ? OFF_X + 2 * XS + ((tmB * 32 + 2 * tnB + gl) * 2 + (wid >> 1)) * QS
                       : (tmB * 128 + wid * 32) * 2048 + tnB * 128 + gl * 64, 16));
        };
        ldres();
        kloop<16, 16, 12, 0>(so,
          [&](int s_, auto) { pA = lane * 16 + wid * 2048 + s_; pB = lane * 16 + s_; pin2(pA, pB); },
          [&](auto SET, auto Q) {
            constexpr int set = decltype(SET)::value, q = decltype(Q)::value, s = q / 6, m = q % 6;
            if constexpr (m < 2) dsrd<s * 8192 + m * 1024>(fA[set][s][m], pA);
            else dsrd<16384 + s * 4096 + (m - 2) * 1024>(fB[set][s][m - 2], pB);
          },
          [&](auto SET, auto M) {
            constexpr int set = decltype(SET)::value, m = decltype(M)::value, s = m >> 3, j = (m >> 1) & 3, i = m & 1;
            mfma(acc[i][j], fB[set][s][j], fA[set][s][i]);
          },
          [](int) {}, [](auto) {});
        STAMP(0, 16 * pair + 6);
        finish(acc, res, bb, wid, tnB, 2 * pair + 1, 4 * pair + 1);
      }
      {
        f32x4 acc[4][7];
        {
        FRESH_LANE();
#pragma unroll
        for (int i = 0; i < 4; ++i)
#pragma unroll
          for (int j = 0; j < 7; ++j) acc[i][j] = zero4();
        bf16x8 fA[2][4], fB[2][7];
        u32 pA, pB;
        kloop<32, 28, 11, 1>(so,
          [&](int s_, auto) { pA = lane * 16 + wmi * 4096 + s_; pB = lane * 16 + wni * 7168 + s_; pin2(pA, pB); },
          [&](auto SET, auto Q) {
            constexpr int set = decltype(SET)::value, q = decltype(Q)::value;
            if constexpr (q < 4) dsrd<q * 1024>(fA[set][q], pA);
            else dsrd<8192 + (q - 4) * 1024>(fB[set][q - 4], pB);
          },
          [&](auto SET, auto M) {
            constexpr int set = decltype(SET)::value, m = decltype(M)::value, j = m >> 2, i = m & 3;
            mfma(acc[i][j], fB[set][j], fA[set][i]);
          }, [](int) {}, [](auto) {});
        }
        FRESH_LANE();
        STAMP(0, 16 * pair + 10);
        float iv[4], mv[4];
#pragma unroll
        for (int i = 0; i < 4; ++i) { iv[i] = t_inv[16 + wmi * 64 + 16 * i + r]; mv[i] = t_mnv[16 + wmi * 64 + 16 * i + r]; }
        const u32 hbase = OFF_H + pair * HS + (tmA * 224 + wmi) * QS;
        auto act = [&](const f32x4& a, const f32x4& cs, const f32x4& bs, int i) {
          const f32x4 y = a * iv[i] + (bs - cs * mv[i]);
          return (u32x2){gelu_tanh2((f32x2){y[0], y[1]}), gelu_tanh2((f32x2){y[2], y[3]})};
        };
#pragma unroll
        for (int jp = 0; jp < 3; ++jp) {
          const int t0 = (wni * 7 + 2 * jp) * 16 + 4 * g;
          const f32x4 cs0 = *(const f32x4*)(t_cs + t0), cs1 = *(const f32x4*)(t_cs + t0 + 16);
          const f32x4 bs0 = *(const f32x4*)(t_bs + t0), bs1 = *(const f32x4*)(t_bs + t0 + 16);
#pragma unroll
          for (int i = 0; i < 4; ++i) {
            const u32x2 a = act(acc[i][2 * jp], cs0, bs0, i), b = act(acc[i][2 * jp + 1], cs1, bs1, i);
            const u32x4 v = {a[0], a[1], b[0], b[1]};
            st16(v, hbase + hpos(tnA, wni * 4 + jp) * 2 * QS + qoff(i, lane));
            if (4 * jp + i == 3 + PUBJ) publish(IC<PUBJ>{}, 4 * pair + 2, tmA, tnA, 0);
          }
          if (jp == 0) SB();
        }
        {
          const int t0 = (wni * 7 + 6) * 16 + 4 * g;
          const f32x4 cs0 = *(const f32x4*)(t_cs + t0), bs0 = *(const f32x4*)(t_bs + t0);
#pragma unroll
          for (int i = 0; i < 4; i += 2) {
            const u32x2 a = act(acc[i][6], cs0, bs0, i), b = act(acc[i + 1][6], cs0, bs0, i + 1);
            const auto s0 = __builtin_amdgcn_permlane16_swap(a[0], b[0], false, false);
            const auto s1 = __builtin_amdgcn_permlane16_swap(a[1], b[1], false, false);
            const u32x4 v = {s0[0], s1[0], s0[1], s1[1]};
            st16(v, hbase + hpos(tnA, 3) * 2 * QS + qoff(i + (g & 1), (2 * wni + (g >> 1)) * 16 + r));
          }
        }
        STAMP(0, 16 * pair + 11);
      }
      {
        FRESH_LANE();
        f32x4 acc[4][4];
#pragma unroll
        for (int i = 0; i < 4; ++i)
#pragma unroll
          for (int j = 0; j < 4; ++j) acc[i][j] = zero4();
        bf16x8 fA[2][2][4], fB[2][2][4];
        u32 pA, pB;
        const u32 xc = OFF_XC + pair * (4 * MB) + wid * 4096;
        const int w = 2 * (tnB & 1) + wmi, tn = (tnB & 14) + wni;
        u32x4 res[2][2], bb[2];
        if (HKT == -2) publish(IC<0>{}, 4 * pair + 2, tmA, tnA, 1);
        kloop<28, 32, 16, 1>(so,
          [&](int s_, auto) { pA = lane * 16 + wmi * 2048 + s_; pB = lane * 16 + wni * 4096 + s_; pin2(pA, pB); },
          [&](auto SET, auto Q) {
            constexpr int set = decltype(SET)::value, q = decltype(Q)::value, s = q >> 3, m = q & 7;
            if constexpr (m < 4) dsrd<s * 8192 + (m >> 1) * 4096 + (m & 1) * 1024>(fA[set][s][m], pA);
            else dsrd<16384 + s * 8192 + (m - 4) * 1024>(fB[set][s][m - 4], pB);
          },
          [&](auto SET, auto M) {
            constexpr int set = decltype(SET)::value, m = decltype(M)::value;
            constexpr int s = (m >> 3) & 1, j = (m >> 1) & 3, i = (m & 1) + (m < 16 ? 2 : 0);
            mfma(acc[i][j], fB[set][s][j], fA[set][s][i]);
          },
          [&](int t) {
            if (t == HKT) publish(IC<0>{}, 4 * pair + 2, tmA, tnA, 1);
            if (t == RST) {
              FRESH_LANE();
#pragma unroll
              for (int i = 0; i < 2; ++i)
#pragma unroll
                for (int gl = 0; gl < 2; ++gl)
                  res[i][gl] = __builtin_bit_cast(u32x4, __builtin_amdgcn_raw_buffer_load_b128(rs, qoff((w & 1) * 2 + i, lane),
                      OFF_X + (2 * pair + 1) * XS + ((tmB * 32 + 2 * tn + gl) * 2 + (w >> 1)) * QS, 16));
#pragma unroll
              for (int gl = 0; gl < 2; ++gl)
                bb[gl] = __builtin_bit_cast(u32x4, __builtin_amdgcn_raw_buffer_load_b128(rsrc(b2, DM * 2), g * 16, tn * 128 + gl * 64, 0));
            }
          },
          [&](auto M) {
            constexpr int k = decltype(M)::value - SND - 1;
            if constexpr (k >= 0 && k < 8 && k % 2 == 0) {
              const f32x4 a = acc[2][k >> 1], b = acc[3][k >> 1];
              const u32x4 v = {pack2(a[0], a[1]), pack2(a[2], a[3]), pack2(b[0], b[1]), pack2(b[2], b[3])};
              st16(__builtin_elementwise_min(v, (u32x4){~1u, ~1u, ~1u, ~1u}), xc + blockIdx.x * 16384 + lane * 16 + k * 512);
            }
          });
        STAMP(0, 16 * pair + 14);
        u32x4 xv[4];
#pragma nounroll
        for (int spin = 0; spin < (1 << 22); ++spin) {
#pragma unroll
          for (int k = 0; k < 4; ++k)
            xv[k] = __builtin_bit_cast(u32x4, __builtin_amdgcn_raw_buffer_load_b128(rs, lane * 16 + k * 1024, xc + (blockIdx.x ^ PX) * 16384, 16));
          const u32x4 o = __builtin_elementwise_max(__builtin_elementwise_max(xv[0], xv[1]), __builtin_elementwise_max(xv[2], xv[3]));
          if (!__any(max(max(o[0], o[1]), max(o[2], o[3])) == ~0u)) break;
          asm volatile("" ::: "memory");
        }
#pragma unroll
        for (int k = 0; k < 4; ++k) {
          acc[0][k] += (f32x4){bf_lo(xv[k][0]), bf_hi(xv[k][0]), bf_lo(xv[k][1]), bf_hi(xv[k][1])};
          acc[1][k] += (f32x4){bf_lo(xv[k][2]), bf_hi(xv[k][2]), bf_lo(xv[k][3]), bf_hi(xv[k][3])};
        }
        finish(acc, res, bb, w, tn, pair == 0 ? 2 : 4, 4 * pair + 3);
      }
    }
  } else if (wid == 4) {
    {
      FRESH_LANE();
      STAMP(1, 0);
      const __amdgpu_buffer_rsrc_t rx = rsrc(x, SEQ * DM * 2);
      u32 vo[4];
#pragma unroll
      for (int e = 0; e < 4; ++e)
        vo[e] = (8 * e + (lane >> 3)) * 2048 + (((lane & 7) ^ (4 * (e & 1) + (lane >> 4))) * 16) - e * 1024;
      const u32 sx = tmA * 128 * 2048, sh = (tmA > 0 ? tmA * 128 - 16 : 0) * 2048;
      auto half = [&](auto H, int U) {
        constexpr int h = decltype(H)::value;
        U = (U + 2 * rot0) & 15;
        for_n<8>([&](auto E) {
          constexpr int e = decltype(E)::value;
          DMA(rx, (LDS_AS void*)(smem + so + h * SLOT + (e >> 2) * 4096), 16, vo[e & 3],
              sx + (h * 64 + (e >> 2) * 32) * 2048 + U * 128, (e & 3) * 1024, AX(0));
        });
        DMA(rx, (LDS_AS void*)(smem + so + SLOT + 21504), 16, vo[h], sh + U * 128, h * 1024, AX(0));
        if constexpr (h == 1) { so += 2 * SLOT; if (so == RING) so = 0; }
      };
#pragma nounroll
      for (int U = 0; U < RA; ++U) { half(IC<0>{}, U); half(IC<1>{}, U); }
      wait_vm<18 * (RA - 1)>();
      bar();
      if (RA < 3) { half(IC<0>{}, RA); half(IC<1>{}, RA); }
      bar();
      if (RA < 2) { half(IC<0>{}, 2); half(IC<1>{}, 2); }
#pragma nounroll
      for (int U = 3; U < 16; ++U) {
        wait_vm<18>();
        bar();
        half(IC<0>{}, U);
        bar();
        half(IC<1>{}, U);
      }
      wait_vm<18>(); bar(); bar();
      wait_vm<0>(); bar(); bar();
    }
    auto phase = [&](auto NSL_, auto PA_, auto HALO_, u32 src, u32 hsrc, int ph  , int tm, int tmh) {
      constexpr int NSL = decltype(NSL_)::value, PA = decltype(PA_)::value, HALO = decltype(HALO_)::value;
      constexpr int BIG = NSL == 28, PT = PA + HALO, D = BIG ? 3 : PA == 16 ? DN : DW;
      FRESH_LANE();
      u32 vo[4];
#pragma unroll
      for (int e = 0; e < 4; ++e) vo[e] = qoff(e, lane) - e * 1024;
      poll(flag(ph + 1, (lane & 16) ? tmh : tm, BIG ? 8 * (tnB & 1) + ((rotA >> 1) + (r < 3 ? r : 0))
                                               : PA == 16 ? (rotB + (r < DN ? r : 0)) & 15 : rotA + (HALO || 2 * (r & 3) < DW ? r & 3 : 0)));
      static_assert(DW <= 6, "four producers");
      STAMP(1, 4 * (ph + 1));
      const u32 flip = BIG ? (tnB & 1) * QS : 0;
      auto issue = [&](int u) {
        u = BIG ? (u < 8 ? u ^ (rotA >> 1) : u) : PA == 16 ? (u + rotB) & 15 : u ^ (2 * rotA);
        if constexpr (PA == 16 && !BIG) wait_vm<47>();
        for_n<PA>([&](auto E) {
          constexpr int e = decltype(E)::value;
          DMA(rs, (LDS_AS void*)(smem + so + (e >> 2) * 4096), 16, vo[e & 3],
              src + u * (PA / 4 * QS) + (e >> 2) * QS + ((e & 4) ? -flip : flip), (e & 3) * 1024, 16);
        });
        if constexpr (HALO) DMA(rs, (LDS_AS void*)(smem + so + 8192), 16, vo[3], hsrc + u * (2 * QS), 0, 16);
        next_slot<BIG>(so);
      };
#pragma nounroll
      for (int u = 0; u < D; ++u) issue(u);
#pragma nounroll
      for (int u = 0; u < NSL - D; ++u) {
        wait_vm<(D - 1) * PT>();
        bar();
        issue(u + D);
      }
      for_n<D>([&](auto M) { wait_vm<(D - 1 - decltype(M)::value) * PT>(); bar(); });
    };
#pragma nounroll
    for (int pair = 0; pair < 2; ++pair) {
      const int tmh = tmA > 0 ? tmA - 1 : 0;
      const u32 xq = OFF_X + 2 * pair * XS;
      if (pair) phase(IC<32>{}, IC<8>{}, IC<1>{}, xq + tmA * (64 * QS), xq + tmh * (64 * QS) + QS + 3072, 3, tmA, tmh);
      bar();
      phase(IC<16>{}, IC<16>{}, IC<0>{}, OFF_O + pair * XS + tmB * (64 * QS), 0, 4 * pair, tmB, tmB);
      phase(IC<32>{}, IC<8>{}, IC<0>{}, xq + XS + tmA * (64 * QS), 0, 4 * pair + 1, tmA, tmA);
      bar();
      so = 0;
      phase(IC<28>{}, IC<16>{}, IC<0>{}, OFF_H + pair * HS + (tmB * 224 + (tnB & 1) * 112) * QS, 0, 4 * pair + 2, tmB, tmB);
      bar();
      so = 0;
    }
  } else if (wid == 5) {
    FRESH_LANE();
    auto phase = [&](auto NSL_, auto PB_, int rot, bool ramp, auto&& piece) {
      constexpr int NSL = decltype(NSL_)::value, PB = decltype(PB_)::value;
      auto issue = [&](int u) {
        u = PB == 16 ? (u < 8 ? u ^ rot : u) : PB == 8 ? (u + rot) & 15 : u ^ rot;
        wait_vm<63 - PB>();
        for_n<PB>([&](auto I) { piece(I, u); });
        next_slot<PB == 16>(so);
      };
      constexpr int D = PB == 16 ? 3 : PB == 8 ? (DN < 4 ? DN : 5) : DW;
      int u0 = 0;
      if (PB == 12 && R0 < D && ramp) {
#pragma nounroll
        for (int u = 0; u < R0; ++u) issue(u);
        for_n<(PB == 12 && D > R0 ? D - R0 : 0)>([&](auto M) {
          constexpr int m = decltype(M)::value;
          wait_vm<(R0 + m - 1) * PB>();
          bar();
          issue(R0 + 2 * m);
          issue(R0 + 2 * m + 1);
        });
        u0 = D - R0;
      } else {
#pragma nounroll
        for (int u = 0; u < D; ++u) issue(u);
      }
#pragma nounroll
      for (int u = u0; u < NSL - D; ++u) {
        wait_vm<(D - 1) * PB>();
        bar();
        issue(u + D);
      }
      for_n<D>([&](auto M) { wait_vm<(D - 1 - decltype(M)::value) * PB>(); bar(); });
    };
    auto tiles = [&](auto NSL_, auto PB_, auto BOFF_, u32 src) {
      constexpr int PB = decltype(PB_)::value, BOFF = decltype(BOFF_)::value;
      u32 vo[4];
#pragma unroll
      for (int e = 0; e < 4; ++e) vo[e] = qoff(e, lane) - e * 1024;
      phase(NSL_, PB_, PB == 16 ? rotA >> 1 : PB == 8 ? rotB : 2 * rotA, false, [&](auto I, int u) {
        constexpr int i = decltype(I)::value;
        DMA(rs, (LDS_AS void*)(smem + so + BOFF + (i >> 2) * 4096), 16, vo[i & 3],
            PB == 16 ? src + u * (2 * QS) + (i >> 3) * QS + ((i >> 2) & 1) * (112 * QS)
                     : src + u * ((PB + 3) / 4 * QS) + (i >> 2) * QS, (i & 3) * 1024, 16);
      });
    };
#pragma nounroll
    for (int pair = 0; pair < 2; ++pair) {
      {
        const __amdgpu_buffer_rsrc_t rq = rsrc(wqkv, DM * 3 * DM * 2);
        u32 vo[4];
#pragma unroll
        for (int e = 0; e < 4; ++e)
          vo[e] = (8 * e + (lane >> 3)) * 6144 + (((lane & 7) ^ (2 * ((lane >> 4) & 1) + 4 * (e & 1))) * 16) - e * 1024;
        phase(IC<32>{}, IC<12>{}, pair ? 2 * rotA : 0, pair == 0, [&](auto I, int u) {
          constexpr int i = decltype(I)::value;
          if (pair == 0) u = (u + 4 * rot0) & 31;
          DMA(rq, (LDS_AS void*)(smem + so + 9216 + (i >> 2) * 4096), 16, vo[i & 3],
              u * (32 * 6144) + (i >> 2) * 2048 + tnA * 128, (i & 3) * 1024, AX(1));
        });
      }
      bar();
      if (pair == 0) poll(flag(F_WO, r, tnB));
      tiles(IC<16>{}, IC<8>{}, IC<16384>{}, OFF_WO + tnB * (32 * QS));
      if (pair == 0) poll(flag(F_W1, r, (tnA & 14) + (g & 1)));
      tiles(IC<32>{}, IC<14>{}, IC<8192>{}, OFF_W1 + tnA * (32 * 4 * QS));
      bar();
      so = 0;
      if (pair == 0) poll(flag(F_W2, 8 * (tnB & 1) + (r & 7), (tnB & 14) + (r >> 3)));
      tiles(IC<28>{}, IC<16>{}, IC<16384>{}, OFF_W2 + ((tnB & 14) * 112 + (tnB & 1) * 56) * QS);
      bar();
      so = 0;
    }
  } else if (wid == 6) {
    FRESH_LANE();
    const int rr = lane >> 3, c16 = (lane & 7) * 16;
    const __amdgpu_buffer_rsrc_t rx = rsrc(x, SEQ * DM * 2), rq = rsrc(wqkv, DM * 3 * DM * 2);
    auto warm = [&](int j) {
      const int n = idx * 64 + lane, m = idx * 48 + (lane < 48 ? lane : 47), k = (m * 5462) >> 16, p = m - 12 * k;
      j = (j + rot0) & 7;
      if (GRP) {
        DMA(rx, (LDS_AS void*)(smem + L_PF), 4, (n & 511) * 2048 + ((n >> 9) & 1) * 128, ((idx >> 4) * 1024 + (xcd >> 2) * 512) * 2048 + j * 256, 0, AX(2));
        DMA(rq, (LDS_AS void*)(smem + L_PF), 4, k * 6144 + (p >> 2) * 2048 + (p & 3) * 128, (xcd & 3) * 512 + j * (128 * 6144), 0, AX(3));
        if (STG) DMA(rq, (LDS_AS void*)(smem + L_PF), 4, (k ^ 64) * 6144 + (p >> 2) * 2048 + (p & 3) * 128, (xcd & 3) * 512 + j * (128 * 6144), 0, 0);
        return;
      }
      if (PFM & 1) DMA(rx, (LDS_AS void*)(smem + L_PF), 4, (n & 1023) * 2048 + (n >> 10) * 128, (xcd >> 2) * (1024 * 2048) + j * 256, 0, 0);
      if (PFM & 2) DMA(rq, (LDS_AS void*)(smem + L_PF), 4, k * 6144 + (p >> 2) * 2048 + (p & 3) * 128, (xcd & 3) * 512 + j * (128 * 6144), 0, 0);
    };
    auto touch = [&](int j) {
      const int n = blockIdx.x * 16 + lane, m = blockIdx.x * 24 + min(lane, 39) - 16, k = (m * 1366) >> 16;
      const char* a = lane < 16 ? (const char*)x + (n >> 1) * 2048 + (n & 1) * 128 + j * 256
                                : (const char*)wqkv + j * (128 * 6144) + m * 128;
      __builtin_amdgcn_global_load_lds((const GLB_AS void*)a, (LDS_AS void*)(smem + L_PF), 4, 0, 0);
    };
    u32x4 U[7][4], F[4];
    auto u_load = [&](auto I) {
      constexpr int i = decltype(I)::value;
#pragma unroll
      for (int e = 0; e < 4; ++e) {
        if constexpr (i < 2)
          U[(i + 5) % 7][e] = __builtin_bit_cast(u32x4, __builtin_amdgcn_raw_buffer_load_b128(rsrc(wo, DM * DM * 2),
              ((rr & 3) + 16 * (rr >> 2)) * 2048 + c16, ((2 * tmA + i) * 32 + 4 * e) * 2048 + tnA * 128, UAUX));
        else if constexpr (i < 9)
          U[(i + 5) % 7][e] = __builtin_bit_cast(u32x4, __builtin_amdgcn_raw_buffer_load_b128(rsrc(w1, DM * FF * 2),
              rr * 7168 + c16, ((2 * tmA + (tnA & 1)) * 32 + 8 * e) * 7168 + (7 * (tnA >> 1) + i - 2) * 128, UAUX));
        else
          U[(i + 5) % 7][e] = __builtin_bit_cast(u32x4, __builtin_amdgcn_raw_buffer_load_b128(rsrc(w2, DM * FF * 2),
              rr * 2048 + c16, ((7 * tmA + i - 9) * 32 + 8 * e) * 2048 + tnA * 128, UAUX));
      }
    };
    auto u_write = [&](auto I) {
      constexpr int i = decltype(I)::value;
#pragma unroll
      for (int e = 0; e < 4; ++e) {
        const u32x4 v = U[(i + 5) % 7][e];
        *(u32x4*)(smem + L_IM + e * 1024 + rr * 128 + (c16 ^ ((rr & 2) << 5))) = (e & 1) ? (u32x4){v[2], v[3], v[0], v[1]} : v;
      }
    };
    auto u_store = [&](auto I) {
      constexpr int i = decltype(I)::value;
      const u32 sw = ((r & 8) << 3) | ((g & 1) << 3);
      const char* im = smem + L_IM + (8 * g + (r >> 2)) * 128;
      for_n<4>([&](auto E) {
        constexpr int e = decltype(E)::value;
        constexpr bool split = i >= 2 && i < 9 && (2 * (i - 2) + (e >> 1)) % 7 == 3;
        const char* p = im + (((e >> 1) * 64 + (split ? (e & 1) * 32 + (r & 3) * 8 : (r & 3) * 16 + (e & 1) * 8)) ^ sw);
        const u32x2 lo = __builtin_bit_cast(u32x2, __builtin_amdgcn_ds_read_tr16_b64_v4i16((LDS_AS s16x4*)p));
        const u32x2 hi = __builtin_bit_cast(u32x2, __builtin_amdgcn_ds_read_tr16_b64_v4i16((LDS_AS s16x4*)(p + 512)));
        F[e] = (u32x4){lo[0], lo[1], hi[0], hi[1]};
      });
      for_n<4>([&](auto E) {
        constexpr int e = decltype(E)::value;
        if constexpr (i < 2) st16(F[e], OFF_WO + (tnA * 32 + 2 * tmA + i) * QS + qoff(e, lane));
        else if constexpr (i < 9) {
          constexpr int f = w1_frag(i, e);
          st16(F[e], OFF_W1 + ((((tnA & 14) + (f >> 4)) * 32 + 2 * tmA + (tnA & 1)) * 4 + ((f & 15) >> 2)) * QS + qoff(f & 3, lane));
        } else st16(F[e], OFF_W2 + (tnA * 112 + hpos(tmA, i - 9)) * QS + qoff(e, lane));
      });
    };
    auto u_sums = [&](auto I) {
      constexpr int i = decltype(I)::value;
      constexpr u32 K = w1_frag(i, 0) | (w1_frag(i, 1) << 8) | (w1_frag(i, 2) << 16) | (w1_frag(i, 3) << 24);
      const float cs = fold16(fold32(sum8(F[0]), sum8(F[2])), fold32(sum8(F[1]), sum8(F[3])));
      __builtin_amdgcn_raw_buffer_store_b32(__float_as_uint(cs), rs,
          OFF_CS1 + ((2 * tmA + (tnA & 1)) * 4096 + (tnA & 14) * 256 + ((K >> (8 * g)) & 255) * 16 + r) * 4, 0, 16);
    };
    auto up = [&](int set) { if (lane == 0) raise(set, tmA, tnA); };
    u32x4 Q[24];
    float qs[8];
    __builtin_amdgcn_s_sleep(PFW0);
#pragma unroll
    for (int j = J0; j < PFJ; ++j) warm(j);
#pragma unroll
    for (int j = MTJ; j < MTJ + MT; ++j) { if (j > MTJ) __builtin_amdgcn_s_sleep(MTS); touch(j); }
    auto first = [&](auto C) {
      constexpr int c = decltype(C)::value;
      if constexpr (c < 3) {
#pragma unroll
        for (int e = 8 * c; e < 8 * c + 8; ++e)
          Q[e] = __builtin_bit_cast(u32x4, __builtin_amdgcn_raw_buffer_load_b128(rq, rr * 6144 + c16,
              (tmA * 64 + 8 * (e & 7)) * 6144 + (e >> 3) * 2048 + tnA * 128, AX(4)));
      } else {
        u_load(IC<0>{});
        u_load(IC<1>{});
      }
    };
    if constexpr (QB < 0) for_n<4>(first);
    for_n<33>([&](auto S) {
      constexpr int s = decltype(S)::value;
      constexpr int SKP = STG ? 12 : 0;
      if (s >= SKP || idx < 16) bar();
      if constexpr (s == CQP + 5 + FLD) {
        wait_vm<between(CQP + 5, s)>();
        up(F_CQ);
        if (idx >= 16)
#pragma nounroll
          for (int u = 0; u < SKP; ++u) bar();
      }
      if constexpr (s == 26) { wait_vm<0>(); up(F_WO); }
      if constexpr (QB >= 0 && s >= QB && s < QB + 3) first(IC<s - QB>{});
      if constexpr (QB >= 0 && s == WOB) first(IC<3>{});
      if constexpr (MT > 0 && MTJ + MT + s < 8) touch(MTJ + MT + s);
      if constexpr (s >= PF0 && (s - PF0) % PFS == 0 && (s - PF0) / PFS < 8 - PFJ) warm(PFJ + (s - PF0) / PFS);
      if constexpr (s >= CQP && s < CQP + 6) {
        constexpr int h = s - CQP;
#pragma unroll
        for (int e = 0; e < 4; ++e) {
          u32x4 v = Q[4 * h + e];
          asm volatile("" : "+v"(v));
#pragma unroll
          for (int k = 0; k < 4; ++k) {
            if (h % 2 == 0 && e == 0) { qs[2 * k] = bf_lo(v[k]); qs[2 * k + 1] = bf_hi(v[k]); }
            else { qs[2 * k] += bf_lo(v[k]); qs[2 * k + 1] += bf_hi(v[k]); }
          }
        }
        if constexpr (h % 2 == 0) {
#pragma unroll
          for (int k = 0; k < 8; ++k) asm volatile("" : "+v"(qs[k]));
        } else {
          float c0 = fold16(fold32(qs[0], qs[4]), fold32(qs[2], qs[6])), c1 = fold16(fold32(qs[1], qs[5]), fold32(qs[3], qs[7]));
          c0 += __uint_as_float(__builtin_amdgcn_mov_dpp(__float_as_uint(c0), 0x128, 0xf, 0xf, false));
          c1 += __uint_as_float(__builtin_amdgcn_mov_dpp(__float_as_uint(c1), 0x128, 0xf, 0xf, false));
          const int col = 8 * (lane & 7) + rr;
          __builtin_amdgcn_raw_buffer_store_b32(__float_as_uint((rr & 1) ? c1 : c0), rs,
              OFF_CSQ + (tmA * 4096 + tnA * 256 + (col >> 5) * 96 + (h >> 1) * 32 + (col & 31)) * 4, 0, 16);
        }
      }
      if constexpr (s >= HL && s < HL + 4) {
        if constexpr ((s - HL) % 2 == 0) u_write(IC<(s - HL) / 2>{}); else u_store(IC<(s - HL) / 2>{});
      }
      if constexpr (s == G0) for_n<7>([&](auto I) { u_load(IC<2 + decltype(I)::value>{}); });
    });
    static_assert(HL >= CQP + 6 && HL + 4 < 26 && CQP + 5 + FLD < 26 && G0 >= 26, "schedule");
    for_n<7>([&](auto I) {
      constexpr int i = 2 + decltype(I)::value;
      u_write(IC<i>{});
      u_store(IC<i>{});
      u_sums(IC<i>{});
    });
    bar(); bar(); bar();
    wait_vm<0>();
    up(F_W1);
#pragma nounroll
    for (int u = 3; u <= G1; ++u) bar();
    for_n<7>([&](auto I) { u_load(IC<9 + decltype(I)::value>{}); });
#pragma nounroll
    for (int u = G1 + 1; u < 16 + H2; ++u) bar();
    for_n<14>([&](auto S) {
      constexpr int s = decltype(S)::value, i = 9 + s / 2;
      bar();
      if constexpr (s % 2 == 0) u_write(IC<i>{}); else u_store(IC<i>{});
    });
    bar(); bar(); bar(); bar();
    wait_vm<0>();
    up(F_W2);
  } else {
    FRESH_LANE();
    const int tmh = tmA > 0 ? tmA - 1 : 0;
    auto ln_phase = [&](bool qkv, int xi) {
      f32x4 sv[16], cv[16];
      f32x2 hv[4];
      const u32 so0 = OFF_ST + xi * 262144 + tmA * 16384, so1 = OFF_ST + xi * 262144 + (qkv ? tmh : tmA) * 16384;
      const u32 sc = (qkv ? OFF_CSQ : OFF_CS1) + tnA * 1024;
      auto ldcs = [&](int p0) {
#pragma unroll
        for (int p = 0; p < 16; ++p)
          cv[p] = __builtin_bit_cast(f32x4, __builtin_amdgcn_raw_buffer_load_b128(rs, lane * 16, sc + (p0 + p) * 16384, 16));
      };
#pragma unroll
      for (int p = 0; p < 16; ++p)
        sv[p] = __builtin_bit_cast(f32x4, __builtin_amdgcn_raw_buffer_load_b128(rs, lane * 16, so0 + p * 1024, 16));
#pragma unroll
      for (int p = 0; p < 4; ++p)
        hv[p] = __builtin_bit_cast(f32x2, __builtin_amdgcn_raw_buffer_load_b64(rs, (112 + r) * 8 + g * 4096, so1 + p * 1024, 16));
      bar();
      if (xi < 2) ldcs(0);
      bar(); bar();
      float mean[3] = {0.f, 0.f, 0.f}, m2[3] = {0.f, 0.f, 0.f}, dd[3] = {0.f, 0.f, 0.f};
#pragma unroll
      for (int p = 0; p < 16; ++p) { mean[0] += sv[p][0]; mean[1] += sv[p][2]; }
      mean[2] = xsum(hv[0][0] + hv[1][0] + hv[2][0] + hv[3][0]);
#pragma unroll
      for (int h = 0; h < 3; ++h) mean[h] *= (1.f / 16.f);
#pragma unroll
      for (int p = 0; p < 4; ++p) { const float d = hv[p][0] - mean[2]; m2[2] += hv[p][1]; dd[2] += d * d; }
      auto cut = [&]() {
        asm volatile("" : "+v"(mean[0]), "+v"(mean[1]), "+v"(mean[2]), "+v"(m2[0]), "+v"(m2[1]), "+v"(m2[2]),
                          "+v"(dd[0]), "+v"(dd[1]), "+v"(dd[2]));
        bar();
        asm volatile("" : "+v"(mean[0]), "+v"(mean[1]), "+v"(mean[2]), "+v"(m2[0]), "+v"(m2[1]), "+v"(m2[2]),
                          "+v"(dd[0]), "+v"(dd[1]), "+v"(dd[2]));
      };
#pragma unroll
      for (int p = 0; p < 16; ++p) {
        if (p % 8 == 0) cut();
        const float d0 = sv[p][0] - mean[0], d1 = sv[p][2] - mean[1];
        m2[0] += sv[p][1]; dd[0] += d0 * d0;
        m2[1] += sv[p][3]; dd[1] += d1 * d1;
      }
      m2[0] += 64.f * dd[0]; m2[1] += 64.f * dd[1];
      m2[2] = xsum(m2[2] + 64.f * dd[2]);
      cut();
#pragma unroll
      for (int h = 0; h < 3; ++h) { mean[h] = bfr(mean[h]); m2[h] = bfr(sqrtf(bfr(bfr(m2[h] * (1.f / DM)) + LN_EPS))); }
      cut();
#pragma unroll
      for (int h = 0; h < 3; ++h) m2[h] = 1.f / m2[h];
      *(f32x2*)(smem + L_INV + 64 + lane * 8) = (f32x2){m2[0], m2[1]};
      *(f32x2*)(smem + L_MNV + 64 + lane * 8) = (f32x2){mean[0] * m2[0], mean[1] * m2[1]};
      *(float*)(smem + L_INV + r * 4) = m2[2];
      *(float*)(smem + L_MNV + r * 4) = mean[2] * m2[2];
      bar();
      f32x4 ca = {0.f, 0.f, 0.f, 0.f};
      if (xi < 2) {
#pragma unroll
        for (int p = 0; p < 16; ++p) ca += cv[p];
        if (qkv) *(f32x4*)(smem + L_CSQ + lane * 16) = ca;
      }
      if (!qkv) {
        asm volatile("" : "+v"(ca));
        if (xi < 2) ldcs(16);
        bar(); bar(); bar(); bar();
        if (xi < 2) {
#pragma unroll
          for (int p = 0; p < 16; ++p) ca += cv[p];
          *(f32x4*)(smem + L_CS1 + lane * 16) = ca;
        }
      }
      wait_lgkm0();
    };
    constexpr int NLN = 8;
    auto bars = [&](int n) {
#pragma nounroll
      for (int u = 0; u < n; ++u) bar();
    };
    auto check = [&](const GLB_AS u64* f, int n) {
      const u64 v = ldf(f);
      bars(n);
      if (!__all(v == token())) poll(f);
    };
    auto guard = [&](const GLB_AS u64* f, int msk, int spp, int D, int K, auto&& step) {
      const u64 tok = token();
      int b = 0, lim = 0;
#pragma nounroll
      for (int spin = 0; spin < (1 << 22); ++spin) {
        const u64 v = ldf(f);
        if (b < lim) { bar(); step(b++); } else __builtin_amdgcn_s_sleep(1);
        if (b < lim) { bar(); step(b++); }
        const u32 m = (u32)__ballot(v == tok);
        const int n = __builtin_ctz(~(m & (m >> 16)));
        if (n >= 16) break;
        lim = min(K, (n & msk) * spp - D);
      }
#pragma nounroll
      while (b < K) { bar(); step(b++); }
    };
    {
      const u64 ep = __hip_atomic_load((const GLB_AS u64*)(ws + OFF_EP), __ATOMIC_RELAXED, __HIP_MEMORY_SCOPE_AGENT);
      u16 bv[4];
#pragma unroll
      for (int h = 0; h < 4; ++h) {
        const int t = min(lane + 64 * h, 223), f = t >> 4, rho = t & 15, wn = f / 7, j = f % 7;
        bv[h] = b1[tnA * 224 + (j == 6 ? 96 + 16 * wn + rho : (4 * wn + (j >> 1)) * 32 + 8 * (rho >> 2) + 4 * (j & 1) + (rho & 3))];
      }
      DMA(rsrc(b2, DM * 2), (LDS_AS void*)(smem + L_PF), 4, (lane & 15) * 128, 0, 0, 0);
      __builtin_amdgcn_s_sleep(XW);
      bars(XL);
      const __amdgpu_buffer_rsrc_t rx = rsrc(x, SEQ * DM * 2);
      bf16x8 xf[8][2];
#pragma unroll
      for (int i = 0; i < 8; ++i)
#pragma unroll
        for (int gl = 0; gl < 2; ++gl)
          xf[i][gl] = __builtin_bit_cast(bf16x8, __builtin_amdgcn_raw_buffer_load_b128(rx, r * 2048 + g * 16,
              (tmA * 128 + 16 * i) * 2048 + tnA * 128 + gl * 64, AX(5)));
      bars(TOKB - XL);
#pragma unroll
      for (int h = 0; h < 4; ++h) ((float*)(smem + L_BS))[lane + 64 * h] = bf_lo(bv[h]);
      if (lane == 0) {
        *(LDS_AS u64*)(smem + L_CNT) = 0ull;
        *(LDS_AS u64*)(smem + L_TOK) = ep * 6364136223846793005ull + 1442695040888963407ull;
      }
      wait_lgkm0();
      bars(XP - TOKB);
      const u32x4 one4 = {0x3f803f80u, 0x3f803f80u, 0x3f803f80u, 0x3f803f80u};
      const bf16x8 ones = __builtin_bit_cast(bf16x8, one4);
      const u32 svo = g == (r >> 2) ? OFF_ST + (tmA * 16 + tnA) * 1024 + r * 8 : 0x80000000u;
#pragma unroll
      for (int i = 0; i < 8; ++i) {
        if (i == 4) bar();
        const f32x4 z = {0.f, 0.f, 0.f, 0.f};
        f32x4 s = __builtin_amdgcn_mfma_f32_16x16x32_bf16(ones, xf[i][0], z, 0, 0, 0);
        f32x4 q = __builtin_amdgcn_mfma_f32_16x16x32_bf16(xf[i][0], xf[i][0], z, 0, 0, 0);
        s = __builtin_amdgcn_mfma_f32_16x16x32_bf16(ones, xf[i][1], s, 0, 0, 0);
        q = __builtin_amdgcn_mfma_f32_16x16x32_bf16(xf[i][1], xf[i][1], q, 0, 0, 0);
        const float qq = (r & 2) ? ((r & 1) ? q[3] : q[2]) : ((r & 1) ? q[1] : q[0]);
        const float m = s[0] * (1.f / 64.f);
        const f32x2 o = {m, fmaxf(__builtin_fmaf(-s[0], m, qq), 0.f)};
        __builtin_amdgcn_raw_buffer_store_b64(__builtin_bit_cast(i32x2, o), rs, svo + i * 128, 0, 16);
      }
      bars(FLD);
      wait_vm<0>();
      if (lane == 0) raise(F_ST, tmA, tnA);
      bars(POLB - (XP + 1 + FLD));
      static_assert(TOKB >= XL && XP >= TOKB && POLB >= XP + 1 + FLD && POLB + 2 + NLN <= 31 && POLB + 2 > CQP + 5 + FLD, "schedule");
    }
#pragma nounroll
    for (int pair = 0; pair < 2; ++pair) {
      if (pair) {
        const __amdgpu_buffer_rsrc_t rq = rsrc(wqkv, DM * 3 * DM * 2);
        const int m = idx * 48 + (lane < 48 ? lane : 47), k = (m * 5462) >> 16, p = m - 12 * k;
        auto warm = [&](int j) {
          DMA(rq, (LDS_AS void*)(smem + L_PF), 4, k * 6144 + (p >> 2) * 2048 + (p & 3) * 128,
              (xcd & 3) * 512 + ((j + (rotA >> 1)) & 7) * (128 * 6144), 0, 0);
          if (STG) DMA(rq, (LDS_AS void*)(smem + L_PF), 4, (k ^ 64) * 6144 + (p >> 2) * 2048 + (p & 3) * 128,
              (xcd & 3) * 512 + ((j + (rotA >> 1)) & 7) * (128 * 6144), 0, 0);
        };
        warm(0); warm(1); warm(2);
        guard(flag(4, (g & 1) ? tmh : tmA, (rotA + r) & 15), 14, 2, DW, POLB, [&](int b) { if ((b & 1) && b < 10) warm(3 + (b >> 1)); });
        if (blockIdx.x == 0 && lane == 0)
          __hip_atomic_store((GLB_AS u64*)(ws + OFF_EP), token(), __ATOMIC_RELAXED, __HIP_MEMORY_SCOPE_AGENT);
      }
      check(g < 2 ? flag(pair ? 4 + F_2ND : F_ST, (g & 1) ? tmh : tmA, r) : pair ? flag(4, tmB, tnB ^ 1) : flag(F_CQ, r, tnA), 2);
      ln_phase(true, 2 * pair);
      bars(33 - CLB - (POLB + 2 + NLN));
#pragma unroll
      for (int e = 0; e < 16; ++e)
        st16(((u32x4){~0u, ~0u, ~0u, ~0u}), OFF_XC + pair * (4 * MB) + blockIdx.x * 16384 + e * 1024 + lane * 16);
      bars(CLB);
      wait_vm<0>();
      guard(flag(4 * pair + 1, tmB, (rotB + r) & 15), 15, 1, DN, 16, [](int) {});
      guard(flag(4 * pair + 2, tmA, (rotA + r) & 15), 15, 2, DW, KW1, [](int) {});
      check(flag(4 * pair + 2 + F_2ND, tmA, r), 2);
      ln_phase(false, 2 * pair + 1);
      bars(33 - (KW1 + 2 + NLN + 4));
      guard(flag(4 * pair + 3, tmB, 8 * (tnB & 1) + ((r ^ (rotA >> 1)) & 7)), 15, 1, 3, 5 - CB, [&](int b) {
      });
      check(flag(4 * pair + 3 + F_2ND, tmB, 8 * (tnB & 1) + (r & 7)), CB);
      bars(PFX - 5);
#pragma unroll
      for (int e = 0; e < (PFX < 29 ? 2 : 0); ++e)
        DMA(rs, (LDS_AS void*)(smem + L_PF), 4, lane * 128, OFF_XC + pair * (4 * MB) + (blockIdx.x ^ PX) * 16384 + e * 8192, 0, 16);
      bars(29 - PFX);
    }
  }
  asm volatile(".Lmega_end:");
#endif
}

extern "C" void kernel_launch(void* const* d_in, const int* in_sizes, int n_in, void* d_out, int out_size,
                              void* d_ws, size_t ws_size, hipStream_t stream) {
  const u16* x0 = (const u16*)d_in[0];
  const u16* wqkv = (const u16*)d_in[1];
  const u16* wo = (const u16*)d_in[2];
  const u16* w1 = (const u16*)d_in[4];
  const u16* b1 = (const u16*)d_in[5];
  const u16* w2 = (const u16*)d_in[6];
  const u16* b2 = (const u16*)d_in[7];
  char* ws = (char*)(((uintptr_t)d_ws + 1023) & ~(uintptr_t)1023);
  (void)hipFuncSetAttribute((const void*)mega_k, hipFuncAttributeMaxDynamicSharedMemorySize, LDS_BYTES);
  mega_k<<<256, 512, LDS_BYTES, stream>>>(ws, x0, wqkv, wo, w1, b1, w2, b2, (u16*)d_out);
}
```

```cpp
#include <hip/hip_runtime.h>
#include <stdint.h>
#include <type_traits>
#include <utility>

typedef unsigned short u16;
typedef unsigned int u32;
typedef unsigned long long u64;
typedef __bf16 bf16x8 __attribute__((ext_vector_type(8)));
typedef __bf16 bf16x2 __attribute__((ext_vector_type(2)));
typedef short s16x4 __attribute__((ext_vector_type(4)));
typedef float f32x4 __attribute__((ext_vector_type(4)));
typedef float f32x2 __attribute__((ext_vector_type(2)));
typedef u32 u32x4 __attribute__((ext_vector_type(4)));
typedef u32 u32x2 __attribute__((ext_vector_type(2)));
typedef int i32x4 __attribute__((ext_vector_type(4)));
typedef int i32x2 __attribute__((ext_vector_type(2)));
#define LDS_AS __attribute__((address_space(3)))
#define GLB_AS __attribute__((address_space(1)))

constexpr int SEQ = 2048, DM = 1024, FF = 3584;
constexpr float LN_EPS = 1e-5f;

#define DN 4
#define DW 5
#define UAUX 2
#define PFW0 30
#define PF0 4
#define PFS 2
#define PFJ 2
#define CQP 7
#define HL 16
#define FLD 3
#define XW 30
#define TOKB 3
#define XP 7
#define POLB 18
#define G0 27
#define G1 12
#define H2 4
#define PUBJ 2
#define CB 1
#define KW1 11
#define CLB 1
#define SND 12
#define RST 9
#define PRB 1
#ifndef NVOTE
#define NVOTE 16
#endif
constexpr u32 MB = 1u << 20;
constexpr u32 QS = 5120;
constexpr u32 XS = 8 * MB, HS = 32 * MB;
constexpr u32 OFF_WO = 0;
constexpr u32 OFF_W1 = 3 * MB;
constexpr u32 OFF_W2 = 13 * MB;
constexpr u32 OFF_CSQ = 22 * MB;
constexpr u32 OFF_CS1 = OFF_CSQ + 262144;
constexpr u32 OFF_ST = 24 * MB;
constexpr u32 OFF_FL = OFF_ST + MB;
constexpr u32 OFF_EP = OFF_FL + 26 * 32768;
constexpr u32 OFF_DBG = 26 * MB;
constexpr u32 OFF_XC = 28 * MB;
constexpr u32 OFF_X = 28 * MB;
constexpr u32 OFF_O = OFF_X + 4 * XS;
constexpr u32 OFF_H = OFF_O + 2 * XS;
constexpr u32 WS_USED = OFF_H + 2 * HS, WS_ALIGN = 4 * MB;
#define HAS_ALIGN
constexpr int F_ST = 0, F_CQ = 8, F_WO = 9, F_W1 = 10, F_W2 = 11, F_2ND = 10, F_XC = 18, F_PR = 19;
__device__ __forceinline__ u32 qoff(u32 e, u32 slot) {
  const u32 n = 8 * e + (slot >> 3);
  return ((n + (((n + 4) * 37) >> 8)) << 7) + (slot & 7) * 16;
}
__device__ __forceinline__ u32 hpos(u32 t, u32 j) {
  return (j & 3) == 0 ? 2 * (t & 7) + (j >> 2) : 16 + 5 * (t & 7) + j - 1 - (j >> 2);
}
__device__ __forceinline__ u32 aq(u32 kg, u32 half) { return kg * 32768 + (kg & 1) * 3072 + half * 16384; }
__device__ __forceinline__ u32 ppair(u32 tm) { return (tm & 3) + (tm >> 3) * 4; }
__device__ __forceinline__ u32 xpan(u32 tm, u32 c13) { return ppair(tm) * MB + (((((tm >> 2) ^ (tm >> 1)) & 1) << 13) ^ c13); }
__device__ __forceinline__ u32 hpan(u32 tm, u32 kh, u32 c13) {
  return ppair(tm) * (4 * MB) + (((kh ^ tm) & 1) << 21) + (((((tm >> 2) ^ kh ^ tm) & 1) << 13) ^ c13);
}
__device__ __forceinline__ u32 span(u32 tm, u32 c13) { return ppair(tm) * 32768 + (((tm & 4) << 11) ^ c13); }

constexpr int SLOT = 24576, NRING = 6, RING = SLOT * NRING;
constexpr int L_INV = RING, L_MNV = L_INV + 576, L_CNT = L_MNV + 576, L_TOK = L_CNT + 8, L_C13 = L_TOK + 8;
constexpr int L_CSQ = RING + 1280, L_CS1 = L_CSQ + 1024, L_BS = L_CS1 + 1024;
constexpr int L_XC = L_BS + 1024;
constexpr int L_IM = L_XC + 6144;
constexpr int L_PF = L_IM + 4096;
constexpr int LDS_BYTES = L_PF + 256;

__device__ __forceinline__ float bf_lo(u32 w) { return __uint_as_float(w << 16); }
__device__ __forceinline__ float bf_hi(u32 w) { return __uint_as_float(w & 0xffff0000u); }
__device__ __forceinline__ u32 pack2(float lo, float hi) {
  f32x2 v = {lo, hi};
  bf16x2 r = __builtin_convertvector(v, bf16x2);
  return __builtin_bit_cast(u32, r);
}
__device__ __forceinline__ float bfr(float x) { return bf_lo(pack2(x, 0.f) & 0xffffu); }
__device__ __forceinline__ u32 gelu_tanh2(f32x2 x) {
  const float c0 = -2.3022081983f, c1 = -0.1029432396f;
  const f32x2 p = x * x * c1 + c0;
  const f32x2 a = x * p;
  const f32x2 d = (f32x2){__builtin_amdgcn_exp2f(a[0]), __builtin_amdgcn_exp2f(a[1])} + 1.f;
  const f32x2 g = x * (f32x2){__builtin_amdgcn_rcpf(d[0]), __builtin_amdgcn_rcpf(d[1])};
  return pack2(g[0], g[1]);
}
template <int N_> __device__ __forceinline__ void wait_vm() { asm volatile("s_waitcnt vmcnt(%0)" ::"n"(N_) : "memory"); }
__device__ __forceinline__ void wait_lgkm0() { asm volatile("s_waitcnt lgkmcnt(0)" ::: "memory"); }
__device__ __forceinline__ void bar() {
  __builtin_amdgcn_sched_barrier(0);
  asm volatile("s_barrier" ::: "memory");
  __builtin_amdgcn_sched_barrier(0);
}
template <int V> using IC = std::integral_constant<int, V>;
template <typename F, int... I>
__device__ __forceinline__ void for_seq(F&& f, std::integer_sequence<int, I...>) { (f(IC<I>{}), ...); }
template <int N, typename F> __device__ __forceinline__ void for_n(F&& f) { for_seq(f, std::make_integer_sequence<int, N>{}); }

__device__ __forceinline__ float xsum(float x) {
#if defined(__HIP_DEVICE_COMPILE__)
  auto a = __builtin_amdgcn_permlane16_swap(__float_as_uint(x), __float_as_uint(x), false, false);
  x = __uint_as_float(a[0]) + __uint_as_float(a[1]);
  auto b = __builtin_amdgcn_permlane32_swap(__float_as_uint(x), __float_as_uint(x), false, false);
  x = __uint_as_float(b[0]) + __uint_as_float(b[1]);
#endif
  return x;
}
__device__ __forceinline__ float xmax(float x) {
#if defined(__HIP_DEVICE_COMPILE__)
  auto a = __builtin_amdgcn_permlane16_swap(__float_as_uint(x), __float_as_uint(x), false, false);
  x = fmaxf(__uint_as_float(a[0]), __uint_as_float(a[1]));
  auto b = __builtin_amdgcn_permlane32_swap(__float_as_uint(x), __float_as_uint(x), false, false);
  x = fmaxf(__uint_as_float(b[0]), __uint_as_float(b[1]));
#endif
  return x;
}
__device__ __forceinline__ f32x2 row_part(const u32x4& c0, const u32x4& c1) {
  float v[16];
#pragma unroll
  for (int k = 0; k < 4; ++k) {
    v[2 * k] = bf_lo(c0[k]); v[2 * k + 1] = bf_hi(c0[k]);
    v[8 + 2 * k] = bf_lo(c1[k]); v[8 + 2 * k + 1] = bf_hi(c1[k]);
  }
  float s = 0.f;
#pragma unroll
  for (int k = 0; k < 16; ++k) s += v[k];
  const float m = xsum(s) * (1.f / 64.f);
  float q = 0.f;
#pragma unroll
  for (int k = 0; k < 16; ++k) { const float d = v[k] - m; q += d * d; }
  return (f32x2){m, xsum(q)};
}

#define st16(v, off) __builtin_amdgcn_raw_buffer_store_b128(__builtin_bit_cast(i32x4, v), rs, (off), 0, 16)
__device__ __forceinline__ float sum8(const u32x4& f) {
  float s = bf_lo(f[0]) + bf_hi(f[0]);
#pragma unroll
  for (int k = 1; k < 4; ++k) { s += bf_lo(f[k]); s += bf_hi(f[k]); }
  return s;
}
__device__ __forceinline__ float fold32(float a, float b) {
#if defined(__HIP_DEVICE_COMPILE__)
  const auto x = __builtin_amdgcn_permlane32_swap(__float_as_uint(a), __float_as_uint(b), false, false);
  a = __uint_as_float(x[0]) + __uint_as_float(x[1]);
#endif
  return a;
}
__device__ __forceinline__ float fold16(float a, float b) {
#if defined(__HIP_DEVICE_COMPILE__)
  const auto x = __builtin_amdgcn_permlane16_swap(__float_as_uint(a), __float_as_uint(b), false, false);
  a = __uint_as_float(x[0]) + __uint_as_float(x[1]);
#endif
  return a;
}

__device__ __forceinline__ int fresh_lane(int tid) { int l = tid & 63; asm volatile("" : "+v"(l)); return l; }
__device__ __forceinline__ f32x4 zero4() {
  f32x4 z = {0.f, 0.f, 0.f, 0.f};
  asm volatile("" : "+v"(z));
  return z;
}
template <int BIG = 0> __device__ __forceinline__ void next_slot(int& so) {
  if constexpr (BIG) so = (so + 32768) & 131071; else { so += SLOT; if (so == RING) so = 0; }
}

#define DMA __builtin_amdgcn_raw_ptr_buffer_load_lds
#define SB() __builtin_amdgcn_sched_barrier(0)
__device__ __forceinline__ void mfma(f32x4& c, const bf16x8& a, const bf16x8& b) {
  asm volatile("v_mfma_f32_16x16x32_bf16 %0, %1, %2, %0" : "+v"(c) : "v"(a), "v"(b));
}
template <int OFF> __device__ __forceinline__ void dsrd(bf16x8& d, u32 addr) {
  asm volatile("ds_read_b128 %0, %1 offset:%2" : "=v"(d) : "v"(addr), "n"(OFF));
}
template <int OFF> __device__ __forceinline__ void dstr(bf16x8& d, u32 addr) {
  u32x2 lo, hi;
  asm volatile("ds_read_b64_tr_b16 %0, %2 offset:%3\n\tds_read_b64_tr_b16 %1, %2 offset:%4"
               : "=&v"(lo), "=&v"(hi) : "v"(addr), "n"(OFF), "n"(OFF + 512));
  const u32x4 v = {lo[0], lo[1], hi[0], hi[1]};
  d = __builtin_bit_cast(bf16x8, v);
}
__device__ __forceinline__ void pin1(u32& a) { asm volatile("" : "+v"(a)); }
__device__ __forceinline__ void pin2(u32& a, u32& b) { asm volatile("" : "+v"(a), "+v"(b)); }
template <int NSL, int NM, int NR, int ENDB, class AD, class RD, class MM, class HK, class TL>
__device__ __forceinline__ void kloop(int& so, AD&& ad, RD&& rd, MM&& mm, HK&& hook, TL&& tail) {
  constexpr int BIG = NM == 32;
  ad(so, IC<0>{});
  bar();
  for_n<NR>([&](auto Q) { rd(IC<0>{}, Q); });
  next_slot<BIG>(so);
  ad(so, IC<1>{});
  hook(-1);
  auto body = [&](auto S) {
    constexpr int s = decltype(S)::value;
    wait_lgkm0();
    bar();
    for_n<NM>([&](auto M) {
      constexpr int m = decltype(M)::value;
      mm(S, M);
      if constexpr (m < NR) rd(IC<1 - s>{}, IC<m>{});
      if constexpr (m == NR) { next_slot<BIG>(so); asm volatile("" : "+s"(so)); }
      if constexpr (m == NR + 1) ad(so, S);
      SB();
    });
  };
#pragma nounroll
  for (int u = NSL / 2 - 1; u > 0; --u) {
    body(IC<0>{});
    body(IC<1>{});
    hook(NSL / 2 - 1 - u);
  }
  body(IC<0>{});
  wait_lgkm0();
  if constexpr (ENDB) { bar(); so = 0; }
  SB();
  for_n<NM>([&](auto M) { mm(IC<1>{}, M); SB(); tail(M); SB(); });
  asm volatile("s_nop 15");
  SB();
}

#define STAMP(w, i)

constexpr int w1_frag(int i, int e) {
  const int gam = 2 * (i - 2) + (e >> 1), gl = gam % 7;
  return (gam / 7) * 16 + (gl == 3 ? 6 + 7 * (e & 1) : (gl < 3 ? gl * 2 : 7 + (gl - 4) * 2) + (e & 1));
}
constexpr int between(int s0, int s1) {
  int n = 0;
  for (int s = s0 + 1; s < s1; ++s) {
    if (s >= PF0 && (s - PF0) % PFS == 0 && (s - PF0) / PFS < 8 - PFJ) n += 2;
    if (s >= CQP && s < CQP + 6 && (s - CQP) % 2 == 1) n += 1;
    if (s >= HL && s < HL + 4 && (s - HL) % 2 == 1) n += 4;
  }
  return n < 63 ? n : 63;
}

__global__ __launch_bounds__(512, 2) void mega_k(char* __restrict__ ws0, const u16* __restrict__ x,
                                                 const u16* __restrict__ wqkv, const u16* __restrict__ wo,
                                                 const u16* __restrict__ w1, const u16* __restrict__ b1,
                                                 const u16* __restrict__ w2, const u16* __restrict__ b2,
                                                 u16* __restrict__ out) {
#if defined(__HIP_DEVICE_COMPILE__)
  extern __shared__ __attribute__((aligned(16))) char smem[];
  char* const ws = ws0 + (-(u32)(uintptr_t)ws0 & (WS_ALIGN - 1));
  const int tid = threadIdx.x;
  const int wid = __builtin_amdgcn_readfirstlane(tid >> 6);
#define FRESH_LANE() const int lane = fresh_lane(tid), r = lane & 15, g = lane >> 4; (void)r; (void)g
  const int xcd = blockIdx.x & 7, idx = blockIdx.x >> 3;
  const int tmA = (idx >> 4) * 8 + (xcd >> 2) * 4 + ((idx >> 2) & 3), tnA = (xcd & 3) * 4 + (idx & 3);
  const int tmB = (idx >> 4) * 8 + (xcd >> 1) * 2 + ((idx >> 3) & 1), tnB = (idx & 7) * 2 + (xcd & 1);
  const int rotA = 8 - 8 * (xcd & 1), rotB = 4 * ((xcd + 1) & 3), rot0 = xcd;
  const __amdgpu_buffer_rsrc_t rs = __builtin_amdgcn_make_buffer_rsrc((void*)ws, 0, WS_USED, 0x00020000);
  u32 c13 = 0;
  auto side = [&]() { c13 = __builtin_amdgcn_readfirstlane(*(const volatile LDS_AS u32*)(smem + L_C13)); };
  auto flag = [&](int set, int tm, int tn) { return (GLB_AS u64*)(ws + OFF_FL + ((((set * 16 + tm) * 16 + tn) * 128) ^ c13)); };
  auto xbox = [&](int pair, int o) { return OFF_XC + pair * (4 * MB) + ((idx * 4 + (xcd & 3)) ^ o) * 32768 + (((((tmB >> 2) ^ (idx >> 4)) & 1) << 13) ^ c13); };
  auto rsrc = [&](const u16* p, u32 bytes) { return __builtin_amdgcn_make_buffer_rsrc((void*)p, 0, bytes, 0x00020000); };
  auto token = [&]() {
    const u32x2 tk = *(const volatile LDS_AS u32x2*)(smem + L_TOK);
    return (u64)(u32)__builtin_amdgcn_readfirstlane(tk[0]) | ((u64)(u32)__builtin_amdgcn_readfirstlane(tk[1]) << 32);
  };
  auto ldf = [&](const GLB_AS u64* f) { return __hip_atomic_load(f, __ATOMIC_RELAXED, __HIP_MEMORY_SCOPE_AGENT); };
  auto poll = [&](const GLB_AS u64* f) {
    const u64 tok = token();
#pragma nounroll
    for (int spin = 0; spin < (1 << 22); ++spin) {
      if (__all(ldf(f) == tok)) break;
      __builtin_amdgcn_s_sleep(1);
    }
  };
  auto raise = [&](int set, int tm, int tn) {
    __hip_atomic_store(flag(set, tm, tn), token(), __ATOMIC_RELAXED, __HIP_MEMORY_SCOPE_AGENT);
  };
  int so = 0;

  if (wid < 4) {
    auto publish = [&](auto N, int ph, int tm, int tn, int st) {
      FRESH_LANE();
      SB();
      wait_vm<decltype(N)::value>();
      u32 old = 0;
      if (lane == 0) old = __hip_atomic_fetch_add((LDS_AS u32*)(smem + L_CNT) + st, 1u, __ATOMIC_RELAXED, __HIP_MEMORY_SCOPE_WORKGROUP);
      old = __builtin_amdgcn_readfirstlane(old);
      if ((old & 3) == 3 && lane == 0) raise(ph + 1 + F_2ND * st, tm, tn);
      SB();
    };
    const float* t_inv = (const float*)(smem + L_INV);
    const float* t_mnv = (const float*)(smem + L_MNV);
    const float* t_cs = (const float*)(smem + L_CS1);
    const float* t_bs = (const float*)(smem + L_BS);
    const int wmi = wid >> 1, wni = wid & 1;

    auto finish = [&](const f32x4 (&acc)[4][4], const u32x4 (&res)[2][2], const u32x4 (&bb)[2], int w, int tn,
                      int xo  , int ph) {
      FRESH_LANE();
      u32x4 c[2][2];
#pragma unroll
      for (int i = 0; i < 2; ++i)
#pragma unroll
        for (int gl = 0; gl < 2; ++gl) {
#pragma unroll
          for (int k = 0; k < 4; ++k) {
            const f32x4& a = acc[i][2 * gl + (k >> 1)];
            const float y0 = a[2 * (k & 1)] + bf_lo(bb[gl][k]), y1 = a[2 * (k & 1) + 1] + bf_hi(bb[gl][k]);
            c[i][gl][k] = pack2(bf_lo(res[i][gl][k]) + y0, bf_hi(res[i][gl][k]) + y1);
          }
          if (xo < 4) st16(c[i][gl], OFF_X + xo * XS + xpan(tmB, c13) + tn * 65536 + aq(gl, w >> 1) + qoff((w & 1) * 2 + i, lane));
          else __builtin_amdgcn_raw_buffer_store_b128(__builtin_bit_cast(i32x4, c[i][gl]), rsrc(out, SEQ * DM * 2),
                   (tmB * 128 + (2 * w + i) * 16 + r) * 2048 + tn * 128 + gl * 64 + g * 16, 0, 16);
        }
      if (xo < 4) {
        SB();
        const f32x2 s0 = row_part(c[0][0], c[0][1]), s1 = row_part(c[1][0], c[1][1]);
        __builtin_amdgcn_raw_buffer_store_b64(__builtin_bit_cast(i32x2, (g & 1) ? s1 : s0), rs,
            lane < 32 ? OFF_ST + xo * 262144 + span(tmB, c13) + (tn + (tn & 8)) * 1024 + (w * 32 + lane) * 8 : 0x80000000u, 0, 16);
        publish(IC<1>{}, ph, tmB, tnB, 0);
        publish(IC<0>{}, ph, tmB, tnB, 1);
      }
      STAMP(0, 4 * ph + 3);
    };

#pragma nounroll
    for (int pair = 0; pair < 2; ++pair) {
      {
        f32x4 acc[4][6], acch[2];
        {
        FRESH_LANE();
        STAMP(0, 16 * pair);
        u32 pA, pB0, pB1;
        const u32 lB = 9216 + (8 * g + (r >> 2)) * 128 + (((2 * wni) ^ ((r >> 3) & 1) ^ ((g & 1) << 1)) * 32) + (r & 3) * 8;
        const u32 lA = r * 128 + ((g ^ (r >> 1)) * 16);
        auto loop = [&](auto WM, auto RAW) {
          constexpr int wm = decltype(WM)::value, raw = decltype(RAW)::value;
#pragma unroll
          for (int i = 0; i < 4; ++i)
#pragma unroll
            for (int j = 0; j < 6; ++j) acc[i][j] = zero4();
          acch[0] = zero4(); acch[1] = zero4();
          bf16x8 fA[2][4], fH[2], fB[2][6];
          u32 lA0 = raw ? lA : lane * 16, lA1 = raw ? (lA ^ 64) - SLOT : lane * 16, lB1 = lB ^ 32;
          pin2(lA0, lA1); pin1(lB1);
          kloop<32, 26, 11, 0>(so,
            [&](int s_, auto PAR) {
              pA = (decltype(PAR)::value ? lA1 : lA0) + s_; pB0 = lB + s_; pB1 = lB1 + s_;
              pin2(pA, pB0); pin1(pB1);
            },
            [&](auto SET, auto Q) {
              constexpr int set = decltype(SET)::value, q = decltype(Q)::value;
              if constexpr (q < 4) dsrd<raw ? wm * SLOT + q * 2048 : wm * 4096 + q * 1024>(fA[set][q], pA);
              else if constexpr (q < 10) dstr<((q - 4) >> 1) * 4096>(fB[set][q - 4], (q & 1) ? pB1 : pB0);
              else dsrd<raw ? SLOT + 21504 : 8192>(fH[set], pA);
            },
            [&](auto SET, auto M) {
              constexpr int set = decltype(SET)::value, m = decltype(M)::value, j = m >> 2, i = m & 3;
              if constexpr (m < 16) mfma(acc[i][j], fB[set][j], fA[set][i]);
              else if constexpr (m < 24) mfma(acc[i][j], fA[set][i], fB[set][j]);
              else if constexpr (wm == 0) mfma(acch[i], fB[set][2 + i], fH[set]);
              else mfma(acch[i], fH[set], fB[set][4 + i]);
            }, [](int) {}, [](auto) {});
        };
        if (pair == 0) { if (wmi == 0) loop(IC<0>{}, IC<1>{}); else loop(IC<1>{}, IC<1>{}); }
        else { if (wmi == 0) loop(IC<0>{}, IC<0>{}); else loop(IC<1>{}, IC<0>{}); }
        }
        FRESH_LANE();
        STAMP(0, 16 * pair + 2);
        side();
        const float* csw = (const float*)(smem + L_CSQ) + wni * 96;
        auto fix_qk = [&](const f32x4& a0, const f32x4& a1, int trow, int f) {
          const float iv = t_inv[trow + r], mv = t_mnv[trow + r];
          const f32x4 c0 = *(const f32x4*)(csw + f * 16 + 4 * g), c1 = *(const f32x4*)(csw + f * 16 + 16 + 4 * g);
          const f32x4 y0 = a0 * iv - c0 * mv, y1 = a1 * iv - c1 * mv;
          return (u32x4){pack2(y0[0], y0[1]), pack2(y0[2], y0[3]), pack2(y1[0], y1[1]), pack2(y1[2], y1[3])};
        };
        auto fix_v = [&](const f32x4& a0, const f32x4& a1, int trow) {
          const f32x4 iv = *(const f32x4*)(t_inv + trow + 4 * g), mv = *(const f32x4*)(t_mnv + trow + 4 * g);
          const float c0 = csw[64 + r], c1 = csw[80 + r];
          const f32x4 y0 = a0 * iv - mv * c0, y1 = a1 * iv - mv * c1;
          return (u32x4){pack2(y0[0], y0[1]), pack2(y0[2], y0[3]), pack2(y1[0], y1[1]), pack2(y1[2], y1[3])};
        };
        u32x4 Qf[4], Kf[5], Vf[5];
        char* xc = smem + L_XC + wni * 3072 + lane * 16;
        const int row0 = 16 + wmi * 64;
        Kf[4] = fix_qk(acc[3][2], acc[3][3], row0 + 48, 2);
        Vf[4] = fix_v(acc[3][4], acc[3][5], row0 + 48);
        if (wmi == 0) {
          *(u32x4*)xc = Kf[4];
          *(u32x4*)(xc + 1024) = Vf[4];
          Kf[0] = fix_qk(acch[0], acch[1], 0, 2);
        } else {
          *(u32x4*)(xc + 2048) = fix_v(acch[0], acch[1], 0);
        }
#pragma unroll
        for (int i = 0; i < 3; ++i) {
          Kf[i + 1] = fix_qk(acc[i][2], acc[i][3], row0 + 16 * i, 2);
          Vf[i + 1] = fix_v(acc[i][4], acc[i][5], row0 + 16 * i);
        }
        wait_lgkm0();
        bar();
        if (wmi == 1) Kf[0] = *(const u32x4*)xc;
        Vf[0] = *(const u32x4*)(xc + (wmi == 0 ? 2048 : 1024));
#pragma unroll
        for (int i = 0; i < 4; ++i) Qf[i] = fix_qk(acc[i][0], acc[i][1], row0 + 16 * i, 0);
        const bool par = r & 1;
        const float m0 = par ? 0.f : 1.f, m1 = par ? 1.f : 0.f;
        const bool own0 = 4 * g + (r & 1) <= r, own1 = 4 * g + (r & 1) + 2 <= r;
        const bool first = (tmA == 0) && (wmi == 0);
#pragma unroll
        for (int ii = 0; ii < 4; ++ii) {
          const int i = 3 - ii;
          const f32x4 z = {0.f, 0.f, 0.f, 0.f};
          const f32x4 S0 = __builtin_amdgcn_mfma_f32_16x16x32_bf16(__builtin_bit_cast(bf16x8, Kf[i]), __builtin_bit_cast(bf16x8, Qf[i]), z, 0, 0, 0);
          const f32x4 S1 = __builtin_amdgcn_mfma_f32_16x16x32_bf16(__builtin_bit_cast(bf16x8, Kf[i + 1]), __builtin_bit_cast(bf16x8, Qf[i]), z, 0, 0, 0);
          float l0 = own0 ? (par ? S1[1] : S1[0]) : (par ? S0[1] : S0[0]);
          float l1 = own1 ? (par ? S1[3] : S1[2]) : (par ? S0[3] : S0[2]);
          const float c = 0.17677669529663687f * 1.4426950408889634f;
          l0 *= c; l1 *= c;
          if (i == 0 && first) {
            if (!own0) l0 = -1.4426950e9f;
            if (!own1) l1 = -1.4426950e9f;
          }
          const float mx = xmax(fmaxf(l0, l1));
          const float p0 = __builtin_amdgcn_exp2f(l0 - mx), p1 = __builtin_amdgcn_exp2f(l1 - mx);
          const float rsum = __builtin_amdgcn_rcpf(xsum(p0 + p1));
          const float q0 = p0 * rsum, q1 = p1 * rsum;
          const u32 w0 = pack2(q0 * m0, q0 * m1), w1 = pack2(q1 * m0, q1 * m1);
          const u32x4 P = {own0 ? 0u : w0, own1 ? 0u : w1, own0 ? w0 : 0u, own1 ? w1 : 0u};
          const u32x4 V0 = {Vf[i][0], Vf[i][1], Vf[i + 1][0], Vf[i + 1][1]};
          const u32x4 V1 = {Vf[i][2], Vf[i][3], Vf[i + 1][2], Vf[i + 1][3]};
          const f32x4 O0 = __builtin_amdgcn_mfma_f32_16x16x32_bf16(__builtin_bit_cast(bf16x8, V0), __builtin_bit_cast(bf16x8, P), z, 0, 0, 0);
          const f32x4 O1 = __builtin_amdgcn_mfma_f32_16x16x32_bf16(__builtin_bit_cast(bf16x8, V1), __builtin_bit_cast(bf16x8, P), z, 0, 0, 0);
          const u32x4 o = {pack2(O0[0], O0[1]), pack2(O0[2], O0[3]), pack2(O1[0], O1[1]), pack2(O1[2], O1[3])};
          st16(o, OFF_O + pair * XS + xpan(tmA, c13) + tnA * 65536 + aq(wni, wmi) + qoff(i, lane));
        }
        publish(IC<0>{}, 4 * pair, tmA, tnA, 0);
        STAMP(0, 16 * pair + 3);
      }
      {
        FRESH_LANE();
        u32x4 res[2][2];
        const u32x4 bb[2] = {{0u, 0u, 0u, 0u}, {0u, 0u, 0u, 0u}};
        f32x4 acc[4][4];
#pragma unroll
        for (int i = 0; i < 2; ++i)
#pragma unroll
          for (int j = 0; j < 4; ++j) acc[i][j] = zero4();
        bf16x8 fA[2][2][2], fB[2][2][4];
        u32 pA, pB;
        auto ldres = [&]() {
#pragma unroll
          for (int i = 0; i < 2; ++i)
#pragma unroll
            for (int gl = 0; gl < 2; ++gl)
              res[i][gl] = __builtin_bit_cast(u32x4, __builtin_amdgcn_raw_buffer_load_b128(
                  pair ? rs : rsrc(x, SEQ * DM * 2), pair ? qoff((wid & 1) * 2 + i, lane) : (16 * i + r) * 2048 + g * 16,
                  pair ? OFF_X + 2 * XS + xpan(tmB, c13) + tnB * 65536 + aq(gl, wid >> 1)
                       : (tmB * 128 + wid * 32) * 2048 + tnB * 128 + gl * 64, 16));
        };
        ldres();
        kloop<16, 16, 12, 0>(so,
          [&](int s_, auto) { pA = lane * 16 + wid * 2048 + s_; pB = lane * 16 + s_; pin2(pA, pB); },
          [&](auto SET, auto Q) {
            constexpr int set = decltype(SET)::value, q = decltype(Q)::value, s = q / 6, m = q % 6;
            if constexpr (m < 2) dsrd<s * 8192 + m * 1024>(fA[set][s][m], pA);
            else dsrd<16384 + s * 4096 + (m - 2) * 1024>(fB[set][s][m - 2], pB);
          },
          [&](auto SET, auto M) {
            constexpr int set = decltype(SET)::value, m = decltype(M)::value, s = m >> 3, j = (m >> 1) & 3, i = m & 1;
            mfma(acc[i][j], fB[set][s][j], fA[set][s][i]);
          },
          [](int) {}, [](auto) {});
        STAMP(0, 16 * pair + 6);
        finish(acc, res, bb, wid, tnB, 2 * pair + 1, 4 * pair + 1);
      }
      {
        f32x4 acc[4][7];
        {
        FRESH_LANE();
#pragma unroll
        for (int i = 0; i < 4; ++i)
#pragma unroll
          for (int j = 0; j < 7; ++j) acc[i][j] = zero4();
        bf16x8 fA[2][4], fB[2][7];
        u32 pA, pB;
        kloop<32, 28, 11, 1>(so,
          [&](int s_, auto) { pA = lane * 16 + wmi * 4096 + s_; pB = lane * 16 + wni * 7168 + s_; pin2(pA, pB); },
          [&](auto SET, auto Q) {
            constexpr int set = decltype(SET)::value, q = decltype(Q)::value;
            if constexpr (q < 4) dsrd<q * 1024>(fA[set][q], pA);
            else dsrd<8192 + (q - 4) * 1024>(fB[set][q - 4], pB);
          },
          [&](auto SET, auto M) {
            constexpr int set = decltype(SET)::value, m = decltype(M)::value, j = m >> 2, i = m & 3;
            mfma(acc[i][j], fB[set][j], fA[set][i]);
          }, [](int) {}, [](auto) {});
        }
        FRESH_LANE();
        STAMP(0, 16 * pair + 10);
        float iv[4], mv[4];
#pragma unroll
        for (int i = 0; i < 4; ++i) { iv[i] = t_inv[16 + wmi * 64 + 16 * i + r]; mv[i] = t_mnv[16 + wmi * 64 + 16 * i + r]; }
        const u32 hbase = OFF_H + pair * HS + hpan(tmA, tnA >> 3, c13);
        auto act = [&](const f32x4& a, const f32x4& cs, const f32x4& bs, int i) {
          const f32x4 y = a * iv[i] + (bs - cs * mv[i]);
          return (u32x2){gelu_tanh2((f32x2){y[0], y[1]}), gelu_tanh2((f32x2){y[2], y[3]})};
        };
#pragma unroll
        for (int jp = 0; jp < 3; ++jp) {
          const int t0 = (wni * 7 + 2 * jp) * 16 + 4 * g;
          const f32x4 cs0 = *(const f32x4*)(t_cs + t0), cs1 = *(const f32x4*)(t_cs + t0 + 16);
          const f32x4 bs0 = *(const f32x4*)(t_bs + t0), bs1 = *(const f32x4*)(t_bs + t0 + 16);
#pragma unroll
          for (int i = 0; i < 4; ++i) {
            const u32x2 a = act(acc[i][2 * jp], cs0, bs0, i), b = act(acc[i][2 * jp + 1], cs1, bs1, i);
            const u32x4 v = {a[0], a[1], b[0], b[1]};
            st16(v, hbase + aq(hpos(tnA, wni * 4 + jp), wmi) + qoff(i, lane));
            if (4 * jp + i == 3 + PUBJ) publish(IC<PUBJ>{}, 4 * pair + 2, tmA, tnA, 0);
          }
          if (jp == 0) SB();
        }
        {
          const int t0 = (wni * 7 + 6) * 16 + 4 * g;
          const f32x4 cs0 = *(const f32x4*)(t_cs + t0), bs0 = *(const f32x4*)(t_bs + t0);
#pragma unroll
          for (int i = 0; i < 4; i += 2) {
            const u32x2 a = act(acc[i][6], cs0, bs0, i), b = act(acc[i + 1][6], cs0, bs0, i + 1);
            const auto s0 = __builtin_amdgcn_permlane16_swap(a[0], b[0], false, false);
            const auto s1 = __builtin_amdgcn_permlane16_swap(a[1], b[1], false, false);
            const u32x4 v = {s0[0], s1[0], s0[1], s1[1]};
            st16(v, hbase + aq(hpos(tnA, 3), wmi) + qoff(i + (g & 1), (2 * wni + (g >> 1)) * 16 + r));
          }
        }
        STAMP(0, 16 * pair + 11);
      }
      {
        FRESH_LANE();
        f32x4 acc[4][4];
#pragma unroll
        for (int i = 0; i < 4; ++i)
#pragma unroll
          for (int j = 0; j < 4; ++j) acc[i][j] = zero4();
        bf16x8 fA[2][2][4], fB[2][2][4];
        u32 pA, pB;
        const u32 xc = wmi * 16384 + wni * 4096;
        const int w = 2 * (tnB & 1) + wmi, tn = (tnB & 14) + wni;
        u32x4 res[2][2], bb[2];
        publish(IC<0>{}, 4 * pair + 2, tmA, tnA, 1);
        kloop<28, 32, 16, 1>(so,
          [&](int s_, auto) { pA = lane * 16 + wmi * 2048 + s_; pB = lane * 16 + wni * 4096 + s_; pin2(pA, pB); },
          [&](auto SET, auto Q) {
            constexpr int set = decltype(SET)::value, q = decltype(Q)::value, s = q >> 3, m = q & 7;
            if constexpr (m < 4) dsrd<s * 8192 + (m >> 1) * 4096 + (m & 1) * 1024>(fA[set][s][m], pA);
            else dsrd<16384 + s * 8192 + (m - 4) * 1024>(fB[set][s][m - 4], pB);
          },
          [&](auto SET, auto M) {
            constexpr int set = decltype(SET)::value, m = decltype(M)::value;
            constexpr int s = (m >> 3) & 1, j = (m >> 1) & 3, i = (m & 1) + (m < 16 ? 2 : 0);
            mfma(acc[i][j], fB[set][s][j], fA[set][s][i]);
          },
          [&](int t) {
            if (t == RST) {
              FRESH_LANE();
#pragma unroll
              for (int i = 0; i < 2; ++i)
#pragma unroll
                for (int gl = 0; gl < 2; ++gl)
                  res[i][gl] = __builtin_bit_cast(u32x4, __builtin_amdgcn_raw_buffer_load_b128(rs, qoff((w & 1) * 2 + i, lane),
                      OFF_X + (2 * pair + 1) * XS + xpan(tmB, c13) + tn * 65536 + aq(gl, w >> 1), 16));
#pragma unroll
              for (int gl = 0; gl < 2; ++gl)
                bb[gl] = __builtin_bit_cast(u32x4, __builtin_amdgcn_raw_buffer_load_b128(rsrc(b2, DM * 2), g * 16, tn * 128 + gl * 64, 0));
            }
          },
          [&](auto M) {
            constexpr int k = decltype(M)::value - SND - 1;
            if constexpr (k >= 0 && k < 8 && k % 2 == 0) {
              const f32x4 a = acc[2][k >> 1], b = acc[3][k >> 1];
              const u32x4 v = {pack2(a[0], a[1]), pack2(a[2], a[3]), pack2(b[0], b[1]), pack2(b[2], b[3])};
              st16(__builtin_elementwise_min(v, (u32x4){~1u, ~1u, ~1u, ~1u}), xbox(pair, 0) + xc + lane * 16 + k * 512);
            }
          });
        STAMP(0, 16 * pair + 14);
        u32x4 xv[4];
#pragma nounroll
        for (int spin = 0; spin < (1 << 22); ++spin) {
#pragma unroll
          for (int k = 0; k < 4; ++k)
            xv[k] = __builtin_bit_cast(u32x4, __builtin_amdgcn_raw_buffer_load_b128(rs, lane * 16 + k * 1024, xbox(pair, 1) + xc, 16));
          const u32x4 o = __builtin_elementwise_max(__builtin_elementwise_max(xv[0], xv[1]), __builtin_elementwise_max(xv[2], xv[3]));
          if (!__any(max(max(o[0], o[1]), max(o[2], o[3])) == ~0u)) break;
          asm volatile("" ::: "memory");
        }
#pragma unroll
        for (int k = 0; k < 4; ++k) {
          acc[0][k] += (f32x4){bf_lo(xv[k][0]), bf_hi(xv[k][0]), bf_lo(xv[k][1]), bf_hi(xv[k][1])};
          acc[1][k] += (f32x4){bf_lo(xv[k][2]), bf_hi(xv[k][2]), bf_lo(xv[k][3]), bf_hi(xv[k][3])};
        }
        finish(acc, res, bb, w, tn, pair == 0 ? 2 : 4, 4 * pair + 3);
      }
    }
  } else if (wid == 4) {
    {
      FRESH_LANE();
      STAMP(1, 0);
      const __amdgpu_buffer_rsrc_t rx = rsrc(x, SEQ * DM * 2);
      u32 vo[4];
#pragma unroll
      for (int e = 0; e < 4; ++e)
        vo[e] = (8 * e + (lane >> 3)) * 2048 + (((lane & 7) ^ (4 * (e & 1) + (lane >> 4))) * 16) - e * 1024;
      const u32 sx = tmA * 128 * 2048, sh = (tmA > 0 ? tmA * 128 - 16 : 0) * 2048;
      auto half = [&](auto H, int U) {
        constexpr int h = decltype(H)::value;
        U = (U + 2 * rot0) & 15;
        for_n<8>([&](auto E) {
          constexpr int e = decltype(E)::value;
          DMA(rx, (LDS_AS void*)(smem + so + h * SLOT + (e >> 2) * 4096), 16, vo[e & 3],
              sx + (h * 64 + (e >> 2) * 32) * 2048 + U * 128, (e & 3) * 1024, 0);
        });
        DMA(rx, (LDS_AS void*)(smem + so + SLOT + 21504), 16, vo[h], sh + U * 128, h * 1024, 0);
        if constexpr (h == 1) { so += 2 * SLOT; if (so == RING) so = 0; }
      };
#pragma nounroll
      for (int U = 0; U < 3; ++U) { half(IC<0>{}, U); half(IC<1>{}, U); }
      wait_vm<36>();
      bar();
      bar();
#pragma nounroll
      for (int U = 3; U < 16; ++U) {
        wait_vm<18>();
        bar();
        half(IC<0>{}, U);
        bar();
        half(IC<1>{}, U);
      }
      wait_vm<18>(); bar(); bar();
      wait_vm<0>(); bar(); bar();
    }
    auto phase = [&](auto NSL_, auto PA_, auto HALO_, u32 src, u32 hsrc, int ph  , int tm, int tmh) {
      constexpr int NSL = decltype(NSL_)::value, PA = decltype(PA_)::value, HALO = decltype(HALO_)::value;
      constexpr int BIG = NSL == 28, PT = PA + HALO, D = BIG ? 3 : PA == 16 ? DN : DW;
      FRESH_LANE();
      u32 vo[4];
#pragma unroll
      for (int e = 0; e < 4; ++e) vo[e] = qoff(e, lane) - e * 1024;
      poll(flag(ph + 1, (lane & 16) ? tmh : tm, BIG ? 8 * (tnB & 1) + ((rotA >> 1) + (r < 3 ? r : 0))
                                               : PA == 16 ? (rotB + (r < DN ? r : 0)) & 15 : rotA + (HALO || 2 * (r & 3) < DW ? r & 3 : 0)));
      static_assert(DW <= 6, "four producers");
      STAMP(1, 4 * (ph + 1));
      const u32 flip = BIG ? (tnB & 1) * 16384 : 0;
      auto issue = [&](int u) {
        u = BIG ? (u < 8 ? u ^ (rotA >> 1) : u) : PA == 16 ? (u + rotB) & 15 : u ^ (2 * rotA);
        if constexpr (PA == 16 && !BIG) wait_vm<47>();
        const u32 ko = aq(u * (PA / 8), 0);
        for_n<PA>([&](auto E) {
          constexpr int e = decltype(E)::value;
          DMA(rs, (LDS_AS void*)(smem + so + (e >> 2) * 4096), 16, vo[e & 3],
              src + ko + aq(e >> 3, (e >> 2) & 1) + ((e & 4) ? -flip : flip), (e & 3) * 1024, 16);
        });
        if constexpr (HALO) DMA(rs, (LDS_AS void*)(smem + so + 8192), 16, vo[3], hsrc + ko, 0, 16);
        next_slot<BIG>(so);
      };
#pragma nounroll
      for (int u = 0; u < D; ++u) issue(u);
#pragma nounroll
      for (int u = 0; u < NSL - D; ++u) {
        wait_vm<(D - 1) * PT>();
        bar();
        issue(u + D);
      }
      for_n<D>([&](auto M) { wait_vm<(D - 1 - decltype(M)::value) * PT>(); bar(); });
    };
#pragma nounroll
    for (int pair = 0; pair < 2; ++pair) {
      const int tmh = tmA > 0 ? tmA - 1 : 0;
      const u32 xq = OFF_X + 2 * pair * XS;
      if (pair) phase(IC<32>{}, IC<8>{}, IC<1>{}, xq + xpan(tmA, c13), xq + xpan(tmh, c13) + 16384 + 3072, 3, tmA, tmh);
      side();
      bar();
      phase(IC<16>{}, IC<16>{}, IC<0>{}, OFF_O + pair * XS + xpan(tmB, c13), 0, 4 * pair, tmB, tmB);
      phase(IC<32>{}, IC<8>{}, IC<0>{}, xq + XS + xpan(tmA, c13), 0, 4 * pair + 1, tmA, tmA);
      bar();
      so = 0;
      phase(IC<28>{}, IC<16>{}, IC<0>{}, OFF_H + pair * HS + hpan(tmB, tnB & 1, c13), 0, 4 * pair + 2, tmB, tmB);
      bar();
      so = 0;
    }
  } else if (wid == 5) {
    FRESH_LANE();
    auto phase = [&](auto NSL_, auto PB_, int rot, auto&& piece) {
      constexpr int NSL = decltype(NSL_)::value, PB = decltype(PB_)::value;
      auto issue = [&](int u) {
        u = PB == 16 ? (u < 8 ? u ^ rot : u) : PB == 8 ? (u + rot) & 15 : u ^ rot;
        wait_vm<63 - PB>();
        for_n<PB>([&](auto I) { piece(I, u); });
        next_slot<PB == 16>(so);
      };
      constexpr int D = PB == 16 ? 3 : PB == 8 ? (DN < 4 ? DN : 5) : DW;
#pragma nounroll
      for (int u = 0; u < D; ++u) issue(u);
#pragma nounroll
      for (int u = 0; u < NSL - D; ++u) {
        wait_vm<(D - 1) * PB>();
        bar();
        issue(u + D);
      }
      for_n<D>([&](auto M) { wait_vm<(D - 1 - decltype(M)::value) * PB>(); bar(); });
    };
    auto tiles = [&](auto NSL_, auto PB_, auto BOFF_, u32 src) {
      constexpr int PB = decltype(PB_)::value, BOFF = decltype(BOFF_)::value;
      u32 vo[4];
#pragma unroll
      for (int e = 0; e < 4; ++e) vo[e] = qoff(e, lane) - e * 1024;
      phase(NSL_, PB_, PB == 16 ? rotA >> 1 : PB == 8 ? rotB : 2 * rotA, [&](auto I, int u) {
        constexpr int i = decltype(I)::value;
        DMA(rs, (LDS_AS void*)(smem + so + BOFF + (i >> 2) * 4096), 16, vo[i & 3],
            PB == 16 ? src + u * (2 * QS) + (i >> 3) * QS + ((i >> 2) & 1) * (112 * QS)
                     : src + u * ((PB + 3) / 4 * QS) + (i >> 2) * QS, (i & 3) * 1024, 16);
      });
    };
#pragma nounroll
    for (int pair = 0; pair < 2; ++pair) {
      {
        const __amdgpu_buffer_rsrc_t rq = rsrc(wqkv, DM * 3 * DM * 2);
        u32 vo[4];
#pragma unroll
        for (int e = 0; e < 4; ++e)
          vo[e] = (8 * e + (lane >> 3)) * 6144 + (((lane & 7) ^ (2 * ((lane >> 4) & 1) + 4 * (e & 1))) * 16) - e * 1024;
        phase(IC<32>{}, IC<12>{}, pair ? 2 * rotA : 0, [&](auto I, int u) {
          constexpr int i = decltype(I)::value;
          if (pair == 0) u = (u + 4 * rot0) & 31;
          DMA(rq, (LDS_AS void*)(smem + so + 9216 + (i >> 2) * 4096), 16, vo[i & 3],
              u * (32 * 6144) + (i >> 2) * 2048 + tnA * 128, (i & 3) * 1024, 0);
        });
      }
      bar();
      if (pair == 0) poll(flag(F_WO, r, tnB));
      tiles(IC<16>{}, IC<8>{}, IC<16384>{}, OFF_WO + tnB * (32 * QS));
      if (pair == 0) poll(flag(F_W1, r, (tnA & 14) + (g & 1)));
      tiles(IC<32>{}, IC<14>{}, IC<8192>{}, OFF_W1 + tnA * (32 * 4 * QS));
      bar();
      so = 0;
      if (pair == 0) poll(flag(F_W2, 8 * (tnB & 1) + (r & 7), (tnB & 14) + (r >> 3)));
      tiles(IC<28>{}, IC<16>{}, IC<16384>{}, OFF_W2 + ((tnB & 14) * 112 + (tnB & 1) * 56) * QS);
      bar();
      so = 0;
    }
  } else if (wid == 6) {
    FRESH_LANE();
    const int rr = lane >> 3, c16 = (lane & 7) * 16;
    const __amdgpu_buffer_rsrc_t rx = rsrc(x, SEQ * DM * 2), rq = rsrc(wqkv, DM * 3 * DM * 2);
    auto warm = [&](int j) {
      const int n = idx * 64 + lane, m = idx * 48 + (lane < 48 ? lane : 47), k = (m * 5462) >> 16, p = m - 12 * k;
      j = (j + rot0) & 7;
      DMA(rx, (LDS_AS void*)(smem + L_PF), 4, (n & 511) * 2048 + ((n >> 9) & 1) * 128, ((idx >> 4) * 1024 + (xcd >> 2) * 512) * 2048 + j * 256, 0, 0);
      DMA(rq, (LDS_AS void*)(smem + L_PF), 4, k * 6144 + (p >> 2) * 2048 + (p & 3) * 128, (xcd & 3) * 512 + j * (128 * 6144), 0, 0);
    };
    u32x4 U[7][4], F[4];
    auto u_load = [&](auto I) {
      constexpr int i = decltype(I)::value;
#pragma unroll
      for (int e = 0; e < 4; ++e) {
        if constexpr (i < 2)
          U[(i + 5) % 7][e] = __builtin_bit_cast(u32x4, __builtin_amdgcn_raw_buffer_load_b128(rsrc(wo, DM * DM * 2),
              ((rr & 3) + 16 * (rr >> 2)) * 2048 + c16, ((2 * tmA + i) * 32 + 4 * e) * 2048 + tnA * 128, UAUX));
        else if constexpr (i < 9)
          U[(i + 5) % 7][e] = __builtin_bit_cast(u32x4, __builtin_amdgcn_raw_buffer_load_b128(rsrc(w1, DM * FF * 2),
              rr * 7168 + c16, ((2 * tmA + (tnA & 1)) * 32 + 8 * e) * 7168 + (7 * (tnA >> 1) + i - 2) * 128, UAUX));
        else
          U[(i + 5) % 7][e] = __builtin_bit_cast(u32x4, __builtin_amdgcn_raw_buffer_load_b128(rsrc(w2, DM * FF * 2),
              rr * 2048 + c16, ((7 * tmA + i - 9) * 32 + 8 * e) * 2048 + tnA * 128, UAUX));
      }
    };
    auto u_write = [&](auto I) {
      constexpr int i = decltype(I)::value;
#pragma unroll
      for (int e = 0; e < 4; ++e) {
        const u32x4 v = U[(i + 5) % 7][e];
        *(u32x4*)(smem + L_IM + e * 1024 + rr * 128 + (c16 ^ ((rr & 2) << 5))) = (e & 1) ? (u32x4){v[2], v[3], v[0], v[1]} : v;
      }
    };
    auto u_store = [&](auto I) {
      constexpr int i = decltype(I)::value;
      const u32 sw = ((r & 8) << 3) | ((g & 1) << 3);
      const char* im = smem + L_IM + (8 * g + (r >> 2)) * 128;
      for_n<4>([&](auto E) {
        constexpr int e = decltype(E)::value;
        constexpr bool split = i >= 2 && i < 9 && (2 * (i - 2) + (e >> 1)) % 7 == 3;
        const char* p = im + (((e >> 1) * 64 + (split ? (e & 1) * 32 + (r & 3) * 8 : (r & 3) * 16 + (e & 1) * 8)) ^ sw);
        const u32x2 lo = __builtin_bit_cast(u32x2, __builtin_amdgcn_ds_read_tr16_b64_v4i16((LDS_AS s16x4*)p));
        const u32x2 hi = __builtin_bit_cast(u32x2, __builtin_amdgcn_ds_read_tr16_b64_v4i16((LDS_AS s16x4*)(p + 512)));
        F[e] = (u32x4){lo[0], lo[1], hi[0], hi[1]};
      });
      for_n<4>([&](auto E) {
        constexpr int e = decltype(E)::value;
        if constexpr (i < 2) st16(F[e], OFF_WO + (tnA * 32 + 2 * tmA + i) * QS + qoff(e, lane));
        else if constexpr (i < 9) {
          constexpr int f = w1_frag(i, e);
          st16(F[e], OFF_W1 + ((((tnA & 14) + (f >> 4)) * 32 + 2 * tmA + (tnA & 1)) * 4 + ((f & 15) >> 2)) * QS + qoff(f & 3, lane));
        } else st16(F[e], OFF_W2 + (tnA * 112 + (tmA >> 3) * 56 + hpos(tmA, i - 9)) * QS + qoff(e, lane));
      });
    };
    auto u_sums = [&](auto I) {
      constexpr int i = decltype(I)::value;
      constexpr u32 K = w1_frag(i, 0) | (w1_frag(i, 1) << 8) | (w1_frag(i, 2) << 16) | (w1_frag(i, 3) << 24);
      const float cs = fold16(fold32(sum8(F[0]), sum8(F[2])), fold32(sum8(F[1]), sum8(F[3])));
      __builtin_amdgcn_raw_buffer_store_b32(__float_as_uint(cs), rs,
          OFF_CS1 + ((2 * tmA + (tnA & 1)) * 4096 + (tnA & 14) * 256 + ((K >> (8 * g)) & 255) * 16 + r) * 4, 0, 16);
    };
    auto up = [&](int set) { if (lane == 0) raise(set, tmA, tnA); };
    u32x4 Q[24];
    float qs[8];
    __builtin_amdgcn_s_sleep(PFW0);
#pragma unroll
    for (int e = 0; e < 24; ++e)
      Q[e] = __builtin_bit_cast(u32x4, __builtin_amdgcn_raw_buffer_load_b128(rq, rr * 6144 + c16,
          (tmA * 64 + 8 * (e & 7)) * 6144 + (e >> 3) * 2048 + tnA * 128, 0));
    u_load(IC<0>{});
    u_load(IC<1>{});
    for_n<33>([&](auto S) {
      constexpr int s = decltype(S)::value;
      bar();
      if constexpr (s == CQP + 5 + FLD) { wait_vm<between(CQP + 5, s)>(); up(F_CQ); }
      if constexpr (s == 26) { wait_vm<0>(); up(F_WO); }
      if constexpr (s >= PF0 && (s - PF0) % PFS == 0 && (s - PF0) / PFS < 8 - PFJ) warm(PFJ + (s - PF0) / PFS);
      if constexpr (s >= CQP && s < CQP + 6) {
        constexpr int h = s - CQP;
#pragma unroll
        for (int e = 0; e < 4; ++e) {
          u32x4 v = Q[4 * h + e];
          asm volatile("" : "+v"(v));
#pragma unroll
          for (int k = 0; k < 4; ++k) {
            if (h % 2 == 0 && e == 0) { qs[2 * k] = bf_lo(v[k]); qs[2 * k + 1] = bf_hi(v[k]); }
            else { qs[2 * k] += bf_lo(v[k]); qs[2 * k + 1] += bf_hi(v[k]); }
          }
        }
        if constexpr (h % 2 == 0) {
#pragma unroll
          for (int k = 0; k < 8; ++k) asm volatile("" : "+v"(qs[k]));
        } else {
          float c0 = fold16(fold32(qs[0], qs[4]), fold32(qs[2], qs[6])), c1 = fold16(fold32(qs[1], qs[5]), fold32(qs[3], qs[7]));
          c0 += __uint_as_float(__builtin_amdgcn_mov_dpp(__float_as_uint(c0), 0x128, 0xf, 0xf, false));
          c1 += __uint_as_float(__builtin_amdgcn_mov_dpp(__float_as_uint(c1), 0x128, 0xf, 0xf, false));
          const int col = 8 * (lane & 7) + rr;
          __builtin_amdgcn_raw_buffer_store_b32(__float_as_uint((rr & 1) ? c1 : c0), rs,
              OFF_CSQ + (tmA * 4096 + tnA * 256 + (col >> 5) * 96 + (h >> 1) * 32 + (col & 31)) * 4, 0, 16);
        }
      }
      if constexpr (s >= HL && s < HL + 4) {
        if constexpr ((s - HL) % 2 == 0) u_write(IC<(s - HL) / 2>{}); else u_store(IC<(s - HL) / 2>{});
      }
      if constexpr (s == G0) for_n<7>([&](auto I) { u_load(IC<2 + decltype(I)::value>{}); });
    });
    static_assert(HL >= CQP + 6 && HL + 4 < 26 && CQP + 5 + FLD < 26 && G0 >= 26, "schedule");
    for_n<7>([&](auto I) {
      constexpr int i = 2 + decltype(I)::value;
      u_write(IC<i>{});
      u_store(IC<i>{});
      u_sums(IC<i>{});
    });
    bar(); bar(); bar();
    wait_vm<0>();
    up(F_W1);
#pragma nounroll
    for (int u = 3; u <= G1; ++u) bar();
    for_n<7>([&](auto I) { u_load(IC<9 + decltype(I)::value>{}); });
#pragma nounroll
    for (int u = G1 + 1; u < 16 + H2; ++u) bar();
    for_n<14>([&](auto S) {
      constexpr int s = decltype(S)::value, i = 9 + s / 2;
      bar();
      if constexpr (s % 2 == 0) u_write(IC<i>{}); else u_store(IC<i>{});
    });
    bar(); bar(); bar(); bar();
    wait_vm<0>();
    up(F_W2);
  } else {
    FRESH_LANE();
    const int tmh = tmA > 0 ? tmA - 1 : 0;
    auto ln_phase = [&](bool qkv, int xi) {
      f32x4 sv[16], cv[16];
      f32x2 hv[4];
      const u32 so0 = OFF_ST + xi * 262144 + span(tmA, c13), so1 = OFF_ST + xi * 262144 + span(qkv ? tmh : tmA, c13);
      const u32 sc = (qkv ? OFF_CSQ : OFF_CS1) + tnA * 1024;
      auto ldcs = [&](int p0) {
#pragma unroll
        for (int p = 0; p < 16; ++p)
          cv[p] = __builtin_bit_cast(f32x4, __builtin_amdgcn_raw_buffer_load_b128(rs, lane * 16, sc + (p0 + p) * 16384, 16));
      };
#pragma unroll
      for (int p = 0; p < 16; ++p)
        sv[p] = __builtin_bit_cast(f32x4, __builtin_amdgcn_raw_buffer_load_b128(rs, lane * 16, so0 + (p + (p & 8)) * 1024, 16));
#pragma unroll
      for (int p = 0; p < 4; ++p)
        hv[p] = __builtin_bit_cast(f32x2, __builtin_amdgcn_raw_buffer_load_b64(rs, (112 + r) * 8 + (g + (g & 2)) * 4096, so1 + p * 1024, 16));
      bar();
      if (xi < 2) ldcs(0);
      bar(); bar();
      float mean[3] = {0.f, 0.f, 0.f}, m2[3] = {0.f, 0.f, 0.f}, dd[3] = {0.f, 0.f, 0.f};
#pragma unroll
      for (int p = 0; p < 16; ++p) { mean[0] += sv[p][0]; mean[1] += sv[p][2]; }
      mean[2] = xsum(hv[0][0] + hv[1][0] + hv[2][0] + hv[3][0]);
#pragma unroll
      for (int h = 0; h < 3; ++h) mean[h] *= (1.f / 16.f);
#pragma unroll
      for (int p = 0; p < 4; ++p) { const float d = hv[p][0] - mean[2]; m2[2] += hv[p][1]; dd[2] += d * d; }
      auto cut = [&]() {
        asm volatile("" : "+v"(mean[0]), "+v"(mean[1]), "+v"(mean[2]), "+v"(m2[0]), "+v"(m2[1]), "+v"(m2[2]),
                          "+v"(dd[0]), "+v"(dd[1]), "+v"(dd[2]));
        bar();
        asm volatile("" : "+v"(mean[0]), "+v"(mean[1]), "+v"(mean[2]), "+v"(m2[0]), "+v"(m2[1]), "+v"(m2[2]),
                          "+v"(dd[0]), "+v"(dd[1]), "+v"(dd[2]));
      };
#pragma unroll
      for (int p = 0; p < 16; ++p) {
        if (p % 8 == 0) cut();
        const float d0 = sv[p][0] - mean[0], d1 = sv[p][2] - mean[1];
        m2[0] += sv[p][1]; dd[0] += d0 * d0;
        m2[1] += sv[p][3]; dd[1] += d1 * d1;
      }
      m2[0] += 64.f * dd[0]; m2[1] += 64.f * dd[1];
      m2[2] = xsum(m2[2] + 64.f * dd[2]);
      cut();
#pragma unroll
      for (int h = 0; h < 3; ++h) { mean[h] = bfr(mean[h]); m2[h] = bfr(sqrtf(bfr(bfr(m2[h] * (1.f / DM)) + LN_EPS))); }
      cut();
#pragma unroll
      for (int h = 0; h < 3; ++h) m2[h] = 1.f / m2[h];
      *(f32x2*)(smem + L_INV + 64 + lane * 8) = (f32x2){m2[0], m2[1]};
      *(f32x2*)(smem + L_MNV + 64 + lane * 8) = (f32x2){mean[0] * m2[0], mean[1] * m2[1]};
      *(float*)(smem + L_INV + r * 4) = m2[2];
      *(float*)(smem + L_MNV + r * 4) = mean[2] * m2[2];
      bar();
      f32x4 ca = {0.f, 0.f, 0.f, 0.f};
      if (xi < 2) {
#pragma unroll
        for (int p = 0; p < 16; ++p) ca += cv[p];
        if (qkv) *(f32x4*)(smem + L_CSQ + lane * 16) = ca;
      }
      if (!qkv) {
        asm volatile("" : "+v"(ca));
        if (xi < 2) ldcs(16);
        bar(); bar(); bar(); bar();
        if (xi < 2) {
#pragma unroll
          for (int p = 0; p < 16; ++p) ca += cv[p];
          *(f32x4*)(smem + L_CS1 + lane * 16) = ca;
        }
      }
      wait_lgkm0();
    };
    constexpr int NLN = 8;
    auto bars = [&](int n) {
#pragma nounroll
      for (int u = 0; u < n; ++u) bar();
    };
    auto check = [&](const GLB_AS u64* f, int n) {
      const u64 v = ldf(f);
      bars(n);
      if (!__all(v == token())) poll(f);
    };
    auto guard = [&](const GLB_AS u64* f, int msk, int spp, int D, int K, auto&& step) {
      const u64 tok = token();
      int b = 0, lim = 0;
#pragma nounroll
      for (int spin = 0; spin < (1 << 22); ++spin) {
        const u64 v = ldf(f);
        if (b < lim) { bar(); step(b++); } else __builtin_amdgcn_s_sleep(1);
        if (b < lim) { bar(); step(b++); }
        const u32 m = (u32)__ballot(v == tok);
        const int n = __builtin_ctz(~(m & (m >> 16)));
        if (n >= 16) break;
        lim = min(K, (n & msk) * spp - D);
      }
#pragma nounroll
      while (b < K) { bar(); step(b++); }
    };
    {
      const u64 ep = __hip_atomic_load((const GLB_AS u64*)(ws + OFF_EP), __ATOMIC_RELAXED, __HIP_MEMORY_SCOPE_AGENT);
      u16 bv[4];
#pragma unroll
      for (int h = 0; h < 4; ++h) {
        const int t = min(lane + 64 * h, 223), f = t >> 4, rho = t & 15, wn = f / 7, j = f % 7;
        bv[h] = b1[tnA * 224 + (j == 6 ? 96 + 16 * wn + rho : (4 * wn + (j >> 1)) * 32 + 8 * (rho >> 2) + 4 * (j & 1) + (rho & 3))];
      }
      DMA(rsrc(b2, DM * 2), (LDS_AS void*)(smem + L_PF), 4, (lane & 15) * 128, 0, 0, 0);
      __builtin_amdgcn_s_sleep(XW);
      const __amdgpu_buffer_rsrc_t rx = rsrc(x, SEQ * DM * 2);
      bf16x8 xf[8][2];
#pragma unroll
      for (int i = 0; i < 8; ++i)
#pragma unroll
        for (int gl = 0; gl < 2; ++gl)
          xf[i][gl] = __builtin_bit_cast(bf16x8, __builtin_amdgcn_raw_buffer_load_b128(rx, r * 2048 + g * 16,
              (tmA * 128 + 16 * i) * 2048 + tnA * 128 + gl * 64, 0));
      bars(TOKB);
      auto probe = [&](int tn) { return OFF_FL + ((F_PR * 16 + (tmA ^ (4 * (idx & 1)))) * 16 + tn) * 128; };
      if (blockIdx.x < NVOTE)
        __builtin_amdgcn_raw_buffer_store_b32(0, rs, probe(tnA ^ 4) + (lane & 31) * 4, 0, 16);
#pragma unroll
      for (int h = 0; h < 4; ++h) ((float*)(smem + L_BS))[lane + 64 * h] = bf_lo(bv[h]);
      if (lane == 0) {
        *(LDS_AS u64*)(smem + L_CNT) = 0ull;
        *(LDS_AS u64*)(smem + L_TOK) = ep * 6364136223846793005ull + 1442695040888963407ull;
      }
      wait_lgkm0();
      u32 tlb;
      asm volatile("s_load_dword %0, %1, 0x0" : "=s"(tlb) : "s"(ws + OFF_EP + 128) : "memory");
      bars(XP - TOKB);
      const u32x4 one4 = {0x3f803f80u, 0x3f803f80u, 0x3f803f80u, 0x3f803f80u};
      const bf16x8 ones = __builtin_bit_cast(bf16x8, one4);
      const u32 svo = g == (r >> 2) ? OFF_ST + span(tmA, 0) + (tnA + (tnA & 8)) * 1024 + r * 8 : 0x80000000u;
#pragma unroll
      for (int i = 0; i < 8; ++i) {
        if (i == 4) bar();
        const f32x4 z = {0.f, 0.f, 0.f, 0.f};
        f32x4 s = __builtin_amdgcn_mfma_f32_16x16x32_bf16(ones, xf[i][0], z, 0, 0, 0);
        f32x4 q = __builtin_amdgcn_mfma_f32_16x16x32_bf16(xf[i][0], xf[i][0], z, 0, 0, 0);
        s = __builtin_amdgcn_mfma_f32_16x16x32_bf16(ones, xf[i][1], s, 0, 0, 0);
        q = __builtin_amdgcn_mfma_f32_16x16x32_bf16(xf[i][1], xf[i][1], q, 0, 0, 0);
        const float qq = (r & 2) ? ((r & 1) ? q[3] : q[2]) : ((r & 1) ? q[1] : q[0]);
        const float m = s[0] * (1.f / 64.f);
        const f32x2 o = {m, fmaxf(__builtin_fmaf(-s[0], m, qq), 0.f)};
        __builtin_amdgcn_raw_buffer_store_b64(__builtin_bit_cast(i32x2, o), rs, svo + i * 128, 0, 16);
      }
      bars(FLD);
      wait_vm<0>();
      if (lane == 0) raise(F_ST, tmA, tnA);
      bars(PRB);
      wait_lgkm0();
      asm volatile("" :: "s"(tlb));
      if (blockIdx.x < NVOTE) {
        u64 t0, t1;
        u32 v;
        asm volatile("s_memtime %0\n\ts_waitcnt lgkmcnt(0)\n\ts_load_dword %2, %3, 0x0\n\ts_waitcnt lgkmcnt(0)\n\t"
                     "s_memtime %1\n\ts_waitcnt lgkmcnt(0)" : "=&s"(t0), "=&s"(t1), "=&s"(v) : "s"(ws + probe(tnA)) : "memory");
        const u32 t = min((u32)t1 - (u32)t0, 4095u);
        if (lane == 0)
          __hip_atomic_store(flag(F_XC, 0, blockIdx.x), token() ^ t, __ATOMIC_RELAXED, __HIP_MEMORY_SCOPE_AGENT);
      }
      bars(POLB - (XP + 1 + FLD + PRB));
      static_assert(XP >= TOKB && POLB >= XP + 1 + FLD + PRB && POLB + 2 + NLN <= 31 && POLB + 2 > CQP + 5 + FLD, "schedule");
    }
#pragma nounroll
    for (int pair = 0; pair < 2; ++pair) {
      if (pair) {
        const __amdgpu_buffer_rsrc_t rq = rsrc(wqkv, DM * 3 * DM * 2);
        const int m = idx * 48 + (lane < 48 ? lane : 47), k = (m * 5462) >> 16, p = m - 12 * k;
        auto warm = [&](int j) {
          DMA(rq, (LDS_AS void*)(smem + L_PF), 4, k * 6144 + (p >> 2) * 2048 + (p & 3) * 128,
              (xcd & 3) * 512 + ((j + (rotA >> 1)) & 7) * (128 * 6144), 0, 0);
        };
        warm(0); warm(1); warm(2);
        guard(flag(4, (g & 1) ? tmh : tmA, (rotA + r) & 15), 14, 2, DW, POLB, [&](int b) { if ((b & 1) && b < 10) warm(3 + (b >> 1)); });
        if (blockIdx.x == 0 && lane == 0)
          __hip_atomic_store((GLB_AS u64*)(ws + OFF_EP), token(), __ATOMIC_RELAXED, __HIP_MEMORY_SCOPE_AGENT);
      }
      {
        const GLB_AS u64* f = g < 2 ? flag(pair ? 4 + F_2ND : F_ST, (g & 1) ? tmh : tmA, r)
                            : pair ? flag(4, tmB, tnB ^ 1) : g == 2 ? flag(F_CQ, r, tnA) : flag(F_XC, 0, r);
        const u64 lim = (pair == 0 && g == 3) ? 4096 : 1;
        u64 v = ldf(f);
        bars(2);
        const u64 tok = token();
#pragma nounroll
        for (int spin = 0; spin < (1 << 22); ++spin) {
          if (__all((v ^ tok) < lim)) break;
          __builtin_amdgcn_s_sleep(1);
          v = ldf(f);
        }
        if (pair == 0) {
          const u32 t = (u32)(v ^ tok);
          const u64 slower = __ballot(t > (u32)__builtin_amdgcn_mov_dpp(t, 0x128, 0xf, 0xf, false));
          *(volatile LDS_AS u32*)(smem + L_C13) = __builtin_popcount((u32)(slower >> 48) & 255) > 4 ? 8192 : 0;
        }
      }
      ln_phase(true, 2 * pair);
      side();
      bars(33 - CLB - (POLB + 2 + NLN));
#pragma unroll
      for (int e = 0; e < 16; ++e)
        st16(((u32x4){~0u, ~0u, ~0u, ~0u}), xbox(pair, 0) + (e + (e & 8)) * 1024 + lane * 16);
      bars(CLB);
      wait_vm<0>();
      guard(flag(4 * pair + 1, tmB, (rotB + r) & 15), 15, 1, DN, 16, [](int) {});
      guard(flag(4 * pair + 2, tmA, (rotA + r) & 15), 15, 2, DW, KW1, [](int) {});
      check(flag(4 * pair + 2 + F_2ND, tmA, r), 2);
      ln_phase(false, 2 * pair + 1);
      bars(33 - (KW1 + 2 + NLN + 4));
      guard(flag(4 * pair + 3, tmB, 8 * (tnB & 1) + ((r ^ (rotA >> 1)) & 7)), 15, 1, 3, 5 - CB, [](int) {});
      check(flag(4 * pair + 3 + F_2ND, tmB, 8 * (tnB & 1) + (r & 7)), CB);
      bars(29 - 5);
    }
  }
#endif
}

extern "C" void kernel_launch(void* const* d_in, const int* in_sizes, int n_in, void* d_out, int out_size,
                              void* d_ws, size_t ws_size, hipStream_t stream) {
  const u16* x0 = (const u16*)d_in[0];
  const u16* wqkv = (const u16*)d_in[1];
  const u16* wo = (const u16*)d_in[2];
  const u16* w1 = (const u16*)d_in[4];
  const u16* b1 = (const u16*)d_in[5];
  const u16* w2 = (const u16*)d_in[6];
  const u16* b2 = (const u16*)d_in[7];
  (void)hipFuncSetAttribute((const void*)mega_k, hipFuncAttributeMaxDynamicSharedMemorySize, LDS_BYTES);
  mega_k<<<256, 512, LDS_BYTES, stream>>>((char*)d_ws, x0, wqkv, wo, w1, b1, w2, b2, (u16*)d_out);
}
```

```cpp
#include <hip/hip_runtime.h>
#include <stdint.h>
#include <type_traits>
#include <utility>

typedef unsigned short u16;
typedef unsigned int u32;
typedef unsigned long long u64;
typedef __bf16 bf16x8 __attribute__((ext_vector_type(8)));
typedef __bf16 bf16x2 __attribute__((ext_vector_type(2)));
typedef short s16x4 __attribute__((ext_vector_type(4)));
typedef float f32x4 __attribute__((ext_vector_type(4)));
typedef float f32x2 __attribute__((ext_vector_type(2)));
typedef u32 u32x4 __attribute__((ext_vector_type(4)));
typedef u32 u32x2 __attribute__((ext_vector_type(2)));
typedef int i32x4 __attribute__((ext_vector_type(4)));
typedef int i32x2 __attribute__((ext_vector_type(2)));
#define LDS_AS __attribute__((address_space(3)))
#define GLB_AS __attribute__((address_space(1)))

constexpr int SEQ = 2048, DM = 1024, FF = 3584;
constexpr float LN_EPS = 1e-5f;

#define DN 4
#define DW 5
#define UAUX 2
#define PFW0 30
#define PF0 4
#define PFS 2
#define PFJ 2
#define CQP 7
#define HL 16
#define FLD 3
#define XW 30
#define TOKB 3
#define XP 7
#define POLB 18
#define G0 27
#define G1 12
#define H2 4
#define PUBJ 2
#define CB 1
#define KW1 11
#define CLB 1
#define SND 12
#define RST 9
#define XB2 1
#define XA2 1
#define XBO 1
#define CW 1
#define PRB 1
#ifndef NVOTE
#define NVOTE 16
#endif
constexpr u32 MB = 1u << 20;
constexpr u32 QS = 5120;
constexpr u32 XS = 8 * MB, HS = 32 * MB;
constexpr u32 OFF_WO = 0;
constexpr u32 OFF_W1 = 3 * MB;
constexpr u32 OFF_W2 = 13 * MB;
constexpr u32 OFF_CSQ = 22 * MB;
constexpr u32 OFF_CS1 = OFF_CSQ + 262144;
constexpr u32 OFF_ST = 24 * MB;
constexpr u32 OFF_FL = OFF_ST + MB;
constexpr u32 OFF_EP = OFF_FL + 26 * 32768;
constexpr u32 OFF_DBG = 26 * MB;
constexpr u32 OFF_XC = 28 * MB;
constexpr u32 OFF_X = 28 * MB;
constexpr u32 OFF_O = OFF_X + 4 * XS;
constexpr u32 OFF_H = OFF_O + 2 * XS;
constexpr u32 WS_USED = OFF_H + 2 * HS, WS_ALIGN = 4 * MB;
#define HAS_ALIGN
constexpr int F_ST = 0, F_CQ = 8, F_WO = 9, F_W1 = 10, F_W2 = 11, F_2ND = 10, F_XC = 18, F_PR = 19;
__device__ __forceinline__ u32 qoff(u32 e, u32 slot) {
  const u32 n = 8 * e + (slot >> 3);
  return ((n + (((n + 4) * 37) >> 8)) << 7) + (slot & 7) * 16;
}
__device__ __forceinline__ u32 hpos(u32 t, u32 j) {
  return (j & 3) == 0 ? 2 * (t & 7) + (j >> 2) : 16 + 5 * (t & 7) + j - 1 - (j >> 2);
}
__device__ __forceinline__ u32 aq(u32 kg, u32 half) { return kg * 32768 + (kg & 1) * 3072 + half * 16384; }
__device__ __forceinline__ u32 ppair(u32 tm) { return (tm & 3) + (tm >> 3) * 4; }
__device__ __forceinline__ u32 xpan(u32 tm, u32 c13) { return ppair(tm) * MB + (((((tm >> 2) ^ (tm >> 1)) & 1) << 13) ^ c13); }
__device__ __forceinline__ u32 hpan(u32 tm, u32 kh, u32 c13) {
  return ppair(tm) * (4 * MB) + (((kh ^ tm) & 1) << 21) + (((((tm >> 2) ^ kh ^ tm) & 1) << 13) ^ c13);
}
__device__ __forceinline__ u32 span(u32 tm, u32 c13) { return ppair(tm) * 32768 + (((tm & 4) << 11) ^ c13); }

constexpr int SLOT = 24576, NRING = 6, RING = SLOT * NRING;
constexpr int L_INV = RING, L_MNV = L_INV + 576, L_CNT = L_MNV + 576, L_TOK = L_CNT + 8, L_C13 = L_TOK + 8;
constexpr int L_CSQ = RING + 1280, L_CS1 = L_CSQ + 1024, L_BS = L_CS1 + 1024;
constexpr int L_XC = L_BS + 1024;
constexpr int L_IM = L_XC + 6144;
constexpr int L_B2 = L_IM + 4096, L_PF = L_B2 + 256;
constexpr int LDS_BYTES = L_PF + 256;

__device__ __forceinline__ float bf_lo(u32 w) { return __uint_as_float(w << 16); }
__device__ __forceinline__ float bf_hi(u32 w) { return __uint_as_float(w & 0xffff0000u); }
__device__ __forceinline__ u32 pack2(float lo, float hi) {
  f32x2 v = {lo, hi};
  bf16x2 r = __builtin_convertvector(v, bf16x2);
  return __builtin_bit_cast(u32, r);
}
__device__ __forceinline__ float bfr(float x) { return bf_lo(pack2(x, 0.f) & 0xffffu); }
__device__ __forceinline__ u32 gelu_tanh2(f32x2 x) {
  const float c0 = -2.3022081983f, c1 = -0.1029432396f;
  const f32x2 p = x * x * c1 + c0;
  const f32x2 a = x * p;
  const f32x2 d = (f32x2){__builtin_amdgcn_exp2f(a[0]), __builtin_amdgcn_exp2f(a[1])} + 1.f;
  const f32x2 g = x * (f32x2){__builtin_amdgcn_rcpf(d[0]), __builtin_amdgcn_rcpf(d[1])};
  return pack2(g[0], g[1]);
}
template <int N_> __device__ __forceinline__ void wait_vm() { asm volatile("s_waitcnt vmcnt(%0)" ::"n"(N_) : "memory"); }
__device__ __forceinline__ void wait_lgkm0() { asm volatile("s_waitcnt lgkmcnt(0)" ::: "memory"); }
__device__ __forceinline__ void bar() {
  __builtin_amdgcn_sched_barrier(0);
  asm volatile("s_barrier" ::: "memory");
  __builtin_amdgcn_sched_barrier(0);
}
template <int V> using IC = std::integral_constant<int, V>;
template <typename F, int... I>
__device__ __forceinline__ void for_seq(F&& f, std::integer_sequence<int, I...>) { (f(IC<I>{}), ...); }
template <int N, typename F> __device__ __forceinline__ void for_n(F&& f) { for_seq(f, std::make_integer_sequence<int, N>{}); }

__device__ __forceinline__ float xsum(float x) {
#if defined(__HIP_DEVICE_COMPILE__)
  auto a = __builtin_amdgcn_permlane16_swap(__float_as_uint(x), __float_as_uint(x), false, false);
  x = __uint_as_float(a[0]) + __uint_as_float(a[1]);
  auto b = __builtin_amdgcn_permlane32_swap(__float_as_uint(x), __float_as_uint(x), false, false);
  x = __uint_as_float(b[0]) + __uint_as_float(b[1]);
#endif
  return x;
}
__device__ __forceinline__ float xmax(float x) {
#if defined(__HIP_DEVICE_COMPILE__)
  auto a = __builtin_amdgcn_permlane16_swap(__float_as_uint(x), __float_as_uint(x), false, false);
  x = fmaxf(__uint_as_float(a[0]), __uint_as_float(a[1]));
  auto b = __builtin_amdgcn_permlane32_swap(__float_as_uint(x), __float_as_uint(x), false, false);
  x = fmaxf(__uint_as_float(b[0]), __uint_as_float(b[1]));
#endif
  return x;
}
__device__ __forceinline__ f32x2 row_part(const u32x4& c0, const u32x4& c1) {
  float v[16];
#pragma unroll
  for (int k = 0; k < 4; ++k) {
    v[2 * k] = bf_lo(c0[k]); v[2 * k + 1] = bf_hi(c0[k]);
    v[8 + 2 * k] = bf_lo(c1[k]); v[8 + 2 * k + 1] = bf_hi(c1[k]);
  }
  float s = 0.f;
#pragma unroll
  for (int k = 0; k < 16; ++k) s += v[k];
  const float m = xsum(s) * (1.f / 64.f);
  float q = 0.f;
#pragma unroll
  for (int k = 0; k < 16; ++k) { const float d = v[k] - m; q += d * d; }
  return (f32x2){m, xsum(q)};
}

#define st16(v, off) __builtin_amdgcn_raw_buffer_store_b128(__builtin_bit_cast(i32x4, v), rs, (off), 0, 16)
__device__ __forceinline__ float sum8(const u32x4& f) {
  float s = bf_lo(f[0]) + bf_hi(f[0]);
#pragma unroll
  for (int k = 1; k < 4; ++k) { s += bf_lo(f[k]); s += bf_hi(f[k]); }
  return s;
}
__device__ __forceinline__ float fold32(float a, float b) {
#if defined(__HIP_DEVICE_COMPILE__)
  const auto x = __builtin_amdgcn_permlane32_swap(__float_as_uint(a), __float_as_uint(b), false, false);
  a = __uint_as_float(x[0]) + __uint_as_float(x[1]);
#endif
  return a;
}
__device__ __forceinline__ float fold16(float a, float b) {
#if defined(__HIP_DEVICE_COMPILE__)
  const auto x = __builtin_amdgcn_permlane16_swap(__float_as_uint(a), __float_as_uint(b), false, false);
  a = __uint_as_float(x[0]) + __uint_as_float(x[1]);
#endif
  return a;
}

__device__ __forceinline__ int fresh_lane(int tid) { int l = tid & 63; asm volatile("" : "+v"(l)); return l; }
__device__ __forceinline__ f32x4 zero4() {
  f32x4 z = {0.f, 0.f, 0.f, 0.f};
  asm volatile("" : "+v"(z));
  return z;
}
template <int BIG = 0> __device__ __forceinline__ void next_slot(int& so) {
  if constexpr (BIG) so = (so + 32768) & 131071; else { so += SLOT; if (so == RING) so = 0; }
}

#define DMA __builtin_amdgcn_raw_ptr_buffer_load_lds
#define SB() __builtin_amdgcn_sched_barrier(0)
__device__ __forceinline__ void mfma(f32x4& c, const bf16x8& a, const bf16x8& b) {
  asm volatile("v_mfma_f32_16x16x32_bf16 %0, %1, %2, %0" : "+v"(c) : "v"(a), "v"(b));
}
template <int OFF> __device__ __forceinline__ void dsrd(bf16x8& d, u32 addr) {
  asm volatile("ds_read_b128 %0, %1 offset:%2" : "=v"(d) : "v"(addr), "n"(OFF));
}
template <int OFF> __device__ __forceinline__ void dstr(bf16x8& d, u32 addr) {
  u32x2 lo, hi;
  asm volatile("ds_read_b64_tr_b16 %0, %2 offset:%3\n\tds_read_b64_tr_b16 %1, %2 offset:%4"
               : "=&v"(lo), "=&v"(hi) : "v"(addr), "n"(OFF), "n"(OFF + 512));
  const u32x4 v = {lo[0], lo[1], hi[0], hi[1]};
  d = __builtin_bit_cast(bf16x8, v);
}
template <int OFF> __device__ __forceinline__ void dstr1(u32x2& d, u32 addr) {
  asm volatile("ds_read_b64_tr_b16 %0, %1 offset:%2" : "=v"(d) : "v"(addr), "n"(OFF));
}
__device__ __forceinline__ void pin1(u32& a) { asm volatile("" : "+v"(a)); }
__device__ __forceinline__ void pin2(u32& a, u32& b) { asm volatile("" : "+v"(a), "+v"(b)); }
template <int NSL, int NM, int NR, int ENDB, class AD, class RD, class MM, class HK, class TL>
__device__ __forceinline__ void kloop(int& so, AD&& ad, RD&& rd, MM&& mm, HK&& hook, TL&& tail) {
  constexpr int BIG = NM == 32;
  ad(so, IC<0>{});
  bar();
  for_n<NR>([&](auto Q) { rd(IC<0>{}, Q); });
  next_slot<BIG>(so);
  ad(so, IC<1>{});
  hook(-1);
  auto body = [&](auto S) {
    constexpr int s = decltype(S)::value;
    wait_lgkm0();
    bar();
    for_n<NM>([&](auto M) {
      constexpr int m = decltype(M)::value;
      mm(S, M);
      if constexpr (m < NR) rd(IC<1 - s>{}, IC<m>{});
      if constexpr (m == NR) { next_slot<BIG>(so); asm volatile("" : "+s"(so)); }
      if constexpr (m == NR + 1) ad(so, S);
      SB();
    });
  };
#pragma nounroll
  for (int u = NSL / 2 - 1; u > 0; --u) {
    body(IC<0>{});
    body(IC<1>{});
    hook(NSL / 2 - 1 - u);
  }
  body(IC<0>{});
  wait_lgkm0();
  if constexpr (ENDB) { bar(); so = 0; }
  SB();
  for_n<NM>([&](auto M) { mm(IC<1>{}, M); SB(); tail(M); SB(); });
  asm volatile("s_nop 15");
  SB();
}

template <int NM, int NR, int ENDB, class RD, class MM, class HK>
__device__ __forceinline__ void kloop6(RD&& rd, MM&& mm, HK&& hook) {
  bar();
  for_n<NR>([&](auto Q) { rd(IC<0>{}, Q); });
  hook(-1);
  auto body = [&](auto S) {
    constexpr int s = decltype(S)::value;
    wait_lgkm0();
    bar();
    for_n<NM>([&](auto M) {
      constexpr int m = decltype(M)::value;
      mm(IC<s & 1>{}, M);
      if constexpr (m < NR) rd(IC<(s + 1) % NRING>{}, IC<m>{});
      SB();
    });
  };
#pragma nounroll
  for (int u = 5; u > 0; --u) for_n<NRING>(body);
  body(IC<0>{});
  wait_lgkm0();
  if constexpr (ENDB) bar();
  SB();
  for_n<NM>([&](auto M) { mm(IC<1>{}, M); SB(); });
  asm volatile("s_nop 15");
  SB();
}

template <int NSL, int P, int D, int X, class IS, class BR> __device__ __forceinline__ void stream(IS&& issue, BR&& bar) {
#pragma nounroll
  for (int u = 0; u < D + X; ++u) issue(u);
  if constexpr (X) { wait_vm<(D * P < 63 ? D * P : 63)>(); bar(); }
#pragma nounroll
  for (int u = X; u < NSL - D; ++u) {
    wait_vm<((D - 1) * P < 63 ? (D - 1) * P : 63)>();
    bar();
    issue(u + D);
  }
  for_n<D>([&](auto M) { constexpr int n = (D - 1 - decltype(M)::value) * P; wait_vm<(n < 63 ? n : 63)>(); bar(); });
}

#define STAMP(w, i)

constexpr int w1_frag(int i, int e) {
  const int gam = 2 * (i - 2) + (e >> 1), gl = gam % 7;
  return (gam / 7) * 16 + (gl == 3 ? 6 + 7 * (e & 1) : (gl < 3 ? gl * 2 : 7 + (gl - 4) * 2) + (e & 1));
}
constexpr int between(int s0, int s1) {
  int n = 0;
  for (int s = s0 + 1; s < s1; ++s) {
    if (s >= PF0 && (s - PF0) % PFS == 0 && (s - PF0) / PFS < 8 - PFJ) n += 2;
    if (s >= CQP && s < CQP + 6 && (s - CQP) % 2 == 1) n += 1;
    if (s >= HL && s < HL + 4 && (s - HL) % 2 == 1) n += 4;
  }
  return n < 63 ? n : 63;
}

__device__ __forceinline__ void warm_code(u32 n, u32 lds) {
#if defined(__HIP_DEVICE_COMPILE__)
  u64 pc;
  u32 back, len;
  asm volatile("s_getpc_b64 %0\n.Lcw%=:\n\ts_mov_b32 %1, .Lcw%=-mega_k\n\ts_mov_b32 %2, .Lcode_end-mega_k" : "=s"(pc), "=s"(back), "=s"(len));
  pc -= back;
  const u32x4 rc = {(u32)pc, (u32)(pc >> 32), len, 0x00020000u};
  asm volatile("s_mov_b32 %0, m0\n\ts_mov_b32 m0, %1\n\ts_nop 0\n\tbuffer_load_dword %2, %3, 0 offen lds\n\ts_mov_b32 m0, %0"
               : "=&s"(back) : "s"(lds), "v"(n * 128), "s"(rc));
#endif
}

extern "C" __global__ __launch_bounds__(512, 2) void mega_k(char* __restrict__ ws0, const u16* __restrict__ x,
                                                 const u16* __restrict__ wqkv, const u16* __restrict__ wo,
                                                 const u16* __restrict__ w1, const u16* __restrict__ b1,
                                                 const u16* __restrict__ w2, const u16* __restrict__ b2,
                                                 u16* __restrict__ out) {
#if defined(__HIP_DEVICE_COMPILE__)
  extern __shared__ __attribute__((aligned(16))) char smem[];
  char* const ws = ws0 + (-(u32)(uintptr_t)ws0 & (WS_ALIGN - 1));
  const int tid = threadIdx.x;
  const int wid = __builtin_amdgcn_readfirstlane(tid >> 6);
#define FRESH_LANE() const int lane = fresh_lane(tid), r = lane & 15, g = lane >> 4; (void)r; (void)g
  const int xcd = blockIdx.x & 7, idx = blockIdx.x >> 3;
  asm volatile(".subsection 1\n.Lcode_end:\n\t.subsection 0");
  if (CW & 1) warm_code(((tid & 63) * 8 + wid) * 32 + idx, L_PF);
  const int tmA = (idx >> 4) * 8 + (xcd >> 2) * 4 + ((idx >> 2) & 3), tnA = (xcd & 3) * 4 + (idx & 3);
  const int tmB = (idx >> 4) * 8 + (xcd >> 1) * 2 + ((idx >> 3) & 1), tnB = (idx & 7) * 2 + (xcd & 1);
  const int rotA = 8 - 8 * (xcd & 1), rotB = 4 * ((xcd + 1) & 3), rot0 = xcd;
  const __amdgpu_buffer_rsrc_t rs = __builtin_amdgcn_make_buffer_rsrc((void*)ws, 0, WS_USED, 0x00020000);
  u32 c13 = 0;
  auto side = [&]() { c13 = __builtin_amdgcn_readfirstlane(*(const volatile LDS_AS u32*)(smem + L_C13)); };
  auto flag = [&](int set, int tm, int tn) { return (GLB_AS u64*)(ws + OFF_FL + ((((set * 16 + tm) * 16 + tn) * 128) ^ c13)); };
  auto xbox = [&](int pair, int o) { return OFF_XC + pair * (4 * MB) + ((idx * 4 + (xcd & 3)) ^ o) * 32768 + (((((tmB >> 2) ^ (idx >> 4)) & 1) << 13) ^ c13); };
  auto rsrc = [&](const u16* p, u32 bytes) { return __builtin_amdgcn_make_buffer_rsrc((void*)p, 0, bytes, 0x00020000); };
  auto token = [&]() {
    const u32x2 tk = *(const volatile LDS_AS u32x2*)(smem + L_TOK);
    return (u64)(u32)__builtin_amdgcn_readfirstlane(tk[0]) | ((u64)(u32)__builtin_amdgcn_readfirstlane(tk[1]) << 32);
  };
  auto ldf = [&](const GLB_AS u64* f) { return __hip_atomic_load(f, __ATOMIC_RELAXED, __HIP_MEMORY_SCOPE_AGENT); };
  auto poll = [&](const GLB_AS u64* f) {
    const u64 tok = token();
#pragma nounroll
    for (int spin = 0; spin < (1 << 22); ++spin) {
      if (__all(ldf(f) == tok)) break;
      __builtin_amdgcn_s_sleep(1);
    }
  };
  auto raise = [&](int set, int tm, int tn) {
    __hip_atomic_store(flag(set, tm, tn), token(), __ATOMIC_RELAXED, __HIP_MEMORY_SCOPE_AGENT);
  };
  int so = 0;

  if (wid < 4) {
    auto publish = [&](auto N, int ph, int tm, int tn, int st) {
      FRESH_LANE();
      SB();
      wait_vm<decltype(N)::value>();
      u32 old = 0;
      if (lane == 0) old = __hip_atomic_fetch_add((LDS_AS u32*)(smem + L_CNT) + st, 1u, __ATOMIC_RELAXED, __HIP_MEMORY_SCOPE_WORKGROUP);
      old = __builtin_amdgcn_readfirstlane(old);
      if ((old & 3) == 3 && lane == 0) raise(ph + 1 + F_2ND * st, tm, tn);
      SB();
    };
    const float* t_inv = (const float*)(smem + L_INV);
    const float* t_mnv = (const float*)(smem + L_MNV);
    const float* t_cs = (const float*)(smem + L_CS1);
    const float* t_bs = (const float*)(smem + L_BS);
    const int wmi = wid >> 1, wni = wid & 1;

    const int w = 2 * (tnB & 1) + wmi, tn = (tnB & 14) + wni;
    u32x4 c[2][2];
    auto start = [&](f32x4 (&acc)[4][4], const u32x4 (&bb)[2], bool bias) {
#pragma unroll
      for (int i = 0; i < 2; ++i)
#pragma unroll
        for (int j = 0; j < 4; ++j) {
          const u32 c0 = c[i][j >> 1][2 * (j & 1)], c1 = c[i][j >> 1][2 * (j & 1) + 1];
          const u32 b0 = bb[j >> 1][2 * (j & 1)], b1 = bb[j >> 1][2 * (j & 1) + 1];
          f32x4 a = {bf_lo(c0), bf_hi(c0), bf_lo(c1), bf_hi(c1)};
          if (bias) a += (f32x4){bf_lo(b0), bf_hi(b0), bf_lo(b1), bf_hi(b1)};
          asm volatile("" : "+v"(a));
          acc[i][j] = a;
        }
    };
    auto finish = [&](const f32x4 (&acc)[4][4], int xo  , int ph) {
      FRESH_LANE();
#pragma unroll
      for (int i = 0; i < 2; ++i)
#pragma unroll
        for (int gl = 0; gl < 2; ++gl) {
#pragma unroll
          for (int k = 0; k < 4; ++k) {
            const f32x4& a = acc[i][2 * gl + (k >> 1)];
            c[i][gl][k] = pack2(a[2 * (k & 1)], a[2 * (k & 1) + 1]);
          }
          if (xo < 4) st16(c[i][gl], OFF_X + xo * XS + xpan(tmB, c13) + tn * 65536 + aq(gl, w >> 1) + qoff((w & 1) * 2 + i, lane));
          else __builtin_amdgcn_raw_buffer_store_b128(__builtin_bit_cast(i32x4, c[i][gl]), rsrc(out, SEQ * DM * 2),
                   (tmB * 128 + (2 * w + i) * 16 + r) * 2048 + tn * 128 + gl * 64 + g * 16, 0, 16);
        }
      if (xo < 4) {
        SB();
        const f32x2 s0 = row_part(c[0][0], c[0][1]), s1 = row_part(c[1][0], c[1][1]);
        __builtin_amdgcn_raw_buffer_store_b64(__builtin_bit_cast(i32x2, (g & 1) ? s1 : s0), rs,
            lane < 32 ? OFF_ST + xo * 262144 + span(tmB, c13) + (tn + (tn & 8)) * 1024 + (w * 32 + lane) * 8 : 0x80000000u, 0, 16);
        publish(IC<1>{}, ph, tmB, tnB, 0);
        publish(IC<0>{}, ph, tmB, tnB, 1);
      }
      STAMP(0, 4 * ph + 3);
    };

#pragma nounroll
    for (int pair = 0; pair < 2; ++pair) {
      f32x4 aco[4][4];
      {
        f32x4 acc[4][6], acch[2];
        {
        FRESH_LANE();
        STAMP(0, 16 * pair);
        const u32 lB = 9216 + (8 * g + (r >> 2)) * 128 + (((2 * wni) ^ ((r >> 3) & 1) ^ ((g & 1) << 1)) * 32) + (r & 3) * 8;
        const u32 lA = r * 128 + ((g ^ (r >> 1)) * 16);
        auto loop = [&](auto WM, auto RAW) {
          constexpr int wm = decltype(WM)::value, raw = decltype(RAW)::value;
#pragma unroll
          for (int i = 0; i < 4; ++i)
#pragma unroll
            for (int j = 0; j < 6; ++j) acc[i][j] = zero4();
          acch[0] = zero4(); acch[1] = zero4();
          bf16x8 fA[2][4], fH[2];
          u32x2 hB[2][12];
          u32 pA[3][2], pB[3][2];
#pragma unroll
          for (int k = 0; k < 3; ++k) {
            pA[k][0] = (raw ? lA : lane * 16) + 2 * k * SLOT; pA[k][1] = (lA ^ 64) + 2 * k * SLOT;
            pB[k][0] = lB + 2 * k * SLOT; pB[k][1] = (lB ^ 32) + 2 * k * SLOT;
            pin1(pA[k][0]); pin2(pB[k][0], pB[k][1]);
            if (raw) pin1(pA[k][1]);
          }
          auto fB = [&](int set, int j) {
            return __builtin_bit_cast(bf16x8, (u32x4){hB[set][2 * j][0], hB[set][2 * j][1], hB[set][2 * j + 1][0], hB[set][2 * j + 1][1]});
          };
          kloop6<26, 17, 0>(
            [&](auto SL, auto Q) {
              constexpr int set = decltype(SL)::value & 1, k = decltype(SL)::value >> 1, q = decltype(Q)::value, h = q - 5;
              if constexpr (q < 4) dsrd<raw ? wm * SLOT + q * 2048 : set * SLOT + wm * 4096 + q * 1024>(fA[set][q], pA[k][raw & set]);
              else if constexpr (q == 4) dsrd<raw ? SLOT + 21504 : set * SLOT + 8192>(fH[set], pA[k][raw & set]);
              else dstr1<set * SLOT + (h >> 2) * 4096 + (h & 1) * 512>(hB[set][h], pB[k][(h >> 1) & 1]);
            },
            [&](auto SET, auto M) {
              constexpr int set = decltype(SET)::value, m = decltype(M)::value, j = m >> 2, i = m & 3;
              if constexpr (m < 16) mfma(acc[i][j], fB(set, j), fA[set][i]);
              else if constexpr (m < 24) mfma(acc[i][j], fA[set][i], fB(set, j));
              else if constexpr (wm == 0) mfma(acch[i], fB(set, 2 + i), fH[set]);
              else mfma(acch[i], fH[set], fB(set, 4 + i));
            }, [&](int t) { if (t < 0) STAMP(0, 16 * pair + 1); });
        };
        if (pair == 0) { if (wmi == 0) loop(IC<0>{}, IC<1>{}); else loop(IC<1>{}, IC<1>{}); }
        else { if (wmi == 0) loop(IC<0>{}, IC<0>{}); else loop(IC<1>{}, IC<0>{}); }
        }
        FRESH_LANE();
        STAMP(0, 16 * pair + 2);
        side();
        if (pair == 0) {
#pragma unroll
          for (int i = 0; i < 2; ++i)
#pragma unroll
            for (int gl = 0; gl < 2; ++gl)
              c[i][gl] = __builtin_bit_cast(u32x4, __builtin_amdgcn_raw_buffer_load_b128(rsrc(x, SEQ * DM * 2),
                  (16 * i + r) * 2048 + g * 16, (tmB * 128 + w * 32) * 2048 + tn * 128 + gl * 64, 0));
        }
        const float* csw = (const float*)(smem + L_CSQ) + wni * 96;
        auto fix_qk = [&](const f32x4& a0, const f32x4& a1, int trow, int f) {
          const float iv = t_inv[trow + r], mv = t_mnv[trow + r];
          const f32x4 c0 = *(const f32x4*)(csw + f * 16 + 4 * g), c1 = *(const f32x4*)(csw + f * 16 + 16 + 4 * g);
          const f32x4 y0 = a0 * iv - c0 * mv, y1 = a1 * iv - c1 * mv;
          return (u32x4){pack2(y0[0], y0[1]), pack2(y0[2], y0[3]), pack2(y1[0], y1[1]), pack2(y1[2], y1[3])};
        };
        auto fix_v = [&](const f32x4& a0, const f32x4& a1, int trow) {
          const f32x4 iv = *(const f32x4*)(t_inv + trow + 4 * g), mv = *(const f32x4*)(t_mnv + trow + 4 * g);
          const float c0 = csw[64 + r], c1 = csw[80 + r];
          const f32x4 y0 = a0 * iv - mv * c0, y1 = a1 * iv - mv * c1;
          return (u32x4){pack2(y0[0], y0[1]), pack2(y0[2], y0[3]), pack2(y1[0], y1[1]), pack2(y1[2], y1[3])};
        };
        u32x4 Qf[4], Kf[5], Vf[5];
        char* xc = smem + L_XC + wni * 3072 + lane * 16;
        const int row0 = 16 + wmi * 64;
        Kf[4] = fix_qk(acc[3][2], acc[3][3], row0 + 48, 2);
        Vf[4] = fix_v(acc[3][4], acc[3][5], row0 + 48);
        if (wmi == 0) {
          *(u32x4*)xc = Kf[4];
          *(u32x4*)(xc + 1024) = Vf[4];
          Kf[0] = fix_qk(acch[0], acch[1], 0, 2);
        } else {
          Kf[0] = fix_v(acch[0], acch[1], 0);
          *(u32x4*)(xc + 2048) = Kf[0];
        }
#pragma unroll
        for (int i = 0; i < 3; ++i) {
          Kf[i + 1] = fix_qk(acc[i][2], acc[i][3], row0 + 16 * i, 2);
          Vf[i + 1] = fix_v(acc[i][4], acc[i][5], row0 + 16 * i);
        }
        wait_lgkm0();
        bar();
        if (wmi == 1) Kf[0] = *(const u32x4*)xc;
        Vf[0] = *(const u32x4*)(xc + (wmi == 0 ? 2048 : 1024));
#pragma unroll
        for (int i = 0; i < 4; ++i) Qf[i] = fix_qk(acc[i][0], acc[i][1], row0 + 16 * i, 0);
        const bool par = r & 1;
        const float m0 = par ? 0.f : 1.f, m1 = par ? 1.f : 0.f;
        const bool own0 = 4 * g + (r & 1) <= r, own1 = 4 * g + (r & 1) + 2 <= r;
        const bool first = (tmA == 0) && (wmi == 0);
#pragma unroll
        for (int ii = 0; ii < 4; ++ii) {
          const int i = 3 - ii;
          const f32x4 z = {0.f, 0.f, 0.f, 0.f};
          const f32x4 S0 = __builtin_amdgcn_mfma_f32_16x16x32_bf16(__builtin_bit_cast(bf16x8, Kf[i]), __builtin_bit_cast(bf16x8, Qf[i]), z, 0, 0, 0);
          const f32x4 S1 = __builtin_amdgcn_mfma_f32_16x16x32_bf16(__builtin_bit_cast(bf16x8, Kf[i + 1]), __builtin_bit_cast(bf16x8, Qf[i]), z, 0, 0, 0);
          float l0 = own0 ? (par ? S1[1] : S1[0]) : (par ? S0[1] : S0[0]);
          float l1 = own1 ? (par ? S1[3] : S1[2]) : (par ? S0[3] : S0[2]);
          const float c = 0.17677669529663687f * 1.4426950408889634f;
          l0 *= c; l1 *= c;
          if (i == 0 && first) {
            if (!own0) l0 = -1.4426950e9f;
            if (!own1) l1 = -1.4426950e9f;
          }
          const float mx = xmax(fmaxf(l0, l1));
          const float p0 = __builtin_amdgcn_exp2f(l0 - mx), p1 = __builtin_amdgcn_exp2f(l1 - mx);
          const float rsum = __builtin_amdgcn_rcpf(xsum(p0 + p1));
          const float q0 = p0 * rsum, q1 = p1 * rsum;
          const u32 w0 = pack2(q0 * m0, q0 * m1), w1 = pack2(q1 * m0, q1 * m1);
          const u32x4 P = {own0 ? 0u : w0, own1 ? 0u : w1, own0 ? w0 : 0u, own1 ? w1 : 0u};
          const u32x4 V0 = {Vf[i][0], Vf[i][1], Vf[i + 1][0], Vf[i + 1][1]};
          const u32x4 V1 = {Vf[i][2], Vf[i][3], Vf[i + 1][2], Vf[i + 1][3]};
          const f32x4 O0 = __builtin_amdgcn_mfma_f32_16x16x32_bf16(__builtin_bit_cast(bf16x8, V0), __builtin_bit_cast(bf16x8, P), z, 0, 0, 0);
          const f32x4 O1 = __builtin_amdgcn_mfma_f32_16x16x32_bf16(__builtin_bit_cast(bf16x8, V1), __builtin_bit_cast(bf16x8, P), z, 0, 0, 0);
          const u32x4 o = {pack2(O0[0], O0[1]), pack2(O0[2], O0[3]), pack2(O1[0], O1[1]), pack2(O1[2], O1[3])};
          st16(o, OFF_O + pair * XS + xpan(tmA, c13) + tnA * 65536 + aq(wni, wmi) + qoff(i, lane));
        }
        SB();
        start(aco, c[0], false);
        publish(IC<0>{}, 4 * pair, tmA, tnA, 0);
        STAMP(0, 16 * pair + 3);
      }
      {
        FRESH_LANE();
        bf16x8 fA[2][2][2], fB[2][2][4];
        u32 pA, pB;
        so = 2 * SLOT;
        kloop<16, 16, 12, 0>(so,
          [&](int s_, auto) { pA = lane * 16 + wmi * 2048 + s_; pB = lane * 16 + wni * 4096 + s_; pin2(pA, pB); },
          [&](auto SET, auto Q) {
            constexpr int set = decltype(SET)::value, q = decltype(Q)::value, s = q / 6, m = q % 6;
            if constexpr (m < 2) dsrd<s * 4096 + m * 1024>(fA[set][s][m], pA);
            else dsrd<8192 + s * 8192 + (m - 2) * 1024>(fB[set][s][m - 2], pB);
          },
          [&](auto SET, auto M) {
            constexpr int set = decltype(SET)::value, m = decltype(M)::value, s = m >> 3, j = (m >> 1) & 3, i = m & 1;
            mfma(aco[i][j], fB[set][s][j], fA[set][s][i]);
          },
          [&](int t) { if (t < 0) STAMP(0, 16 * pair + 5); }, [](auto) {});
        STAMP(0, 16 * pair + 6);
        finish(aco, 2 * pair + 1, 4 * pair + 1);
      }
      {
        f32x4 acc[4][7];
        {
        FRESH_LANE();
#pragma unroll
        for (int i = 0; i < 4; ++i)
#pragma unroll
          for (int j = 0; j < 7; ++j) acc[i][j] = zero4();
        bf16x8 fA[2][4], fB[2][7];
        u32 pA[3], pB[3];
#pragma unroll
        for (int k = 0; k < 3; ++k) {
          pA[k] = lane * 16 + wmi * 4096 + 2 * k * SLOT; pB[k] = lane * 16 + wni * 7168 + 2 * k * SLOT;
          pin2(pA[k], pB[k]);
        }
        kloop6<28, 11, 1>(
          [&](auto SL, auto Q) {
            constexpr int set = decltype(SL)::value & 1, k = decltype(SL)::value >> 1, q = decltype(Q)::value;
            if constexpr (q < 4) dsrd<set * SLOT + q * 1024>(fA[set][q], pA[k]);
            else dsrd<set * SLOT + 8192 + (q - 4) * 1024>(fB[set][q - 4], pB[k]);
          },
          [&](auto SET, auto M) {
            constexpr int set = decltype(SET)::value, m = decltype(M)::value, j = m >> 2, i = m & 3;
            mfma(acc[i][j], fB[set][j], fA[set][i]);
          }, [&](int t) { if (t < 0) STAMP(0, 16 * pair + 9); });
        }
        FRESH_LANE();
        STAMP(0, 16 * pair + 10);
        float iv[4], mv[4];
#pragma unroll
        for (int i = 0; i < 4; ++i) { iv[i] = t_inv[16 + wmi * 64 + 16 * i + r]; mv[i] = t_mnv[16 + wmi * 64 + 16 * i + r]; }
        const u32 hbase = OFF_H + pair * HS + hpan(tmA, tnA >> 3, c13);
        auto act = [&](const f32x4& a, const f32x4& cs, const f32x4& bs, int i) {
          const f32x4 y = a * iv[i] + (bs - cs * mv[i]);
          return (u32x2){gelu_tanh2((f32x2){y[0], y[1]}), gelu_tanh2((f32x2){y[2], y[3]})};
        };
#pragma unroll
        for (int jp = 0; jp < 3; ++jp) {
          const int t0 = (wni * 7 + 2 * jp) * 16 + 4 * g;
          const f32x4 cs0 = *(const f32x4*)(t_cs + t0), cs1 = *(const f32x4*)(t_cs + t0 + 16);
          const f32x4 bs0 = *(const f32x4*)(t_bs + t0), bs1 = *(const f32x4*)(t_bs + t0 + 16);
#pragma unroll
          for (int i = 0; i < 4; ++i) {
            const u32x2 a = act(acc[i][2 * jp], cs0, bs0, i), b = act(acc[i][2 * jp + 1], cs1, bs1, i);
            const u32x4 v = {a[0], a[1], b[0], b[1]};
            st16(v, hbase + aq(hpos(tnA, wni * 4 + jp), wmi) + qoff(i, lane));
            if (4 * jp + i == 3 + PUBJ) publish(IC<PUBJ>{}, 4 * pair + 2, tmA, tnA, 0);
          }
          if (jp == 0) SB();
        }
        {
          const int t0 = (wni * 7 + 6) * 16 + 4 * g;
          const f32x4 cs0 = *(const f32x4*)(t_cs + t0), bs0 = *(const f32x4*)(t_bs + t0);
#pragma unroll
          for (int i = 0; i < 4; i += 2) {
            const u32x2 a = act(acc[i][6], cs0, bs0, i), b = act(acc[i + 1][6], cs0, bs0, i + 1);
            const auto s0 = __builtin_amdgcn_permlane16_swap(a[0], b[0], false, false);
            const auto s1 = __builtin_amdgcn_permlane16_swap(a[1], b[1], false, false);
            const u32x4 v = {s0[0], s1[0], s0[1], s1[1]};
            st16(v, hbase + aq(hpos(tnA, 3), wmi) + qoff(i + (g & 1), (2 * wni + (g >> 1)) * 16 + r));
          }
        }
        STAMP(0, 16 * pair + 11);
      }
      {
        FRESH_LANE();
        f32x4 acc[4][4];
        SB();
        const u32x4 bb[2] = {*(const u32x4*)(smem + L_B2 + wni * 128 + g * 16), *(const u32x4*)(smem + L_B2 + wni * 128 + 64 + g * 16)};
#pragma unroll
        for (int i = 2; i < 4; ++i)
#pragma unroll
          for (int j = 0; j < 4; ++j) acc[i][j] = zero4();
        start(acc, bb, true);
        bf16x8 fA[2][2][4], fB[2][2][4];
        u32 pA, pB;
        const u32 xc = wmi * 16384 + wni * 4096;
        publish(IC<0>{}, 4 * pair + 2, tmA, tnA, 1);
        so = 0;
        kloop<28, 32, 16, 1>(so,
          [&](int s_, auto) { pA = lane * 16 + wmi * 2048 + s_; pB = lane * 16 + wni * 4096 + s_; pin2(pA, pB); },
          [&](auto SET, auto Q) {
            constexpr int set = decltype(SET)::value, q = decltype(Q)::value, s = q >> 3, m = q & 7;
            if constexpr (m < 4) dsrd<s * 8192 + (m >> 1) * 4096 + (m & 1) * 1024>(fA[set][s][m], pA);
            else dsrd<16384 + s * 8192 + (m - 4) * 1024>(fB[set][s][m - 4], pB);
          },
          [&](auto SET, auto M) {
            constexpr int set = decltype(SET)::value, m = decltype(M)::value;
            constexpr int s = (m >> 3) & 1, j = (m >> 1) & 3, i = (m & 1) + (m < 16 ? 2 : 0);
            mfma(acc[i][j], fB[set][s][j], fA[set][s][i]);
          },
          [&](int t) { if (t < 0) STAMP(0, 16 * pair + 13); },
          [&](auto M) {
            constexpr int k = decltype(M)::value - SND - 1;
            if constexpr (k >= 0 && k < 8 && k % 2 == 0) {
              const f32x4 a = acc[2][k >> 1], b = acc[3][k >> 1];
              const u32x4 v = {pack2(a[0], a[1]), pack2(a[2], a[3]), pack2(b[0], b[1]), pack2(b[2], b[3])};
              st16(__builtin_elementwise_min(v, (u32x4){~1u, ~1u, ~1u, ~1u}), xbox(pair, 0) + xc + lane * 16 + k * 512);
            }
          });
        STAMP(0, 16 * pair + 14);
        u32x4 xv[4];
#pragma nounroll
        for (int spin = 0; spin < (1 << 22); ++spin) {
#pragma unroll
          for (int k = 0; k < 4; ++k)
            xv[k] = __builtin_bit_cast(u32x4, __builtin_amdgcn_raw_buffer_load_b128(rs, lane * 16 + k * 1024, xbox(pair, 1) + xc, 16));
          const u32x4 o = __builtin_elementwise_max(__builtin_elementwise_max(xv[0], xv[1]), __builtin_elementwise_max(xv[2], xv[3]));
          if (!__any(max(max(o[0], o[1]), max(o[2], o[3])) == ~0u)) break;
          asm volatile("" ::: "memory");
        }
#pragma unroll
        for (int k = 0; k < 4; ++k) {
          acc[0][k] += (f32x4){bf_lo(xv[k][0]), bf_hi(xv[k][0]), bf_lo(xv[k][1]), bf_hi(xv[k][1])};
          acc[1][k] += (f32x4){bf_lo(xv[k][2]), bf_hi(xv[k][2]), bf_lo(xv[k][3]), bf_hi(xv[k][3])};
        }
        finish(acc, pair == 0 ? 2 : 4, 4 * pair + 3);
      }
    }
  } else if (wid == 4) {
    {
      FRESH_LANE();
      STAMP(1, 0);
      const __amdgpu_buffer_rsrc_t rx = rsrc(x, SEQ * DM * 2);
      u32 vo[4];
#pragma unroll
      for (int e = 0; e < 4; ++e)
        vo[e] = (8 * e + (lane >> 3)) * 2048 + (((lane & 7) ^ (4 * (e & 1) + (lane >> 4))) * 16) - e * 1024;
      const u32 sx = tmA * 128 * 2048, sh = (tmA > 0 ? tmA * 128 - 16 : 0) * 2048;
      auto half = [&](auto H, int U) {
        constexpr int h = decltype(H)::value;
        U = (U + 2 * rot0) & 15;
        for_n<8>([&](auto E) {
          constexpr int e = decltype(E)::value;
          DMA(rx, (LDS_AS void*)(smem + so + h * SLOT + (e >> 2) * 4096), 16, vo[e & 3],
              sx + (h * 64 + (e >> 2) * 32) * 2048 + U * 128, (e & 3) * 1024, 0);
        });
        DMA(rx, (LDS_AS void*)(smem + so + SLOT + 21504), 16, vo[h], sh + U * 128, h * 1024, 0);
        if constexpr (h == 1) { so += 2 * SLOT; if (so == RING) so = 0; }
      };
#pragma nounroll
      for (int U = 0; U < 3; ++U) { half(IC<0>{}, U); half(IC<1>{}, U); }
      wait_vm<36>();
      bar();
      bar();
#pragma nounroll
      for (int U = 3; U < 16; ++U) {
        wait_vm<18>();
        bar();
        half(IC<0>{}, U);
        bar();
        half(IC<1>{}, U);
      }
      wait_vm<18>(); bar(); bar();
      wait_vm<0>(); bar(); bar();
    }
    auto phase = [&](auto NSL_, auto HALO_, u32 src, u32 hsrc, int ph  , int tm, int tmh) {
      constexpr int NSL = decltype(NSL_)::value, HALO = decltype(HALO_)::value;
      constexpr int BIG = NSL == 28, PA = BIG ? 16 : 8, PT = PA + HALO, D = BIG ? 3 : NSL == 16 ? DN : DW, X = BIG ? XA2 : 0;
      FRESH_LANE();
      u32 vo[4];
#pragma unroll
      for (int e = 0; e < 4; ++e) vo[e] = qoff(e, lane) - e * 1024;
      poll(flag(ph + 1, (lane & 16) ? tmh : tm, BIG ? 8 * (tnB & 1) + ((rotA >> 1) + (r < D + X ? r : 0))
                                               : NSL == 16 ? (rotB + (r < DN ? r : 0)) & 15 : rotA + (r & (DW > 4 ? 3 : 1))));
      STAMP(1, 4 * (ph + 1));
      const u32 flip = BIG ? (tnB & 1) * 16384 : 0;
      auto issue = [&](int u) {
        u = BIG ? (u < 8 ? u ^ (rotA >> 1) : u) : NSL == 16 ? (u + rotB) & 15 : u ^ (2 * rotA);
        if constexpr (BIG && X) wait_vm<47>();
        const u32 ko = aq(NSL == 32 ? u : 2 * u, 0);
        for_n<PA>([&](auto E) {
          constexpr int e = decltype(E)::value;
          DMA(rs, (LDS_AS void*)(smem + so + (e >> 2) * 4096), 16, vo[e & 3],
              src + ko + (NSL == 16 ? aq(e >> 2, 0) : aq(e >> 3, (e >> 2) & 1) + ((e & 4) ? -flip : flip)), (e & 3) * 1024, 16);
        });
        if constexpr (HALO) DMA(rs, (LDS_AS void*)(smem + so + 8192), 16, vo[3], hsrc + ko, 0, 16);
        next_slot<BIG>(so);
      };
      stream<NSL, PT, D, X>(issue, [&]() { bar(); });
    };
#pragma nounroll
    for (int pair = 0; pair < 2; ++pair) {
      const int tmh = tmA > 0 ? tmA - 1 : 0;
      const u32 xq = OFF_X + 2 * pair * XS;
      if (pair) phase(IC<32>{}, IC<1>{}, xq + xpan(tmA, c13), xq + xpan(tmh, c13) + 16384 + 3072, 3, tmA, tmh);
      side();
      bar();
      phase(IC<16>{}, IC<0>{}, OFF_O + pair * XS + xpan(tmB, c13) + aq(0, tnB & 1), 0, 4 * pair, tmB, tmB);
      phase(IC<32>{}, IC<0>{}, xq + XS + xpan(tmA, c13), 0, 4 * pair + 1, tmA, tmA);
      bar();
      so = 0;
      phase(IC<28>{}, IC<0>{}, OFF_H + pair * HS + hpan(tmB, tnB & 1, c13), 0, 4 * pair + 2, tmB, tmB);
      bar();
      so = 0;
    }
  } else if (wid == 5) {
    FRESH_LANE();
    auto phase = [&](auto NSL_, auto PB_, int rot, auto&& piece) {
      constexpr int NSL = decltype(NSL_)::value, PB = decltype(PB_)::value;
      auto issue = [&](int u) {
        u = NSL == 28 ? (u < 8 ? u ^ rot : u) : NSL == 16 ? (u + rot) & 15 : u ^ rot;
        wait_vm<63 - PB>();
        for_n<PB>([&](auto I) { piece(I, u); });
        next_slot<NSL == 28>(so);
      };
      stream<NSL, PB, (NSL == 28 ? 3 : 5), (NSL == 28 ? XB2 : NSL == 16 ? XBO : 0)>(issue, [&]() { bar(); });
    };
    auto tiles = [&](auto NSL_, auto PB_, auto BOFF_, u32 src) {
      constexpr int NSL = decltype(NSL_)::value, PB = decltype(PB_)::value, BOFF = decltype(BOFF_)::value;
      u32 vo[4];
#pragma unroll
      for (int e = 0; e < 4; ++e) vo[e] = qoff(e, lane) - e * 1024;
      phase(NSL_, PB_, NSL == 28 ? rotA >> 1 : NSL == 16 ? rotB : 2 * rotA, [&](auto I, int u) {
        constexpr int i = decltype(I)::value;
        DMA(rs, (LDS_AS void*)(smem + so + BOFF + (i >> 2) * 4096), 16, vo[i & 3],
            PB == 16 ? src + u * (2 * QS) + (i >> 3) * QS + ((i >> 2) & 1) * ((NSL == 28 ? 112 : 32) * QS)
                     : src + u * (4 * QS) + (i >> 2) * QS, (i & 3) * 1024, 16);
      });
    };
#pragma nounroll
    for (int pair = 0; pair < 2; ++pair) {
      {
        const __amdgpu_buffer_rsrc_t rq = rsrc(wqkv, DM * 3 * DM * 2);
        u32 vo[4];
#pragma unroll
        for (int e = 0; e < 4; ++e)
          vo[e] = (8 * e + (lane >> 3)) * 6144 + (((lane & 7) ^ (2 * ((lane >> 4) & 1) + 4 * (e & 1))) * 16) - e * 1024;
        phase(IC<32>{}, IC<12>{}, pair ? 2 * rotA : 0, [&](auto I, int u) {
          constexpr int i = decltype(I)::value;
          if (pair == 0) u = (u + 4 * rot0) & 31;
          DMA(rq, (LDS_AS void*)(smem + so + 9216 + (i >> 2) * 4096), 16, vo[i & 3],
              u * (32 * 6144) + (i >> 2) * 2048 + tnA * 128, (i & 3) * 1024, 0);
        });
      }
      bar();
      if (pair == 0) poll(flag(F_WO, r, (tnB & 14) + (g & 1)));
      tiles(IC<16>{}, IC<16>{}, IC<8192>{}, OFF_WO + (tnB & 14) * (32 * QS));
      if (pair == 0) poll(flag(F_W1, r, (tnA & 14) + (g & 1)));
      tiles(IC<32>{}, IC<14>{}, IC<8192>{}, OFF_W1 + tnA * (32 * 4 * QS));
      bar();
      so = 0;
      if (pair == 0) poll(flag(F_W2, 8 * (tnB & 1) + (r & 7), (tnB & 14) + (r >> 3)));
      tiles(IC<28>{}, IC<16>{}, IC<16384>{}, OFF_W2 + ((tnB & 14) * 112 + (tnB & 1) * 56) * QS);
      bar();
      so = 0;
    }
  } else if (wid == 6) {
    FRESH_LANE();
    const int rr = lane >> 3, c16 = (lane & 7) * 16;
    const __amdgpu_buffer_rsrc_t rx = rsrc(x, SEQ * DM * 2), rq = rsrc(wqkv, DM * 3 * DM * 2);
    auto warm = [&](int j) {
      const int n = idx * 64 + lane, m = idx * 48 + (lane < 48 ? lane : 47), k = (m * 5462) >> 16, p = m - 12 * k;
      j = (j + rot0) & 7;
      DMA(rx, (LDS_AS void*)(smem + L_PF), 4, (n & 511) * 2048 + ((n >> 9) & 1) * 128, ((idx >> 4) * 1024 + (xcd >> 2) * 512) * 2048 + j * 256, 0, 0);
      DMA(rq, (LDS_AS void*)(smem + L_PF), 4, k * 6144 + (p >> 2) * 2048 + (p & 3) * 128, (xcd & 3) * 512 + j * (128 * 6144), 0, 0);
    };
    u32x4 U[7][4], F[4];
    auto u_load = [&](auto I) {
      constexpr int i = decltype(I)::value;
#pragma unroll
      for (int e = 0; e < 4; ++e) {
        if constexpr (i < 2)
          U[(i + 5) % 7][e] = __builtin_bit_cast(u32x4, __builtin_amdgcn_raw_buffer_load_b128(rsrc(wo, DM * DM * 2),
              ((rr & 3) + 16 * (rr >> 2)) * 2048 + c16, ((2 * tmA + i) * 32 + 4 * e) * 2048 + tnA * 128, UAUX));
        else if constexpr (i < 9)
          U[(i + 5) % 7][e] = __builtin_bit_cast(u32x4, __builtin_amdgcn_raw_buffer_load_b128(rsrc(w1, DM * FF * 2),
              rr * 7168 + c16, ((2 * tmA + (tnA & 1)) * 32 + 8 * e) * 7168 + (7 * (tnA >> 1) + i - 2) * 128, UAUX));
        else
          U[(i + 5) % 7][e] = __builtin_bit_cast(u32x4, __builtin_amdgcn_raw_buffer_load_b128(rsrc(w2, DM * FF * 2),
              rr * 2048 + c16, ((7 * tmA + i - 9) * 32 + 8 * e) * 2048 + tnA * 128, UAUX));
      }
    };
    auto u_write = [&](auto I) {
      constexpr int i = decltype(I)::value;
#pragma unroll
      for (int e = 0; e < 4; ++e) {
        const u32x4 v = U[(i + 5) % 7][e];
        *(u32x4*)(smem + L_IM + e * 1024 + rr * 128 + (c16 ^ ((rr & 2) << 5))) = (e & 1) ? (u32x4){v[2], v[3], v[0], v[1]} : v;
      }
    };
    auto u_store = [&](auto I) {
      constexpr int i = decltype(I)::value;
      const u32 sw = ((r & 8) << 3) | ((g & 1) << 3);
      const char* im = smem + L_IM + (8 * g + (r >> 2)) * 128;
      for_n<4>([&](auto E) {
        constexpr int e = decltype(E)::value;
        constexpr bool split = i >= 2 && i < 9 && (2 * (i - 2) + (e >> 1)) % 7 == 3;
        const char* p = im + (((e >> 1) * 64 + (split ? (e & 1) * 32 + (r & 3) * 8 : (r & 3) * 16 + (e & 1) * 8)) ^ sw);
        const u32x2 lo = __builtin_bit_cast(u32x2, __builtin_amdgcn_ds_read_tr16_b64_v4i16((LDS_AS s16x4*)p));
        const u32x2 hi = __builtin_bit_cast(u32x2, __builtin_amdgcn_ds_read_tr16_b64_v4i16((LDS_AS s16x4*)(p + 512)));
        F[e] = (u32x4){lo[0], lo[1], hi[0], hi[1]};
      });
      for_n<4>([&](auto E) {
        constexpr int e = decltype(E)::value;
        if constexpr (i < 2) st16(F[e], OFF_WO + (tnA * 32 + 2 * tmA + i) * QS + qoff(e, lane));
        else if constexpr (i < 9) {
          constexpr int f = w1_frag(i, e);
          st16(F[e], OFF_W1 + ((((tnA & 14) + (f >> 4)) * 32 + 2 * tmA + (tnA & 1)) * 4 + ((f & 15) >> 2)) * QS + qoff(f & 3, lane));
        } else st16(F[e], OFF_W2 + (tnA * 112 + (tmA >> 3) * 56 + hpos(tmA, i - 9)) * QS + qoff(e, lane));
      });
    };
    auto u_sums = [&](auto I) {
      constexpr int i = decltype(I)::value;
      constexpr u32 K = w1_frag(i, 0) | (w1_frag(i, 1) << 8) | (w1_frag(i, 2) << 16) | (w1_frag(i, 3) << 24);
      const float cs = fold16(fold32(sum8(F[0]), sum8(F[2])), fold32(sum8(F[1]), sum8(F[3])));
      __builtin_amdgcn_raw_buffer_store_b32(__float_as_uint(cs), rs,
          OFF_CS1 + ((2 * tmA + (tnA & 1)) * 4096 + (tnA & 14) * 256 + ((K >> (8 * g)) & 255) * 16 + r) * 4, 0, 16);
    };
    auto up = [&](int set) { if (lane == 0) raise(set, tmA, tnA); };
    u32x4 Q[24];
    float qs[8];
    __builtin_amdgcn_s_sleep(PFW0);
#pragma unroll
    for (int e = 0; e < 24; ++e)
      Q[e] = __builtin_bit_cast(u32x4, __builtin_amdgcn_raw_buffer_load_b128(rq, rr * 6144 + c16,
          (tmA * 64 + 8 * (e & 7)) * 6144 + (e >> 3) * 2048 + tnA * 128, 0));
    u_load(IC<0>{});
    u_load(IC<1>{});
    for_n<33>([&](auto S) {
      constexpr int s = decltype(S)::value;
      bar();
      if constexpr (s == CQP + 5 + FLD) { wait_vm<between(CQP + 5, s)>(); up(F_CQ); }
      if constexpr (s == 26) { wait_vm<0>(); up(F_WO); }
      if constexpr (s >= PF0 && (s - PF0) % PFS == 0 && (s - PF0) / PFS < 8 - PFJ) warm(PFJ + (s - PF0) / PFS);
      if constexpr (s >= CQP && s < CQP + 6) {
        constexpr int h = s - CQP;
#pragma unroll
        for (int e = 0; e < 4; ++e) {
          u32x4 v = Q[4 * h + e];
          asm volatile("" : "+v"(v));
#pragma unroll
          for (int k = 0; k < 4; ++k) {
            if (h % 2 == 0 && e == 0) { qs[2 * k] = bf_lo(v[k]); qs[2 * k + 1] = bf_hi(v[k]); }
            else { qs[2 * k] += bf_lo(v[k]); qs[2 * k + 1] += bf_hi(v[k]); }
          }
        }
        if constexpr (h % 2 == 0) {
#pragma unroll
          for (int k = 0; k < 8; ++k) asm volatile("" : "+v"(qs[k]));
        } else {
          float c0 = fold16(fold32(qs[0], qs[4]), fold32(qs[2], qs[6])), c1 = fold16(fold32(qs[1], qs[5]), fold32(qs[3], qs[7]));
          c0 += __uint_as_float(__builtin_amdgcn_mov_dpp(__float_as_uint(c0), 0x128, 0xf, 0xf, false));
          c1 += __uint_as_float(__builtin_amdgcn_mov_dpp(__float_as_uint(c1), 0x128, 0xf, 0xf, false));
          const int col = 8 * (lane & 7) + rr;
          __builtin_amdgcn_raw_buffer_store_b32(__float_as_uint((rr & 1) ? c1 : c0), rs,
              OFF_CSQ + (tmA * 4096 + tnA * 256 + (col >> 5) * 96 + (h >> 1) * 32 + (col & 31)) * 4, 0, 16);
        }
      }
      if constexpr (s >= HL && s < HL + 4) {
        if constexpr ((s - HL) % 2 == 0) u_write(IC<(s - HL) / 2>{}); else u_store(IC<(s - HL) / 2>{});
      }
      if constexpr (s == 22 && (CW & 2)) warm_code(lane * 32 + idx, L_PF);
      if constexpr (s == G0) for_n<7>([&](auto I) { u_load(IC<2 + decltype(I)::value>{}); });
    });
    static_assert(HL >= CQP + 6 && HL + 4 < 26 && CQP + 5 + FLD < 26 && G0 >= 26, "schedule");
    for_n<7>([&](auto I) {
      constexpr int i = 2 + decltype(I)::value;
      u_write(IC<i>{});
      u_store(IC<i>{});
      u_sums(IC<i>{});
    });
    bar(); bar(); bar();
    wait_vm<0>();
    up(F_W1);
    if (CW & 4) warm_code(lane * 32 + idx, L_PF);
#pragma nounroll
    for (int u = 3; u <= G1; ++u) bar();
    for_n<7>([&](auto I) { u_load(IC<9 + decltype(I)::value>{}); });
#pragma nounroll
    for (int u = G1 + 1; u < 16 + H2; ++u) bar();
    for_n<14>([&](auto S) {
      constexpr int s = decltype(S)::value, i = 9 + s / 2;
      bar();
      if constexpr (s % 2 == 0) u_write(IC<i>{}); else u_store(IC<i>{});
    });
    if (CW & 8) warm_code(lane * 32 + idx, L_PF);
    bar(); bar(); bar(); bar();
    wait_vm<0>();
    up(F_W2);
  } else {
    FRESH_LANE();
    const int tmh = tmA > 0 ? tmA - 1 : 0;
    auto ln_phase = [&](bool qkv, int xi) {
      f32x4 sv[16], cv[16];
      f32x2 hv[4];
      const u32 so0 = OFF_ST + xi * 262144 + span(tmA, c13), so1 = OFF_ST + xi * 262144 + span(qkv ? tmh : tmA, c13);
      const u32 sc = (qkv ? OFF_CSQ : OFF_CS1) + tnA * 1024;
      auto ldcs = [&](int p0) {
#pragma unroll
        for (int p = 0; p < 16; ++p)
          cv[p] = __builtin_bit_cast(f32x4, __builtin_amdgcn_raw_buffer_load_b128(rs, lane * 16, sc + (p0 + p) * 16384, 16));
      };
#pragma unroll
      for (int p = 0; p < 16; ++p)
        sv[p] = __builtin_bit_cast(f32x4, __builtin_amdgcn_raw_buffer_load_b128(rs, lane * 16, so0 + (p + (p & 8)) * 1024, 16));
#pragma unroll
      for (int p = 0; p < 4; ++p)
        hv[p] = __builtin_bit_cast(f32x2, __builtin_amdgcn_raw_buffer_load_b64(rs, (112 + r) * 8 + (g + (g & 2)) * 4096, so1 + p * 1024, 16));
      bar();
      if (xi < 2) ldcs(0);
      bar(); bar();
      float mean[3] = {0.f, 0.f, 0.f}, m2[3] = {0.f, 0.f, 0.f}, dd[3] = {0.f, 0.f, 0.f};
#pragma unroll
      for (int p = 0; p < 16; ++p) { mean[0] += sv[p][0]; mean[1] += sv[p][2]; }
      mean[2] = xsum(hv[0][0] + hv[1][0] + hv[2][0] + hv[3][0]);
#pragma unroll
      for (int h = 0; h < 3; ++h) mean[h] *= (1.f / 16.f);
#pragma unroll
      for (int p = 0; p < 4; ++p) { const float d = hv[p][0] - mean[2]; m2[2] += hv[p][1]; dd[2] += d * d; }
      auto cut = [&]() {
        asm volatile("" : "+v"(mean[0]), "+v"(mean[1]), "+v"(mean[2]), "+v"(m2[0]), "+v"(m2[1]), "+v"(m2[2]),
                          "+v"(dd[0]), "+v"(dd[1]), "+v"(dd[2]));
        bar();
        asm volatile("" : "+v"(mean[0]), "+v"(mean[1]), "+v"(mean[2]), "+v"(m2[0]), "+v"(m2[1]), "+v"(m2[2]),
                          "+v"(dd[0]), "+v"(dd[1]), "+v"(dd[2]));
      };
#pragma unroll
      for (int p = 0; p < 16; ++p) {
        if (p % 8 == 0) cut();
        const float d0 = sv[p][0] - mean[0], d1 = sv[p][2] - mean[1];
        m2[0] += sv[p][1]; dd[0] += d0 * d0;
        m2[1] += sv[p][3]; dd[1] += d1 * d1;
      }
      m2[0] += 64.f * dd[0]; m2[1] += 64.f * dd[1];
      m2[2] = xsum(m2[2] + 64.f * dd[2]);
      cut();
#pragma unroll
      for (int h = 0; h < 3; ++h) { mean[h] = bfr(mean[h]); m2[h] = bfr(sqrtf(bfr(bfr(m2[h] * (1.f / DM)) + LN_EPS))); }
      cut();
#pragma unroll
      for (int h = 0; h < 3; ++h) m2[h] = 1.f / m2[h];
      *(f32x2*)(smem + L_INV + 64 + lane * 8) = (f32x2){m2[0], m2[1]};
      *(f32x2*)(smem + L_MNV + 64 + lane * 8) = (f32x2){mean[0] * m2[0], mean[1] * m2[1]};
      *(float*)(smem + L_INV + r * 4) = m2[2];
      *(float*)(smem + L_MNV + r * 4) = mean[2] * m2[2];
      bar();
      f32x4 ca = {0.f, 0.f, 0.f, 0.f};
      if (xi < 2) {
#pragma unroll
        for (int p = 0; p < 16; ++p) ca += cv[p];
        if (qkv) *(f32x4*)(smem + L_CSQ + lane * 16) = ca;
      }
      if (!qkv) {
        asm volatile("" : "+v"(ca));
        if (xi < 2) ldcs(16);
        bar(); bar(); bar(); bar();
        if (xi < 2) {
#pragma unroll
          for (int p = 0; p < 16; ++p) ca += cv[p];
          *(f32x4*)(smem + L_CS1 + lane * 16) = ca;
        }
      }
      wait_lgkm0();
    };
    constexpr int NLN = 8;
    auto bars = [&](int n) {
#pragma nounroll
      for (int u = 0; u < n; ++u) bar();
    };
    auto check = [&](const GLB_AS u64* f, int n) {
      const u64 v = ldf(f);
      bars(n);
      if (!__all(v == token())) poll(f);
    };
    auto guard = [&](const GLB_AS u64* f, int msk, int spp, int D, int K, auto&& step) {
      const u64 tok = token();
      int b = 0, lim = 0;
#pragma nounroll
      for (int spin = 0; spin < (1 << 22); ++spin) {
        const u64 v = ldf(f);
        if (b < lim) { bar(); step(b++); } else __builtin_amdgcn_s_sleep(1);
        if (b < lim) { bar(); step(b++); }
        const u32 m = (u32)__ballot(v == tok);
        const int n = __builtin_ctz(~(m & (m >> 16)));
        if (n >= 16) break;
        lim = min(K, (n & msk) * spp - D);
      }
#pragma nounroll
      while (b < K) { bar(); step(b++); }
    };
    {
      const u64 ep = __hip_atomic_load((const GLB_AS u64*)(ws + OFF_EP), __ATOMIC_RELAXED, __HIP_MEMORY_SCOPE_AGENT);
      u16 bv[4];
#pragma unroll
      for (int h = 0; h < 4; ++h) {
        const int t = min(lane + 64 * h, 223), f = t >> 4, rho = t & 15, wn = f / 7, j = f % 7;
        bv[h] = b1[tnA * 224 + (j == 6 ? 96 + 16 * wn + rho : (4 * wn + (j >> 1)) * 32 + 8 * (rho >> 2) + 4 * (j & 1) + (rho & 3))];
      }
      DMA(rsrc(b2, DM * 2), (LDS_AS void*)(smem + L_B2), 4, lane * 4, (tnB & 14) * 128, 0, 0);
      __builtin_amdgcn_s_sleep(XW);
      const __amdgpu_buffer_rsrc_t rx = rsrc(x, SEQ * DM * 2);
      bf16x8 xf[8][2];
#pragma unroll
      for (int i = 0; i < 8; ++i)
#pragma unroll
        for (int gl = 0; gl < 2; ++gl)
          xf[i][gl] = __builtin_bit_cast(bf16x8, __builtin_amdgcn_raw_buffer_load_b128(rx, r * 2048 + g * 16,
              (tmA * 128 + 16 * i) * 2048 + tnA * 128 + gl * 64, 0));
      bars(TOKB);
      auto probe = [&](int tn) { return OFF_FL + ((F_PR * 16 + (tmA ^ (4 * (idx & 1)))) * 16 + tn) * 128; };
      if (blockIdx.x < NVOTE)
        __builtin_amdgcn_raw_buffer_store_b32(0, rs, probe(tnA ^ 4) + (lane & 31) * 4, 0, 16);
#pragma unroll
      for (int h = 0; h < 4; ++h) ((float*)(smem + L_BS))[lane + 64 * h] = bf_lo(bv[h]);
      if (lane == 0) {
        *(LDS_AS u64*)(smem + L_CNT) = 0ull;
        *(LDS_AS u64*)(smem + L_TOK) = ep * 6364136223846793005ull + 1442695040888963407ull;
      }
      wait_lgkm0();
      u32 tlb;
      asm volatile("s_load_dword %0, %1, 0x0" : "=s"(tlb) : "s"(ws + OFF_EP + 128) : "memory");
      bars(XP - TOKB);
      const u32x4 one4 = {0x3f803f80u, 0x3f803f80u, 0x3f803f80u, 0x3f803f80u};
      const bf16x8 ones = __builtin_bit_cast(bf16x8, one4);
      const u32 svo = g == (r >> 2) ? OFF_ST + span(tmA, 0) + (tnA + (tnA & 8)) * 1024 + r * 8 : 0x80000000u;
#pragma unroll
      for (int i = 0; i < 8; ++i) {
        if (i == 4) bar();
        const f32x4 z = {0.f, 0.f, 0.f, 0.f};
        f32x4 s = __builtin_amdgcn_mfma_f32_16x16x32_bf16(ones, xf[i][0], z, 0, 0, 0);
        f32x4 q = __builtin_amdgcn_mfma_f32_16x16x32_bf16(xf[i][0], xf[i][0], z, 0, 0, 0);
        s = __builtin_amdgcn_mfma_f32_16x16x32_bf16(ones, xf[i][1], s, 0, 0, 0);
        q = __builtin_amdgcn_mfma_f32_16x16x32_bf16(xf[i][1], xf[i][1], q, 0, 0, 0);
        const float qq = (r & 2) ? ((r & 1) ? q[3] : q[2]) : ((r & 1) ? q[1] : q[0]);
        const float m = s[0] * (1.f / 64.f);
        const f32x2 o = {m, fmaxf(__builtin_fmaf(-s[0], m, qq), 0.f)};
        __builtin_amdgcn_raw_buffer_store_b64(__builtin_bit_cast(i32x2, o), rs, svo + i * 128, 0, 16);
      }
      bars(FLD);
      wait_vm<0>();
      if (lane == 0) raise(F_ST, tmA, tnA);
      bars(PRB);
      wait_lgkm0();
      asm volatile("" :: "s"(tlb));
      if (blockIdx.x < NVOTE) {
        u64 t0, t1;
        u32 v;
        asm volatile("s_memtime %0\n\ts_waitcnt lgkmcnt(0)\n\ts_load_dword %2, %3, 0x0\n\ts_waitcnt lgkmcnt(0)\n\t"
                     "s_memtime %1\n\ts_waitcnt lgkmcnt(0)" : "=&s"(t0), "=&s"(t1), "=&s"(v) : "s"(ws + probe(tnA)) : "memory");
        const u32 t = min((u32)t1 - (u32)t0, 4095u);
        if (lane == 0)
          __hip_atomic_store(flag(F_XC, 0, blockIdx.x), token() ^ t, __ATOMIC_RELAXED, __HIP_MEMORY_SCOPE_AGENT);
      }
      bars(POLB - (XP + 1 + FLD + PRB));
      static_assert(XP >= TOKB && POLB >= XP + 1 + FLD + PRB && POLB + 2 + NLN <= 31 && POLB + 2 > CQP + 5 + FLD, "schedule");
    }
#pragma nounroll
    for (int pair = 0; pair < 2; ++pair) {
      if (pair) {
        const __amdgpu_buffer_rsrc_t rq = rsrc(wqkv, DM * 3 * DM * 2);
        const int m = idx * 48 + (lane < 48 ? lane : 47), k = (m * 5462) >> 16, p = m - 12 * k;
        auto warm = [&](int j) {
          DMA(rq, (LDS_AS void*)(smem + L_PF), 4, k * 6144 + (p >> 2) * 2048 + (p & 3) * 128,
              (xcd & 3) * 512 + ((j + (rotA >> 1)) & 7) * (128 * 6144), 0, 0);
        };
        warm(0); warm(1); warm(2);
        guard(flag(4, (g & 1) ? tmh : tmA, (rotA + r) & 15), 14, 2, DW, POLB, [&](int b) { if ((b & 1) && b < 10) warm(3 + (b >> 1)); });
        if (blockIdx.x == 0 && lane == 0)
          __hip_atomic_store((GLB_AS u64*)(ws + OFF_EP), token(), __ATOMIC_RELAXED, __HIP_MEMORY_SCOPE_AGENT);
      }
      {
        const GLB_AS u64* f = g < 2 || pair ? flag(pair ? 4 + F_2ND : F_ST, (g & 1) ? tmh : tmA, r)
                            : g == 2 ? flag(F_CQ, r, tnA) : flag(F_XC, 0, r);
        const u64 lim = (pair == 0 && g == 3) ? 4096 : 1;
        u64 v = ldf(f);
        bars(2);
        const u64 tok = token();
#pragma nounroll
        for (int spin = 0; spin < (1 << 22); ++spin) {
          if (__all((v ^ tok) < lim)) break;
          __builtin_amdgcn_s_sleep(1);
          v = ldf(f);
        }
        if (pair == 0) {
          const u32 t = (u32)(v ^ tok);
          const u64 slower = __ballot(t > (u32)__builtin_amdgcn_mov_dpp(t, 0x128, 0xf, 0xf, false));
          *(volatile LDS_AS u32*)(smem + L_C13) = __builtin_popcount((u32)(slower >> 48) & 255) > 4 ? 8192 : 0;
        }
      }
      ln_phase(true, 2 * pair);
      side();
      bars(33 - CLB - (POLB + 2 + NLN));
#pragma unroll
      for (int e = 0; e < 16; ++e)
        st16(((u32x4){~0u, ~0u, ~0u, ~0u}), xbox(pair, 0) + (e + (e & 8)) * 1024 + lane * 16);
      bars(CLB);
      wait_vm<0>();
      guard(flag(4 * pair + 1, tmB, (rotB + r) & 15), 15, 1, DN, 16, [](int) {});
      guard(flag(4 * pair + 2, tmA, (rotA + r) & 15), 14, 2, DW, KW1, [](int) {});
      check(flag(4 * pair + 2 + F_2ND, tmA, r), 2);
      ln_phase(false, 2 * pair + 1);
      bars(33 - (KW1 + 2 + NLN + 4));
      guard(flag(4 * pair + 3, tmB, 8 * (tnB & 1) + ((r ^ (rotA >> 1)) & 7)), 15, 1, 3, 5 - CB, [](int) {});
      check(flag(4 * pair + 3 + F_2ND, tmB, 8 * (tnB & 1) + (r & 7)), CB);
      bars(29 - 5);
    }
  }
#endif
}

extern "C" void kernel_launch(void* const* d_in, const int* in_sizes, int n_in, void* d_out, int out_size,
                              void* d_ws, size_t ws_size, hipStream_t stream) {
  const u16* x0 = (const u16*)d_in[0];
  const u16* wqkv = (const u16*)d_in[1];
  const u16* wo = (const u16*)d_in[2];
  const u16* w1 = (const u16*)d_in[4];
  const u16* b1 = (const u16*)d_in[5];
  const u16* w2 = (const u16*)d_in[6];
  const u16* b2 = (const u16*)d_in[7];
  (void)hipFuncSetAttribute((const void*)mega_k, hipFuncAttributeMaxDynamicSharedMemorySize, LDS_BYTES);
  mega_k<<<256, 512, LDS_BYTES, stream>>>((char*)d_ws, x0, wqkv, wo, w1, b1, w2, b2, (u16*)d_out);
}
```

```cpp
#include <hip/hip_runtime.h>
#include <stdint.h>
#include <type_traits>
#include <utility>

typedef unsigned short u16;
typedef unsigned int u32;
typedef unsigned long long u64;
typedef __bf16 bf16x8 __attribute__((ext_vector_type(8)));
typedef __bf16 bf16x2 __attribute__((ext_vector_type(2)));
typedef short s16x4 __attribute__((ext_vector_type(4)));
typedef float f32x4 __attribute__((ext_vector_type(4)));
typedef float f32x2 __attribute__((ext_vector_type(2)));
typedef u32 u32x4 __attribute__((ext_vector_type(4)));
typedef u32 u32x2 __attribute__((ext_vector_type(2)));
typedef int i32x4 __attribute__((ext_vector_type(4)));
typedef int i32x2 __attribute__((ext_vector_type(2)));
#define LDS_AS __attribute__((address_space(3)))
#define GLB_AS __attribute__((address_space(1)))

constexpr int SEQ = 2048, DM = 1024, FF = 3584;
constexpr float LN_EPS = 1e-5f;

#define DN 4
#define DW 5
#define UAUX 2
#define PFW0 30
#define PF0 4
#define PFS 2
#define PFJ 2
#define CQP 5
#define HL 8
#define FLD 3
#define XW 30
#define TOKB 3
#define XP 8
#define LG0 24
#define LG 25
#define WQ 24
#define G0 27
#define G1 12
#define H2 4
#define PUBJ 2
#define CB 1
#define KW1 11
#define CLB 1
#define SND 12
#define RST 9
#define XB2 1
#define XA2 1
#define XBO 1
#define CW 1
#define PRB 13
#define CQG 22
#define R3W 1
#ifndef NVOTE
#define NVOTE 16
#endif
constexpr u32 MB = 1u << 20;
constexpr u32 QS = 5120;
constexpr u32 XS = 8 * MB, HS = 32 * MB;
constexpr u32 OFF_WO = 0;
constexpr u32 OFF_W1 = 3 * MB;
constexpr u32 OFF_W2 = 13 * MB;
constexpr u32 OFF_CS1 = 22 * MB;
constexpr u32 OFF_FL = 25 * MB;
constexpr u32 OFF_EP = OFF_FL + 26 * 32768;
constexpr u32 OFF_DBG = 26 * MB;
constexpr u32 OFF_XC = 28 * MB;
constexpr u32 OFF_X = 28 * MB;
constexpr u32 OFF_O = OFF_X + 4 * XS;
constexpr u32 OFF_H = OFF_O + 2 * XS;
constexpr u32 OFF_ST = OFF_H + 2 * HS;
constexpr u32 WS_USED = OFF_ST + 2 * MB, WS_ALIGN = 4 * MB;
#define HAS_ALIGN
constexpr int F_WO = 9, F_W1 = 10, F_W2 = 11, F_2ND = 12, F_3RD = 14, F_XC = 18, F_PR = 19, F_LN = 22;
__device__ __forceinline__ u32 qoff(u32 e, u32 slot) {
  const u32 n = 8 * e + (slot >> 3);
  return ((n + (((n + 4) * 37) >> 8)) << 7) + (slot & 7) * 16;
}
__device__ __forceinline__ u32 hpos(u32 t, u32 j) {
  return (j & 2) == 0 ? 16 * (j & 1) + 2 * (t & 7) + (j >> 2) : 32 + 3 * (t & 7) + (j & 1) + (j >> 2) * 2;
}
__device__ __forceinline__ u32 aq(u32 kg, u32 half) { return kg * 32768 + (kg & 1) * 3072 + half * 16384; }
__device__ __forceinline__ u32 ppair(u32 tm) { return (tm & 3) + (tm >> 3) * 4; }
__device__ __forceinline__ u32 xpan(u32 tm, u32 c13) { return ppair(tm) * MB + (((((tm >> 2) ^ (tm >> 1)) & 1) << 13) ^ c13); }
__device__ __forceinline__ u32 hpan(u32 tm, u32 kh, u32 c13) {
  return ppair(tm) * (4 * MB) + (((kh ^ tm) & 1) << 21) + (((((tm >> 2) ^ kh ^ tm) & 1) << 13) ^ c13);
}
__device__ __forceinline__ u32 span(u32 tm, u32 c13) { return ppair(tm) * 65536 + (((tm & 4) << 11) ^ c13); }

constexpr int SLOT = 24576, NRING = 6, RING = SLOT * NRING;
constexpr int L_INV = RING, L_MNV = L_INV + 576, L_CNT = L_MNV + 576, L_TOK = L_CNT + 8, L_C13 = L_TOK + 8;
constexpr int L_CSQ = RING + 1280, L_CS1 = L_CSQ + 1024, L_BS = L_CS1 + 1024;
constexpr int L_XC = L_BS + 1024;
constexpr int L_IM = L_XC + 6144;
constexpr int L_B2 = L_IM + 4096, L_PF = L_B2 + 256;
constexpr int LDS_BYTES = L_PF + 256;

__device__ __forceinline__ float bf_lo(u32 w) { return __uint_as_float(w << 16); }
__device__ __forceinline__ float bf_hi(u32 w) { return __uint_as_float(w & 0xffff0000u); }
__device__ __forceinline__ u32 pack2(float lo, float hi) {
  f32x2 v = {lo, hi};
  bf16x2 r = __builtin_convertvector(v, bf16x2);
  return __builtin_bit_cast(u32, r);
}
__device__ __forceinline__ float bfr(float x) { return bf_lo(pack2(x, 0.f) & 0xffffu); }
__device__ __forceinline__ u32 gelu_tanh2(f32x2 x) {
  const float c0 = -2.3022081983f, c1 = -0.1029432396f;
  const f32x2 p = x * x * c1 + c0;
  const f32x2 a = x * p;
  const f32x2 d = (f32x2){__builtin_amdgcn_exp2f(a[0]), __builtin_amdgcn_exp2f(a[1])} + 1.f;
  const f32x2 g = x * (f32x2){__builtin_amdgcn_rcpf(d[0]), __builtin_amdgcn_rcpf(d[1])};
  return pack2(g[0], g[1]);
}
template <int N_> __device__ __forceinline__ void wait_vm() { asm volatile("s_waitcnt vmcnt(%0)" ::"n"(N_) : "memory"); }
__device__ __forceinline__ void wait_lgkm0() { asm volatile("s_waitcnt lgkmcnt(0)" ::: "memory"); }
__device__ __forceinline__ void bar() {
  __builtin_amdgcn_sched_barrier(0);
  asm volatile("s_barrier" ::: "memory");
  __builtin_amdgcn_sched_barrier(0);
}
template <int V> using IC = std::integral_constant<int, V>;
template <typename F, int... I>
__device__ __forceinline__ void for_seq(F&& f, std::integer_sequence<int, I...>) { (f(IC<I>{}), ...); }
template <int N, typename F> __device__ __forceinline__ void for_n(F&& f) { for_seq(f, std::make_integer_sequence<int, N>{}); }

__device__ __forceinline__ float xsum(float x) {
#if defined(__HIP_DEVICE_COMPILE__)
  auto a = __builtin_amdgcn_permlane16_swap(__float_as_uint(x), __float_as_uint(x), false, false);
  x = __uint_as_float(a[0]) + __uint_as_float(a[1]);
  auto b = __builtin_amdgcn_permlane32_swap(__float_as_uint(x), __float_as_uint(x), false, false);
  x = __uint_as_float(b[0]) + __uint_as_float(b[1]);
#endif
  return x;
}
__device__ __forceinline__ float xmax(float x) {
#if defined(__HIP_DEVICE_COMPILE__)
  auto a = __builtin_amdgcn_permlane16_swap(__float_as_uint(x), __float_as_uint(x), false, false);
  x = fmaxf(__uint_as_float(a[0]), __uint_as_float(a[1]));
  auto b = __builtin_amdgcn_permlane32_swap(__float_as_uint(x), __float_as_uint(x), false, false);
  x = fmaxf(__uint_as_float(b[0]), __uint_as_float(b[1]));
#endif
  return x;
}
__device__ __forceinline__ f32x2 row_part(const u32x4& c0, const u32x4& c1) {
  float v[16];
#pragma unroll
  for (int k = 0; k < 4; ++k) {
    v[2 * k] = bf_lo(c0[k]); v[2 * k + 1] = bf_hi(c0[k]);
    v[8 + 2 * k] = bf_lo(c1[k]); v[8 + 2 * k + 1] = bf_hi(c1[k]);
  }
  float s = 0.f;
#pragma unroll
  for (int k = 0; k < 16; ++k) s += v[k];
  const float m = xsum(s) * (1.f / 64.f);
  float q = 0.f;
#pragma unroll
  for (int k = 0; k < 16; ++k) { const float d = v[k] - m; q += d * d; }
  return (f32x2){m, xsum(q)};
}

#define st16(v, off) __builtin_amdgcn_raw_buffer_store_b128(__builtin_bit_cast(i32x4, v), rs, (off), 0, 16)
__device__ __forceinline__ float sum8(const u32x4& f) {
  float s = bf_lo(f[0]) + bf_hi(f[0]);
#pragma unroll
  for (int k = 1; k < 4; ++k) { s += bf_lo(f[k]); s += bf_hi(f[k]); }
  return s;
}
__device__ __forceinline__ float fold32(float a, float b) {
#if defined(__HIP_DEVICE_COMPILE__)
  const auto x = __builtin_amdgcn_permlane32_swap(__float_as_uint(a), __float_as_uint(b), false, false);
  a = __uint_as_float(x[0]) + __uint_as_float(x[1]);
#endif
  return a;
}
__device__ __forceinline__ float fold16(float a, float b) {
#if defined(__HIP_DEVICE_COMPILE__)
  const auto x = __builtin_amdgcn_permlane16_swap(__float_as_uint(a), __float_as_uint(b), false, false);
  a = __uint_as_float(x[0]) + __uint_as_float(x[1]);
#endif
  return a;
}

__device__ __forceinline__ int fresh_lane(int tid) { int l = tid & 63; asm volatile("" : "+v"(l)); return l; }
__device__ __forceinline__ f32x4 zero4() {
  f32x4 z = {0.f, 0.f, 0.f, 0.f};
  asm volatile("" : "+v"(z));
  return z;
}
template <int BIG = 0> __device__ __forceinline__ void next_slot(int& so) {
  if constexpr (BIG) so = (so + 32768) & 131071; else { so += SLOT; if (so == RING) so = 0; }
}

#define DMA __builtin_amdgcn_raw_ptr_buffer_load_lds
#define SB() __builtin_amdgcn_sched_barrier(0)
__device__ __forceinline__ void mfma(f32x4& c, const bf16x8& a, const bf16x8& b) {
  asm volatile("v_mfma_f32_16x16x32_bf16 %0, %1, %2, %0" : "+v"(c) : "v"(a), "v"(b));
}
template <int OFF> __device__ __forceinline__ void dsrd(bf16x8& d, u32 addr) {
  asm volatile("ds_read_b128 %0, %1 offset:%2" : "=v"(d) : "v"(addr), "n"(OFF));
}
template <int OFF> __device__ __forceinline__ void dstr(bf16x8& d, u32 addr) {
  u32x2 lo, hi;
  asm volatile("ds_read_b64_tr_b16 %0, %2 offset:%3\n\tds_read_b64_tr_b16 %1, %2 offset:%4"
               : "=&v"(lo), "=&v"(hi) : "v"(addr), "n"(OFF), "n"(OFF + 512));
  const u32x4 v = {lo[0], lo[1], hi[0], hi[1]};
  d = __builtin_bit_cast(bf16x8, v);
}
template <int OFF> __device__ __forceinline__ void dstr1(u32x2& d, u32 addr) {
  asm volatile("ds_read_b64_tr_b16 %0, %1 offset:%2" : "=v"(d) : "v"(addr), "n"(OFF));
}
__device__ __forceinline__ void pin1(u32& a) { asm volatile("" : "+v"(a)); }
__device__ __forceinline__ void pin2(u32& a, u32& b) { asm volatile("" : "+v"(a), "+v"(b)); }
template <int NSL, int NM, int NR, int ENDB, class AD, class RD, class MM, class HK, class TL>
__device__ __forceinline__ void kloop(int& so, AD&& ad, RD&& rd, MM&& mm, HK&& hook, TL&& tail) {
  constexpr int BIG = NM == 32;
  ad(so, IC<0>{});
  bar();
  for_n<NR>([&](auto Q) { rd(IC<0>{}, Q); });
  next_slot<BIG>(so);
  ad(so, IC<1>{});
  hook(-1);
  auto body = [&](auto S) {
    constexpr int s = decltype(S)::value;
    wait_lgkm0();
    bar();
    for_n<NM>([&](auto M) {
      constexpr int m = decltype(M)::value;
      mm(S, M);
      if constexpr (m < NR) rd(IC<1 - s>{}, IC<m>{});
      if constexpr (m == NR) { next_slot<BIG>(so); asm volatile("" : "+s"(so)); }
      if constexpr (m == NR + 1) ad(so, S);
      SB();
    });
  };
#pragma nounroll
  for (int u = NSL / 2 - 1; u > 0; --u) {
    body(IC<0>{});
    body(IC<1>{});
    hook(NSL / 2 - 1 - u);
  }
  body(IC<0>{});
  wait_lgkm0();
  if constexpr (ENDB) { bar(); so = 0; }
  SB();
  for_n<NM>([&](auto M) { mm(IC<1>{}, M); SB(); tail(M); SB(); });
  asm volatile("s_nop 15");
  SB();
}

template <int NM, int NR, int ENDB, class RD, class MM, class HK>
__device__ __forceinline__ void kloop6(RD&& rd, MM&& mm, HK&& hook) {
  bar();
  for_n<NR>([&](auto Q) { rd(IC<0>{}, Q); });
  hook(-1);
  auto body = [&](auto S) {
    constexpr int s = decltype(S)::value;
    wait_lgkm0();
    bar();
    for_n<NM>([&](auto M) {
      constexpr int m = decltype(M)::value;
      mm(IC<s & 1>{}, M);
      if constexpr (m < NR) rd(IC<(s + 1) % NRING>{}, IC<m>{});
      SB();
    });
  };
#pragma nounroll
  for (int u = 5; u > 0; --u) for_n<NRING>(body);
  body(IC<0>{});
  wait_lgkm0();
  if constexpr (ENDB) bar();
  SB();
  for_n<NM>([&](auto M) { mm(IC<1>{}, M); SB(); });
  asm volatile("s_nop 15");
  SB();
}

template <int NSL, int P, int D, int X, class IS, class BR> __device__ __forceinline__ void stream(IS&& issue, BR&& bar) {
#pragma nounroll
  for (int u = 0; u < D + X; ++u) issue(u);
  if constexpr (X) { wait_vm<(D * P < 63 ? D * P : 63)>(); bar(); }
#pragma nounroll
  for (int u = X; u < NSL - D; ++u) {
    wait_vm<((D - 1) * P < 63 ? (D - 1) * P : 63)>();
    bar();
    issue(u + D);
  }
  for_n<D>([&](auto M) { constexpr int n = (D - 1 - decltype(M)::value) * P; wait_vm<(n < 63 ? n : 63)>(); bar(); });
}

#define STAMP(w, i)
#define TS(v)
#define TSW(v, i)
#define TSP 1

constexpr int w1_frag(int i, int e) {
  const int gam = 2 * (i - 2) + (e >> 1), gl = gam % 7;
  return (gam / 7) * 16 + (gl == 3 ? 6 + 7 * (e & 1) : (gl < 3 ? gl * 2 : 7 + (gl - 4) * 2) + (e & 1));
}
__device__ __forceinline__ void warm_code(u32 n, u32 lds) {
#if defined(__HIP_DEVICE_COMPILE__)
  u64 pc;
  u32 back, len;
  asm volatile("s_getpc_b64 %0\n.Lcw%=:\n\ts_mov_b32 %1, .Lcw%=-mega_k\n\ts_mov_b32 %2, .Lcode_end-mega_k" : "=s"(pc), "=s"(back), "=s"(len));
  pc -= back;
  const u32x4 rc = {(u32)pc, (u32)(pc >> 32), len, 0x00020000u};
  asm volatile("s_mov_b32 %0, m0\n\ts_mov_b32 m0, %1\n\ts_nop 0\n\tbuffer_load_dword %2, %3, 0 offen lds\n\ts_mov_b32 m0, %0"
               : "=&s"(back) : "s"(lds), "v"(n * 128), "s"(rc));
#endif
}

template <int N> __device__ __forceinline__ void announce(u32 fl, __amdgpu_buffer_rsrc_t rs) {
  u32x2 tk;
  u32 a, o, f, n;
  asm volatile("s_mov_b64 exec, 1\n\tv_mov_b32 %1, %7\n\tds_read_b64 %0, %1 offset:8\n\tv_mov_b32 %2, 1\n\tv_mov_b32 %3, %5\n\t"
               "s_waitcnt vmcnt(%8)\n\tds_add_rtn_u32 %1, %1, %2\n\ts_waitcnt lgkmcnt(0)\n\t"
               "v_readfirstlane_b32 %4, %1\n\ts_and_b32 %4, %4, 3\n\ts_cmp_eq_u32 %4, 3\n\ts_cbranch_scc0 .Lan%=\n\t"
               "buffer_store_dwordx2 %0, %3, %6, 0 offen sc1\n.Lan%=:\n\ts_mov_b64 exec, -1"
               : "=&v"(tk), "=&v"(a), "=&v"(o), "=&v"(f), "=&s"(n)
               : "s"(fl), "s"(rs), "n"(L_CNT), "n"(N) : "memory", "scc");
  static_assert(L_TOK == L_CNT + 8, "layout");
}

extern "C" __global__ __launch_bounds__(512, 2) void mega_k(char* __restrict__ ws0, const u16* __restrict__ x,
                                                 const u16* __restrict__ wqkv, const u16* __restrict__ wo,
                                                 const u16* __restrict__ w1, const u16* __restrict__ b1,
                                                 const u16* __restrict__ w2, const u16* __restrict__ b2,
                                                 u16* __restrict__ out) {
#if defined(__HIP_DEVICE_COMPILE__)
  extern __shared__ __attribute__((aligned(16))) char smem[];
  char* const ws = ws0 + (-(u32)(uintptr_t)ws0 & (WS_ALIGN - 1));
  const int tid = threadIdx.x;
  const int wid = __builtin_amdgcn_readfirstlane(tid >> 6);
#define FRESH_LANE() const int lane = fresh_lane(tid), r = lane & 15, g = lane >> 4; (void)r; (void)g
  const int xcd = blockIdx.x & 7, idx = blockIdx.x >> 3;
  asm volatile(".subsection 1\n.Lcode_end:\n\t.subsection 0");
  if (CW & 1) warm_code(((tid & 63) * 8 + wid) * 32 + idx, L_PF);
  const int tmA = (idx >> 4) * 8 + (xcd >> 2) * 4 + ((idx >> 2) & 3), tnA = (xcd & 3) * 4 + (idx & 3);
  const int tmB = (idx >> 4) * 8 + (xcd >> 1) * 2 + ((idx >> 3) & 1), tnB = (idx & 7) * 2 + (xcd & 1);
  const int rotA = 8 - 8 * (xcd & 1), rotB = 4 * ((xcd + 1) & 3), rot0 = xcd;
  const __amdgpu_buffer_rsrc_t rs = __builtin_amdgcn_make_buffer_rsrc((void*)ws, 0, WS_USED, 0x00020000);
  u32 c13 = 0;
  auto side = [&]() { c13 = __builtin_amdgcn_readfirstlane(*(const volatile LDS_AS u32*)(smem + L_C13)); };
  auto flag = [&](int set, int tm, int tn) { return OFF_FL + ((((set * 16 + tm) * 16 + tn) * 128) ^ c13); };
  auto xbox = [&](int pair, int o) { return OFF_XC + pair * (4 * MB) + ((idx * 4 + (xcd & 3)) ^ o) * 32768 + (((((tmB >> 2) ^ (idx >> 4)) & 1) << 13) ^ c13); };
  auto rsrc = [&](const u16* p, u32 bytes) { return __builtin_amdgcn_make_buffer_rsrc((void*)p, 0, bytes, 0x00020000); };
  auto token = [&]() {
    const u32x2 tk = *(const volatile LDS_AS u32x2*)(smem + L_TOK);
    return (u64)(u32)__builtin_amdgcn_readfirstlane(tk[0]) | ((u64)(u32)__builtin_amdgcn_readfirstlane(tk[1]) << 32);
  };
  auto ldf = [&](u32 f) {
    asm volatile("" ::: "memory");
    return __builtin_bit_cast(u64, __builtin_amdgcn_raw_buffer_load_b64(rs, f, 0, 16));
  };
  auto poll = [&](u32 f, u64 tok) {
#pragma nounroll
    for (int spin = 0; spin < (1 << 22); ++spin) {
      if (__all(ldf(f) == tok)) break;
      __builtin_amdgcn_s_sleep(1);
    }
    asm volatile("" ::: "memory");
  };
  auto st8 = [&](u64 v, bool on, u32 f) {
    __builtin_amdgcn_raw_buffer_store_b64(__builtin_bit_cast(i32x2, v), rs, on ? f : 0x80000000u, 0, 16);
  };
  int so = 0;
#define QUIT() __builtin_amdgcn_endpgm()

  if (wid < 4) {
    auto publish = [&](auto N, int ph, int tm, int tn) {
      SB();
      announce<decltype(N)::value>(flag(ph + 1, tm, tn), rs);
      SB();
    };
    const float* t_inv = (const float*)(smem + L_INV);
    const float* t_mnv = (const float*)(smem + L_MNV);
    const float* t_cs = (const float*)(smem + L_CS1);
    const float* t_bs = (const float*)(smem + L_BS);
    const int wmi = wid >> 1, wni = wid & 1;

    const int w = 2 * (tnB & 1) + wmi, tn = (tnB & 14) + wni;
    u32x4 c[2][2];
    auto start = [&](f32x4 (&acc)[4][4], const u32x4 (&bb)[2], bool bias) {
#pragma unroll
      for (int i = 0; i < 2; ++i)
#pragma unroll
        for (int j = 0; j < 4; ++j) {
          const u32 c0 = c[i][j >> 1][2 * (j & 1)], c1 = c[i][j >> 1][2 * (j & 1) + 1];
          const u32 b0 = bb[j >> 1][2 * (j & 1)], b1 = bb[j >> 1][2 * (j & 1) + 1];
          f32x4 a = {bf_lo(c0), bf_hi(c0), bf_lo(c1), bf_hi(c1)};
          if (bias) a += (f32x4){bf_lo(b0), bf_hi(b0), bf_lo(b1), bf_hi(b1)};
          asm volatile("" : "+v"(a));
          acc[i][j] = a;
        }
    };
    auto finish = [&](const f32x4 (&acc)[4][4], int xo  , int ph) {
      FRESH_LANE();
#pragma unroll
      for (int i = 0; i < 2; ++i)
#pragma unroll
        for (int gl = 0; gl < 2; ++gl) {
#pragma unroll
          for (int k = 0; k < 4; ++k) {
            const f32x4& a = acc[i][2 * gl + (k >> 1)];
            c[i][gl][k] = pack2(a[2 * (k & 1)], a[2 * (k & 1) + 1]);
          }
          if (xo < 4) st16(c[i][gl], OFF_X + xo * XS + xpan(tmB, c13) + tn * 65536 + aq(gl, w >> 1) + qoff((w & 1) * 2 + i, lane));
          else __builtin_amdgcn_raw_buffer_store_b128(__builtin_bit_cast(i32x4, c[i][gl]), rsrc(out, SEQ * DM * 2),
                   (tmB * 128 + (2 * w + i) * 16 + r) * 2048 + tn * 128 + gl * 64 + g * 16, 0, 16);
        }
      if (xo < 4) {
        SB();
        TS(ta);
        const u32 tag = *(const volatile LDS_AS u32*)(smem + L_TOK);
        const f32x2 s0 = row_part(c[0][0], c[0][1]), s1 = row_part(c[1][0], c[1][1]), s = (g & 1) ? s1 : s0;
        __builtin_amdgcn_raw_buffer_store_b64((i32x2){(int)__float_as_uint((g & 2) ? s[1] : s[0]), (int)tag}, rs,
            OFF_ST + xo * 524288 + span(tmB, c13) + w * 16384 + tn * 128 + (lane & 24) * 256 + (lane & 7) * 16 + (g >> 1) * 8, 0, 16);
        SB();
        TS(tb);
        publish(IC<1>{}, ph, tmB, tnB);
        TS(tc);
      }
      STAMP(0, 4 * ph + 3);
    };

#pragma nounroll
    for (int pair = 0; pair < 2; ++pair) {
      f32x4 aco[4][4];
      {
        f32x4 acc[4][6], acch[3];
        {
        FRESH_LANE();
        STAMP(0, 16 * pair);
        const u32 lB = 9216 + (8 * g + (r >> 2)) * 128 + (((2 * wni) ^ ((r >> 3) & 1) ^ ((g & 1) << 1)) * 32) + (r & 3) * 8;
        const u32 lA = r * 128 + ((g ^ (r >> 1)) * 16);
        auto loop = [&](auto WM, auto RAW) {
          constexpr int wm = decltype(WM)::value, raw = decltype(RAW)::value;
#pragma unroll
          for (int i = 0; i < 4; ++i)
#pragma unroll
            for (int j = 0; j < 6; ++j) acc[i][j] = zero4();
          acch[0] = zero4(); acch[1] = zero4();
          bf16x8 fA[2][4], fH[2];
          u32x2 hB[2][12];
          u32 pA[3][2], pB[3][2], pH[3][2];
          acch[2] = zero4();
          if (raw) *(u32*)(smem + L_XC + lane * 4) = 0x3f803f80u;
#pragma unroll
          for (int k = 0; k < 3; ++k) {
            pA[k][0] = (raw ? lA : lane * 16) + 2 * k * SLOT; pA[k][1] = (lA ^ 64) + 2 * k * SLOT;
            pB[k][0] = lB + 2 * k * SLOT; pB[k][1] = (lB ^ 32) + 2 * k * SLOT;
            pin1(pA[k][0]); pin2(pB[k][0], pB[k][1]);
            if (raw) {
              pH[k][0] = r ? pA[k][0] : L_XC - (SLOT + 21504) + lA; pH[k][1] = r ? pA[k][1] : L_XC - (SLOT + 21504) + (lA ^ 64);
              pin1(pA[k][1]); pin2(pH[k][0], pH[k][1]);
            }
          }
          auto fB = [&](int set, int j) {
            return __builtin_bit_cast(bf16x8, (u32x4){hB[set][2 * j][0], hB[set][2 * j][1], hB[set][2 * j + 1][0], hB[set][2 * j + 1][1]});
          };
          kloop6<26 + raw, 17, 0>(
            [&](auto SL, auto Q) {
              constexpr int set = decltype(SL)::value & 1, k = decltype(SL)::value >> 1, q = decltype(Q)::value, h = q - 5;
              if constexpr (q < 4) dsrd<raw ? wm * SLOT + q * 2048 : set * SLOT + wm * 4096 + q * 1024>(fA[set][q], pA[k][raw & set]);
              else if constexpr (q == 4) dsrd<raw ? SLOT + 21504 : set * SLOT + 8192>(fH[set], raw ? pH[k][set] : pA[k][0]);
              else dstr1<set * SLOT + (h >> 2) * 4096 + (h & 1) * 512>(hB[set][h], pB[k][(h >> 1) & 1]);
            },
            [&](auto SET, auto M) {
              constexpr int set = decltype(SET)::value, m = decltype(M)::value, j = m >> 2, i = m & 3;
              if constexpr (m < 16) mfma(acc[i][j], fB(set, j), fA[set][i]);
              else if constexpr (m < 24) mfma(acc[i][j], fA[set][i], fB(set, j));
              else if constexpr (m == 26) mfma(acch[2], fB(set, wm), fH[set]);
              else if constexpr (wm == 0) mfma(acch[i], fB(set, 2 + i), fH[set]);
              else mfma(acch[i], fH[set], fB(set, 4 + i));
            }, [&](int t) { if (t < 0) STAMP(0, 16 * pair + 1); });
        };
        if (pair == 0) { if (wmi == 0) loop(IC<0>{}, IC<1>{}); else loop(IC<1>{}, IC<1>{}); }
        else { if (wmi == 0) loop(IC<0>{}, IC<0>{}); else loop(IC<1>{}, IC<0>{}); }
        }
        FRESH_LANE();
        STAMP(0, 16 * pair + 2);
        side();
        float* csw = (float*)(smem + L_CSQ) + wni * 96;
        if (pair == 0) {
          if (r == 0) {
            *(f32x4*)(csw + wmi * 16 + 4 * g) = acch[2];
            if (wmi == 0) { *(f32x4*)(csw + 32 + 4 * g) = acch[0]; *(f32x4*)(csw + 48 + 4 * g) = acch[1]; }
          }
          if (wmi == 1 && g == 0) { csw[64 + r] = acch[0][0]; csw[80 + r] = acch[1][0]; }
#pragma unroll
          for (int i = 0; i < 2; ++i)
#pragma unroll
            for (int gl = 0; gl < 2; ++gl)
              c[i][gl] = __builtin_bit_cast(u32x4, __builtin_amdgcn_raw_buffer_load_b128(rsrc(x, SEQ * DM * 2),
                  (16 * i + r) * 2048 + g * 16, (tmB * 128 + w * 32) * 2048 + tn * 128 + gl * 64, 0));
          wait_lgkm0();
          bar();
        }
        auto fix_qk = [&](const f32x4& a0, const f32x4& a1, int trow, int f) {
          const float iv = t_inv[trow + r], mv = t_mnv[trow + r];
          const f32x4 c0 = *(const f32x4*)(csw + f * 16 + 4 * g), c1 = *(const f32x4*)(csw + f * 16 + 16 + 4 * g);
          const f32x4 y0 = a0 * iv - c0 * mv, y1 = a1 * iv - c1 * mv;
          return (u32x4){pack2(y0[0], y0[1]), pack2(y0[2], y0[3]), pack2(y1[0], y1[1]), pack2(y1[2], y1[3])};
        };
        auto fix_v = [&](const f32x4& a0, const f32x4& a1, int trow) {
          const f32x4 iv = *(const f32x4*)(t_inv + trow + 4 * g), mv = *(const f32x4*)(t_mnv + trow + 4 * g);
          const float c0 = csw[64 + r], c1 = csw[80 + r];
          const f32x4 y0 = a0 * iv - mv * c0, y1 = a1 * iv - mv * c1;
          return (u32x4){pack2(y0[0], y0[1]), pack2(y0[2], y0[3]), pack2(y1[0], y1[1]), pack2(y1[2], y1[3])};
        };
        u32x4 Qf[4], Kf[5], Vf[5];
        char* xc = smem + L_XC + wni * 3072 + lane * 16;
        const int row0 = 16 + wmi * 64;
        Kf[4] = fix_qk(acc[3][2], acc[3][3], row0 + 48, 2);
        Vf[4] = fix_v(acc[3][4], acc[3][5], row0 + 48);
        if (wmi == 0) {
          *(u32x4*)xc = Kf[4];
          *(u32x4*)(xc + 1024) = Vf[4];
          Kf[0] = fix_qk(acch[0], acch[1], 0, 2);
        } else {
          Kf[0] = fix_v(acch[0], acch[1], 0);
          *(u32x4*)(xc + 2048) = Kf[0];
        }
#pragma unroll
        for (int i = 0; i < 3; ++i) {
          Kf[i + 1] = fix_qk(acc[i][2], acc[i][3], row0 + 16 * i, 2);
          Vf[i + 1] = fix_v(acc[i][4], acc[i][5], row0 + 16 * i);
        }
        wait_lgkm0();
        bar();
        TS(td);
        if (wmi == 1) Kf[0] = *(const u32x4*)xc;
        Vf[0] = *(const u32x4*)(xc + (wmi == 0 ? 2048 : 1024));
#pragma unroll
        for (int i = 0; i < 4; ++i) Qf[i] = fix_qk(acc[i][0], acc[i][1], row0 + 16 * i, 0);
        const bool par = r & 1;
        const float m0 = par ? 0.f : 1.f, m1 = par ? 1.f : 0.f;
        const bool own0 = 4 * g + (r & 1) <= r, own1 = 4 * g + (r & 1) + 2 <= r;
        const bool first = (tmA == 0) && (wmi == 0);
#pragma unroll
        for (int ii = 0; ii < 4; ++ii) {
          const int i = 3 - ii;
          const f32x4 z = {0.f, 0.f, 0.f, 0.f};
          const f32x4 S0 = __builtin_amdgcn_mfma_f32_16x16x32_bf16(__builtin_bit_cast(bf16x8, Kf[i]), __builtin_bit_cast(bf16x8, Qf[i]), z, 0, 0, 0);
          const f32x4 S1 = __builtin_amdgcn_mfma_f32_16x16x32_bf16(__builtin_bit_cast(bf16x8, Kf[i + 1]), __builtin_bit_cast(bf16x8, Qf[i]), z, 0, 0, 0);
          float l0 = own0 ? (par ? S1[1] : S1[0]) : (par ? S0[1] : S0[0]);
          float l1 = own1 ? (par ? S1[3] : S1[2]) : (par ? S0[3] : S0[2]);
          const float c = 0.17677669529663687f * 1.4426950408889634f;
          l0 *= c; l1 *= c;
          if (i == 0 && first) {
            if (!own0) l0 = -1.4426950e9f;
            if (!own1) l1 = -1.4426950e9f;
          }
          const float mx = xmax(fmaxf(l0, l1));
          const float p0 = __builtin_amdgcn_exp2f(l0 - mx), p1 = __builtin_amdgcn_exp2f(l1 - mx);
          const float rsum = __builtin_amdgcn_rcpf(xsum(p0 + p1));
          const float q0 = p0 * rsum, q1 = p1 * rsum;
          const u32 w0 = pack2(q0 * m0, q0 * m1), w1 = pack2(q1 * m0, q1 * m1);
          const u32x4 P = {own0 ? 0u : w0, own1 ? 0u : w1, own0 ? w0 : 0u, own1 ? w1 : 0u};
          const u32x4 V0 = {Vf[i][0], Vf[i][1], Vf[i + 1][0], Vf[i + 1][1]};
          const u32x4 V1 = {Vf[i][2], Vf[i][3], Vf[i + 1][2], Vf[i + 1][3]};
          const f32x4 O0 = __builtin_amdgcn_mfma_f32_16x16x32_bf16(__builtin_bit_cast(bf16x8, V0), __builtin_bit_cast(bf16x8, P), z, 0, 0, 0);
          const f32x4 O1 = __builtin_amdgcn_mfma_f32_16x16x32_bf16(__builtin_bit_cast(bf16x8, V1), __builtin_bit_cast(bf16x8, P), z, 0, 0, 0);
          const u32x4 o = {pack2(O0[0], O0[1]), pack2(O0[2], O0[3]), pack2(O1[0], O1[1]), pack2(O1[2], O1[3])};
          st16(o, OFF_O + pair * XS + xpan(tmA, c13) + tnA * 65536 + aq(wni, wmi) + qoff(i, lane));
        }
        SB();
        TS(te);
        start(aco, c[0], false);
        publish(IC<0>{}, 4 * pair, tmA, tnA);
        TSW(td, 35); TSW(te, 34);
        STAMP(0, 16 * pair + 3);
      }
      {
        FRESH_LANE();
        bf16x8 fA[2][2][2], fB[2][2][4];
        u32 pA, pB;
        so = 2 * SLOT;
        kloop<16, 16, 12, 0>(so,
          [&](int s_, auto) { pA = lane * 16 + wmi * 2048 + s_; pB = lane * 16 + wni * 4096 + s_; pin2(pA, pB); },
          [&](auto SET, auto Q) {
            constexpr int set = decltype(SET)::value, q = decltype(Q)::value, s = q / 6, m = q % 6;
            if constexpr (m < 2) dsrd<s * 4096 + m * 1024>(fA[set][s][m], pA);
            else dsrd<8192 + s * 8192 + (m - 2) * 1024>(fB[set][s][m - 2], pB);
          },
          [&](auto SET, auto M) {
            constexpr int set = decltype(SET)::value, m = decltype(M)::value, s = m >> 3, j = (m >> 1) & 3, i = m & 1;
            mfma(aco[i][j], fB[set][s][j], fA[set][s][i]);
          },
          [&](int t) { if (t < 0) STAMP(0, 16 * pair + 5); }, [](auto) {});
        STAMP(0, 16 * pair + 6);
        finish(aco, 2 * pair + 1, 4 * pair + 1);
      }
      {
        f32x4 acc[4][7];
        {
        FRESH_LANE();
#pragma unroll
        for (int i = 0; i < 4; ++i)
#pragma unroll
          for (int j = 0; j < 7; ++j) acc[i][j] = zero4();
        bf16x8 fA[2][4], fB[2][7];
        u32 pA[3], pB[3];
#pragma unroll
        for (int k = 0; k < 3; ++k) {
          pA[k] = lane * 16 + wmi * 4096 + 2 * k * SLOT; pB[k] = lane * 16 + wni * 7168 + 2 * k * SLOT;
          pin2(pA[k], pB[k]);
        }
        kloop6<28, 11, 1>(
          [&](auto SL, auto Q) {
            constexpr int set = decltype(SL)::value & 1, k = decltype(SL)::value >> 1, q = decltype(Q)::value;
            if constexpr (q < 4) dsrd<set * SLOT + q * 1024>(fA[set][q], pA[k]);
            else dsrd<set * SLOT + 8192 + (q - 4) * 1024>(fB[set][q - 4], pB[k]);
          },
          [&](auto SET, auto M) {
            constexpr int set = decltype(SET)::value, m = decltype(M)::value, j = m >> 2, i = m & 3;
            mfma(acc[i][j], fB[set][j], fA[set][i]);
          }, [&](int t) { if (t < 0) STAMP(0, 16 * pair + 9); });
        }
        FRESH_LANE();
        STAMP(0, 16 * pair + 10);
        float iv[4], mv[4];
#pragma unroll
        for (int i = 0; i < 4; ++i) { iv[i] = t_inv[16 + wmi * 64 + 16 * i + r]; mv[i] = t_mnv[16 + wmi * 64 + 16 * i + r]; }
        const u32 hbase = OFF_H + pair * HS + hpan(tmA, tnA >> 3, c13);
        auto act = [&](const f32x4& a, const f32x4& cs, const f32x4& bs, int i) {
          const f32x4 y = a * iv[i] + (bs - cs * mv[i]);
          return (u32x2){gelu_tanh2((f32x2){y[0], y[1]}), gelu_tanh2((f32x2){y[2], y[3]})};
        };
#pragma unroll
        for (int jp = 0; jp < 3; ++jp) {
          const int t0 = (wni * 7 + 2 * jp) * 16 + 4 * g;
          const f32x4 cs0 = *(const f32x4*)(t_cs + t0), cs1 = *(const f32x4*)(t_cs + t0 + 16);
          const f32x4 bs0 = *(const f32x4*)(t_bs + t0), bs1 = *(const f32x4*)(t_bs + t0 + 16);
#pragma unroll
          for (int i = 0; i < 4; ++i) {
            const u32x2 a = act(acc[i][2 * jp], cs0, bs0, i), b = act(acc[i][2 * jp + 1], cs1, bs1, i);
            const u32x4 v = {a[0], a[1], b[0], b[1]};
            st16(v, hbase + aq(hpos(tnA, wni * 4 + jp), wmi) + qoff(i, lane));
            if (4 * jp + i == 3 + PUBJ) publish(IC<PUBJ>{}, 4 * pair + 2, tmA, tnA);
          }
          if (jp < 2) SB();
        }
        {
          const int t0 = (wni * 7 + 6) * 16 + 4 * g;
          const f32x4 cs0 = *(const f32x4*)(t_cs + t0), bs0 = *(const f32x4*)(t_bs + t0);
#pragma unroll
          for (int i = 0; i < 4; i += 2) {
            const u32x2 a = act(acc[i][6], cs0, bs0, i), b = act(acc[i + 1][6], cs0, bs0, i + 1);
            const auto s0 = __builtin_amdgcn_permlane16_swap(a[0], b[0], false, false);
            const auto s1 = __builtin_amdgcn_permlane16_swap(a[1], b[1], false, false);
            const u32x4 v = {s0[0], s1[0], s0[1], s1[1]};
            st16(v, hbase + aq(hpos(tnA, 3), wmi) + qoff(i + (g & 1), (2 * wni + (g >> 1)) * 16 + r));
          }
        }
        STAMP(0, 16 * pair + 11);
      }
      {
        FRESH_LANE();
        f32x4 acc[4][4];
        SB();
        const u32x4 bb[2] = {*(const u32x4*)(smem + L_B2 + wni * 128 + g * 16), *(const u32x4*)(smem + L_B2 + wni * 128 + 64 + g * 16)};
#pragma unroll
        for (int i = 2; i < 4; ++i)
#pragma unroll
          for (int j = 0; j < 4; ++j) acc[i][j] = zero4();
        start(acc, bb, true);
        bf16x8 fA[2][2][4], fB[2][2][4];
        u32 pA, pB;
        const u32 xc = wmi * 16384 + wni * 4096;
        wait_vm<6>();
        so = 0;
        kloop<28, 32, 16, 1>(so,
          [&](int s_, auto) { pA = lane * 16 + wmi * 2048 + s_; pB = lane * 16 + wni * 4096 + s_; pin2(pA, pB); },
          [&](auto SET, auto Q) {
            constexpr int set = decltype(SET)::value, q = decltype(Q)::value, s = q >> 3, m = q & 7;
            if constexpr (m < 4) dsrd<s * 8192 + (m >> 1) * 4096 + (m & 1) * 1024>(fA[set][s][m], pA);
            else dsrd<16384 + s * 8192 + (m - 4) * 1024>(fB[set][s][m - 4], pB);
          },
          [&](auto SET, auto M) {
            constexpr int set = decltype(SET)::value, m = decltype(M)::value;
            constexpr int s = (m >> 3) & 1, j = (m >> 1) & 3, i = (m & 1) + (m < 16 ? 2 : 0);
            mfma(acc[i][j], fB[set][s][j], fA[set][s][i]);
          },
          [&](int t) { if (t < 0) { STAMP(0, 16 * pair + 13); } else wait_vm<0>(); },
          [&](auto M) {
            constexpr int k = decltype(M)::value - SND - 1;
            if constexpr (k >= 0 && k < 8 && k % 2 == 0) {
              const f32x4 a = acc[2][k >> 1], b = acc[3][k >> 1];
              const u32x4 v = {pack2(a[0], a[1]), pack2(a[2], a[3]), pack2(b[0], b[1]), pack2(b[2], b[3])};
              st16(__builtin_elementwise_min(v, (u32x4){~1u, ~1u, ~1u, ~1u}), xbox(pair, 0) + xc + lane * 16 + k * 512);
            }
          });
        STAMP(0, 16 * pair + 14);
        u32x4 xv[4];
#pragma nounroll
        for (int spin = 0; spin < (1 << 22); ++spin) {
#pragma unroll
          for (int k = 0; k < 4; ++k)
            xv[k] = __builtin_bit_cast(u32x4, __builtin_amdgcn_raw_buffer_load_b128(rs, lane * 16 + k * 1024, xbox(pair, 1) + xc, 16));
          const u32x4 o = __builtin_elementwise_max(__builtin_elementwise_max(xv[0], xv[1]), __builtin_elementwise_max(xv[2], xv[3]));
          if (!__any(max(max(o[0], o[1]), max(o[2], o[3])) == ~0u)) break;
          asm volatile("" ::: "memory");
        }
#pragma unroll
        for (int k = 0; k < 4; ++k) {
          acc[0][k] += (f32x4){bf_lo(xv[k][0]), bf_hi(xv[k][0]), bf_lo(xv[k][1]), bf_hi(xv[k][1])};
          acc[1][k] += (f32x4){bf_lo(xv[k][2]), bf_hi(xv[k][2]), bf_lo(xv[k][3]), bf_hi(xv[k][3])};
        }
        finish(acc, pair == 0 ? 2 : 4, 4 * pair + 3);
      }
    }
  } else if (wid == 4) {
    {
      FRESH_LANE();
      STAMP(1, 0);
      const __amdgpu_buffer_rsrc_t rx = rsrc(x, SEQ * DM * 2);
      u32 vo[4];
#pragma unroll
      for (int e = 0; e < 4; ++e)
        vo[e] = (8 * e + (lane >> 3)) * 2048 + (((lane & 7) ^ (4 * (e & 1) + (lane >> 4))) * 16) - e * 1024;
      const u32 sx = tmA * 128 * 2048, sh = (tmA > 0 ? tmA * 128 - 16 : 0) * 2048;
      auto half = [&](auto H, int U) {
        constexpr int h = decltype(H)::value;
        U = (U + 2 * rot0) & 15;
        for_n<8>([&](auto E) {
          constexpr int e = decltype(E)::value;
          DMA(rx, (LDS_AS void*)(smem + so + h * SLOT + (e >> 2) * 4096), 16, vo[e & 3],
              sx + (h * 64 + (e >> 2) * 32) * 2048 + U * 128, (e & 3) * 1024, 0);
        });
        DMA(rx, (LDS_AS void*)(smem + so + SLOT + 21504), 16, vo[h], sh + U * 128, h * 1024, 0);
        if constexpr (h == 1) { so += 2 * SLOT; if (so == RING) so = 0; }
      };
#pragma nounroll
      for (int U = 0; U < 3; ++U) { half(IC<0>{}, U); half(IC<1>{}, U); }
      wait_vm<36>();
      bar();
      bar();
#pragma nounroll
      for (int U = 3; U < 16; ++U) {
        wait_vm<18>();
        bar();
        half(IC<0>{}, U);
        bar();
        half(IC<1>{}, U);
      }
      wait_vm<18>(); bar(); bar();
      wait_vm<0>(); bar(); bar(); bar();
    }
    auto phase = [&](auto NSL_, auto HALO_, u32 src, u32 hsrc, int ph  , int tm, int tmh) {
      constexpr int NSL = decltype(NSL_)::value, HALO = decltype(HALO_)::value;
      constexpr int BIG = NSL == 28, PA = BIG ? 16 : 8, PT = PA + HALO, D = BIG ? 3 : NSL == 16 ? DN : DW, X = BIG ? XA2 : 0;
      FRESH_LANE();
      u32 vo[4];
#pragma unroll
      for (int e = 0; e < 4; ++e) vo[e] = qoff(e, lane) - e * 1024;
      poll(flag(ph + 1, (lane & 16) ? tmh : tm, BIG ? 8 * (tnB & 1) + ((rotA >> 1) + (r < D + X ? r : 0))
                                               : NSL == 16 ? (rotB + (r < DN ? r : 0)) & 15 : rotA + (r & (DW > 4 ? 3 : 1))), token());
      STAMP(1, 4 * (ph + 1));
      const u32 flip = BIG ? (tnB & 1) * 16384 : 0;
      auto issue = [&](int u) {
        u = BIG ? (u < 8 ? u ^ (rotA >> 1) : u) : NSL == 16 ? (u + rotB) & 15 : u ^ (2 * rotA);
        if constexpr (BIG && X) wait_vm<47>();
        const u32 ko = aq(NSL == 32 ? u : 2 * u, 0);
        for_n<PA>([&](auto E) {
          constexpr int e = decltype(E)::value;
          DMA(rs, (LDS_AS void*)(smem + so + (e >> 2) * 4096), 16, vo[e & 3],
              src + ko + (NSL == 16 ? aq(e >> 2, 0) : aq(e >> 3, (e >> 2) & 1) + ((e & 4) ? -flip : flip)), (e & 3) * 1024, 16);
        });
        if constexpr (HALO) DMA(rs, (LDS_AS void*)(smem + so + 8192), 16, vo[3], hsrc + ko, 0, 16);
        next_slot<BIG>(so);
      };
      stream<NSL, PT, D, X>(issue, [&]() { bar(); });
    };
#pragma nounroll
    for (int pair = 0; pair < 2; ++pair) {
      const int tmh = tmA > 0 ? tmA - 1 : 0;
      const u32 xq = OFF_X + 2 * pair * XS;
      if (pair) phase(IC<32>{}, IC<1>{}, xq + xpan(tmA, c13), xq + xpan(tmh, c13) + 16384 + 3072, 3, tmA, tmh);
      side();
      bar();
      phase(IC<16>{}, IC<0>{}, OFF_O + pair * XS + xpan(tmB, c13) + aq(0, tnB & 1), 0, 4 * pair, tmB, tmB);
      phase(IC<32>{}, IC<0>{}, xq + XS + xpan(tmA, c13), 0, 4 * pair + 1, tmA, tmA);
      bar();
      so = 0;
      phase(IC<28>{}, IC<0>{}, OFF_H + pair * HS + hpan(tmB, tnB & 1, c13), 0, 4 * pair + 2, tmB, tmB);
      bar();
      so = 0;
    }
  } else if (wid == 5) {
    FRESH_LANE();
    auto phase = [&](auto NSL_, auto PB_, int rot, auto&& piece) {
      constexpr int NSL = decltype(NSL_)::value, PB = decltype(PB_)::value;
      auto issue = [&](int u) {
        u = NSL == 28 ? (u < 8 ? u ^ rot : u) : NSL == 16 ? (u + rot) & 15 : u ^ rot;
        wait_vm<63 - PB>();
        for_n<PB>([&](auto I) { piece(I, u); });
        next_slot<NSL == 28>(so);
      };
      stream<NSL, PB, (NSL == 28 ? 3 : 5), (NSL == 28 ? XB2 : NSL == 16 ? XBO : 0)>(issue, [&]() { bar(); });
    };
    auto tiles = [&](auto NSL_, auto PB_, auto BOFF_, u32 src) {
      constexpr int NSL = decltype(NSL_)::value, PB = decltype(PB_)::value, BOFF = decltype(BOFF_)::value;
      u32 vo[4];
#pragma unroll
      for (int e = 0; e < 4; ++e) vo[e] = qoff(e, lane) - e * 1024;
      phase(NSL_, PB_, NSL == 28 ? rotA >> 1 : NSL == 16 ? rotB : 2 * rotA, [&](auto I, int u) {
        constexpr int i = decltype(I)::value;
        DMA(rs, (LDS_AS void*)(smem + so + BOFF + (i >> 2) * 4096), 16, vo[i & 3],
            PB == 16 ? src + u * (2 * QS) + (i >> 3) * QS + ((i >> 2) & 1) * ((NSL == 28 ? 112 : 32) * QS)
                     : src + u * (4 * QS) + (i >> 2) * QS, (i & 3) * 1024, 16);
      });
    };
#pragma nounroll
    for (int pair = 0; pair < 2; ++pair) {
      {
        const __amdgpu_buffer_rsrc_t rq = rsrc(wqkv, DM * 3 * DM * 2);
        u32 vo[4];
#pragma unroll
        for (int e = 0; e < 4; ++e)
          vo[e] = (8 * e + (lane >> 3)) * 6144 + (((lane & 7) ^ (2 * ((lane >> 4) & 1) + 4 * (e & 1))) * 16) - e * 1024;
        phase(IC<32>{}, IC<12>{}, pair ? 2 * rotA : 0, [&](auto I, int u) {
          constexpr int i = decltype(I)::value;
          if (pair == 0) u = (u + 4 * rot0) & 31;
          DMA(rq, (LDS_AS void*)(smem + so + 9216 + (i >> 2) * 4096), 16, vo[i & 3],
              u * (32 * 6144) + (i >> 2) * 2048 + tnA * 128, (i & 3) * 1024, 0);
        });
      }
      if (pair == 0) bar();
      bar();
      tiles(IC<16>{}, IC<16>{}, IC<8192>{}, OFF_WO + (tnB & 14) * (32 * QS));
      if (pair == 0) poll(flag(F_W1, r, (tnA & 14) + (g & 1)), token());
      tiles(IC<32>{}, IC<14>{}, IC<8192>{}, OFF_W1 + tnA * (32 * 4 * QS));
      bar();
      so = 0;
      if (pair == 0) poll(flag(F_W2, 8 * (tnB & 1) + (r & 7), (tnB & 14) + (r >> 3)), token());
      tiles(IC<28>{}, IC<16>{}, IC<16384>{}, OFF_W2 + ((tnB & 14) * 112 + (tnB & 1) * 56) * QS);
      bar();
      so = 0;
    }
  } else if (wid == 6) {
    FRESH_LANE();
    const int rr = lane >> 3, c16 = (lane & 7) * 16;
    const __amdgpu_buffer_rsrc_t rq = rsrc(wqkv, DM * 3 * DM * 2);
    if (R3W)
      DMA(rq, (LDS_AS void*)(smem + L_PF), 4, lane < 24 && !(xcd & 1) ? ((rot0 * 128 + idx * 8 + (lane & 7)) & 1023) * 6144 + (lane >> 3) * 2048 : 0x80000000u,
          (xcd & 3) * 512 + 384, 0, 0);
    u32x4 U[7][4], F[4];
    auto u_load = [&](auto I) {
      constexpr int i = decltype(I)::value;
#pragma unroll
      for (int e = 0; e < 4; ++e) {
        if constexpr (i < 2)
          U[(i + 5) % 7][e] = __builtin_bit_cast(u32x4, __builtin_amdgcn_raw_buffer_load_b128(rsrc(wo, DM * DM * 2),
              ((rr & 3) + 16 * (rr >> 2)) * 2048 + c16, ((2 * tmA + i) * 32 + 4 * e) * 2048 + (tnA ^ (4 * i)) * 128, UAUX));
        else if constexpr (i < 9)
          U[(i + 5) % 7][e] = __builtin_bit_cast(u32x4, __builtin_amdgcn_raw_buffer_load_b128(rsrc(w1, DM * FF * 2),
              rr * 7168 + c16, ((2 * tmA + (tnA & 1)) * 32 + 8 * e) * 7168 + (7 * (tnA >> 1) + i - 2) * 128, UAUX));
        else
          U[(i + 5) % 7][e] = __builtin_bit_cast(u32x4, __builtin_amdgcn_raw_buffer_load_b128(rsrc(w2, DM * FF * 2),
              rr * 2048 + c16, ((7 * tmA + i - 9) * 32 + 8 * e) * 2048 + tnA * 128, UAUX));
      }
    };
    auto u_write = [&](auto I) {
      constexpr int i = decltype(I)::value;
#pragma unroll
      for (int e = 0; e < 4; ++e) {
        const u32x4 v = U[(i + 5) % 7][e];
        *(u32x4*)(smem + L_IM + e * 1024 + rr * 128 + (c16 ^ ((rr & 2) << 5))) = (e & 1) ? (u32x4){v[2], v[3], v[0], v[1]} : v;
      }
    };
    auto u_store = [&](auto I) {
      constexpr int i = decltype(I)::value;
      const u32 sw = ((r & 8) << 3) | ((g & 1) << 3);
      const char* im = smem + L_IM + (8 * g + (r >> 2)) * 128;
      for_n<4>([&](auto E) {
        constexpr int e = decltype(E)::value;
        constexpr bool split = i >= 2 && i < 9 && (2 * (i - 2) + (e >> 1)) % 7 == 3;
        const char* p = im + (((e >> 1) * 64 + (split ? (e & 1) * 32 + (r & 3) * 8 : (r & 3) * 16 + (e & 1) * 8)) ^ sw);
        const u32x2 lo = __builtin_bit_cast(u32x2, __builtin_amdgcn_ds_read_tr16_b64_v4i16((LDS_AS s16x4*)p));
        const u32x2 hi = __builtin_bit_cast(u32x2, __builtin_amdgcn_ds_read_tr16_b64_v4i16((LDS_AS s16x4*)(p + 512)));
        F[e] = (u32x4){lo[0], lo[1], hi[0], hi[1]};
      });
      for_n<4>([&](auto E) {
        constexpr int e = decltype(E)::value;
        if constexpr (i < 2) st16(F[e], OFF_WO + ((tnA ^ (4 * i)) * 32 + 2 * tmA + i) * QS + qoff(e, lane));
        else if constexpr (i < 9) {
          constexpr int f = w1_frag(i, e);
          st16(F[e], OFF_W1 + ((((tnA & 14) + (f >> 4)) * 32 + 2 * tmA + (tnA & 1)) * 4 + ((f & 15) >> 2)) * QS + qoff(f & 3, lane));
        } else st16(F[e], OFF_W2 + (tnA * 112 + (tmA >> 3) * 56 + hpos(tmA, i - 9)) * QS + qoff(e, lane));
      });
    };
    auto u_sums = [&](auto I) {
      constexpr int i = decltype(I)::value;
      constexpr u32 K = w1_frag(i, 0) | (w1_frag(i, 1) << 8) | (w1_frag(i, 2) << 16) | (w1_frag(i, 3) << 24);
      f32x4 cs = {0.f, 0.f, 0.f, 0.f};
#pragma unroll
      for (int e = 0; e < 4; ++e) {
        const u32 o = (r >> 2) == e ? 0x3f803f80u : 0u;
        cs = __builtin_amdgcn_mfma_f32_16x16x32_bf16(__builtin_bit_cast(bf16x8, (u32x4){o, o, o, o}), __builtin_bit_cast(bf16x8, F[e]), cs, 0, 0, 0);
      }
      const u32 f = (K >> (8 * g)) & 255;
      __builtin_amdgcn_raw_buffer_store_b32(__float_as_uint(cs[0]), rs,
          OFF_CS1 + ((((tnA & 14) + (f >> 4)) * 32 + 2 * tmA + (tnA & 1)) * 256 + (f & 15) * 16 + r) * 4, 0, 16);
    };
    u64 tok = 0;
    auto up = [&](int set) { st8(tok, lane == 0, flag(set, tmA, tnA)); };
    f32x4 cv[32], ca[2];
    auto cs_load = [&](u32 src, int p0, int p1) {
#pragma unroll
      for (int p = p0; p < p1; ++p)
        cv[p] = __builtin_bit_cast(f32x4, __builtin_amdgcn_raw_buffer_load_b128(rs, lane * 16 + (p & 3) * 1024, src + (p >> 2) * 4096, 16));
    };
    auto cs_add = [&](int p0, int p1) {
#pragma unroll
      for (int p = p0; p < p1; ++p) {
        asm volatile("" : "+v"(cv[p]));
        if (p < 2) ca[p] = cv[p];
        else {
#pragma unroll
          for (int k = 0; k < 4; ++k) {
            float t = ca[p & 1][k];
            const float u = cv[p][k];
            asm("v_add_f32 %0, %0, %1" : "+v"(t) : "v"(u));
            ca[p & 1][k] = t;
          }
        }
      }
      asm volatile("" : "+v"(ca[0]), "+v"(ca[1]));
    };
    u64 vf = 0;
    const u32 fo = flag(F_WO, r, ((tnB & 14) + (g & 1)) ^ (2 * (g & 2)));
    __builtin_amdgcn_s_sleep(PFW0);
    u_load(IC<0>{});
    u_load(IC<1>{});
    for_n<34>([&](auto S) {
      constexpr int s = decltype(S)::value;
      bar();
      if constexpr (s == TOKB) { tok = token(); asm volatile("" : "+s"(tok)); }
      if constexpr (s >= HL && s < HL + 4) {
        if constexpr ((s - HL) % 2 == 0) u_write(IC<(s - HL) / 2>{}); else u_store(IC<(s - HL) / 2>{});
      }
      if constexpr (s == HL + 3 + FLD) { wait_vm<0>(); up(F_WO); }
      if constexpr (s == G0 - 4) vf = ldf(fo);
      if constexpr (s == G0 - 1) { if (!__all(vf == tok)) poll(fo, tok); }
      if constexpr (s == G0) for_n<4>([&](auto I) { u_load(IC<2 + decltype(I)::value>{}); });
      if constexpr (s == G0 + 1) for_n<3>([&](auto I) { u_load(IC<6 + decltype(I)::value>{}); });
    });
    static_assert(TOKB < HL && HL + 3 + FLD < G0 - 4 && G0 + 1 < 33, "schedule");
    for_n<7>([&](auto I) {
      constexpr int i = 2 + decltype(I)::value;
      u_write(IC<i>{});
      u_store(IC<i>{});
      u_sums(IC<i>{});
    });
    bar(); bar(); bar();
    wait_vm<0>();
    up(F_W1);
#pragma nounroll
    for (int u = 3; u <= G1; ++u) bar();
    for_n<7>([&](auto I) {
      u_load(IC<9 + decltype(I)::value>{});
      if constexpr (decltype(I)::value == 1 || decltype(I)::value == 3) bar();
    });
#pragma nounroll
    for (int u = G1 + 3; u < 16 + H2; ++u) bar();
    for_n<14>([&](auto S) {
      constexpr int s = decltype(S)::value, i = 9 + s / 2;
      bar();
      if constexpr (s % 2 == 0) u_write(IC<i>{}); else u_store(IC<i>{});
    });
    for_n<12>([&](auto S) {
      constexpr int s = decltype(S)::value;
      bar();
      if constexpr (s == 3) { wait_vm<16>(); up(F_W2); }
      if constexpr (s < 6) cs_load(OFF_CS1 + tnA * 32768, s * 16 / 3, (s + 1) * 16 / 3);
      if constexpr (s >= 8) cs_add(8 * (s - 8), 8 * (s - 7));
    });
    static_assert(H2 + 14 + 12 <= 31, "schedule");
    *(f32x4*)(smem + L_CS1 + lane * 16) = ca[0] + ca[1];
    wait_lgkm0();
    QUIT();
  } else {
    FRESH_LANE();
    const int tmh = tmA > 0 ? tmA - 1 : 0;
    u64 tok;
    auto bars = [&](int n) {
#pragma nounroll
      for (int u = 0; u < n; ++u) bar();
    };
    auto ror8 = [](float v) { return __uint_as_float(__builtin_amdgcn_mov_dpp(__float_as_uint(v), 0x128, 0xf, 0xf, false)); };
    auto put = [&](int xi, float mean, float m2, bool on, int row) {
      const float sd = __builtin_amdgcn_sqrtf(bfr(bfr(m2 * (1.f / DM)) + LN_EPS));
      st8(pack2(mean, sd) | (tok << 32), on, flag(F_LN + xi, tmA, tnA) + row * 8);
    };
    auto reduce = [&](int xi) {
      const u32 src = OFF_ST + xi * 524288 + span(tmA, c13) + (tnA >> 2) * 16384 + (tnA & 3) * 2048, tag = (u32)tok;
      u32x4 a, b;
      auto ld = [&]() {
        asm volatile("" ::: "memory");
        a = __builtin_bit_cast(u32x4, __builtin_amdgcn_raw_buffer_load_b128(rs, lane * 16, src, 16));
        b = __builtin_bit_cast(u32x4, __builtin_amdgcn_raw_buffer_load_b128(rs, lane * 16 + 1024, src, 16));
      };
      ld();
      bar(); bar(); bar();
#pragma nounroll
      for (int spin = 0; spin < (1 << 22); ++spin) {
        if (__all((((a[1] ^ tag) | (a[3] ^ tag)) | ((b[1] ^ tag) | (b[3] ^ tag))) == 0)) break;
        __builtin_amdgcn_s_sleep(1);
        ld();
      }
      float m = __uint_as_float(a[0]) + __uint_as_float(b[0]);
      m = xsum(m + ror8(m)) * (1.f / 16.f);
      const float d0 = __uint_as_float(a[0]) - m, d1 = __uint_as_float(b[0]) - m;
      float q = __uint_as_float(a[2]) + __uint_as_float(b[2]) + 64.f * (d0 * d0 + d1 * d1);
      asm volatile("" : "+v"(m), "+v"(q));
      bar();
      asm volatile("" : "+v"(m), "+v"(q));
      put(xi, m, xsum(q + ror8(q)), lane < 8, lane);
    };
    auto gather = [&](auto H, int xi) {
      constexpr int n = 2 + decltype(H)::value;
      const u32 tag = (u32)tok, f0 = flag(F_LN + xi, tmA, lane >> 3) + (lane & 7) * 8;
      const u32 f[3] = {f0, f0 + 1024, flag(F_LN + xi, tmh, 14 + (r >> 3)) + (lane & 7) * 8};
      u32x2 v[3];
      auto ld = [&]() {
#pragma unroll
        for (int h = 0; h < n; ++h) v[h] = __builtin_bit_cast(u32x2, ldf(f[h]));
      };
      ld();
      bar(); bar(); bar();
#pragma nounroll
      for (int spin = 0; spin < (1 << 22); ++spin) {
        if (__all(((v[0][1] ^ tag) | (v[1][1] ^ tag) | (v[n - 1][1] ^ tag)) == 0)) break;
        __builtin_amdgcn_s_sleep(1);
        ld();
      }
#pragma unroll
      for (int h = 0; h < n; ++h) {
        const float iv = __builtin_amdgcn_rcpf(bf_hi(v[h][0]));
        const int o = h < 2 ? 64 + 256 * h + lane * 4 : r * 4;
        *(float*)(smem + L_INV + o) = iv;
        *(float*)(smem + L_MNV + o) = bf_lo(v[h][0]) * iv;
      }
      wait_lgkm0();
    };
    auto guard = [&](u32 f, int K, auto&& lim_, auto&& step) {
      int b = 0, lim = 0;
#pragma nounroll
      for (int spin = 0; spin < (1 << 22); ++spin) {
        const u64 v = ldf(f);
        if (b < lim) { bar(); step(b++); } else __builtin_amdgcn_s_sleep(1);
        if (b < lim) { bar(); step(b++); }
        lim = lim_((u32)__ballot(v == tok));
        if (lim >= 100) break;
        lim = min(K, lim);
      }
#pragma nounroll
      while (b < K) { bar(); step(b++); }
    };
    auto ahead = [](int msk, int spp, int D) {
      return [=](u32 m) { const int n = __builtin_ctz(~(m & (m >> 16))); return n >= 16 ? 100 : (n & msk) * spp - D; };
    };
    const __amdgpu_buffer_rsrc_t rx = rsrc(x, SEQ * DM * 2), rq = rsrc(wqkv, DM * 3 * DM * 2);
    u32 wx, ww;
    {
      const int n = idx * 64 + lane, m = idx * 48 + (lane < 48 ? lane : 47), k = (m * 5462) >> 16, p = m - 12 * k;
      wx = (n & 511) * 2048 + ((n >> 9) & 1) * 128 + ((idx >> 4) * 1024 + (xcd >> 2) * 512) * 2048;
      ww = k * 6144 + (p >> 2) * 2048 + (p & 3) * 128 + (xcd & 3) * 512;
    }
    {
      const u64 ep = ldf(OFF_EP);
      u16 bv[4];
#pragma unroll
      for (int h = 0; h < 4; ++h) {
        const int t = min(lane + 64 * h, 223), f = t >> 4, rho = t & 15, wn = f / 7, j = f % 7;
        bv[h] = b1[tnA * 224 + (j == 6 ? 96 + 16 * wn + rho : (4 * wn + (j >> 1)) * 32 + 8 * (rho >> 2) + 4 * (j & 1) + (rho & 3))];
      }
      DMA(rsrc(b2, DM * 2), (LDS_AS void*)(smem + L_B2), 4, lane * 4, (tnB & 14) * 128, 0, 0);
      pin2(wx, ww);
      __builtin_amdgcn_s_sleep(XW);
      bf16x8 xf[16];
#pragma unroll
      for (int e = 0; e < 16; ++e)
        xf[e] = __builtin_bit_cast(bf16x8, __builtin_amdgcn_raw_buffer_load_b128(rx, (r >> 1) * 2048 + (r & 1) * 64 + g * 16 + e * 128,
            (tmA * 128 + tnA * 8) * 2048, 0));
      auto probe = [&](int tn) { return flag(F_PR, tmA ^ (4 * (idx & 1)), tn); };
      const u32x4 one4 = {0x3f803f80u, 0x3f803f80u, 0x3f803f80u, 0x3f803f80u};
      const bf16x8 ones = __builtin_bit_cast(bf16x8, one4);
      f32x4 sm[2], sq[2];
      u32 tlb;
      u64 vt;
      for_n<LG0>([&](auto S) {
        constexpr int s = decltype(S)::value;
        bar();
        if constexpr (s == TOKB - 1) {
          __builtin_amdgcn_raw_buffer_store_b32(0, rs, blockIdx.x < NVOTE ? probe(tnA ^ 4) + (lane & 31) * 4 : 0x80000000u, 0, 16);
#pragma unroll
          for (int h = 0; h < 4; ++h) ((float*)(smem + L_BS))[lane + 64 * h] = bf_lo(bv[h]);
          tok = ((u64)(u32)__builtin_amdgcn_readfirstlane((u32)ep) | ((u64)(u32)__builtin_amdgcn_readfirstlane((u32)(ep >> 32)) << 32))
                * 6364136223846793005ull + 1442695040888963407ull;
          *(LDS_AS u32x4*)(smem + L_CNT) = (u32x4){0u, 0u, (u32)tok, (u32)(tok >> 32)};
          wait_lgkm0();
          asm volatile("s_load_dword %0, %1, 0x0" : "=s"(tlb) : "s"(ws + OFF_EP + 128) : "memory");
        }
        if constexpr (s >= PF0 && (s - PF0) % PFS == 0 && (s - PF0) / PFS < 8 - PFJ) {
          const int j = (PFJ + (s - PF0) / PFS + rot0) & 7;
          DMA(rx, (LDS_AS void*)(smem + L_PF), 4, wx, j * 256, 0, 0);
          DMA(rq, (LDS_AS void*)(smem + L_PF), 4, ww, j * (128 * 6144), 0, 0);
        }
        if constexpr (s >= XP && s < XP + 4) {
#pragma unroll
          for (int e = 4 * (s - XP); e < 4 * (s - XP) + 4; ++e) {
            const f32x4 z = {0.f, 0.f, 0.f, 0.f};
            asm volatile("" : "+v"(xf[e]));
            sm[e & 1] = __builtin_amdgcn_mfma_f32_16x16x32_bf16(ones, xf[e], e < 2 ? z : sm[e & 1], 0, 0, 0);
            sq[e & 1] = __builtin_amdgcn_mfma_f32_16x16x32_bf16(xf[e], xf[e], e < 2 ? z : sq[e & 1], 0, 0, 0);
          }
        }
        if constexpr (s >= XP && s <= XP + 4) asm volatile("" : "+v"(sm[0]), "+v"(sm[1]), "+v"(sq[0]), "+v"(sq[1]));
        if constexpr (s == XP + 4) {
          const f32x4 q4 = sq[0] + sq[1];
          float q = (r & 2) ? ((r & 1) ? q4[3] : q4[2]) : ((r & 1) ? q4[1] : q4[0]), t = sm[0][0] + sm[1][0];
          q += __uint_as_float(__builtin_amdgcn_mov_dpp(__float_as_uint(q), 0xb1, 0xf, 0xf, false));
          t += __uint_as_float(__builtin_amdgcn_mov_dpp(__float_as_uint(t), 0xb1, 0xf, 0xf, false));
          const float m = t * (1.f / DM);
          put(0, m, fmaxf(__builtin_fmaf(-t, m, q), 0.f), g == (r >> 2) && !(r & 1), r >> 1);
        }
        if constexpr (s == PRB) {
          wait_lgkm0();
          asm volatile("" :: "s"(tlb));
          if (blockIdx.x < NVOTE) {
            u64 t0, t1;
            u32 v;
            asm volatile("s_memtime %0\n\ts_waitcnt lgkmcnt(0)\n\ts_load_dword %2, %3, 0x0\n\ts_waitcnt lgkmcnt(0)\n\t"
                         "s_memtime %1\n\ts_waitcnt lgkmcnt(0)" : "=&s"(t0), "=&s"(t1), "=&s"(v) : "s"(ws + probe(tnA)) : "memory");
            const u32 t = min((u32)t1 - (u32)t0, 4095u);
            st8(tok ^ t, lane == 0, flag(F_XC, 0, blockIdx.x));
          }
        }
        if constexpr (s == LG0 - 4) vt = ldf(flag(F_XC, 0, r));
      });
      static_assert(XP >= TOKB && PRB > XP + 4 && LG0 - 4 > PRB && LG0 + 3 <= 31 && L_CNT % 16 == 0, "schedule");
#pragma nounroll
      for (int spin = 0; spin < (1 << 22); ++spin) {
        if (__all((vt ^ tok) < 4096)) break;
        __builtin_amdgcn_s_sleep(1);
        vt = ldf(flag(F_XC, 0, r));
      }
      const u32 t = (u32)(vt ^ tok);
      const u64 slower = __ballot(t > (u32)__builtin_amdgcn_mov_dpp(t, 0x128, 0xf, 0xf, false));
      const u32 c = __builtin_popcount((u32)slower & 255) > 4 ? 8192 : 0;
      *(volatile LDS_AS u32*)(smem + L_C13) = c;
      gather(IC<1>{}, 0);
      c13 = c;
      bars(34 - CLB - (LG0 + 3));
    }
#pragma nounroll
    for (int pair = 0; pair < 2; ++pair) {
#pragma unroll
      for (int e = 0; e < 16; ++e)
        st16(((u32x4){~0u, ~0u, ~0u, ~0u}), xbox(pair, 0) + (e + (e & 8)) * 1024 + lane * 16);
      bars(CLB);
      wait_vm<0>();
      guard(flag(4 * pair + 1, tmB, (rotB + r) & 15), 16, ahead(15, 1, DN), [](int) {});
      guard(flag(4 * pair + 2, tmA, (rotA + r) & 15), KW1, ahead(14, 2, DW), [](int) {});
      reduce(2 * pair + 1);
      bars(LG - (KW1 + 4));
      gather(IC<0>{}, 2 * pair + 1);
      bars(33 - (LG + 3));
      guard(flag(lane < 8 ? 4 * pair + 3 : lane < 16 ? F_2ND + pair : F_3RD + pair, tmB, 8 * (tnB & 1) + ((lane < 8 ? r ^ (rotA >> 1) : r) & 7)), 13,
            [](u32 m) { const int n = __builtin_ctz(~m | (1u << 24)); return n >= 24 ? 100 : min(n, 16) - 3; },
            [&](int b) { if (b == 0 || b == 3) st8(tok, lane == 0, flag(b ? F_3RD + pair : F_2ND + pair, tmA, tnA)); });
      if (pair) break;
      bars(WQ - 13);
      for_n<29 - WQ>([&](auto S) {
        constexpr int s = decltype(S)::value;
        if constexpr (s < 4) {
          DMA(rq, (LDS_AS void*)(smem + L_PF), 4, ww, ((2 * s + (rotA >> 1)) & 7) * (128 * 6144), 0, 0);
          DMA(rq, (LDS_AS void*)(smem + L_PF), 4, ww, ((2 * s + 1 + (rotA >> 1)) & 7) * (128 * 6144), 0, 0);
        }
        bar();
      });
      guard(flag(4, (g & 1) ? tmh : tmA, (rotA + r) & 15), KW1, ahead(14, 2, DW), [](int) {});
      st8(tok, lane == 0 && blockIdx.x == 0, OFF_EP);
      reduce(2);
      bars(LG - (KW1 + 4));
      gather(IC<1>{}, 2);
      bars(33 - CLB - (LG + 3));
      static_assert(WQ <= 25 && LG + 3 <= 31, "schedule");
    }
    QUIT();
  }
#endif
}

extern "C" void kernel_launch(void* const* d_in, const int* in_sizes, int n_in, void* d_out, int out_size,
                              void* d_ws, size_t ws_size, hipStream_t stream) {
  const u16* x0 = (const u16*)d_in[0];
  const u16* wqkv = (const u16*)d_in[1];
  const u16* wo = (const u16*)d_in[2];
  const u16* w1 = (const u16*)d_in[4];
  const u16* b1 = (const u16*)d_in[5];
  const u16* w2 = (const u16*)d_in[6];
  const u16* b2 = (const u16*)d_in[7];
  (void)hipFuncSetAttribute((const void*)mega_k, hipFuncAttributeMaxDynamicSharedMemorySize, LDS_BYTES);
  mega_k<<<256, 512, LDS_BYTES, stream>>>((char*)d_ws, x0, wqkv, wo, w1, b1, w2, b2, (u16*)d_out);
}
```

```cpp
#include <hip/hip_runtime.h>
#include <stdint.h>
#include <type_traits>
#include <utility>

typedef unsigned short u16;
typedef unsigned int u32;
typedef unsigned long long u64;
typedef __bf16 bf16x8 __attribute__((ext_vector_type(8)));
typedef __bf16 bf16x2 __attribute__((ext_vector_type(2)));
typedef short s16x4 __attribute__((ext_vector_type(4)));
typedef float f32x4 __attribute__((ext_vector_type(4)));
typedef float f32x2 __attribute__((ext_vector_type(2)));
typedef u32 u32x4 __attribute__((ext_vector_type(4)));
typedef u32 u32x2 __attribute__((ext_vector_type(2)));
typedef u32 u32x10 __attribute__((ext_vector_type(10)));
typedef int i32x4 __attribute__((ext_vector_type(4)));
typedef int i32x2 __attribute__((ext_vector_type(2)));
#define LDS_AS __attribute__((address_space(3)))
#define GLB_AS __attribute__((address_space(1)))

constexpr int SEQ = 2048, DM = 1024, FF = 3584;
constexpr float LN_EPS = 1e-5f;

#define DN 4
#define DW 5
#define UAUX 2
#define PFW0 30
#define PF0 4
#define PFS 2
#define PFJ 2
#define CQP 5
#define HL 8
#define FLD 3
#define XW 30
#define TOKB 3
#define XP 8
#define LG0 24
#define LG 25
#define WQ 24
#define G0 27
#define G1 12
#define H2 4
#define PUBJ 2
#define CB 1
#define KW1 11
#define CLB 1
#define SND 12
#define RST 9
#define XB2 1
#define XA2 1
#define XBO 1
#define CW 1
#define PRB 13
#define CQG 22
#define R3W 1
#ifndef NVOTE
#define NVOTE 16
#endif
constexpr u32 MB = 1u << 20;
constexpr u32 QS = 5120;
constexpr u32 XS = 8 * MB, HS = 32 * MB;
constexpr u32 OFF_WO = 0;
constexpr u32 OFF_W1 = 3 * MB;
constexpr u32 OFF_W2 = 13 * MB;
constexpr u32 OFF_CS1 = 22 * MB;
constexpr u32 OFF_FL = 25 * MB;
constexpr u32 OFF_EP = OFF_FL + 26 * 32768;
constexpr u32 OFF_DBG = 26 * MB;
constexpr u32 OFF_XC = 28 * MB;
constexpr u32 OFF_X = 28 * MB;
constexpr u32 OFF_O = OFF_X + 4 * XS;
constexpr u32 OFF_H = OFF_O + 2 * XS;
constexpr u32 OFF_ST = OFF_H + 2 * HS;
constexpr u32 WS_USED = OFF_ST + 2 * MB, WS_ALIGN = 4 * MB;
#define HAS_ALIGN
constexpr int F_WO = 9, F_W1 = 10, F_W2 = 11, F_2ND = 12, F_3RD = 14, F_XC = 18, F_PR = 19, F_LN = 22;
__device__ __forceinline__ u32 qoff(u32 e, u32 slot) {
  return slot * 16 + (e * 1152 + (e == 3 ? 128 : 0)) + (slot >= (e == 3 ? 56 : 24 - 8 * e) ? 128 : 0);
}
__device__ __forceinline__ u32 hpos(u32 t, u32 j) {
  return (j & 2) == 0 ? 16 * (j & 1) + 2 * (t & 7) + (j >> 2) : 32 + 3 * (t & 7) + (j & 1) + (j >> 2) * 2;
}
__device__ __forceinline__ u32 aq(u32 kg, u32 half) { return kg * 32768 + (kg & 1) * 3072 + half * 16384; }
__device__ __forceinline__ u32 ppair(u32 tm) { return (tm & 3) + (tm >> 3) * 4; }
__device__ __forceinline__ u32 xpan(u32 tm, u32 c13) { return ppair(tm) * MB + (((((tm >> 2) ^ (tm >> 1)) & 1) << 13) ^ c13); }
__device__ __forceinline__ u32 hpan(u32 tm, u32 kh, u32 c13) {
  return ppair(tm) * (4 * MB) + (((kh ^ tm) & 1) << 21) + (((((tm >> 2) ^ kh ^ tm) & 1) << 13) ^ c13);
}
__device__ __forceinline__ u32 span(u32 tm, u32 c13) { return ppair(tm) * 65536 + (((tm & 4) << 11) ^ c13); }

constexpr int SLOT = 24576, NRING = 6, RING = SLOT * NRING;
constexpr int L_INV = RING, L_MNV = L_INV + 576, L_CNT = L_MNV + 576, L_TOK = L_CNT + 8, L_C13 = L_TOK + 8;
constexpr int L_CSQ = RING + 1280, L_CS1 = L_CSQ + 1024, L_BS = L_CS1 + 1024;
constexpr int L_XC = L_BS + 1024;
constexpr int L_IM = L_XC + 6144;
constexpr int L_B2 = L_IM + 4096, L_PF = L_B2 + 256;
constexpr int LDS_BYTES = L_PF + 256;

__device__ __forceinline__ float bf_lo(u32 w) { return __uint_as_float(w << 16); }
__device__ __forceinline__ float bf_hi(u32 w) { return __uint_as_float(w & 0xffff0000u); }
__device__ __forceinline__ u32 pack2(float lo, float hi) {
  f32x2 v = {lo, hi};
  bf16x2 r = __builtin_convertvector(v, bf16x2);
  return __builtin_bit_cast(u32, r);
}
__device__ __forceinline__ float bfr(float x) { return bf_lo(pack2(x, 0.f) & 0xffffu); }
__device__ __forceinline__ u32 gelu_tanh2(f32x2 x) {
  const float c0 = -2.3022081983f, c1 = -0.1029432396f;
  const f32x2 p = x * x * c1 + c0;
  const f32x2 a = x * p;
  const f32x2 d = (f32x2){__builtin_amdgcn_exp2f(a[0]), __builtin_amdgcn_exp2f(a[1])} + 1.f;
  const f32x2 g = x * (f32x2){__builtin_amdgcn_rcpf(d[0]), __builtin_amdgcn_rcpf(d[1])};
  return pack2(g[0], g[1]);
}
template <int N_> __device__ __forceinline__ void wait_vm() { asm volatile("s_waitcnt vmcnt(%0)" ::"n"(N_) : "memory"); }
__device__ __forceinline__ void wait_lgkm0() { asm volatile("s_waitcnt lgkmcnt(0)" ::: "memory"); }
__device__ __forceinline__ void bar() {
  __builtin_amdgcn_sched_barrier(0);
  asm volatile("s_barrier" ::: "memory");
  __builtin_amdgcn_sched_barrier(0);
}
template <int V> using IC = std::integral_constant<int, V>;
template <typename F, int... I>
__device__ __forceinline__ void for_seq(F&& f, std::integer_sequence<int, I...>) { (f(IC<I>{}), ...); }
template <int N, typename F> __device__ __forceinline__ void for_n(F&& f) { for_seq(f, std::make_integer_sequence<int, N>{}); }

__device__ __forceinline__ float xsum(float x) {
#if defined(__HIP_DEVICE_COMPILE__)
  auto a = __builtin_amdgcn_permlane16_swap(__float_as_uint(x), __float_as_uint(x), false, false);
  x = __uint_as_float(a[0]) + __uint_as_float(a[1]);
  auto b = __builtin_amdgcn_permlane32_swap(__float_as_uint(x), __float_as_uint(x), false, false);
  x = __uint_as_float(b[0]) + __uint_as_float(b[1]);
#endif
  return x;
}
__device__ __forceinline__ float xmax(float x) {
#if defined(__HIP_DEVICE_COMPILE__)
  auto a = __builtin_amdgcn_permlane16_swap(__float_as_uint(x), __float_as_uint(x), false, false);
  x = fmaxf(__uint_as_float(a[0]), __uint_as_float(a[1]));
  auto b = __builtin_amdgcn_permlane32_swap(__float_as_uint(x), __float_as_uint(x), false, false);
  x = fmaxf(__uint_as_float(b[0]), __uint_as_float(b[1]));
#endif
  return x;
}
__device__ __forceinline__ f32x2 row_part(const u32x4& c0, const u32x4& c1) {
  float v[16];
#pragma unroll
  for (int k = 0; k < 4; ++k) {
    v[2 * k] = bf_lo(c0[k]); v[2 * k + 1] = bf_hi(c0[k]);
    v[8 + 2 * k] = bf_lo(c1[k]); v[8 + 2 * k + 1] = bf_hi(c1[k]);
  }
  float s = 0.f;
#pragma unroll
  for (int k = 0; k < 16; ++k) s += v[k];
  const float m = xsum(s) * (1.f / 64.f);
  float q = 0.f;
#pragma unroll
  for (int k = 0; k < 16; ++k) { const float d = v[k] - m; q += d * d; }
  return (f32x2){m, xsum(q)};
}

#define st16(v, off) __builtin_amdgcn_raw_buffer_store_b128(__builtin_bit_cast(i32x4, v), rs, (off), 0, 16)
__device__ __forceinline__ float sum8(const u32x4& f) {
  float s = bf_lo(f[0]) + bf_hi(f[0]);
#pragma unroll
  for (int k = 1; k < 4; ++k) { s += bf_lo(f[k]); s += bf_hi(f[k]); }
  return s;
}
__device__ __forceinline__ float fold32(float a, float b) {
#if defined(__HIP_DEVICE_COMPILE__)
  const auto x = __builtin_amdgcn_permlane32_swap(__float_as_uint(a), __float_as_uint(b), false, false);
  a = __uint_as_float(x[0]) + __uint_as_float(x[1]);
#endif
  return a;
}
__device__ __forceinline__ float fold16(float a, float b) {
#if defined(__HIP_DEVICE_COMPILE__)
  const auto x = __builtin_amdgcn_permlane16_swap(__float_as_uint(a), __float_as_uint(b), false, false);
  a = __uint_as_float(x[0]) + __uint_as_float(x[1]);
#endif
  return a;
}

__device__ __forceinline__ int fresh_lane(int tid) { int l = tid & 63; asm volatile("" : "+v"(l)); return l; }
__device__ __forceinline__ f32x4 zero4() {
  f32x4 z = {0.f, 0.f, 0.f, 0.f};
  asm volatile("" : "+v"(z));
  return z;
}
template <int BIG = 0> __device__ __forceinline__ void next_slot(int& so) {
  if constexpr (BIG) so = (so + 32768) & 131071; else { so += SLOT; if (so == RING) so = 0; }
}

#define DMA __builtin_amdgcn_raw_ptr_buffer_load_lds
#define SB() __builtin_amdgcn_sched_barrier(0)
__device__ __forceinline__ void mfma(f32x4& c, const bf16x8& a, const bf16x8& b) {
  asm volatile("v_mfma_f32_16x16x32_bf16 %0, %1, %2, %0" : "+v"(c) : "v"(a), "v"(b));
}
template <int OFF> __device__ __forceinline__ void dsrd(bf16x8& d, u32 addr) {
  asm volatile("ds_read_b128 %0, %1 offset:%2" : "=v"(d) : "v"(addr), "n"(OFF));
}
template <int OFF> __device__ __forceinline__ void dstr(bf16x8& d, u32 addr) {
  u32x2 lo, hi;
  asm volatile("ds_read_b64_tr_b16 %0, %2 offset:%3\n\tds_read_b64_tr_b16 %1, %2 offset:%4"
               : "=&v"(lo), "=&v"(hi) : "v"(addr), "n"(OFF), "n"(OFF + 512));
  const u32x4 v = {lo[0], lo[1], hi[0], hi[1]};
  d = __builtin_bit_cast(bf16x8, v);
}
template <int OFF> __device__ __forceinline__ void dstr1(u32x2& d, u32 addr) {
  asm volatile("ds_read_b64_tr_b16 %0, %1 offset:%2" : "=v"(d) : "v"(addr), "n"(OFF));
}
template <int OFF> __device__ __forceinline__ void dsrd1(float& d, u32 addr) {
  asm volatile("ds_read_b32 %0, %1 offset:%2" : "=v"(d) : "v"(addr), "n"(OFF));
}
template <int O0, int O1> __device__ __forceinline__ void dsrd2(f32x2& d, u32 addr) {
  asm volatile("ds_read2_b32 %0, %1 offset0:%2 offset1:%3" : "=v"(d) : "v"(addr), "n"(O0), "n"(O1));
}
template <int OFF> __device__ __forceinline__ void dsrd4(f32x4& d, u32 addr) {
  asm volatile("ds_read_b128 %0, %1 offset:%2" : "=v"(d) : "v"(addr), "n"(OFF));
}
template <class T> __device__ __forceinline__ void pin(T& a) { asm volatile("" : "+v"(a)); }
template <class T, class... R> __device__ __forceinline__ void pin(T& a, R&... r) { pin(a); pin(r...); }
template <class F> constexpr u64 lanes(F f) {
  u64 m = 0;
  for (int l = 0; l < 64; ++l) m |= (u64)(f(l & 15, l >> 4) ? 1 : 0) << l;
  return m;
}
constexpr u64 M_PAR = lanes([](int r, int) { return r & 1; });
constexpr u64 M_OWN0 = lanes([](int r, int g) { return 4 * g + (r & 1) <= r; });
constexpr u64 M_OWN1 = lanes([](int r, int g) { return 4 * g + (r & 1) + 2 <= r; });
#define LANE_IN(m) __builtin_amdgcn_inverse_ballot_w64(m)
__device__ __forceinline__ void pin1(u32& a) { asm volatile("" : "+v"(a)); }
__device__ __forceinline__ void pin2(u32& a, u32& b) { asm volatile("" : "+v"(a), "+v"(b)); }
template <int NSL, int NM, int NR, int ENDB, class AD, class RD, class MM, class HK, class TL>
__device__ __forceinline__ void kloop(int& so, AD&& ad, RD&& rd, MM&& mm, HK&& hook, TL&& tail) {
  constexpr int BIG = NM == 32;
  ad(so, IC<0>{});
  bar();
  for_n<NR>([&](auto Q) { rd(IC<0>{}, Q); });
  next_slot<BIG>(so);
  ad(so, IC<1>{});
  hook(-1);
  auto body = [&](auto S) {
    constexpr int s = decltype(S)::value;
    wait_lgkm0();
    bar();
    for_n<NM>([&](auto M) {
      constexpr int m = decltype(M)::value;
      mm(S, M);
      if constexpr (m < NR) rd(IC<1 - s>{}, IC<m>{});
      if constexpr (m == NR) { next_slot<BIG>(so); asm volatile("" : "+s"(so)); }
      if constexpr (m == NR + 1) ad(so, S);
      SB();
    });
  };
#pragma nounroll
  for (int u = NSL / 2 - 1; u > 0; --u) {
    body(IC<0>{});
    body(IC<1>{});
    hook(NSL / 2 - 1 - u);
  }
  body(IC<0>{});
  wait_lgkm0();
  if constexpr (ENDB) { bar(); so = 0; }
  SB();
  for_n<NM>([&](auto M) { mm(IC<1>{}, M); SB(); tail(M); SB(); });
  asm volatile("s_nop 15");
  SB();
}

template <int NM, int NR, int ENDB, class RD, class MM, class HK, class TL>
__device__ __forceinline__ void kloop6(RD&& rd, MM&& mm, HK&& hook, TL&& tail) {
  bar();
  for_n<NR>([&](auto Q) { rd(IC<0>{}, Q); });
  hook(-1);
  auto body = [&](auto S) {
    constexpr int s = decltype(S)::value;
    wait_lgkm0();
    bar();
    for_n<NM>([&](auto M) {
      constexpr int m = decltype(M)::value;
      mm(IC<s & 1>{}, M);
      if constexpr (m < NR) rd(IC<(s + 1) % NRING>{}, IC<m>{});
      SB();
    });
  };
#pragma nounroll
  for (int u = 5; u > 0; --u) for_n<NRING>(body);
  body(IC<0>{});
  wait_lgkm0();
  if constexpr (ENDB) bar();
  SB();
  for_n<NM>([&](auto M) { mm(IC<1>{}, M); SB(); tail(M); SB(); });
  asm volatile("s_nop 15");
  SB();
}

template <int NSL, int P, int D, int X, class IS, class BR> __device__ __forceinline__ void stream(IS&& issue, BR&& bar) {
#pragma nounroll
  for (int u = 0; u < D + X; ++u) issue(u);
  if constexpr (X) { wait_vm<(D * P < 63 ? D * P : 63)>(); bar(); }
#pragma nounroll
  for (int u = X; u < NSL - D; ++u) {
    wait_vm<((D - 1) * P < 63 ? (D - 1) * P : 63)>();
    bar();
    issue(u + D);
  }
  for_n<D>([&](auto M) { constexpr int n = (D - 1 - decltype(M)::value) * P; wait_vm<(n < 63 ? n : 63)>(); bar(); });
}

#define STAMP(w, i)
#define TS(v)
#define TSW(v, i)
#define TSP 1

constexpr int w1_frag(int i, int e) {
  const int gam = 2 * (i - 2) + (e >> 1), gl = gam % 7;
  return (gam / 7) * 16 + (gl == 3 ? 6 + 7 * (e & 1) : (gl < 3 ? gl * 2 : 7 + (gl - 4) * 2) + (e & 1));
}
__device__ __forceinline__ void warm_code(u32 n, u32 lds) {
#if defined(__HIP_DEVICE_COMPILE__)
  u64 pc;
  u32 back, len;
  asm volatile("s_getpc_b64 %0\n.Lcw%=:\n\ts_mov_b32 %1, .Lcw%=-mega_k\n\ts_mov_b32 %2, .Lcode_end-mega_k" : "=s"(pc), "=s"(back), "=s"(len));
  pc -= back;
  const u32x4 rc = {(u32)pc, (u32)(pc >> 32), len, 0x00020000u};
  asm volatile("s_mov_b32 %0, m0\n\ts_mov_b32 m0, %1\n\ts_nop 0\n\tbuffer_load_dword %2, %3, 0 offen lds\n\ts_mov_b32 m0, %0"
               : "=&s"(back) : "s"(lds), "v"(n * 128), "s"(rc));
#endif
}

template <int N> __device__ __forceinline__ void announce(u32 fl, __amdgpu_buffer_rsrc_t rs) {
  u32x2 tk;
  u32 a, o, f, n;
  asm volatile("s_mov_b64 exec, 1\n\tv_mov_b32 %1, %7\n\tds_read_b64 %0, %1 offset:8\n\tv_mov_b32 %2, 1\n\tv_mov_b32 %3, %5\n\t"
               "s_waitcnt vmcnt(%8)\n\tds_add_rtn_u32 %1, %1, %2\n\ts_waitcnt lgkmcnt(0)\n\t"
               "v_readfirstlane_b32 %4, %1\n\ts_and_b32 %4, %4, 3\n\ts_cmp_eq_u32 %4, 3\n\ts_cbranch_scc0 .Lan%=\n\t"
               "buffer_store_dwordx2 %0, %3, %6, 0 offen sc1\n.Lan%=:\n\ts_mov_b64 exec, -1"
               : "=&v"(tk), "=&v"(a), "=&v"(o), "=&v"(f), "=&s"(n)
               : "s"(fl), "s"(rs), "n"(L_CNT), "n"(N) : "memory", "scc");
  static_assert(L_TOK == L_CNT + 8, "layout");
}

extern "C" __global__ __launch_bounds__(512, 2) void mega_k(char* __restrict__ ws0, const u16* __restrict__ x,
                                                 const u16* __restrict__ wqkv, const u16* __restrict__ wo,
                                                 const u16* __restrict__ w1, const u16* __restrict__ b1,
                                                 const u16* __restrict__ w2, const u16* __restrict__ b2,
                                                 u16* __restrict__ out) {
#if defined(__HIP_DEVICE_COMPILE__)
  extern __shared__ __attribute__((aligned(16))) char smem[];
  char* const ws = ws0 + (-(u32)(uintptr_t)ws0 & (WS_ALIGN - 1));
  const int tid = threadIdx.x;
  const int wid = __builtin_amdgcn_readfirstlane(tid >> 6);
#define FRESH_LANE() const int lane = fresh_lane(tid), r = lane & 15, g = lane >> 4; (void)r; (void)g
  const int xcd = blockIdx.x & 7, idx = blockIdx.x >> 3;
  asm volatile(".subsection 1\n.Lcode_end:\n\t.subsection 0");
  if (CW & 1) warm_code(((tid & 63) * 8 + wid) * 32 + idx, L_PF);
  const int tmA = (idx >> 4) * 8 + (xcd >> 2) * 4 + ((idx >> 2) & 3), tnA = (xcd & 3) * 4 + (idx & 3);
  const int tmB = (idx >> 4) * 8 + (xcd >> 1) * 2 + ((idx >> 3) & 1), tnB = (idx & 7) * 2 + (xcd & 1);
  const int rotA = 8 - 8 * (xcd & 1), rotB = 4 * ((xcd + 1) & 3), rot0 = xcd;
  const __amdgpu_buffer_rsrc_t rs = __builtin_amdgcn_make_buffer_rsrc((void*)ws, 0, WS_USED, 0x00020000);
  u32 c13 = 0;
  auto side = [&]() { c13 = __builtin_amdgcn_readfirstlane(*(const volatile LDS_AS u32*)(smem + L_C13)); };
  auto flag = [&](int set, int tm, int tn) { return OFF_FL + ((((set * 16 + tm) * 16 + tn) * 128) ^ c13); };
  auto xbox = [&](int pair, int o) { return OFF_XC + pair * (4 * MB) + ((idx * 4 + (xcd & 3)) ^ o) * 32768 + (((((tmB >> 2) ^ (idx >> 4)) & 1) << 13) ^ c13); };
  auto rsrc = [&](const u16* p, u32 bytes) { return __builtin_amdgcn_make_buffer_rsrc((void*)p, 0, bytes, 0x00020000); };
  auto token = [&]() {
    const u32x2 tk = *(const volatile LDS_AS u32x2*)(smem + L_TOK);
    return (u64)(u32)__builtin_amdgcn_readfirstlane(tk[0]) | ((u64)(u32)__builtin_amdgcn_readfirstlane(tk[1]) << 32);
  };
  auto ldf = [&](u32 f) {
    asm volatile("" ::: "memory");
    return __builtin_bit_cast(u64, __builtin_amdgcn_raw_buffer_load_b64(rs, f, 0, 16));
  };
  auto poll = [&](u32 f, u64 tok) {
#pragma nounroll
    for (int spin = 0; spin < (1 << 22); ++spin) {
      if (__all(ldf(f) == tok)) break;
      __builtin_amdgcn_s_sleep(1);
    }
    asm volatile("" ::: "memory");
  };
  auto st8 = [&](u64 v, bool on, u32 f) {
    __builtin_amdgcn_raw_buffer_store_b64(__builtin_bit_cast(i32x2, v), rs, on ? f : 0x80000000u, 0, 16);
  };
  int so = 0;
#define QUIT() __builtin_amdgcn_endpgm()

  if (wid < 4) {
    auto publish = [&](auto N, int ph, int tm, int tn) {
      SB();
      announce<decltype(N)::value>(flag(ph + 1, tm, tn), rs);
      SB();
    };
    const float* t_inv = (const float*)(smem + L_INV);
    const float* t_mnv = (const float*)(smem + L_MNV);
    const float* t_cs = (const float*)(smem + L_CS1);
    const float* t_bs = (const float*)(smem + L_BS);
    const int wmi = wid >> 1, wni = wid & 1;

    const int w = 2 * (tnB & 1) + wmi, tn = (tnB & 14) + wni;
    u32x4 c[2][2];
    auto start = [&](f32x4 (&acc)[4][4], const u32x4 (&bb)[2], bool bias) {
#pragma unroll
      for (int i = 0; i < 2; ++i)
#pragma unroll
        for (int j = 0; j < 4; ++j) {
          const u32 c0 = c[i][j >> 1][2 * (j & 1)], c1 = c[i][j >> 1][2 * (j & 1) + 1];
          const u32 b0 = bb[j >> 1][2 * (j & 1)], b1 = bb[j >> 1][2 * (j & 1) + 1];
          f32x4 a = {bf_lo(c0), bf_hi(c0), bf_lo(c1), bf_hi(c1)};
          if (bias) a += (f32x4){bf_lo(b0), bf_hi(b0), bf_lo(b1), bf_hi(b1)};
          asm volatile("" : "+v"(a));
          acc[i][j] = a;
        }
    };
    auto finish = [&](const f32x4 (&acc)[4][4], int xo  , int ph) {
      FRESH_LANE();
#pragma unroll
      for (int i = 0; i < 2; ++i)
#pragma unroll
        for (int gl = 0; gl < 2; ++gl) {
#pragma unroll
          for (int k = 0; k < 4; ++k) {
            const f32x4& a = acc[i][2 * gl + (k >> 1)];
            c[i][gl][k] = pack2(a[2 * (k & 1)], a[2 * (k & 1) + 1]);
          }
          if (xo < 4) st16(c[i][gl], OFF_X + xo * XS + xpan(tmB, c13) + tn * 65536 + aq(gl, w >> 1) + qoff((w & 1) * 2 + i, lane));
          else __builtin_amdgcn_raw_buffer_store_b128(__builtin_bit_cast(i32x4, c[i][gl]), rsrc(out, SEQ * DM * 2),
                   (tmB * 128 + (2 * w + i) * 16 + r) * 2048 + tn * 128 + gl * 64 + g * 16, 0, 16);
        }
      if (xo < 4) {
        SB();
        TS(ta);
        const u32 tag = *(const volatile LDS_AS u32*)(smem + L_TOK);
        const f32x2 s0 = row_part(c[0][0], c[0][1]), s1 = row_part(c[1][0], c[1][1]), s = (g & 1) ? s1 : s0;
        __builtin_amdgcn_raw_buffer_store_b64((i32x2){(int)__float_as_uint((g & 2) ? s[1] : s[0]), (int)tag}, rs,
            OFF_ST + xo * 524288 + span(tmB, c13) + w * 16384 + tn * 128 + (lane & 24) * 256 + (lane & 7) * 16 + (g >> 1) * 8, 0, 16);
        SB();
        TS(tb);
        publish(IC<1>{}, ph, tmB, tnB);
        TS(tc);
      }
      STAMP(0, 4 * ph + 3);
    };

#pragma nounroll
    for (int pair = 0; pair < 2; ++pair) {
      f32x4 aco[4][4];
      {
        f32x4 acc[4][6], acch[2];
        f32x4 muv[4], muh, cs[4];
        f32x2 mu2[2], ivk[2][2], fk[2][2], csv;
        u32 sel[4], oa[2];
        {
        FRESH_LANE();
        STAMP(0, 16 * pair);
        const u32 lB = 9216 + (8 * g + (r >> 2)) * 128 + (((2 * wni) ^ ((r >> 3) & 1) ^ ((g & 1) << 1)) * 32) + (r & 3) * 8;
        const u32 lA = r * 128 + ((g ^ (r >> 1)) * 16);
        auto loop = [&](auto WM, auto RAW) {
          constexpr int wm = decltype(WM)::value, raw = decltype(RAW)::value;
#pragma unroll
          for (int i = 0; i < 4; ++i)
#pragma unroll
            for (int j = 0; j < 6; ++j) acc[i][j] = zero4();
          acch[0] = zero4(); acch[1] = zero4();
          bf16x8 fA[2][4], fH[2];
          u32x2 hB[2][12];
          u32 pA[3][2], pB[3][2], pH[3][2];
          f32x4 accq;
          if (raw) { accq = zero4(); *(u32*)(smem + L_XC + lane * 4) = 0x3f803f80u; }
#pragma unroll
          for (int k = 0; k < 3; ++k) {
            pA[k][0] = (raw ? lA : lane * 16) + 2 * k * SLOT; pA[k][1] = (lA ^ 64) + 2 * k * SLOT;
            pB[k][0] = lB + 2 * k * SLOT; pB[k][1] = (lB ^ 32) + 2 * k * SLOT;
            pin1(pA[k][0]); pin2(pB[k][0], pB[k][1]);
            if (raw) {
              pH[k][0] = r ? pA[k][0] : L_XC - (SLOT + 21504) + lA; pH[k][1] = r ? pA[k][1] : L_XC - (SLOT + 21504) + (lA ^ 64);
              pin1(pA[k][1]); pin2(pH[k][0], pH[k][1]);
            }
          }
          int ln;
          u32 aR, aG, aK[2];
          f32x2 ivq[2];
          float mh, cv;
          float fq[2][4];
          auto fB = [&](int set, int j) {
            return __builtin_bit_cast(bf16x8, (u32x4){hB[set][2 * j][0], hB[set][2 * j][1], hB[set][2 * j + 1][0], hB[set][2 * j + 1][1]});
          };
          kloop6<26 + raw, 17, 0>(
            [&](auto SL, auto Q) {
              constexpr int set = decltype(SL)::value & 1, k = decltype(SL)::value >> 1, q = decltype(Q)::value, h = q - 5;
              if constexpr (q < 4) dsrd<raw ? wm * SLOT + q * 2048 : set * SLOT + wm * 4096 + q * 1024>(fA[set][q], pA[k][raw & set]);
              else if constexpr (q == 4) dsrd<raw ? SLOT + 21504 : set * SLOT + 8192>(fH[set], raw ? pH[k][set] : pA[k][0]);
              else dstr1<set * SLOT + (h >> 2) * 4096 + (h & 1) * 512>(hB[set][h], pB[k][(h >> 1) & 1]);
            },
            [&](auto SET, auto M) {
              constexpr int set = decltype(SET)::value, m = decltype(M)::value, j = m >> 2, i = m & 3;
              if constexpr (m < 16) mfma(acc[i][j], fB(set, j), fA[set][i]);
              else if constexpr (m < 24) mfma(acc[i][j], fA[set][i], fB(set, j));
              else if constexpr (m == 26) mfma(accq, fB(set, wm), fH[set]);
              else if constexpr (wm == 0) mfma(acch[i], fB(set, 2 + i), fH[set]);
              else mfma(acch[i], fH[set], fB(set, 4 + i));
            }, [&](int t) { if (t < 0) STAMP(0, 16 * pair + 1); },
            [&](auto M) {
              constexpr int m = decltype(M)::value;
              if constexpr (m == 0) {
                ln = fresh_lane(tid);
                aR = L_INV + wm * 256 + (ln & 15) * 4; aG = L_INV + wm * 256 + (ln & 48);
                pin(aR, aG);
              }
              if constexpr (m == 1) { dsrd2<16, 32>(ivq[0], aR); dsrd2<48, 64>(ivq[1], aR); dsrd4<640 + 192>(muv[3], aG); }
              if constexpr (m == 2) { aK[0] = aG + (ln & 1) * 4; aK[1] = aK[0] + 64; pin(aK[0], aK[1]); }
              if constexpr (m == 3) {
                const u32 a0 = LANE_IN(M_OWN0) ? aK[1] : aK[0], a1 = LANE_IN(M_OWN1) ? aK[1] : aK[0];
                aK[0] = a0; aK[1] = a1;
                pin(aK[0], aK[1]);
              }
              if constexpr (m == 4) { dsrd2<0, 16>(ivk[0][0], aK[0]); dsrd2<32, 48>(ivk[0][1], aK[0]); dsrd2<2, 18>(ivk[1][0], aK[1]); }
              if constexpr (m == 5) { dsrd2<34, 50>(ivk[1][1], aK[1]); dsrd2<160, 176>(mu2[0], aR); dsrd2<192, 208>(mu2[1], aR); }
              if constexpr (m == 6) { dsrd4<640>(muv[0], aG); dsrd4<640 + 64>(muv[1], aG); dsrd4<640 + 128>(muv[2], aG); }
              if constexpr (m == 7) {
                if constexpr (wm) dsrd4<576 - 256>(muh, aG); else dsrd1<576>(mh, aR);
                aK[0] = L_C13;
                pin(aK[0]);
                dsrd1<0>(cv, aK[0]);
              }
              if constexpr (m == 8) { aR += L_CSQ - L_INV - wm * 256 + wni * 384; aG += L_CSQ - L_INV - wm * 256 + wni * 384; pin(aR, aG); }
              if constexpr (!raw) {
                if constexpr (m == 9) { dsrd4<128>(cs[2], aG); dsrd4<192>(cs[3], aG); dsrd2<64, 80>(csv, aR); }
                if constexpr (m == 10) { dsrd4<0>(cs[0], aG); dsrd4<64>(cs[1], aG); }
              }
              if constexpr (m == 11) { sel[0] = LANE_IN(M_PAR) ? 0x01000c0cu : 0x0c0c0100u; pin(sel[0]); }
              if constexpr (m == 12) { sel[1] = LANE_IN(M_PAR) ? 0x03020c0cu : 0x0c0c0302u; pin(sel[1]); }
              if constexpr (m == 13) {
                sel[2] = LANE_IN(M_OWN0) ? sel[0] : 0x0c0c0c0cu; sel[3] = LANE_IN(M_OWN1) ? sel[1] : 0x0c0c0c0cu;
                pin(sel[2], sel[3]);
              }
              if constexpr (m == 14) {
                sel[0] = LANE_IN(M_OWN0) ? 0x0c0c0c0cu : sel[0]; sel[1] = LANE_IN(M_OWN1) ? 0x0c0c0c0cu : sel[1];
                pin(sel[0], sel[1]);
              }
              if constexpr (m == 16) {
                wait_lgkm0();
                pin(ivq[0], ivq[1], muv[0], muv[1], muv[2], muv[3], ivk[0][0], ivk[0][1], ivk[1][0], ivk[1][1]);
                pin(mu2[0], mu2[1], cv);
                if constexpr (wm) pin(muh); else { pin(mh); muh = (f32x4){mh, mh, mh, mh}; }
                if constexpr (!raw) pin(cs[0], cs[1], cs[2], cs[3], csv);
              }
              if constexpr (m >= 17 && m < 21) {
                constexpr int i = m - 17;
                float cq = ivq[i >> 1][i & 1] * (0.17677669529663687f * 1.4426950408889634f);
                pin(cq);
                fq[0][i] = ivk[0][i >> 1][i & 1] * cq; fq[1][i] = ivk[1][i >> 1][i & 1] * cq;
                pin(fq[0][i], fq[1][i]);
                if constexpr (i & 1) { fk[0][i >> 1] = (f32x2){fq[0][i - 1], fq[0][i]}; fk[1][i >> 1] = (f32x2){fq[1][i - 1], fq[1][i]}; }
              }
              if constexpr (m == 21) {
                oa[0] = ((OFF_O + pair * XS + xpan(tmA, 0) + tnA * 65536 + aq(wni, wm)) ^ __float_as_uint(cv)) + ln * 16;
                oa[1] = oa[0] + 128;
                pin(oa[0], oa[1]);
              }
              if constexpr (m == 22) c13 = __builtin_amdgcn_readfirstlane(__float_as_uint(cv));
            });
          if constexpr (raw) {
            FRESH_LANE();
            if (LANE_IN(0x0001000100010001ull)) {
              *(f32x4*)(smem + aG + wm * 64) = accq;
              if (wm == 0) { *(f32x4*)(smem + aG + 128) = acch[0]; *(f32x4*)(smem + aG + 192) = acch[1]; }
            }
            if (wm == 1 && LANE_IN(0xffffull)) { *(float*)(smem + aR + 256) = acch[0][0]; *(float*)(smem + aR + 320) = acch[1][0]; }
            wait_lgkm0();
            bar();
            dsrd4<128>(cs[2], aG); dsrd4<192>(cs[3], aG); dsrd2<64, 80>(csv, aR); dsrd4<0>(cs[0], aG); dsrd4<64>(cs[1], aG);
#pragma unroll
            for (int i = 0; i < 2; ++i)
#pragma unroll
              for (int gl = 0; gl < 2; ++gl)
                c[i][gl] = __builtin_bit_cast(u32x4, __builtin_amdgcn_raw_buffer_load_b128(rsrc(x, SEQ * DM * 2),
                    (16 * i + r) * 2048 + g * 16, (tmB * 128 + w * 32) * 2048 + tn * 128 + gl * 64, 0));
            wait_lgkm0();
            pin(cs[0], cs[1], cs[2], cs[3], csv);
          }
        };
        if (pair == 0) { if (wmi == 0) loop(IC<0>{}, IC<1>{}); else loop(IC<1>{}, IC<1>{}); }
        else { if (wmi == 0) loop(IC<0>{}, IC<0>{}); else loop(IC<1>{}, IC<0>{}); }
        }
        FRESH_LANE();
        STAMP(0, 16 * pair + 2);
        auto fix_qk = [&](const f32x4& a0, const f32x4& a1, float mu, int f) {
          const f32x4 y0 = a0 - cs[f] * mu, y1 = a1 - cs[f + 1] * mu;
          u32x4 v = {pack2(y0[0], y0[1]), pack2(y0[2], y0[3]), pack2(y1[0], y1[1]), pack2(y1[2], y1[3])};
          pin(v);
          return v;
        };
        auto fix_v = [&](const f32x4& a, const f32x4& mu, float cw) {
          const f32x4 y = a - mu * cw;
          u32x2 v = {pack2(y[0], y[1]), pack2(y[2], y[3])};
          pin(v);
          return v;
        };
        u32x4 Qf[4], Kf[5];
        u32x2 Vf[5][2];
        char* xc = smem + L_XC + wni * 3072;
        Kf[4] = fix_qk(acc[3][2], acc[3][3], mu2[1][1], 2);
        Vf[4][0] = fix_v(acc[3][4], muv[3], csv[0]); Vf[4][1] = fix_v(acc[3][5], muv[3], csv[1]);
        if (wmi == 0) {
          *(u32x4*)(xc + lane * 16) = Kf[4];
          *(u32x2*)(xc + 1024 + lane * 8) = Vf[4][0]; *(u32x2*)(xc + 1536 + lane * 8) = Vf[4][1];
          Kf[0] = fix_qk(acch[0], acch[1], muh[0], 2);
        } else {
          Kf[0] = Kf[4];
          *(u32x2*)(xc + 2048 + lane * 8) = fix_v(acch[0], muh, csv[0]);
          *(u32x2*)(xc + 2560 + lane * 8) = fix_v(acch[1], muh, csv[1]);
        }
#pragma unroll
        for (int i = 0; i < 3; ++i) Kf[i + 1] = fix_qk(acc[i][2], acc[i][3], mu2[i >> 1][i & 1], 2);
        wait_lgkm0();
        bar();
        TS(td);
        if (wmi == 1) Kf[0] = *(const u32x4*)(xc + lane * 16);
        Vf[0][0] = *(const u32x2*)(xc + (wmi == 0 ? 2048 : 1024) + lane * 8);
        Vf[0][1] = *(const u32x2*)(xc + (wmi == 0 ? 2560 : 1536) + lane * 8);
        const f32x4 z = {0.f, 0.f, 0.f, 0.f};
        f32x4 S[4][2];
#pragma unroll
        for (int i = 3; i >= 0; --i) {
          Qf[i] = fix_qk(acc[i][0], acc[i][1], mu2[i >> 1][i & 1], 0);
          S[i][1] = __builtin_amdgcn_mfma_f32_16x16x32_bf16(__builtin_bit_cast(bf16x8, Kf[i + 1]), __builtin_bit_cast(bf16x8, Qf[i]), z, 0, 0, 0);
          S[i][0] = __builtin_amdgcn_mfma_f32_16x16x32_bf16(__builtin_bit_cast(bf16x8, Kf[i]), __builtin_bit_cast(bf16x8, Qf[i]), z, 0, 0, 0);
        }
#pragma unroll
        for (int i = 0; i < 3; ++i) { Vf[i + 1][0] = fix_v(acc[i][4], muv[i], csv[0]); Vf[i + 1][1] = fix_v(acc[i][5], muv[i], csv[1]); }
        const bool par = LANE_IN(M_PAR), own0 = LANE_IN(M_OWN0), own1 = LANE_IN(M_OWN1);
        f32x2 l[2][2];
#pragma unroll
        for (int i = 3; i >= 0; --i) {
          l[0][i >> 1][i & 1] = own0 ? (par ? S[i][1][1] : S[i][1][0]) : (par ? S[i][0][1] : S[i][0][0]);
          l[1][i >> 1][i & 1] = own1 ? (par ? S[i][1][3] : S[i][1][2]) : (par ? S[i][0][3] : S[i][0][2]);
        }
#pragma unroll
        for (int i = 0; i < 4; ++i) l[i >> 1][i & 1] *= fk[i >> 1][i & 1];
        {
          const u64 first = tmA == 0 && wmi == 0 ? ~0ull : 0ull;
          if (LANE_IN(first & ~M_OWN0)) l[0][0][0] = -1.4426950e9f;
          if (LANE_IN(first & ~M_OWN1)) l[1][0][0] = -1.4426950e9f;
        }
        auto all4 = [](f32x2 (&v)[2], auto f, auto mid) {
          auto U = [](float a) { return __float_as_uint(a); };
          auto F = [](u32 a) { return __uint_as_float(a); };
          const auto a = __builtin_amdgcn_permlane32_swap(U(v[0][0]), U(v[1][0]), false, false);
          const auto b = __builtin_amdgcn_permlane32_swap(U(v[0][1]), U(v[1][1]), false, false);
          const auto c = __builtin_amdgcn_permlane16_swap(U(f(F(a[0]), F(a[1]))), U(f(F(b[0]), F(b[1]))), false, false);
          const u32 t = U(mid(f(F(c[0]), F(c[1]))));
          const auto d = __builtin_amdgcn_permlane16_swap(t, t, false, false);
          const auto e0 = __builtin_amdgcn_permlane32_swap(d[0], d[0], false, false);
          const auto e1 = __builtin_amdgcn_permlane32_swap(d[1], d[1], false, false);
          v[0] = (f32x2){F(e0[0]), F(e1[0])}; v[1] = (f32x2){F(e0[1]), F(e1[1])};
        };
        f32x2 t[2] = {__builtin_elementwise_maximum(l[0][0], l[1][0]), __builtin_elementwise_maximum(l[0][1], l[1][1])};
        all4(t, [](float a, float b) { return __builtin_elementwise_maximum(a, b); }, [](float a) { return a; });
#pragma unroll
        for (int i = 0; i < 4; ++i) {
          const f32x2 d = l[i >> 1][i & 1] - t[i & 1];
          l[i >> 1][i & 1] = (f32x2){__builtin_amdgcn_exp2f(d[0]), __builtin_amdgcn_exp2f(d[1])};
        }
        t[0] = l[0][0] + l[1][0]; t[1] = l[0][1] + l[1][1];
        all4(t, [](float a, float b) { return a + b; }, [](float a) { return __builtin_amdgcn_rcpf(a); });
#pragma unroll
        for (int i = 0; i < 4; ++i) l[i >> 1][i & 1] = l[i >> 1][i & 1] * ivk[i >> 1][i & 1] * t[i & 1];
        u32x10 V[2];
#pragma unroll
        for (int i = 0; i < 10; ++i) { V[0][i] = Vf[i >> 1][0][i & 1]; V[1][i] = Vf[i >> 1][1][i & 1]; }
        pin(V[0], V[1]);
        for_n<4>([&](auto I) {
          constexpr int i = decltype(I)::value;
          const u32 pw = pack2(l[0][i >> 1][i & 1], l[1][i >> 1][i & 1]);
          const u32x4 P = {__builtin_amdgcn_perm(pw, pw, sel[0]), __builtin_amdgcn_perm(pw, pw, sel[1]),
                           __builtin_amdgcn_perm(pw, pw, sel[2]), __builtin_amdgcn_perm(pw, pw, sel[3])};
          const u32x4 V0 = __builtin_shufflevector(V[0], V[0], 2 * i, 2 * i + 1, 2 * i + 2, 2 * i + 3);
          const u32x4 V1 = __builtin_shufflevector(V[1], V[1], 2 * i, 2 * i + 1, 2 * i + 2, 2 * i + 3);
          const f32x4 O0 = __builtin_amdgcn_mfma_f32_16x16x32_bf16(__builtin_bit_cast(bf16x8, V0), __builtin_bit_cast(bf16x8, P), z, 0, 0, 0);
          const f32x4 O1 = __builtin_amdgcn_mfma_f32_16x16x32_bf16(__builtin_bit_cast(bf16x8, V1), __builtin_bit_cast(bf16x8, P), z, 0, 0, 0);
          const u32x4 o = {pack2(O0[0], O0[1]), pack2(O0[2], O0[3]), pack2(O1[0], O1[1]), pack2(O1[2], O1[3])};
          st16(o, (LANE_IN(~0ull << (i == 3 ? 56 : 24 - 8 * i)) ? oa[1] : oa[0]) + qoff(i, 0));
        });
        SB();
        TS(te);
        start(aco, c[0], false);
        publish(IC<0>{}, 4 * pair, tmA, tnA);
        TSW(td, 35); TSW(te, 34);
        STAMP(0, 16 * pair + 3);
      }
      {
        FRESH_LANE();
        bf16x8 fA[2][2][2], fB[2][2][4];
        u32 pA, pB;
        so = 2 * SLOT;
        kloop<16, 16, 12, 0>(so,
          [&](int s_, auto) { pA = lane * 16 + wmi * 2048 + s_; pB = lane * 16 + wni * 4096 + s_; pin2(pA, pB); },
          [&](auto SET, auto Q) {
            constexpr int set = decltype(SET)::value, q = decltype(Q)::value, s = q / 6, m = q % 6;
            if constexpr (m < 2) dsrd<s * 4096 + m * 1024>(fA[set][s][m], pA);
            else dsrd<8192 + s * 8192 + (m - 2) * 1024>(fB[set][s][m - 2], pB);
          },
          [&](auto SET, auto M) {
            constexpr int set = decltype(SET)::value, m = decltype(M)::value, s = m >> 3, j = (m >> 1) & 3, i = m & 1;
            mfma(aco[i][j], fB[set][s][j], fA[set][s][i]);
          },
          [&](int t) { if (t < 0) STAMP(0, 16 * pair + 5); }, [](auto) {});
        STAMP(0, 16 * pair + 6);
        finish(aco, 2 * pair + 1, 4 * pair + 1);
      }
      {
        f32x4 acc[4][7];
        {
        FRESH_LANE();
#pragma unroll
        for (int i = 0; i < 4; ++i)
#pragma unroll
          for (int j = 0; j < 7; ++j) acc[i][j] = zero4();
        bf16x8 fA[2][4], fB[2][7];
        u32 pA[3], pB[3];
#pragma unroll
        for (int k = 0; k < 3; ++k) {
          pA[k] = lane * 16 + wmi * 4096 + 2 * k * SLOT; pB[k] = lane * 16 + wni * 7168 + 2 * k * SLOT;
          pin2(pA[k], pB[k]);
        }
        kloop6<28, 11, 1>(
          [&](auto SL, auto Q) {
            constexpr int set = decltype(SL)::value & 1, k = decltype(SL)::value >> 1, q = decltype(Q)::value;
            if constexpr (q < 4) dsrd<set * SLOT + q * 1024>(fA[set][q], pA[k]);
            else dsrd<set * SLOT + 8192 + (q - 4) * 1024>(fB[set][q - 4], pB[k]);
          },
          [&](auto SET, auto M) {
            constexpr int set = decltype(SET)::value, m = decltype(M)::value, j = m >> 2, i = m & 3;
            mfma(acc[i][j], fB[set][j], fA[set][i]);
          }, [&](int t) { if (t < 0) STAMP(0, 16 * pair + 9); }, [](auto) {});
        }
        FRESH_LANE();
        STAMP(0, 16 * pair + 10);
        float iv[4], mv[4];
#pragma unroll
        for (int i = 0; i < 4; ++i) { iv[i] = t_inv[16 + wmi * 64 + 16 * i + r]; mv[i] = t_mnv[16 + wmi * 64 + 16 * i + r]; }
        const u32 hbase = OFF_H + pair * HS + hpan(tmA, tnA >> 3, c13);
        auto act = [&](const f32x4& a, const f32x4& cs, const f32x4& bs, int i) {
          const f32x4 y = a * iv[i] + (bs - cs * mv[i]);
          return (u32x2){gelu_tanh2((f32x2){y[0], y[1]}), gelu_tanh2((f32x2){y[2], y[3]})};
        };
#pragma unroll
        for (int jp = 0; jp < 3; ++jp) {
          const int t0 = (wni * 7 + 2 * jp) * 16 + 4 * g;
          const f32x4 cs0 = *(const f32x4*)(t_cs + t0), cs1 = *(const f32x4*)(t_cs + t0 + 16);
          const f32x4 bs0 = *(const f32x4*)(t_bs + t0), bs1 = *(const f32x4*)(t_bs + t0 + 16);
#pragma unroll
          for (int i = 0; i < 4; ++i) {
            const u32x2 a = act(acc[i][2 * jp], cs0, bs0, i), b = act(acc[i][2 * jp + 1], cs1, bs1, i);
            const u32x4 v = {a[0], a[1], b[0], b[1]};
            st16(v, hbase + aq(hpos(tnA, wni * 4 + jp), wmi) + qoff(i, lane));
            if (4 * jp + i == 3 + PUBJ) publish(IC<PUBJ>{}, 4 * pair + 2, tmA, tnA);
          }
          if (jp < 2) SB();
        }
        {
          const int t0 = (wni * 7 + 6) * 16 + 4 * g;
          const f32x4 cs0 = *(const f32x4*)(t_cs + t0), bs0 = *(const f32x4*)(t_bs + t0);
#pragma unroll
          for (int i = 0; i < 4; i += 2) {
            const u32x2 a = act(acc[i][6], cs0, bs0, i), b = act(acc[i + 1][6], cs0, bs0, i + 1);
            const auto s0 = __builtin_amdgcn_permlane16_swap(a[0], b[0], false, false);
            const auto s1 = __builtin_amdgcn_permlane16_swap(a[1], b[1], false, false);
            const u32x4 v = {s0[0], s1[0], s0[1], s1[1]};
            st16(v, hbase + aq(hpos(tnA, 3), wmi) + qoff(i + (g & 1), (2 * wni + (g >> 1)) * 16 + r));
          }
        }
        STAMP(0, 16 * pair + 11);
      }
      {
        FRESH_LANE();
        f32x4 acc[4][4];
        SB();
        const u32x4 bb[2] = {*(const u32x4*)(smem + L_B2 + wni * 128 + g * 16), *(const u32x4*)(smem + L_B2 + wni * 128 + 64 + g * 16)};
#pragma unroll
        for (int i = 2; i < 4; ++i)
#pragma unroll
          for (int j = 0; j < 4; ++j) acc[i][j] = zero4();
        start(acc, bb, true);
        bf16x8 fA[2][2][4], fB[2][2][4];
        u32 pA, pB;
        const u32 xc = wmi * 16384 + wni * 4096;
        wait_vm<6>();
        so = 0;
        kloop<28, 32, 16, 1>(so,
          [&](int s_, auto) { pA = lane * 16 + wmi * 2048 + s_; pB = lane * 16 + wni * 4096 + s_; pin2(pA, pB); },
          [&](auto SET, auto Q) {
            constexpr int set = decltype(SET)::value, q = decltype(Q)::value, s = q >> 3, m = q & 7;
            if constexpr (m < 4) dsrd<s * 8192 + (m >> 1) * 4096 + (m & 1) * 1024>(fA[set][s][m], pA);
            else dsrd<16384 + s * 8192 + (m - 4) * 1024>(fB[set][s][m - 4], pB);
          },
          [&](auto SET, auto M) {
            constexpr int set = decltype(SET)::value, m = decltype(M)::value;
            constexpr int s = (m >> 3) & 1, j = (m >> 1) & 3, i = (m & 1) + (m < 16 ? 2 : 0);
            mfma(acc[i][j], fB[set][s][j], fA[set][s][i]);
          },
          [&](int t) { if (t < 0) { STAMP(0, 16 * pair + 13); } else wait_vm<0>(); },
          [&](auto M) {
            constexpr int k = decltype(M)::value - SND - 1;
            if constexpr (k >= 0 && k < 8 && k % 2 == 0) {
              const f32x4 a = acc[2][k >> 1], b = acc[3][k >> 1];
              const u32x4 v = {pack2(a[0], a[1]), pack2(a[2], a[3]), pack2(b[0], b[1]), pack2(b[2], b[3])};
              st16(__builtin_elementwise_min(v, (u32x4){~1u, ~1u, ~1u, ~1u}), xbox(pair, 0) + xc + lane * 16 + k * 512);
            }
          });
        STAMP(0, 16 * pair + 14);
        u32x4 xv[4];
#pragma nounroll
        for (int spin = 0; spin < (1 << 22); ++spin) {
#pragma unroll
          for (int k = 0; k < 4; ++k)
            xv[k] = __builtin_bit_cast(u32x4, __builtin_amdgcn_raw_buffer_load_b128(rs, lane * 16 + k * 1024, xbox(pair, 1) + xc, 16));
          const u32x4 o = __builtin_elementwise_max(__builtin_elementwise_max(xv[0], xv[1]), __builtin_elementwise_max(xv[2], xv[3]));
          if (!__any(max(max(o[0], o[1]), max(o[2], o[3])) == ~0u)) break;
          asm volatile("" ::: "memory");
        }
#pragma unroll
        for (int k = 0; k < 4; ++k) {
          acc[0][k] += (f32x4){bf_lo(xv[k][0]), bf_hi(xv[k][0]), bf_lo(xv[k][1]), bf_hi(xv[k][1])};
          acc[1][k] += (f32x4){bf_lo(xv[k][2]), bf_hi(xv[k][2]), bf_lo(xv[k][3]), bf_hi(xv[k][3])};
        }
        finish(acc, pair == 0 ? 2 : 4, 4 * pair + 3);
      }
    }
  } else if (wid == 4) {
    {
      FRESH_LANE();
      STAMP(1, 0);
      const __amdgpu_buffer_rsrc_t rx = rsrc(x, SEQ * DM * 2);
      u32 vo[4];
#pragma unroll
      for (int e = 0; e < 4; ++e)
        vo[e] = (8 * e + (lane >> 3)) * 2048 + (((lane & 7) ^ (4 * (e & 1) + (lane >> 4))) * 16) - e * 1024;
      const u32 sx = tmA * 128 * 2048, sh = (tmA > 0 ? tmA * 128 - 16 : 0) * 2048;
      auto half = [&](auto H, int U) {
        constexpr int h = decltype(H)::value;
        U = (U + 2 * rot0) & 15;
        for_n<8>([&](auto E) {
          constexpr int e = decltype(E)::value;
          DMA(rx, (LDS_AS void*)(smem + so + h * SLOT + (e >> 2) * 4096), 16, vo[e & 3],
              sx + (h * 64 + (e >> 2) * 32) * 2048 + U * 128, (e & 3) * 1024, 0);
        });
        DMA(rx, (LDS_AS void*)(smem + so + SLOT + 21504), 16, vo[h], sh + U * 128, h * 1024, 0);
        if constexpr (h == 1) { so += 2 * SLOT; if (so == RING) so = 0; }
      };
#pragma nounroll
      for (int U = 0; U < 3; ++U) { half(IC<0>{}, U); half(IC<1>{}, U); }
      wait_vm<36>();
      bar();
      bar();
#pragma nounroll
      for (int U = 3; U < 16; ++U) {
        wait_vm<18>();
        bar();
        half(IC<0>{}, U);
        bar();
        half(IC<1>{}, U);
      }
      wait_vm<18>(); bar(); bar();
      wait_vm<0>(); bar(); bar(); bar();
    }
    auto phase = [&](auto NSL_, auto HALO_, u32 src, u32 hsrc, int ph  , int tm, int tmh) {
      constexpr int NSL = decltype(NSL_)::value, HALO = decltype(HALO_)::value;
      constexpr int BIG = NSL == 28, PA = BIG ? 16 : 8, PT = PA + HALO, D = BIG ? 3 : NSL == 16 ? DN : DW, X = BIG ? XA2 : 0;
      FRESH_LANE();
      u32 vo[4];
#pragma unroll
      for (int e = 0; e < 4; ++e) vo[e] = qoff(e, lane) - e * 1024;
      poll(flag(ph + 1, (lane & 16) ? tmh : tm, BIG ? 8 * (tnB & 1) + ((rotA >> 1) + (r < D + X ? r : 0))
                                               : NSL == 16 ? (rotB + (r < DN ? r : 0)) & 15 : rotA + (r & (DW > 4 ? 3 : 1))), token());
      STAMP(1, 4 * (ph + 1));
      const u32 flip = BIG ? (tnB & 1) * 16384 : 0;
      auto issue = [&](int u) {
        u = BIG ? (u < 8 ? u ^ (rotA >> 1) : u) : NSL == 16 ? (u + rotB) & 15 : u ^ (2 * rotA);
        if constexpr (BIG && X) wait_vm<47>();
        const u32 ko = aq(NSL == 32 ? u : 2 * u, 0);
        for_n<PA>([&](auto E) {
          constexpr int e = decltype(E)::value;
          DMA(rs, (LDS_AS void*)(smem + so + (e >> 2) * 4096), 16, vo[e & 3],
              src + ko + (NSL == 16 ? aq(e >> 2, 0) : aq(e >> 3, (e >> 2) & 1) + ((e & 4) ? -flip : flip)), (e & 3) * 1024, 16);
        });
        if constexpr (HALO) DMA(rs, (LDS_AS void*)(smem + so + 8192), 16, vo[3], hsrc + ko, 0, 16);
        next_slot<BIG>(so);
      };
      stream<NSL, PT, D, X>(issue, [&]() { bar(); });
    };
#pragma nounroll
    for (int pair = 0; pair < 2; ++pair) {
      const int tmh = tmA > 0 ? tmA - 1 : 0;
      const u32 xq = OFF_X + 2 * pair * XS;
      if (pair) phase(IC<32>{}, IC<1>{}, xq + xpan(tmA, c13), xq + xpan(tmh, c13) + 16384 + 3072, 3, tmA, tmh);
      side();
      bar();
      phase(IC<16>{}, IC<0>{}, OFF_O + pair * XS + xpan(tmB, c13) + aq(0, tnB & 1), 0, 4 * pair, tmB, tmB);
      phase(IC<32>{}, IC<0>{}, xq + XS + xpan(tmA, c13), 0, 4 * pair + 1, tmA, tmA);
      bar();
      so = 0;
      phase(IC<28>{}, IC<0>{}, OFF_H + pair * HS + hpan(tmB, tnB & 1, c13), 0, 4 * pair + 2, tmB, tmB);
      bar();
      so = 0;
    }
  } else if (wid == 5) {
    FRESH_LANE();
    auto phase = [&](auto NSL_, auto PB_, int rot, auto&& piece) {
      constexpr int NSL = decltype(NSL_)::value, PB = decltype(PB_)::value;
      auto issue = [&](int u) {
        u = NSL == 28 ? (u < 8 ? u ^ rot : u) : NSL == 16 ? (u + rot) & 15 : u ^ rot;
        wait_vm<63 - PB>();
        for_n<PB>([&](auto I) { piece(I, u); });
        next_slot<NSL == 28>(so);
      };
      stream<NSL, PB, (NSL == 28 ? 3 : 5), (NSL == 28 ? XB2 : NSL == 16 ? XBO : 0)>(issue, [&]() { bar(); });
    };
    auto tiles = [&](auto NSL_, auto PB_, auto BOFF_, u32 src) {
      constexpr int NSL = decltype(NSL_)::value, PB = decltype(PB_)::value, BOFF = decltype(BOFF_)::value;
      u32 vo[4];
#pragma unroll
      for (int e = 0; e < 4; ++e) vo[e] = qoff(e, lane) - e * 1024;
      phase(NSL_, PB_, NSL == 28 ? rotA >> 1 : NSL == 16 ? rotB : 2 * rotA, [&](auto I, int u) {
        constexpr int i = decltype(I)::value;
        DMA(rs, (LDS_AS void*)(smem + so + BOFF + (i >> 2) * 4096), 16, vo[i & 3],
            PB == 16 ? src + u * (2 * QS) + (i >> 3) * QS + ((i >> 2) & 1) * ((NSL == 28 ? 112 : 32) * QS)
                     : src + u * (4 * QS) + (i >> 2) * QS, (i & 3) * 1024, 16);
      });
    };
#pragma nounroll
    for (int pair = 0; pair < 2; ++pair) {
      {
        const __amdgpu_buffer_rsrc_t rq = rsrc(wqkv, DM * 3 * DM * 2);
        u32 vo[4];
#pragma unroll
        for (int e = 0; e < 4; ++e)
          vo[e] = (8 * e + (lane >> 3)) * 6144 + (((lane & 7) ^ (2 * ((lane >> 4) & 1) + 4 * (e & 1))) * 16) - e * 1024;
        phase(IC<32>{}, IC<12>{}, pair ? 2 * rotA : 0, [&](auto I, int u) {
          constexpr int i = decltype(I)::value;
          if (pair == 0) u = (u + 4 * rot0) & 31;
          DMA(rq, (LDS_AS void*)(smem + so + 9216 + (i >> 2) * 4096), 16, vo[i & 3],
              u * (32 * 6144) + (i >> 2) * 2048 + tnA * 128, (i & 3) * 1024, 0);
        });
      }
      if (pair == 0) bar();
      bar();
      tiles(IC<16>{}, IC<16>{}, IC<8192>{}, OFF_WO + (tnB & 14) * (32 * QS));
      if (pair == 0) poll(flag(F_W1, r, (tnA & 14) + (g & 1)), token());
      tiles(IC<32>{}, IC<14>{}, IC<8192>{}, OFF_W1 + tnA * (32 * 4 * QS));
      bar();
      so = 0;
      if (pair == 0) poll(flag(F_W2, 8 * (tnB & 1) + (r & 7), (tnB & 14) + (r >> 3)), token());
      tiles(IC<28>{}, IC<16>{}, IC<16384>{}, OFF_W2 + ((tnB & 14) * 112 + (tnB & 1) * 56) * QS);
      bar();
      so = 0;
    }
  } else if (wid == 6) {
    FRESH_LANE();
    const int rr = lane >> 3, c16 = (lane & 7) * 16;
    const __amdgpu_buffer_rsrc_t rq = rsrc(wqkv, DM * 3 * DM * 2);
    if (R3W)
      DMA(rq, (LDS_AS void*)(smem + L_PF), 4, lane < 24 && !(xcd & 1) ? ((rot0 * 128 + idx * 8 + (lane & 7)) & 1023) * 6144 + (lane >> 3) * 2048 : 0x80000000u,
          (xcd & 3) * 512 + 384, 0, 0);
    u32x4 U[7][4], F[4];
    auto u_load = [&](auto I) {
      constexpr int i = decltype(I)::value;
#pragma unroll
      for (int e = 0; e < 4; ++e) {
        if constexpr (i < 2)
          U[(i + 5) % 7][e] = __builtin_bit_cast(u32x4, __builtin_amdgcn_raw_buffer_load_b128(rsrc(wo, DM * DM * 2),
              ((rr & 3) + 16 * (rr >> 2)) * 2048 + c16, ((2 * tmA + i) * 32 + 4 * e) * 2048 + (tnA ^ (4 * i)) * 128, UAUX));
        else if constexpr (i < 9)
          U[(i + 5) % 7][e] = __builtin_bit_cast(u32x4, __builtin_amdgcn_raw_buffer_load_b128(rsrc(w1, DM * FF * 2),
              rr * 7168 + c16, ((2 * tmA + (tnA & 1)) * 32 + 8 * e) * 7168 + (7 * (tnA >> 1) + i - 2) * 128, UAUX));
        else
          U[(i + 5) % 7][e] = __builtin_bit_cast(u32x4, __builtin_amdgcn_raw_buffer_load_b128(rsrc(w2, DM * FF * 2),
              rr * 2048 + c16, ((7 * tmA + i - 9) * 32 + 8 * e) * 2048 + tnA * 128, UAUX));
      }
    };
    auto u_write = [&](auto I) {
      constexpr int i = decltype(I)::value;
#pragma unroll
      for (int e = 0; e < 4; ++e) {
        const u32x4 v = U[(i + 5) % 7][e];
        *(u32x4*)(smem + L_IM + e * 1024 + rr * 128 + (c16 ^ ((rr & 2) << 5))) = (e & 1) ? (u32x4){v[2], v[3], v[0], v[1]} : v;
      }
    };
    auto u_store = [&](auto I) {
      constexpr int i = decltype(I)::value;
      const u32 sw = ((r & 8) << 3) | ((g & 1) << 3);
      const char* im = smem + L_IM + (8 * g + (r >> 2)) * 128;
      for_n<4>([&](auto E) {
        constexpr int e = decltype(E)::value;
        constexpr bool split = i >= 2 && i < 9 && (2 * (i - 2) + (e >> 1)) % 7 == 3;
        const char* p = im + (((e >> 1) * 64 + (split ? (e & 1) * 32 + (r & 3) * 8 : (r & 3) * 16 + (e & 1) * 8)) ^ sw);
        const u32x2 lo = __builtin_bit_cast(u32x2, __builtin_amdgcn_ds_read_tr16_b64_v4i16((LDS_AS s16x4*)p));
        const u32x2 hi = __builtin_bit_cast(u32x2, __builtin_amdgcn_ds_read_tr16_b64_v4i16((LDS_AS s16x4*)(p + 512)));
        F[e] = (u32x4){lo[0], lo[1], hi[0], hi[1]};
      });
      for_n<4>([&](auto E) {
        constexpr int e = decltype(E)::value;
        if constexpr (i < 2) st16(F[e], OFF_WO + ((tnA ^ (4 * i)) * 32 + 2 * tmA + i) * QS + qoff(e, lane));
        else if constexpr (i < 9) {
          constexpr int f = w1_frag(i, e);
          st16(F[e], OFF_W1 + ((((tnA & 14) + (f >> 4)) * 32 + 2 * tmA + (tnA & 1)) * 4 + ((f & 15) >> 2)) * QS + qoff(f & 3, lane));
        } else st16(F[e], OFF_W2 + (tnA * 112 + (tmA >> 3) * 56 + hpos(tmA, i - 9)) * QS + qoff(e, lane));
      });
    };
    auto u_sums = [&](auto I) {
      constexpr int i = decltype(I)::value;
      constexpr u32 K = w1_frag(i, 0) | (w1_frag(i, 1) << 8) | (w1_frag(i, 2) << 16) | (w1_frag(i, 3) << 24);
      f32x4 cs = {0.f, 0.f, 0.f, 0.f};
#pragma unroll
      for (int e = 0; e < 4; ++e) {
        const u32 o = (r >> 2) == e ? 0x3f803f80u : 0u;
        cs = __builtin_amdgcn_mfma_f32_16x16x32_bf16(__builtin_bit_cast(bf16x8, (u32x4){o, o, o, o}), __builtin_bit_cast(bf16x8, F[e]), cs, 0, 0, 0);
      }
      const u32 f = (K >> (8 * g)) & 255;
      __builtin_amdgcn_raw_buffer_store_b32(__float_as_uint(cs[0]), rs,
          OFF_CS1 + ((((tnA & 14) + (f >> 4)) * 32 + 2 * tmA + (tnA & 1)) * 256 + (f & 15) * 16 + r) * 4, 0, 16);
    };
    u64 tok = 0;
    auto up = [&](int set) { st8(tok, lane == 0, flag(set, tmA, tnA)); };
    f32x4 cv[32], ca[2];
    auto cs_load = [&](u32 src, int p0, int p1) {
#pragma unroll
      for (int p = p0; p < p1; ++p)
        cv[p] = __builtin_bit_cast(f32x4, __builtin_amdgcn_raw_buffer_load_b128(rs, lane * 16 + (p & 3) * 1024, src + (p >> 2) * 4096, 16));
    };
    auto cs_add = [&](int p0, int p1) {
#pragma unroll
      for (int p = p0; p < p1; ++p) {
        asm volatile("" : "+v"(cv[p]));
        if (p < 2) ca[p] = cv[p];
        else {
#pragma unroll
          for (int k = 0; k < 4; ++k) {
            float t = ca[p & 1][k];
            const float u = cv[p][k];
            asm("v_add_f32 %0, %0, %1" : "+v"(t) : "v"(u));
            ca[p & 1][k] = t;
          }
        }
      }
      asm volatile("" : "+v"(ca[0]), "+v"(ca[1]));
    };
    u64 vf = 0;
    const u32 fo = flag(F_WO, r, ((tnB & 14) + (g & 1)) ^ (2 * (g & 2)));
    __builtin_amdgcn_s_sleep(PFW0);
    u_load(IC<0>{});
    u_load(IC<1>{});
    for_n<34>([&](auto S) {
      constexpr int s = decltype(S)::value;
      bar();
      if constexpr (s == TOKB) { tok = token(); asm volatile("" : "+s"(tok)); }
      if constexpr (s >= HL && s < HL + 4) {
        if constexpr ((s - HL) % 2 == 0) u_write(IC<(s - HL) / 2>{}); else u_store(IC<(s - HL) / 2>{});
      }
      if constexpr (s == HL + 3 + FLD) { wait_vm<0>(); up(F_WO); }
      if constexpr (s == G0 - 4) vf = ldf(fo);
      if constexpr (s == G0 - 1) { if (!__all(vf == tok)) poll(fo, tok); }
      if constexpr (s == G0) for_n<4>([&](auto I) { u_load(IC<2 + decltype(I)::value>{}); });
      if constexpr (s == G0 + 1) for_n<3>([&](auto I) { u_load(IC<6 + decltype(I)::value>{}); });
    });
    static_assert(TOKB < HL && HL + 3 + FLD < G0 - 4 && G0 + 1 < 33, "schedule");
    for_n<7>([&](auto I) {
      constexpr int i = 2 + decltype(I)::value;
      u_write(IC<i>{});
      u_store(IC<i>{});
      u_sums(IC<i>{});
    });
    bar(); bar(); bar();
    wait_vm<0>();
    up(F_W1);
#pragma nounroll
    for (int u = 3; u <= G1; ++u) bar();
    for_n<7>([&](auto I) {
      u_load(IC<9 + decltype(I)::value>{});
      if constexpr (decltype(I)::value == 1 || decltype(I)::value == 3) bar();
    });
#pragma nounroll
    for (int u = G1 + 3; u < 16 + H2; ++u) bar();
    for_n<14>([&](auto S) {
      constexpr int s = decltype(S)::value, i = 9 + s / 2;
      bar();
      if constexpr (s % 2 == 0) u_write(IC<i>{}); else u_store(IC<i>{});
    });
    for_n<12>([&](auto S) {
      constexpr int s = decltype(S)::value;
      bar();
      if constexpr (s == 3) { wait_vm<16>(); up(F_W2); }
      if constexpr (s < 6) cs_load(OFF_CS1 + tnA * 32768, s * 16 / 3, (s + 1) * 16 / 3);
      if constexpr (s >= 8) cs_add(8 * (s - 8), 8 * (s - 7));
    });
    static_assert(H2 + 14 + 12 <= 31, "schedule");
    *(f32x4*)(smem + L_CS1 + lane * 16) = ca[0] + ca[1];
    wait_lgkm0();
    QUIT();
  } else {
    FRESH_LANE();
    const int tmh = tmA > 0 ? tmA - 1 : 0;
    u64 tok;
    auto bars = [&](int n) {
#pragma nounroll
      for (int u = 0; u < n; ++u) bar();
    };
    auto ror8 = [](float v) { return __uint_as_float(__builtin_amdgcn_mov_dpp(__float_as_uint(v), 0x128, 0xf, 0xf, false)); };
    auto put = [&](int xi, float mean, float m2, bool on, int row) {
      const float sd = __builtin_amdgcn_sqrtf(bfr(bfr(m2 * (1.f / DM)) + LN_EPS));
      st8(pack2(mean, sd) | (tok << 32), on, flag(F_LN + xi, tmA, tnA) + row * 8);
    };
    auto reduce = [&](int xi) {
      const u32 src = OFF_ST + xi * 524288 + span(tmA, c13) + (tnA >> 2) * 16384 + (tnA & 3) * 2048, tag = (u32)tok;
      u32x4 a, b;
      auto ld = [&]() {
        asm volatile("" ::: "memory");
        a = __builtin_bit_cast(u32x4, __builtin_amdgcn_raw_buffer_load_b128(rs, lane * 16, src, 16));
        b = __builtin_bit_cast(u32x4, __builtin_amdgcn_raw_buffer_load_b128(rs, lane * 16 + 1024, src, 16));
      };
      ld();
      bar(); bar(); bar();
#pragma nounroll
      for (int spin = 0; spin < (1 << 22); ++spin) {
        if (__all((((a[1] ^ tag) | (a[3] ^ tag)) | ((b[1] ^ tag) | (b[3] ^ tag))) == 0)) break;
        __builtin_amdgcn_s_sleep(1);
        ld();
      }
      float m = __uint_as_float(a[0]) + __uint_as_float(b[0]);
      m = xsum(m + ror8(m)) * (1.f / 16.f);
      const float d0 = __uint_as_float(a[0]) - m, d1 = __uint_as_float(b[0]) - m;
      float q = __uint_as_float(a[2]) + __uint_as_float(b[2]) + 64.f * (d0 * d0 + d1 * d1);
      asm volatile("" : "+v"(m), "+v"(q));
      bar();
      asm volatile("" : "+v"(m), "+v"(q));
      put(xi, m, xsum(q + ror8(q)), lane < 8, lane);
    };
    auto gather = [&](auto H, int xi) {
      constexpr int n = 2 + decltype(H)::value;
      const u32 tag = (u32)tok, f0 = flag(F_LN + xi, tmA, lane >> 3) + (lane & 7) * 8;
      const u32 f[3] = {f0, f0 + 1024, flag(F_LN + xi, tmh, 14 + (r >> 3)) + (lane & 7) * 8};
      u32x2 v[3];
      auto ld = [&]() {
#pragma unroll
        for (int h = 0; h < n; ++h) v[h] = __builtin_bit_cast(u32x2, ldf(f[h]));
      };
      ld();
      bar(); bar(); bar();
#pragma nounroll
      for (int spin = 0; spin < (1 << 22); ++spin) {
        if (__all(((v[0][1] ^ tag) | (v[1][1] ^ tag) | (v[n - 1][1] ^ tag)) == 0)) break;
        __builtin_amdgcn_s_sleep(1);
        ld();
      }
#pragma unroll
      for (int h = 0; h < n; ++h) {
        const float iv = __builtin_amdgcn_rcpf(bf_hi(v[h][0]));
        const int o = h < 2 ? 64 + 256 * h + lane * 4 : r * 4;
        *(float*)(smem + L_INV + o) = iv;
        *(float*)(smem + L_MNV + o) = n == 3 ? bf_lo(v[h][0]) : bf_lo(v[h][0]) * iv;
      }
      wait_lgkm0();
    };
    auto guard = [&](u32 f, int K, auto&& lim_, auto&& step) {
      int b = 0, lim = 0;
#pragma nounroll
      for (int spin = 0; spin < (1 << 22); ++spin) {
        const u64 v = ldf(f);
        if (b < lim) { bar(); step(b++); } else __builtin_amdgcn_s_sleep(1);
        if (b < lim) { bar(); step(b++); }
        lim = lim_((u32)__ballot(v == tok));
        if (lim >= 100) break;
        lim = min(K, lim);
      }
#pragma nounroll
      while (b < K) { bar(); step(b++); }
    };
    auto ahead = [](int msk, int spp, int D) {
      return [=](u32 m) { const int n = __builtin_ctz(~(m & (m >> 16))); return n >= 16 ? 100 : (n & msk) * spp - D; };
    };
    const __amdgpu_buffer_rsrc_t rx = rsrc(x, SEQ * DM * 2), rq = rsrc(wqkv, DM * 3 * DM * 2);
    u32 wx, ww;
    {
      const int n = idx * 64 + lane, m = idx * 48 + (lane < 48 ? lane : 47), k = (m * 5462) >> 16, p = m - 12 * k;
      wx = (n & 511) * 2048 + ((n >> 9) & 1) * 128 + ((idx >> 4) * 1024 + (xcd >> 2) * 512) * 2048;
      ww = k * 6144 + (p >> 2) * 2048 + (p & 3) * 128 + (xcd & 3) * 512;
    }
    {
      const u64 ep = ldf(OFF_EP);
      u16 bv[4];
#pragma unroll
      for (int h = 0; h < 4; ++h) {
        const int t = min(lane + 64 * h, 223), f = t >> 4, rho = t & 15, wn = f / 7, j = f % 7;
        bv[h] = b1[tnA * 224 + (j == 6 ? 96 + 16 * wn + rho : (4 * wn + (j >> 1)) * 32 + 8 * (rho >> 2) + 4 * (j & 1) + (rho & 3))];
      }
      DMA(rsrc(b2, DM * 2), (LDS_AS void*)(smem + L_B2), 4, lane * 4, (tnB & 14) * 128, 0, 0);
      pin2(wx, ww);
      __builtin_amdgcn_s_sleep(XW);
      bf16x8 xf[16];
#pragma unroll
      for (int e = 0; e < 16; ++e)
        xf[e] = __builtin_bit_cast(bf16x8, __builtin_amdgcn_raw_buffer_load_b128(rx, (r >> 1) * 2048 + (r & 1) * 64 + g * 16 + e * 128,
            (tmA * 128 + tnA * 8) * 2048, 0));
      auto probe = [&](int tn) { return flag(F_PR, tmA ^ (4 * (idx & 1)), tn); };
      const u32x4 one4 = {0x3f803f80u, 0x3f803f80u, 0x3f803f80u, 0x3f803f80u};
      const bf16x8 ones = __builtin_bit_cast(bf16x8, one4);
      f32x4 sm[2], sq[2];
      u32 tlb;
      u64 vt;
      for_n<LG0>([&](auto S) {
        constexpr int s = decltype(S)::value;
        bar();
        if constexpr (s == TOKB - 1) {
          __builtin_amdgcn_raw_buffer_store_b32(0, rs, blockIdx.x < NVOTE ? probe(tnA ^ 4) + (lane & 31) * 4 : 0x80000000u, 0, 16);
#pragma unroll
          for (int h = 0; h < 4; ++h) ((float*)(smem + L_BS))[lane + 64 * h] = bf_lo(bv[h]);
          tok = ((u64)(u32)__builtin_amdgcn_readfirstlane((u32)ep) | ((u64)(u32)__builtin_amdgcn_readfirstlane((u32)(ep >> 32)) << 32))
                * 6364136223846793005ull + 1442695040888963407ull;
          *(LDS_AS u32x4*)(smem + L_CNT) = (u32x4){0u, 0u, (u32)tok, (u32)(tok >> 32)};
          wait_lgkm0();
          asm volatile("s_load_dword %0, %1, 0x0" : "=s"(tlb) : "s"(ws + OFF_EP + 128) : "memory");
        }
        if constexpr (s >= PF0 && (s - PF0) % PFS == 0 && (s - PF0) / PFS < 8 - PFJ) {
          const int j = (PFJ + (s - PF0) / PFS + rot0) & 7;
          DMA(rx, (LDS_AS void*)(smem + L_PF), 4, wx, j * 256, 0, 0);
          DMA(rq, (LDS_AS void*)(smem + L_PF), 4, ww, j * (128 * 6144), 0, 0);
        }
        if constexpr (s >= XP && s < XP + 4) {
#pragma unroll
          for (int e = 4 * (s - XP); e < 4 * (s - XP) + 4; ++e) {
            const f32x4 z = {0.f, 0.f, 0.f, 0.f};
            asm volatile("" : "+v"(xf[e]));
            sm[e & 1] = __builtin_amdgcn_mfma_f32_16x16x32_bf16(ones, xf[e], e < 2 ? z : sm[e & 1], 0, 0, 0);
            sq[e & 1] = __builtin_amdgcn_mfma_f32_16x16x32_bf16(xf[e], xf[e], e < 2 ? z : sq[e & 1], 0, 0, 0);
          }
        }
        if constexpr (s >= XP && s <= XP + 4) asm volatile("" : "+v"(sm[0]), "+v"(sm[1]), "+v"(sq[0]), "+v"(sq[1]));
        if constexpr (s == XP + 4) {
          const f32x4 q4 = sq[0] + sq[1];
          float q = (r & 2) ? ((r & 1) ? q4[3] : q4[2]) : ((r & 1) ? q4[1] : q4[0]), t = sm[0][0] + sm[1][0];
          q += __uint_as_float(__builtin_amdgcn_mov_dpp(__float_as_uint(q), 0xb1, 0xf, 0xf, false));
          t += __uint_as_float(__builtin_amdgcn_mov_dpp(__float_as_uint(t), 0xb1, 0xf, 0xf, false));
          const float m = t * (1.f / DM);
          put(0, m, fmaxf(__builtin_fmaf(-t, m, q), 0.f), g == (r >> 2) && !(r & 1), r >> 1);
        }
        if constexpr (s == PRB) {
          wait_lgkm0();
          asm volatile("" :: "s"(tlb));
          if (blockIdx.x < NVOTE) {
            u64 t0, t1;
            u32 v;
            asm volatile("s_memtime %0\n\ts_waitcnt lgkmcnt(0)\n\ts_load_dword %2, %3, 0x0\n\ts_waitcnt lgkmcnt(0)\n\t"
                         "s_memtime %1\n\ts_waitcnt lgkmcnt(0)" : "=&s"(t0), "=&s"(t1), "=&s"(v) : "s"(ws + probe(tnA)) : "memory");
            const u32 t = min((u32)t1 - (u32)t0, 4095u);
            st8(tok ^ t, lane == 0, flag(F_XC, 0, blockIdx.x));
          }
        }
        if constexpr (s == LG0 - 4) vt = ldf(flag(F_XC, 0, r));
      });
      static_assert(XP >= TOKB && PRB > XP + 4 && LG0 - 4 > PRB && LG0 + 3 <= 31 && L_CNT % 16 == 0, "schedule");
#pragma nounroll
      for (int spin = 0; spin < (1 << 22); ++spin) {
        if (__all((vt ^ tok) < 4096)) break;
        __builtin_amdgcn_s_sleep(1);
        vt = ldf(flag(F_XC, 0, r));
      }
      const u32 t = (u32)(vt ^ tok);
      const u64 slower = __ballot(t > (u32)__builtin_amdgcn_mov_dpp(t, 0x128, 0xf, 0xf, false));
      const u32 c = __builtin_popcount((u32)slower & 255) > 4 ? 8192 : 0;
      *(volatile LDS_AS u32*)(smem + L_C13) = c;
      gather(IC<1>{}, 0);
      c13 = c;
      bars(34 - CLB - (LG0 + 3));
    }
#pragma nounroll
    for (int pair = 0; pair < 2; ++pair) {
#pragma unroll
      for (int e = 0; e < 16; ++e)
        st16(((u32x4){~0u, ~0u, ~0u, ~0u}), xbox(pair, 0) + (e + (e & 8)) * 1024 + lane * 16);
      bars(CLB);
      wait_vm<0>();
      guard(flag(4 * pair + 1, tmB, (rotB + r) & 15), 16, ahead(15, 1, DN), [](int) {});
      guard(flag(4 * pair + 2, tmA, (rotA + r) & 15), KW1, ahead(14, 2, DW), [](int) {});
      reduce(2 * pair + 1);
      bars(LG - (KW1 + 4));
      gather(IC<0>{}, 2 * pair + 1);
      bars(33 - (LG + 3));
      guard(flag(lane < 8 ? 4 * pair + 3 : lane < 16 ? F_2ND + pair : F_3RD + pair, tmB, 8 * (tnB & 1) + ((lane < 8 ? r ^ (rotA >> 1) : r) & 7)), 13,
            [](u32 m) { const int n = __builtin_ctz(~m | (1u << 24)); return n >= 24 ? 100 : min(n, 16) - 3; },
            [&](int b) { if (b == 0 || b == 3) st8(tok, lane == 0, flag(b ? F_3RD + pair : F_2ND + pair, tmA, tnA)); });
      if (pair) break;
      bars(WQ - 13);
      for_n<29 - WQ>([&](auto S) {
        constexpr int s = decltype(S)::value;
        if constexpr (s < 4) {
          DMA(rq, (LDS_AS void*)(smem + L_PF), 4, ww, ((2 * s + (rotA >> 1)) & 7) * (128 * 6144), 0, 0);
          DMA(rq, (LDS_AS void*)(smem + L_PF), 4, ww, ((2 * s + 1 + (rotA >> 1)) & 7) * (128 * 6144), 0, 0);
        }
        bar();
      });
      guard(flag(4, (g & 1) ? tmh : tmA, (rotA + r) & 15), KW1, ahead(14, 2, DW), [](int) {});
      st8(tok, lane == 0 && blockIdx.x == 0, OFF_EP);
      reduce(2);
      bars(LG - (KW1 + 4));
      gather(IC<1>{}, 2);
      bars(33 - CLB - (LG + 3));
      static_assert(WQ <= 25 && LG + 3 <= 31, "schedule");
    }
    QUIT();
  }
#endif
}

extern "C" void kernel_launch(void* const* d_in, const int* in_sizes, int n_in, void* d_out, int out_size,
                              void* d_ws, size_t ws_size, hipStream_t stream) {
  const u16* x0 = (const u16*)d_in[0];
  const u16* wqkv = (const u16*)d_in[1];
  const u16* wo = (const u16*)d_in[2];
  const u16* w1 = (const u16*)d_in[4];
  const u16* b1 = (const u16*)d_in[5];
  const u16* w2 = (const u16*)d_in[6];
  const u16* b2 = (const u16*)d_in[7];
  (void)hipFuncSetAttribute((const void*)mega_k, hipFuncAttributeMaxDynamicSharedMemorySize, LDS_BYTES);
  mega_k<<<256, 512, LDS_BYTES, stream>>>((char*)d_ws + (ws_size - WS_USED - WS_ALIGN), x0, wqkv, wo, w1, b1, w2, b2, (u16*)d_out);
}
```

```cpp
#include <hip/hip_runtime.h>
#include <stdint.h>
#include <type_traits>
#include <utility>

typedef unsigned short u16;
typedef unsigned int u32;
typedef unsigned long long u64;
typedef __bf16 bf16x8 __attribute__((ext_vector_type(8)));
typedef __bf16 bf16x2 __attribute__((ext_vector_type(2)));
typedef short s16x4 __attribute__((ext_vector_type(4)));
typedef float f32x4 __attribute__((ext_vector_type(4)));
typedef float f32x2 __attribute__((ext_vector_type(2)));
typedef u32 u32x4 __attribute__((ext_vector_type(4)));
typedef u32 u32x2 __attribute__((ext_vector_type(2)));
typedef u32 u32x10 __attribute__((ext_vector_type(10)));
typedef int i32x4 __attribute__((ext_vector_type(4)));
typedef int i32x2 __attribute__((ext_vector_type(2)));
#define LDS_AS __attribute__((address_space(3)))
#define GLB_AS __attribute__((address_space(1)))

constexpr int SEQ = 2048, DM = 1024, FF = 3584;
constexpr float LN_EPS = 1e-5f;

#define DN 4
#define DW 5
#define UAUX 2
#define PFW0 30
#define PF0 4
#define PFS 2
#define PFJ 2
#define CQP 5
#define HL 8
#define FLD 3
#define XW 30
#define TOKB 3
#define XP 8
#define LG0 24
#define LG 25
#define WQ 24
#define G0 27
#define G1 12
#define H2 4
#define PUBJ 2
#define CB 1
#define KW1 11
#define CLB 1
#define SND 12
#define RST 9
#define XB2 1
#define XA2 1
#define XBO 1
#define CW 1
#define PRB 13
#define CQG 22
#define R3W 1
#define LVA 16
#define LVB 6
#ifndef NVOTE
#define NVOTE 16
#endif
constexpr u32 MB = 1u << 20;
constexpr u32 QS = 5120;
constexpr u32 XS = 8 * MB, HS = 32 * MB;
constexpr u32 OFF_WO = 0;
constexpr u32 OFF_W1 = 3 * MB;
constexpr u32 OFF_W2 = 13 * MB;
constexpr u32 OFF_CS1 = 22 * MB;
constexpr u32 OFF_FL = 25 * MB;
constexpr u32 OFF_EP = OFF_FL + 26 * 32768;
constexpr u32 OFF_DBG = 26 * MB;
constexpr u32 OFF_XC = 28 * MB;
constexpr u32 OFF_X = 28 * MB;
constexpr u32 OFF_O = OFF_X + 4 * XS;
constexpr u32 OFF_H = OFF_O + 2 * XS;
constexpr u32 OFF_ST = OFF_H + 2 * HS;
constexpr u32 WS_USED = OFF_ST + 2 * MB, WS_ALIGN = 4 * MB;
#define HAS_ALIGN
constexpr int F_WO = 9, F_W1 = 10, F_W2 = 11, F_2ND = 12, F_3RD = 14, F_XC = 18, F_PR = 19, F_LN = 22;
__device__ __forceinline__ u32 qoff(u32 e, u32 slot) {
  return slot * 16 + (e * 1152 + (e == 3 ? 128 : 0)) + (slot >= (e == 3 ? 56 : 24 - 8 * e) ? 128 : 0);
}
__device__ __forceinline__ u32 hpos(u32 t, u32 j) {
  return (j & 2) == 0 ? 16 * (j & 1) + 2 * (t & 7) + (j >> 2) : 32 + 3 * (t & 7) + (j & 1) + (j >> 2) * 2;
}
__device__ __forceinline__ u32 aq(u32 kg, u32 half) { return kg * 32768 + (kg & 1) * 3072 + half * 16384; }
__device__ __forceinline__ u32 ppair(u32 tm) { return (tm & 3) + (tm >> 3) * 4; }
__device__ __forceinline__ u32 xpan(u32 tm, u32 c13) { return ppair(tm) * MB + (((((tm >> 2) ^ (tm >> 1)) & 1) << 13) ^ c13); }
__device__ __forceinline__ u32 hpan(u32 tm, u32 kh, u32 c13) {
  return ppair(tm) * (4 * MB) + (((kh ^ tm) & 1) << 21) + (((((tm >> 2) ^ kh ^ tm) & 1) << 13) ^ c13);
}
__device__ __forceinline__ u32 span(u32 tm, u32 c13) { return ppair(tm) * 65536 + (((tm & 4) << 11) ^ c13); }

constexpr int SLOT = 24576, NRING = 6, RING = SLOT * NRING;
constexpr int L_INV = RING, L_MNV = L_INV + 576, L_CNT = L_MNV + 576, L_TOK = L_CNT + 8, L_C13 = L_TOK + 8;
constexpr int L_CSQ = RING + 1280, L_CS1 = L_CSQ + 1024, L_BS = L_CS1 + 1024;
constexpr int L_XC = L_BS + 1024;
constexpr int L_IM = L_XC + 6144;
constexpr int L_B2 = L_IM + 4096, L_PF = L_B2 + 256;
constexpr int LDS_BYTES = L_PF + 256;

__device__ __forceinline__ float bf_lo(u32 w) { return __uint_as_float(w << 16); }
__device__ __forceinline__ float bf_hi(u32 w) { return __uint_as_float(w & 0xffff0000u); }
__device__ __forceinline__ u32 pack2(float lo, float hi) {
  f32x2 v = {lo, hi};
  bf16x2 r = __builtin_convertvector(v, bf16x2);
  return __builtin_bit_cast(u32, r);
}
__device__ __forceinline__ float bfr(float x) { return bf_lo(pack2(x, 0.f) & 0xffffu); }
__device__ __forceinline__ u32 gelu_tanh2(f32x2 x) {
  const float c0 = -2.3022081983f, c1 = -0.1029432396f;
  const f32x2 p = x * x * c1 + c0;
  const f32x2 a = x * p;
  const f32x2 d = (f32x2){__builtin_amdgcn_exp2f(a[0]), __builtin_amdgcn_exp2f(a[1])} + 1.f;
  const f32x2 g = x * (f32x2){__builtin_amdgcn_rcpf(d[0]), __builtin_amdgcn_rcpf(d[1])};
  return pack2(g[0], g[1]);
}
template <int N_> __device__ __forceinline__ void wait_vm() { asm volatile("s_waitcnt vmcnt(%0)" ::"n"(N_) : "memory"); }
__device__ __forceinline__ void wait_lgkm0() { asm volatile("s_waitcnt lgkmcnt(0)" ::: "memory"); }
__device__ __forceinline__ void bar() {
  __builtin_amdgcn_sched_barrier(0);
  asm volatile("s_barrier" ::: "memory");
  __builtin_amdgcn_sched_barrier(0);
}
template <int V> using IC = std::integral_constant<int, V>;
template <typename F, int... I>
__device__ __forceinline__ void for_seq(F&& f, std::integer_sequence<int, I...>) { (f(IC<I>{}), ...); }
template <int N, typename F> __device__ __forceinline__ void for_n(F&& f) { for_seq(f, std::make_integer_sequence<int, N>{}); }

__device__ __forceinline__ float xsum(float x) {
#if defined(__HIP_DEVICE_COMPILE__)
  auto a = __builtin_amdgcn_permlane16_swap(__float_as_uint(x), __float_as_uint(x), false, false);
  x = __uint_as_float(a[0]) + __uint_as_float(a[1]);
  auto b = __builtin_amdgcn_permlane32_swap(__float_as_uint(x), __float_as_uint(x), false, false);
  x = __uint_as_float(b[0]) + __uint_as_float(b[1]);
#endif
  return x;
}
__device__ __forceinline__ float xmax(float x) {
#if defined(__HIP_DEVICE_COMPILE__)
  auto a = __builtin_amdgcn_permlane16_swap(__float_as_uint(x), __float_as_uint(x), false, false);
  x = fmaxf(__uint_as_float(a[0]), __uint_as_float(a[1]));
  auto b = __builtin_amdgcn_permlane32_swap(__float_as_uint(x), __float_as_uint(x), false, false);
  x = fmaxf(__uint_as_float(b[0]), __uint_as_float(b[1]));
#endif
  return x;
}
__device__ __forceinline__ f32x2 row_part(const u32x4& c0, const u32x4& c1) {
  float v[16];
#pragma unroll
  for (int k = 0; k < 4; ++k) {
    v[2 * k] = bf_lo(c0[k]); v[2 * k + 1] = bf_hi(c0[k]);
    v[8 + 2 * k] = bf_lo(c1[k]); v[8 + 2 * k + 1] = bf_hi(c1[k]);
  }
  float s = 0.f;
#pragma unroll
  for (int k = 0; k < 16; ++k) s += v[k];
  const float m = xsum(s) * (1.f / 64.f);
  float q = 0.f;
#pragma unroll
  for (int k = 0; k < 16; ++k) { const float d = v[k] - m; q += d * d; }
  return (f32x2){m, xsum(q)};
}

#define st16(v, off) __builtin_amdgcn_raw_buffer_store_b128(__builtin_bit_cast(i32x4, v), rs, (off), 0, 16)
__device__ __forceinline__ float sum8(const u32x4& f) {
  float s = bf_lo(f[0]) + bf_hi(f[0]);
#pragma unroll
  for (int k = 1; k < 4; ++k) { s += bf_lo(f[k]); s += bf_hi(f[k]); }
  return s;
}
__device__ __forceinline__ float fold32(float a, float b) {
#if defined(__HIP_DEVICE_COMPILE__)
  const auto x = __builtin_amdgcn_permlane32_swap(__float_as_uint(a), __float_as_uint(b), false, false);
  a = __uint_as_float(x[0]) + __uint_as_float(x[1]);
#endif
  return a;
}
__device__ __forceinline__ float fold16(float a, float b) {
#if defined(__HIP_DEVICE_COMPILE__)
  const auto x = __builtin_amdgcn_permlane16_swap(__float_as_uint(a), __float_as_uint(b), false, false);
  a = __uint_as_float(x[0]) + __uint_as_float(x[1]);
#endif
  return a;
}

__device__ __forceinline__ int fresh_lane(int tid) { int l = tid & 63; asm volatile("" : "+v"(l)); return l; }
__device__ __forceinline__ f32x4 zero4() {
  f32x4 z = {0.f, 0.f, 0.f, 0.f};
  asm volatile("" : "+v"(z));
  return z;
}
template <int BIG = 0> __device__ __forceinline__ void next_slot(int& so) {
  if constexpr (BIG) so = (so + 32768) & 131071; else { so += SLOT; if (so == RING) so = 0; }
}

#define DMA __builtin_amdgcn_raw_ptr_buffer_load_lds
#define SB() __builtin_amdgcn_sched_barrier(0)
__device__ __forceinline__ void mfma(f32x4& c, const bf16x8& a, const bf16x8& b) {
  asm volatile("v_mfma_f32_16x16x32_bf16 %0, %1, %2, %0" : "+v"(c) : "v"(a), "v"(b));
}
template <int OFF> __device__ __forceinline__ void dsrd(bf16x8& d, u32 addr) {
  asm volatile("ds_read_b128 %0, %1 offset:%2" : "=v"(d) : "v"(addr), "n"(OFF));
}
template <int OFF> __device__ __forceinline__ void dstr(bf16x8& d, u32 addr) {
  u32x2 lo, hi;
  asm volatile("ds_read_b64_tr_b16 %0, %2 offset:%3\n\tds_read_b64_tr_b16 %1, %2 offset:%4"
               : "=&v"(lo), "=&v"(hi) : "v"(addr), "n"(OFF), "n"(OFF + 512));
  const u32x4 v = {lo[0], lo[1], hi[0], hi[1]};
  d = __builtin_bit_cast(bf16x8, v);
}
template <int OFF> __device__ __forceinline__ void dstr1(u32x2& d, u32 addr) {
  asm volatile("ds_read_b64_tr_b16 %0, %1 offset:%2" : "=v"(d) : "v"(addr), "n"(OFF));
}
template <int OFF> __device__ __forceinline__ void dsrd1(float& d, u32 addr) {
  asm volatile("ds_read_b32 %0, %1 offset:%2" : "=v"(d) : "v"(addr), "n"(OFF));
}
template <int O0, int O1> __device__ __forceinline__ void dsrd2(f32x2& d, u32 addr) {
  asm volatile("ds_read2_b32 %0, %1 offset0:%2 offset1:%3" : "=v"(d) : "v"(addr), "n"(O0), "n"(O1));
}
template <int OFF> __device__ __forceinline__ void dsrd4(f32x4& d, u32 addr) {
  asm volatile("ds_read_b128 %0, %1 offset:%2" : "=v"(d) : "v"(addr), "n"(OFF));
}
template <class T> __device__ __forceinline__ void pin(T& a) { asm volatile("" : "+v"(a)); }
template <class T, class... R> __device__ __forceinline__ void pin(T& a, R&... r) { pin(a); pin(r...); }
template <class F> constexpr u64 lanes(F f) {
  u64 m = 0;
  for (int l = 0; l < 64; ++l) m |= (u64)(f(l & 15, l >> 4) ? 1 : 0) << l;
  return m;
}
constexpr u64 M_PAR = lanes([](int r, int) { return r & 1; });
constexpr u64 M_OWN0 = lanes([](int r, int g) { return 4 * g + (r & 1) <= r; });
constexpr u64 M_OWN1 = lanes([](int r, int g) { return 4 * g + (r & 1) + 2 <= r; });
#define LANE_IN(m) __builtin_amdgcn_inverse_ballot_w64(m)
__device__ __forceinline__ void pin1(u32& a) { asm volatile("" : "+v"(a)); }
__device__ __forceinline__ void pin2(u32& a, u32& b) { asm volatile("" : "+v"(a), "+v"(b)); }
template <int NSL, int NM, int NR, int ENDB, class AD, class RD, class MM, class HK, class TL>
__device__ __forceinline__ void kloop(int& so, AD&& ad, RD&& rd, MM&& mm, HK&& hook, TL&& tail) {
  constexpr int BIG = NM == 32;
  ad(so, IC<0>{});
  bar();
  for_n<NR>([&](auto Q) { rd(IC<0>{}, Q); });
  next_slot<BIG>(so);
  ad(so, IC<1>{});
  hook(-1);
  auto body = [&](auto S) {
    constexpr int s = decltype(S)::value;
    wait_lgkm0();
    bar();
    for_n<NM>([&](auto M) {
      constexpr int m = decltype(M)::value;
      mm(S, M);
      if constexpr (m < NR) rd(IC<1 - s>{}, IC<m>{});
      if constexpr (m == NR) { next_slot<BIG>(so); asm volatile("" : "+s"(so)); }
      if constexpr (m == NR + 1) ad(so, S);
      SB();
    });
  };
#pragma nounroll
  for (int u = NSL / 2 - 1; u > 0; --u) {
    body(IC<0>{});
    body(IC<1>{});
    hook(NSL / 2 - 1 - u);
  }
  body(IC<0>{});
  wait_lgkm0();
  if constexpr (ENDB) { bar(); so = 0; }
  SB();
  for_n<NM>([&](auto M) { mm(IC<1>{}, M); SB(); tail(M); SB(); });
  asm volatile("s_nop 15");
  SB();
}

template <int NM, int NR, int ENDB, class RD, class MM, class HK, class TL>
__device__ __forceinline__ void kloop6(RD&& rd, MM&& mm, HK&& hook, TL&& tail) {
  bar();
  for_n<NR>([&](auto Q) { rd(IC<0>{}, Q); });
  hook(-1);
  auto body = [&](auto S) {
    constexpr int s = decltype(S)::value;
    wait_lgkm0();
    bar();
    for_n<NM>([&](auto M) {
      constexpr int m = decltype(M)::value;
      mm(IC<s & 1>{}, M);
      if constexpr (m < NR) rd(IC<(s + 1) % NRING>{}, IC<m>{});
      SB();
    });
  };
#pragma nounroll
  for (int u = 5; u > 0; --u) for_n<NRING>(body);
  body(IC<0>{});
  wait_lgkm0();
  if constexpr (ENDB) bar();
  SB();
  for_n<NM>([&](auto M) { mm(IC<1>{}, M); SB(); tail(M); SB(); });
  asm volatile("s_nop 15");
  SB();
}

template <int NSL, int P, int D, int X, class IS, class BR> __device__ __forceinline__ void stream(IS&& issue, BR&& bar) {
#pragma nounroll
  for (int u = 0; u < D + X; ++u) issue(u);
  if constexpr (X) { wait_vm<(D * P < 63 ? D * P : 63)>(); bar(); }
#pragma nounroll
  for (int u = X; u < NSL - D; ++u) {
    wait_vm<((D - 1) * P < 63 ? (D - 1) * P : 63)>();
    bar();
    issue(u + D);
  }
  for_n<D>([&](auto M) { constexpr int n = (D - 1 - decltype(M)::value) * P; wait_vm<(n < 63 ? n : 63)>(); bar(); });
}

#define STAMP(w, i)
#define TS(v)
#define TSW(v, i)
#define TSP 1

constexpr int w1_frag(int i, int e) {
  const int gam = 2 * (i - 2) + (e >> 1), gl = gam % 7;
  return (gam / 7) * 16 + (gl == 3 ? 6 + 7 * (e & 1) : (gl < 3 ? gl * 2 : 7 + (gl - 4) * 2) + (e & 1));
}
__device__ __forceinline__ void warm_code(u32 n, u32 lds) {
#if defined(__HIP_DEVICE_COMPILE__)
  u64 pc;
  u32 back, len;
  asm volatile("s_getpc_b64 %0\n.Lcw%=:\n\ts_mov_b32 %1, .Lcw%=-mega_k\n\ts_mov_b32 %2, .Lcode_end-mega_k" : "=s"(pc), "=s"(back), "=s"(len));
  pc -= back;
  const u32x4 rc = {(u32)pc, (u32)(pc >> 32), len, 0x00020000u};
  asm volatile("s_mov_b32 %0, m0\n\ts_mov_b32 m0, %1\n\ts_nop 0\n\tbuffer_load_dword %2, %3, 0 offen lds\n\ts_mov_b32 m0, %0"
               : "=&s"(back) : "s"(lds), "v"(n * 128), "s"(rc));
#endif
}

template <int N> __device__ __forceinline__ void announce(u32 fl, __amdgpu_buffer_rsrc_t rs) {
  u32x2 tk;
  u32 a, o, f, n;
  asm volatile("s_mov_b64 exec, 1\n\tv_mov_b32 %1, %7\n\tds_read_b64 %0, %1 offset:8\n\tv_mov_b32 %2, 1\n\tv_mov_b32 %3, %5\n\t"
               "s_waitcnt vmcnt(%8)\n\tds_add_rtn_u32 %1, %1, %2\n\ts_waitcnt lgkmcnt(0)\n\t"
               "v_readfirstlane_b32 %4, %1\n\ts_and_b32 %4, %4, 3\n\ts_cmp_eq_u32 %4, 3\n\ts_cbranch_scc0 .Lan%=\n\t"
               "buffer_store_dwordx2 %0, %3, %6, 0 offen sc1\n.Lan%=:\n\ts_mov_b64 exec, -1"
               : "=&v"(tk), "=&v"(a), "=&v"(o), "=&v"(f), "=&s"(n)
               : "s"(fl), "s"(rs), "n"(L_CNT), "n"(N) : "memory", "scc");
  static_assert(L_TOK == L_CNT + 8, "layout");
}

extern "C" __global__ __launch_bounds__(512, 2) void mega_k(char* __restrict__ ws0, const u16* __restrict__ x,
                                                 const u16* __restrict__ wqkv, const u16* __restrict__ wo,
                                                 const u16* __restrict__ w1, const u16* __restrict__ b1,
                                                 const u16* __restrict__ w2, const u16* __restrict__ b2,
                                                 u16* __restrict__ out) {
#if defined(__HIP_DEVICE_COMPILE__)
  extern __shared__ __attribute__((aligned(16))) char smem[];
  char* const ws = ws0 + (-(u32)(uintptr_t)ws0 & (WS_ALIGN - 1));
  const int tid = threadIdx.x;
  const int wid = __builtin_amdgcn_readfirstlane(tid >> 6);
#define FRESH_LANE() const int lane = fresh_lane(tid), r = lane & 15, g = lane >> 4; (void)r; (void)g
  const int xcd = blockIdx.x & 7, idx = blockIdx.x >> 3;
  asm volatile(".subsection 1\n.Lcode_end:\n\t.subsection 0");
  if (CW & 1) warm_code(((tid & 63) * 8 + wid) * 32 + idx, L_PF);
  const int tmA = (idx >> 4) * 8 + (xcd >> 2) * 4 + ((idx >> 2) & 3), tnA = (xcd & 3) * 4 + (idx & 3);
  const int tmB = (idx >> 4) * 8 + (xcd >> 1) * 2 + ((idx >> 3) & 1), tnB = (idx & 7) * 2 + (xcd & 1);
  const int rotA = 8 - 8 * (xcd & 1), rotB = 4 * ((xcd + 1) & 3), rot0 = xcd;
  const __amdgpu_buffer_rsrc_t rs = __builtin_amdgcn_make_buffer_rsrc((void*)ws, 0, WS_USED, 0x00020000);
  u32 c13 = 0;
  auto side = [&]() { c13 = __builtin_amdgcn_readfirstlane(*(const volatile LDS_AS u32*)(smem + L_C13)); };
  auto flag = [&](int set, int tm, int tn) { return OFF_FL + ((((set * 16 + tm) * 16 + tn) * 128) ^ c13); };
  auto xbox = [&](int pair, int o) { return OFF_XC + pair * (4 * MB) + ((idx * 4 + (xcd & 3)) ^ o) * 32768 + (((((tmB >> 2) ^ (idx >> 4)) & 1) << 13) ^ c13); };
  auto rsrc = [&](const u16* p, u32 bytes) { return __builtin_amdgcn_make_buffer_rsrc((void*)p, 0, bytes, 0x00020000); };
  auto token = [&]() {
    const u32x2 tk = *(const volatile LDS_AS u32x2*)(smem + L_TOK);
    return (u64)(u32)__builtin_amdgcn_readfirstlane(tk[0]) | ((u64)(u32)__builtin_amdgcn_readfirstlane(tk[1]) << 32);
  };
  auto ldf = [&](u32 f) {
    asm volatile("" ::: "memory");
    return __builtin_bit_cast(u64, __builtin_amdgcn_raw_buffer_load_b64(rs, f, 0, 16));
  };
  auto poll = [&](u32 f, u64 tok) {
#pragma nounroll
    for (int spin = 0; spin < (1 << 22); ++spin) {
      if (__all(ldf(f) == tok)) break;
      __builtin_amdgcn_s_sleep(1);
    }
    asm volatile("" ::: "memory");
  };
  auto st8 = [&](u64 v, bool on, u32 f) {
    __builtin_amdgcn_raw_buffer_store_b64(__builtin_bit_cast(i32x2, v), rs, on ? f : 0x80000000u, 0, 16);
  };
  int so = 0;
#define QUIT() __builtin_amdgcn_endpgm()

  if (wid < 4) {
    auto publish = [&](auto N, int ph, int tm, int tn) {
      SB();
      announce<decltype(N)::value>(flag(ph + 1, tm, tn), rs);
      SB();
    };
    const float* t_inv = (const float*)(smem + L_INV);
    const float* t_mnv = (const float*)(smem + L_MNV);
    const float* t_cs = (const float*)(smem + L_CS1);
    const float* t_bs = (const float*)(smem + L_BS);
    const int wmi = wid >> 1, wni = wid & 1;

    const int w = 2 * (tnB & 1) + wmi, tn = (tnB & 14) + wni;
    u32x4 c[2][2];
    auto start = [&](f32x4 (&acc)[4][4], const u32x4 (&bb)[2], bool bias) {
#pragma unroll
      for (int i = 0; i < 2; ++i)
#pragma unroll
        for (int j = 0; j < 4; ++j) {
          const u32 c0 = c[i][j >> 1][2 * (j & 1)], c1 = c[i][j >> 1][2 * (j & 1) + 1];
          const u32 b0 = bb[j >> 1][2 * (j & 1)], b1 = bb[j >> 1][2 * (j & 1) + 1];
          f32x4 a = {bf_lo(c0), bf_hi(c0), bf_lo(c1), bf_hi(c1)};
          if (bias) a += (f32x4){bf_lo(b0), bf_hi(b0), bf_lo(b1), bf_hi(b1)};
          asm volatile("" : "+v"(a));
          acc[i][j] = a;
        }
    };
    auto finish = [&](const f32x4 (&acc)[4][4], int xo  , int ph) {
      FRESH_LANE();
#pragma unroll
      for (int i = 0; i < 2; ++i)
#pragma unroll
        for (int gl = 0; gl < 2; ++gl) {
#pragma unroll
          for (int k = 0; k < 4; ++k) {
            const f32x4& a = acc[i][2 * gl + (k >> 1)];
            c[i][gl][k] = pack2(a[2 * (k & 1)], a[2 * (k & 1) + 1]);
          }
          if (xo < 4) st16(c[i][gl], OFF_X + xo * XS + xpan(tmB, c13) + tn * 65536 + aq(gl, w >> 1) + qoff((w & 1) * 2 + i, lane));
          else __builtin_amdgcn_raw_buffer_store_b128(__builtin_bit_cast(i32x4, c[i][gl]), rsrc(out, SEQ * DM * 2),
                   (tmB * 128 + (2 * w + i) * 16 + r) * 2048 + tn * 128 + gl * 64 + g * 16, 0, 16);
        }
      if (xo < 4) {
        SB();
        TS(ta);
        const u32 tag = *(const volatile LDS_AS u32*)(smem + L_TOK);
        const f32x2 s0 = row_part(c[0][0], c[0][1]), s1 = row_part(c[1][0], c[1][1]), s = (g & 1) ? s1 : s0;
        __builtin_amdgcn_raw_buffer_store_b64((i32x2){(int)__float_as_uint((g & 2) ? s[1] : s[0]), (int)tag}, rs,
            OFF_ST + xo * 524288 + span(tmB, c13) + w * 16384 + tn * 128 + (lane & 24) * 256 + (lane & 7) * 16 + (g >> 1) * 8, 0, 16);
        SB();
        TS(tb);
        publish(IC<1>{}, ph, tmB, tnB);
        TS(tc);
      }
      STAMP(0, 4 * ph + 3);
    };

    auto level = [&](int n) {
      f32x4 d[8];
      bf16x8 a[2], b[4];
      u32 p = fresh_lane(tid) * 16;
      pin(p);
      dsrd<0>(a[0], p); dsrd<1024>(a[1], p); dsrd<9216>(b[0], p); dsrd<10240>(b[1], p); dsrd<11264>(b[2], p); dsrd<12288>(b[3], p);
#pragma unroll
      for (int i = 0; i < 8; ++i) d[i] = zero4();
      wait_lgkm0();
#pragma nounroll
      for (int t = 0; t < n; ++t)
#pragma unroll
        for (int i = 0; i < 8; ++i) mfma(d[i], b[i & 3], a[i >> 2]);
      asm volatile("s_nop 15\n\ts_nop 15" ::: "memory");
      pin(d[0], d[1], d[2], d[3], d[4], d[5], d[6], d[7]);
    };

#pragma nounroll
    for (int pair = 0; pair < 2; ++pair) {
      f32x4 aco[4][4];
      {
        f32x4 acc[4][6], acch[2];
        f32x4 muv[4], muh, cs[4];
        f32x2 mu2[2], ivk[2][2], fk[2][2], csv;
        u32 sel[4], oa[2];
        {
        FRESH_LANE();
        STAMP(0, 16 * pair);
        const u32 lB = 9216 + (8 * g + (r >> 2)) * 128 + (((2 * wni) ^ ((r >> 3) & 1) ^ ((g & 1) << 1)) * 32) + (r & 3) * 8;
        const u32 lA = r * 128 + ((g ^ (r >> 1)) * 16);
        auto loop = [&](auto WM, auto RAW) {
          constexpr int wm = decltype(WM)::value, raw = decltype(RAW)::value;
#pragma unroll
          for (int i = 0; i < 4; ++i)
#pragma unroll
            for (int j = 0; j < 6; ++j) acc[i][j] = zero4();
          acch[0] = zero4(); acch[1] = zero4();
          bf16x8 fA[2][4], fH[2];
          u32x2 hB[2][12];
          u32 pA[3][2], pB[3][2], pH[3][2];
          f32x4 accq;
          if (raw) { accq = zero4(); *(u32*)(smem + L_XC + lane * 4) = 0x3f803f80u; }
#pragma unroll
          for (int k = 0; k < 3; ++k) {
            pA[k][0] = (raw ? lA : lane * 16) + 2 * k * SLOT; pA[k][1] = (lA ^ 64) + 2 * k * SLOT;
            pB[k][0] = lB + 2 * k * SLOT; pB[k][1] = (lB ^ 32) + 2 * k * SLOT;
            pin1(pA[k][0]); pin2(pB[k][0], pB[k][1]);
            if (raw) {
              pH[k][0] = r ? pA[k][0] : L_XC - (SLOT + 21504) + lA; pH[k][1] = r ? pA[k][1] : L_XC - (SLOT + 21504) + (lA ^ 64);
              pin1(pA[k][1]); pin2(pH[k][0], pH[k][1]);
            }
          }
          int ln;
          u32 aR, aG, aK[2];
          f32x2 ivq[2];
          float mh, cv;
          float fq[2][4];
          auto fB = [&](int set, int j) {
            return __builtin_bit_cast(bf16x8, (u32x4){hB[set][2 * j][0], hB[set][2 * j][1], hB[set][2 * j + 1][0], hB[set][2 * j + 1][1]});
          };
          kloop6<26 + raw, 17, 0>(
            [&](auto SL, auto Q) {
              constexpr int set = decltype(SL)::value & 1, k = decltype(SL)::value >> 1, q = decltype(Q)::value, h = q - 5;
              if constexpr (q < 4) dsrd<raw ? wm * SLOT + q * 2048 : set * SLOT + wm * 4096 + q * 1024>(fA[set][q], pA[k][raw & set]);
              else if constexpr (q == 4) dsrd<raw ? SLOT + 21504 : set * SLOT + 8192>(fH[set], raw ? pH[k][set] : pA[k][0]);
              else dstr1<set * SLOT + (h >> 2) * 4096 + (h & 1) * 512>(hB[set][h], pB[k][(h >> 1) & 1]);
            },
            [&](auto SET, auto M) {
              constexpr int set = decltype(SET)::value, m = decltype(M)::value, j = m >> 2, i = m & 3;
              if constexpr (m < 16) mfma(acc[i][j], fB(set, j), fA[set][i]);
              else if constexpr (m < 24) mfma(acc[i][j], fA[set][i], fB(set, j));
              else if constexpr (m == 26) mfma(accq, fB(set, wm), fH[set]);
              else if constexpr (wm == 0) mfma(acch[i], fB(set, 2 + i), fH[set]);
              else mfma(acch[i], fH[set], fB(set, 4 + i));
            }, [&](int t) { if (t < 0) STAMP(0, 16 * pair + 1); },
            [&](auto M) {
              constexpr int m = decltype(M)::value;
              if constexpr (m == 0) {
                ln = fresh_lane(tid);
                aR = L_INV + wm * 256 + (ln & 15) * 4; aG = L_INV + wm * 256 + (ln & 48);
                pin(aR, aG);
              }
              if constexpr (m == 1) { dsrd2<16, 32>(ivq[0], aR); dsrd2<48, 64>(ivq[1], aR); dsrd4<640 + 192>(muv[3], aG); }
              if constexpr (m == 2) { aK[0] = aG + (ln & 1) * 4; aK[1] = aK[0] + 64; pin(aK[0], aK[1]); }
              if constexpr (m == 3) {
                const u32 a0 = LANE_IN(M_OWN0) ? aK[1] : aK[0], a1 = LANE_IN(M_OWN1) ? aK[1] : aK[0];
                aK[0] = a0; aK[1] = a1;
                pin(aK[0], aK[1]);
              }
              if constexpr (m == 4) { dsrd2<0, 16>(ivk[0][0], aK[0]); dsrd2<32, 48>(ivk[0][1], aK[0]); dsrd2<2, 18>(ivk[1][0], aK[1]); }
              if constexpr (m == 5) { dsrd2<34, 50>(ivk[1][1], aK[1]); dsrd2<160, 176>(mu2[0], aR); dsrd2<192, 208>(mu2[1], aR); }
              if constexpr (m == 6) { dsrd4<640>(muv[0], aG); dsrd4<640 + 64>(muv[1], aG); dsrd4<640 + 128>(muv[2], aG); }
              if constexpr (m == 7) {
                if constexpr (wm) dsrd4<576 - 256>(muh, aG); else dsrd1<576>(mh, aR);
                aK[0] = L_C13;
                pin(aK[0]);
                dsrd1<0>(cv, aK[0]);
              }
              if constexpr (m == 8) { aR += L_CSQ - L_INV - wm * 256 + wni * 384; aG += L_CSQ - L_INV - wm * 256 + wni * 384; pin(aR, aG); }
              if constexpr (!raw) {
                if constexpr (m == 9) { dsrd4<128>(cs[2], aG); dsrd4<192>(cs[3], aG); dsrd2<64, 80>(csv, aR); }
                if constexpr (m == 10) { dsrd4<0>(cs[0], aG); dsrd4<64>(cs[1], aG); }
              }
              if constexpr (m == 11) { sel[0] = LANE_IN(M_PAR) ? 0x01000c0cu : 0x0c0c0100u; pin(sel[0]); }
              if constexpr (m == 12) { sel[1] = LANE_IN(M_PAR) ? 0x03020c0cu : 0x0c0c0302u; pin(sel[1]); }
              if constexpr (m == 13) {
                sel[2] = LANE_IN(M_OWN0) ? sel[0] : 0x0c0c0c0cu; sel[3] = LANE_IN(M_OWN1) ? sel[1] : 0x0c0c0c0cu;
                pin(sel[2], sel[3]);
              }
              if constexpr (m == 14) {
                sel[0] = LANE_IN(M_OWN0) ? 0x0c0c0c0cu : sel[0]; sel[1] = LANE_IN(M_OWN1) ? 0x0c0c0c0cu : sel[1];
                pin(sel[0], sel[1]);
              }
              if constexpr (m == 16) {
                wait_lgkm0();
                pin(ivq[0], ivq[1], muv[0], muv[1], muv[2], muv[3], ivk[0][0], ivk[0][1], ivk[1][0], ivk[1][1]);
                pin(mu2[0], mu2[1], cv);
                if constexpr (wm) pin(muh); else { pin(mh); muh = (f32x4){mh, mh, mh, mh}; }
                if constexpr (!raw) pin(cs[0], cs[1], cs[2], cs[3], csv);
              }
              if constexpr (m >= 17 && m < 21) {
                constexpr int i = m - 17;
                float cq = ivq[i >> 1][i & 1] * (0.17677669529663687f * 1.4426950408889634f);
                pin(cq);
                fq[0][i] = ivk[0][i >> 1][i & 1] * cq; fq[1][i] = ivk[1][i >> 1][i & 1] * cq;
                pin(fq[0][i], fq[1][i]);
                if constexpr (i & 1) { fk[0][i >> 1] = (f32x2){fq[0][i - 1], fq[0][i]}; fk[1][i >> 1] = (f32x2){fq[1][i - 1], fq[1][i]}; }
              }
              if constexpr (m == 21) {
                oa[0] = ((OFF_O + pair * XS + xpan(tmA, 0) + tnA * 65536 + aq(wni, wm)) ^ __float_as_uint(cv)) + ln * 16;
                oa[1] = oa[0] + 128;
                pin(oa[0], oa[1]);
              }
              if constexpr (m == 22) c13 = __builtin_amdgcn_readfirstlane(__float_as_uint(cv));
            });
          if constexpr (raw) {
            FRESH_LANE();
            if (LANE_IN(0x0001000100010001ull)) {
              *(f32x4*)(smem + aG + wm * 64) = accq;
              if (wm == 0) { *(f32x4*)(smem + aG + 128) = acch[0]; *(f32x4*)(smem + aG + 192) = acch[1]; }
            }
            if (wm == 1 && LANE_IN(0xffffull)) { *(float*)(smem + aR + 256) = acch[0][0]; *(float*)(smem + aR + 320) = acch[1][0]; }
            wait_lgkm0();
            bar();
            dsrd4<128>(cs[2], aG); dsrd4<192>(cs[3], aG); dsrd2<64, 80>(csv, aR); dsrd4<0>(cs[0], aG); dsrd4<64>(cs[1], aG);
#pragma unroll
            for (int i = 0; i < 2; ++i)
#pragma unroll
              for (int gl = 0; gl < 2; ++gl)
                c[i][gl] = __builtin_bit_cast(u32x4, __builtin_amdgcn_raw_buffer_load_b128(rsrc(x, SEQ * DM * 2),
                    (16 * i + r) * 2048 + g * 16, (tmB * 128 + w * 32) * 2048 + tn * 128 + gl * 64, 0));
            wait_lgkm0();
            pin(cs[0], cs[1], cs[2], cs[3], csv);
          }
        };
        if (pair == 0) { if (wmi == 0) loop(IC<0>{}, IC<1>{}); else loop(IC<1>{}, IC<1>{}); }
        else { if (wmi == 0) loop(IC<0>{}, IC<0>{}); else loop(IC<1>{}, IC<0>{}); }
        }
        FRESH_LANE();
        STAMP(0, 16 * pair + 2);
        auto fix_qk = [&](const f32x4& a0, const f32x4& a1, float mu, int f) {
          const f32x4 y0 = a0 - cs[f] * mu, y1 = a1 - cs[f + 1] * mu;
          u32x4 v = {pack2(y0[0], y0[1]), pack2(y0[2], y0[3]), pack2(y1[0], y1[1]), pack2(y1[2], y1[3])};
          pin(v);
          return v;
        };
        auto fix_v = [&](const f32x4& a, const f32x4& mu, float cw) {
          const f32x4 y = a - mu * cw;
          u32x2 v = {pack2(y[0], y[1]), pack2(y[2], y[3])};
          pin(v);
          return v;
        };
        u32x4 Qf[4], Kf[5];
        u32x2 Vf[5][2];
        char* xc = smem + L_XC + wni * 3072;
        Kf[4] = fix_qk(acc[3][2], acc[3][3], mu2[1][1], 2);
        Vf[4][0] = fix_v(acc[3][4], muv[3], csv[0]); Vf[4][1] = fix_v(acc[3][5], muv[3], csv[1]);
        if (wmi == 0) {
          *(u32x4*)(xc + lane * 16) = Kf[4];
          *(u32x2*)(xc + 1024 + lane * 8) = Vf[4][0]; *(u32x2*)(xc + 1536 + lane * 8) = Vf[4][1];
          Kf[0] = fix_qk(acch[0], acch[1], muh[0], 2);
        } else {
          Kf[0] = Kf[4];
          *(u32x2*)(xc + 2048 + lane * 8) = fix_v(acch[0], muh, csv[0]);
          *(u32x2*)(xc + 2560 + lane * 8) = fix_v(acch[1], muh, csv[1]);
        }
#pragma unroll
        for (int i = 0; i < 3; ++i) Kf[i + 1] = fix_qk(acc[i][2], acc[i][3], mu2[i >> 1][i & 1], 2);
        wait_lgkm0();
        bar();
        TS(td);
        if (wmi == 1) Kf[0] = *(const u32x4*)(xc + lane * 16);
        Vf[0][0] = *(const u32x2*)(xc + (wmi == 0 ? 2048 : 1024) + lane * 8);
        Vf[0][1] = *(const u32x2*)(xc + (wmi == 0 ? 2560 : 1536) + lane * 8);
        const f32x4 z = {0.f, 0.f, 0.f, 0.f};
        f32x4 S[4][2];
#pragma unroll
        for (int i = 3; i >= 0; --i) {
          Qf[i] = fix_qk(acc[i][0], acc[i][1], mu2[i >> 1][i & 1], 0);
          S[i][1] = __builtin_amdgcn_mfma_f32_16x16x32_bf16(__builtin_bit_cast(bf16x8, Kf[i + 1]), __builtin_bit_cast(bf16x8, Qf[i]), z, 0, 0, 0);
          S[i][0] = __builtin_amdgcn_mfma_f32_16x16x32_bf16(__builtin_bit_cast(bf16x8, Kf[i]), __builtin_bit_cast(bf16x8, Qf[i]), z, 0, 0, 0);
        }
#pragma unroll
        for (int i = 0; i < 3; ++i) { Vf[i + 1][0] = fix_v(acc[i][4], muv[i], csv[0]); Vf[i + 1][1] = fix_v(acc[i][5], muv[i], csv[1]); }
        const bool par = LANE_IN(M_PAR), own0 = LANE_IN(M_OWN0), own1 = LANE_IN(M_OWN1);
        f32x2 l[2][2];
#pragma unroll
        for (int i = 3; i >= 0; --i) {
          l[0][i >> 1][i & 1] = own0 ? (par ? S[i][1][1] : S[i][1][0]) : (par ? S[i][0][1] : S[i][0][0]);
          l[1][i >> 1][i & 1] = own1 ? (par ? S[i][1][3] : S[i][1][2]) : (par ? S[i][0][3] : S[i][0][2]);
        }
#pragma unroll
        for (int i = 0; i < 4; ++i) l[i >> 1][i & 1] *= fk[i >> 1][i & 1];
        {
          const u64 first = tmA == 0 && wmi == 0 ? ~0ull : 0ull;
          if (LANE_IN(first & ~M_OWN0)) l[0][0][0] = -1.4426950e9f;
          if (LANE_IN(first & ~M_OWN1)) l[1][0][0] = -1.4426950e9f;
        }
        auto all4 = [](f32x2 (&v)[2], auto f, auto mid) {
          auto U = [](float a) { return __float_as_uint(a); };
          auto F = [](u32 a) { return __uint_as_float(a); };
          const auto a = __builtin_amdgcn_permlane32_swap(U(v[0][0]), U(v[1][0]), false, false);
          const auto b = __builtin_amdgcn_permlane32_swap(U(v[0][1]), U(v[1][1]), false, false);
          const auto c = __builtin_amdgcn_permlane16_swap(U(f(F(a[0]), F(a[1]))), U(f(F(b[0]), F(b[1]))), false, false);
          const u32 t = U(mid(f(F(c[0]), F(c[1]))));
          const auto d = __builtin_amdgcn_permlane16_swap(t, t, false, false);
          const auto e0 = __builtin_amdgcn_permlane32_swap(d[0], d[0], false, false);
          const auto e1 = __builtin_amdgcn_permlane32_swap(d[1], d[1], false, false);
          v[0] = (f32x2){F(e0[0]), F(e1[0])}; v[1] = (f32x2){F(e0[1]), F(e1[1])};
        };
        f32x2 t[2] = {__builtin_elementwise_maximum(l[0][0], l[1][0]), __builtin_elementwise_maximum(l[0][1], l[1][1])};
        all4(t, [](float a, float b) { return __builtin_elementwise_maximum(a, b); }, [](float a) { return a; });
#pragma unroll
        for (int i = 0; i < 4; ++i) {
          const f32x2 d = l[i >> 1][i & 1] - t[i & 1];
          l[i >> 1][i & 1] = (f32x2){__builtin_amdgcn_exp2f(d[0]), __builtin_amdgcn_exp2f(d[1])};
        }
        t[0] = l[0][0] + l[1][0]; t[1] = l[0][1] + l[1][1];
        all4(t, [](float a, float b) { return a + b; }, [](float a) { return __builtin_amdgcn_rcpf(a); });
#pragma unroll
        for (int i = 0; i < 4; ++i) l[i >> 1][i & 1] = l[i >> 1][i & 1] * ivk[i >> 1][i & 1] * t[i & 1];
        u32x10 V[2];
#pragma unroll
        for (int i = 0; i < 10; ++i) { V[0][i] = Vf[i >> 1][0][i & 1]; V[1][i] = Vf[i >> 1][1][i & 1]; }
        pin(V[0], V[1]);
        for_n<4>([&](auto I) {
          constexpr int i = decltype(I)::value;
          const u32 pw = pack2(l[0][i >> 1][i & 1], l[1][i >> 1][i & 1]);
          const u32x4 P = {__builtin_amdgcn_perm(pw, pw, sel[0]), __builtin_amdgcn_perm(pw, pw, sel[1]),
                           __builtin_amdgcn_perm(pw, pw, sel[2]), __builtin_amdgcn_perm(pw, pw, sel[3])};
          const u32x4 V0 = __builtin_shufflevector(V[0], V[0], 2 * i, 2 * i + 1, 2 * i + 2, 2 * i + 3);
          const u32x4 V1 = __builtin_shufflevector(V[1], V[1], 2 * i, 2 * i + 1, 2 * i + 2, 2 * i + 3);
          const f32x4 O0 = __builtin_amdgcn_mfma_f32_16x16x32_bf16(__builtin_bit_cast(bf16x8, V0), __builtin_bit_cast(bf16x8, P), z, 0, 0, 0);
          const f32x4 O1 = __builtin_amdgcn_mfma_f32_16x16x32_bf16(__builtin_bit_cast(bf16x8, V1), __builtin_bit_cast(bf16x8, P), z, 0, 0, 0);
          const u32x4 o = {pack2(O0[0], O0[1]), pack2(O0[2], O0[3]), pack2(O1[0], O1[1]), pack2(O1[2], O1[3])};
          st16(o, (LANE_IN(~0ull << (i == 3 ? 56 : 24 - 8 * i)) ? oa[1] : oa[0]) + qoff(i, 0));
        });
        SB();
        TS(te);
        start(aco, c[0], false);
        publish(IC<0>{}, 4 * pair, tmA, tnA);
        if (pair == 0) level(LVA);
        TSW(td, 35); TSW(te, 34);
        STAMP(0, 16 * pair + 3);
      }
      {
        FRESH_LANE();
        bf16x8 fA[2][2][2], fB[2][2][4];
        u32 pA, pB;
        so = 2 * SLOT;
        kloop<16, 16, 12, 0>(so,
          [&](int s_, auto) { pA = lane * 16 + wmi * 2048 + s_; pB = lane * 16 + wni * 4096 + s_; pin2(pA, pB); },
          [&](auto SET, auto Q) {
            constexpr int set = decltype(SET)::value, q = decltype(Q)::value, s = q / 6, m = q % 6;
            if constexpr (m < 2) dsrd<s * 4096 + m * 1024>(fA[set][s][m], pA);
            else dsrd<8192 + s * 8192 + (m - 2) * 1024>(fB[set][s][m - 2], pB);
          },
          [&](auto SET, auto M) {
            constexpr int set = decltype(SET)::value, m = decltype(M)::value, s = m >> 3, j = (m >> 1) & 3, i = m & 1;
            mfma(aco[i][j], fB[set][s][j], fA[set][s][i]);
          },
          [&](int t) { if (t < 0) STAMP(0, 16 * pair + 5); }, [](auto) {});
        STAMP(0, 16 * pair + 6);
        finish(aco, 2 * pair + 1, 4 * pair + 1);
        if (pair == 0) level(LVB);
      }
      {
        f32x4 acc[4][7];
        {
        FRESH_LANE();
#pragma unroll
        for (int i = 0; i < 4; ++i)
#pragma unroll
          for (int j = 0; j < 7; ++j) acc[i][j] = zero4();
        bf16x8 fA[2][4], fB[2][7];
        u32 pA[3], pB[3];
#pragma unroll
        for (int k = 0; k < 3; ++k) {
          pA[k] = lane * 16 + wmi * 4096 + 2 * k * SLOT; pB[k] = lane * 16 + wni * 7168 + 2 * k * SLOT;
          pin2(pA[k], pB[k]);
        }
        kloop6<28, 11, 1>(
          [&](auto SL, auto Q) {
            constexpr int set = decltype(SL)::value & 1, k = decltype(SL)::value >> 1, q = decltype(Q)::value;
            if constexpr (q < 4) dsrd<set * SLOT + q * 1024>(fA[set][q], pA[k]);
            else dsrd<set * SLOT + 8192 + (q - 4) * 1024>(fB[set][q - 4], pB[k]);
          },
          [&](auto SET, auto M) {
            constexpr int set = decltype(SET)::value, m = decltype(M)::value, j = m >> 2, i = m & 3;
            mfma(acc[i][j], fB[set][j], fA[set][i]);
          }, [&](int t) { if (t < 0) STAMP(0, 16 * pair + 9); }, [](auto) {});
        }
        FRESH_LANE();
        STAMP(0, 16 * pair + 10);
        float iv[4], mv[4];
#pragma unroll
        for (int i = 0; i < 4; ++i) { iv[i] = t_inv[16 + wmi * 64 + 16 * i + r]; mv[i] = t_mnv[16 + wmi * 64 + 16 * i + r]; }
        const u32 hbase = OFF_H + pair * HS + hpan(tmA, tnA >> 3, c13);
        auto act = [&](const f32x4& a, const f32x4& cs, const f32x4& bs, int i) {
          const f32x4 y = a * iv[i] + (bs - cs * mv[i]);
          return (u32x2){gelu_tanh2((f32x2){y[0], y[1]}), gelu_tanh2((f32x2){y[2], y[3]})};
        };
#pragma unroll
        for (int jp = 0; jp < 3; ++jp) {
          const int t0 = (wni * 7 + 2 * jp) * 16 + 4 * g;
          const f32x4 cs0 = *(const f32x4*)(t_cs + t0), cs1 = *(const f32x4*)(t_cs + t0 + 16);
          const f32x4 bs0 = *(const f32x4*)(t_bs + t0), bs1 = *(const f32x4*)(t_bs + t0 + 16);
#pragma unroll
          for (int i = 0; i < 4; ++i) {
            const u32x2 a = act(acc[i][2 * jp], cs0, bs0, i), b = act(acc[i][2 * jp + 1], cs1, bs1, i);
            const u32x4 v = {a[0], a[1], b[0], b[1]};
            st16(v, hbase + aq(hpos(tnA, wni * 4 + jp), wmi) + qoff(i, lane));
            if (4 * jp + i == 3 + PUBJ) publish(IC<PUBJ>{}, 4 * pair + 2, tmA, tnA);
          }
          if (jp < 2) SB();
        }
        {
          const int t0 = (wni * 7 + 6) * 16 + 4 * g;
          const f32x4 cs0 = *(const f32x4*)(t_cs + t0), bs0 = *(const f32x4*)(t_bs + t0);
#pragma unroll
          for (int i = 0; i < 4; i += 2) {
            const u32x2 a = act(acc[i][6], cs0, bs0, i), b = act(acc[i + 1][6], cs0, bs0, i + 1);
            const auto s0 = __builtin_amdgcn_permlane16_swap(a[0], b[0], false, false);
            const auto s1 = __builtin_amdgcn_permlane16_swap(a[1], b[1], false, false);
            const u32x4 v = {s0[0], s1[0], s0[1], s1[1]};
            st16(v, hbase + aq(hpos(tnA, 3), wmi) + qoff(i + (g & 1), (2 * wni + (g >> 1)) * 16 + r));
          }
        }
        STAMP(0, 16 * pair + 11);
      }
      {
        FRESH_LANE();
        f32x4 acc[4][4];
        SB();
        const u32x4 bb[2] = {*(const u32x4*)(smem + L_B2 + wni * 128 + g * 16), *(const u32x4*)(smem + L_B2 + wni * 128 + 64 + g * 16)};
#pragma unroll
        for (int i = 2; i < 4; ++i)
#pragma unroll
          for (int j = 0; j < 4; ++j) acc[i][j] = zero4();
        start(acc, bb, true);
        bf16x8 fA[2][2][4], fB[2][2][4];
        u32 pA, pB;
        const u32 xc = wmi * 16384 + wni * 4096;
        wait_vm<6>();
        so = 0;
        kloop<28, 32, 16, 1>(so,
          [&](int s_, auto) { pA = lane * 16 + wmi * 2048 + s_; pB = lane * 16 + wni * 4096 + s_; pin2(pA, pB); },
          [&](auto SET, auto Q) {
            constexpr int set = decltype(SET)::value, q = decltype(Q)::value, s = q >> 3, m = q & 7;
            if constexpr (m < 4) dsrd<s * 8192 + (m >> 1) * 4096 + (m & 1) * 1024>(fA[set][s][m], pA);
            else dsrd<16384 + s * 8192 + (m - 4) * 1024>(fB[set][s][m - 4], pB);
          },
          [&](auto SET, auto M) {
            constexpr int set = decltype(SET)::value, m = decltype(M)::value;
            constexpr int s = (m >> 3) & 1, j = (m >> 1) & 3, i = (m & 1) + (m < 16 ? 2 : 0);
            mfma(acc[i][j], fB[set][s][j], fA[set][s][i]);
          },
          [&](int t) { if (t < 0) { STAMP(0, 16 * pair + 13); } else wait_vm<0>(); },
          [&](auto M) {
            constexpr int k = decltype(M)::value - SND - 1;
            if constexpr (k >= 0 && k < 8 && k % 2 == 0) {
              const f32x4 a = acc[2][k >> 1], b = acc[3][k >> 1];
              const u32x4 v = {pack2(a[0], a[1]), pack2(a[2], a[3]), pack2(b[0], b[1]), pack2(b[2], b[3])};
              st16(__builtin_elementwise_min(v, (u32x4){~1u, ~1u, ~1u, ~1u}), xbox(pair, 0) + xc + lane * 16 + k * 512);
            }
          });
        STAMP(0, 16 * pair + 14);
        u32x4 xv[4];
#pragma nounroll
        for (int spin = 0; spin < (1 << 22); ++spin) {
#pragma unroll
          for (int k = 0; k < 4; ++k)
            xv[k] = __builtin_bit_cast(u32x4, __builtin_amdgcn_raw_buffer_load_b128(rs, lane * 16 + k * 1024, xbox(pair, 1) + xc, 16));
          const u32x4 o = __builtin_elementwise_max(__builtin_elementwise_max(xv[0], xv[1]), __builtin_elementwise_max(xv[2], xv[3]));
          if (!__any(max(max(o[0], o[1]), max(o[2], o[3])) == ~0u)) break;
          asm volatile("" ::: "memory");
        }
#pragma unroll
        for (int k = 0; k < 4; ++k) {
          acc[0][k] += (f32x4){bf_lo(xv[k][0]), bf_hi(xv[k][0]), bf_lo(xv[k][1]), bf_hi(xv[k][1])};
          acc[1][k] += (f32x4){bf_lo(xv[k][2]), bf_hi(xv[k][2]), bf_lo(xv[k][3]), bf_hi(xv[k][3])};
        }
        finish(acc, pair == 0 ? 2 : 4, 4 * pair + 3);
      }
    }
  } else if (wid == 4) {
    {
      FRESH_LANE();
      STAMP(1, 0);
      const __amdgpu_buffer_rsrc_t rx = rsrc(x, SEQ * DM * 2);
      u32 vo[4];
#pragma unroll
      for (int e = 0; e < 4; ++e)
        vo[e] = (8 * e + (lane >> 3)) * 2048 + (((lane & 7) ^ (4 * (e & 1) + (lane >> 4))) * 16) - e * 1024;
      const u32 sx = tmA * 128 * 2048, sh = (tmA > 0 ? tmA * 128 - 16 : 0) * 2048;
      auto half = [&](auto H, int U) {
        constexpr int h = decltype(H)::value;
        U = (U + 2 * rot0) & 15;
        for_n<8>([&](auto E) {
          constexpr int e = decltype(E)::value;
          DMA(rx, (LDS_AS void*)(smem + so + h * SLOT + (e >> 2) * 4096), 16, vo[e & 3],
              sx + (h * 64 + (e >> 2) * 32) * 2048 + U * 128, (e & 3) * 1024, 0);
        });
        DMA(rx, (LDS_AS void*)(smem + so + SLOT + 21504), 16, vo[h], sh + U * 128, h * 1024, 0);
        if constexpr (h == 1) { so += 2 * SLOT; if (so == RING) so = 0; }
      };
#pragma nounroll
      for (int U = 0; U < 3; ++U) { half(IC<0>{}, U); half(IC<1>{}, U); }
      wait_vm<36>();
      bar();
      bar();
#pragma nounroll
      for (int U = 3; U < 16; ++U) {
        wait_vm<18>();
        bar();
        half(IC<0>{}, U);
        bar();
        half(IC<1>{}, U);
      }
      wait_vm<18>(); bar(); bar();
      wait_vm<0>(); bar(); bar(); bar();
    }
    auto phase = [&](auto NSL_, auto HALO_, u32 src, u32 hsrc, int ph  , int tm, int tmh) {
      constexpr int NSL = decltype(NSL_)::value, HALO = decltype(HALO_)::value;
      constexpr int BIG = NSL == 28, PA = BIG ? 16 : 8, PT = PA + HALO, D = BIG ? 3 : NSL == 16 ? DN : DW, X = BIG ? XA2 : 0;
      FRESH_LANE();
      u32 vo[4];
#pragma unroll
      for (int e = 0; e < 4; ++e) vo[e] = qoff(e, lane) - e * 1024;
      poll(flag(ph + 1, (lane & 16) ? tmh : tm, BIG ? 8 * (tnB & 1) + ((rotA >> 1) + (r < D + X ? r : 0))
                                               : NSL == 16 ? (rotB + (r < DN ? r : 0)) & 15 : rotA + (r & (DW > 4 ? 3 : 1))), token());
      STAMP(1, 4 * (ph + 1));
      const u32 flip = BIG ? (tnB & 1) * 16384 : 0;
      auto issue = [&](int u) {
        u = BIG ? (u < 8 ? u ^ (rotA >> 1) : u) : NSL == 16 ? (u + rotB) & 15 : u ^ (2 * rotA);
        if constexpr (BIG && X) wait_vm<47>();
        const u32 ko = aq(NSL == 32 ? u : 2 * u, 0);
        for_n<PA>([&](auto E) {
          constexpr int e = decltype(E)::value;
          DMA(rs, (LDS_AS void*)(smem + so + (e >> 2) * 4096), 16, vo[e & 3],
              src + ko + (NSL == 16 ? aq(e >> 2, 0) : aq(e >> 3, (e >> 2) & 1) + ((e & 4) ? -flip : flip)), (e & 3) * 1024, 16);
        });
        if constexpr (HALO) DMA(rs, (LDS_AS void*)(smem + so + 8192), 16, vo[3], hsrc + ko, 0, 16);
        next_slot<BIG>(so);
      };
      stream<NSL, PT, D, X>(issue, [&]() { bar(); });
    };
#pragma nounroll
    for (int pair = 0; pair < 2; ++pair) {
      const int tmh = tmA > 0 ? tmA - 1 : 0;
      const u32 xq = OFF_X + 2 * pair * XS;
      if (pair) phase(IC<32>{}, IC<1>{}, xq + xpan(tmA, c13), xq + xpan(tmh, c13) + 16384 + 3072, 3, tmA, tmh);
      side();
      bar();
      phase(IC<16>{}, IC<0>{}, OFF_O + pair * XS + xpan(tmB, c13) + aq(0, tnB & 1), 0, 4 * pair, tmB, tmB);
      phase(IC<32>{}, IC<0>{}, xq + XS + xpan(tmA, c13), 0, 4 * pair + 1, tmA, tmA);
      bar();
      so = 0;
      phase(IC<28>{}, IC<0>{}, OFF_H + pair * HS + hpan(tmB, tnB & 1, c13), 0, 4 * pair + 2, tmB, tmB);
      bar();
      so = 0;
    }
  } else if (wid == 5) {
    FRESH_LANE();
    auto phase = [&](auto NSL_, auto PB_, int rot, auto&& piece) {
      constexpr int NSL = decltype(NSL_)::value, PB = decltype(PB_)::value;
      auto issue = [&](int u) {
        u = NSL == 28 ? (u < 8 ? u ^ rot : u) : NSL == 16 ? (u + rot) & 15 : u ^ rot;
        wait_vm<63 - PB>();
        for_n<PB>([&](auto I) { piece(I, u); });
        next_slot<NSL == 28>(so);
      };
      stream<NSL, PB, (NSL == 28 ? 3 : 5), (NSL == 28 ? XB2 : NSL == 16 ? XBO : 0)>(issue, [&]() { bar(); });
    };
    auto tiles = [&](auto NSL_, auto PB_, auto BOFF_, u32 src) {
      constexpr int NSL = decltype(NSL_)::value, PB = decltype(PB_)::value, BOFF = decltype(BOFF_)::value;
      u32 vo[4];
#pragma unroll
      for (int e = 0; e < 4; ++e) vo[e] = qoff(e, lane) - e * 1024;
      phase(NSL_, PB_, NSL == 28 ? rotA >> 1 : NSL == 16 ? rotB : 2 * rotA, [&](auto I, int u) {
        constexpr int i = decltype(I)::value;
        DMA(rs, (LDS_AS void*)(smem + so + BOFF + (i >> 2) * 4096), 16, vo[i & 3],
            PB == 16 ? src + u * (2 * QS) + (i >> 3) * QS + ((i >> 2) & 1) * ((NSL == 28 ? 112 : 32) * QS)
                     : src + u * (4 * QS) + (i >> 2) * QS, (i & 3) * 1024, 16);
      });
    };
#pragma nounroll
    for (int pair = 0; pair < 2; ++pair) {
      {
        const __amdgpu_buffer_rsrc_t rq = rsrc(wqkv, DM * 3 * DM * 2);
        u32 vo[4];
#pragma unroll
        for (int e = 0; e < 4; ++e)
          vo[e] = (8 * e + (lane >> 3)) * 6144 + (((lane & 7) ^ (2 * ((lane >> 4) & 1) + 4 * (e & 1))) * 16) - e * 1024;
        phase(IC<32>{}, IC<12>{}, pair ? 2 * rotA : 0, [&](auto I, int u) {
          constexpr int i = decltype(I)::value;
          if (pair == 0) u = (u + 4 * rot0) & 31;
          DMA(rq, (LDS_AS void*)(smem + so + 9216 + (i >> 2) * 4096), 16, vo[i & 3],
              u * (32 * 6144) + (i >> 2) * 2048 + tnA * 128, (i & 3) * 1024, 0);
        });
      }
      if (pair == 0) bar();
      bar();
      tiles(IC<16>{}, IC<16>{}, IC<8192>{}, OFF_WO + (tnB & 14) * (32 * QS));
      if (pair == 0) poll(flag(F_W1, r, (tnA & 14) + (g & 1)), token());
      tiles(IC<32>{}, IC<14>{}, IC<8192>{}, OFF_W1 + tnA * (32 * 4 * QS));
      bar();
      so = 0;
      if (pair == 0) poll(flag(F_W2, 8 * (tnB & 1) + (r & 7), (tnB & 14) + (r >> 3)), token());
      tiles(IC<28>{}, IC<16>{}, IC<16384>{}, OFF_W2 + ((tnB & 14) * 112 + (tnB & 1) * 56) * QS);
      bar();
      so = 0;
    }
  } else if (wid == 6) {
    FRESH_LANE();
    const int rr = lane >> 3, c16 = (lane & 7) * 16;
    const __amdgpu_buffer_rsrc_t rq = rsrc(wqkv, DM * 3 * DM * 2);
    if (R3W)
      DMA(rq, (LDS_AS void*)(smem + L_PF), 4, lane < 24 && !(xcd & 1) ? ((rot0 * 128 + idx * 8 + (lane & 7)) & 1023) * 6144 + (lane >> 3) * 2048 : 0x80000000u,
          (xcd & 3) * 512 + 384, 0, 0);
    u32x4 U[7][4], F[4];
    auto u_load = [&](auto I) {
      constexpr int i = decltype(I)::value;
#pragma unroll
      for (int e = 0; e < 4; ++e) {
        if constexpr (i < 2)
          U[(i + 5) % 7][e] = __builtin_bit_cast(u32x4, __builtin_amdgcn_raw_buffer_load_b128(rsrc(wo, DM * DM * 2),
              ((rr & 3) + 16 * (rr >> 2)) * 2048 + c16, ((2 * tmA + i) * 32 + 4 * e) * 2048 + (tnA ^ (4 * i)) * 128, UAUX));
        else if constexpr (i < 9)
          U[(i + 5) % 7][e] = __builtin_bit_cast(u32x4, __builtin_amdgcn_raw_buffer_load_b128(rsrc(w1, DM * FF * 2),
              rr * 7168 + c16, ((2 * tmA + (tnA & 1)) * 32 + 8 * e) * 7168 + (7 * (tnA >> 1) + i - 2) * 128, UAUX));
        else
          U[(i + 5) % 7][e] = __builtin_bit_cast(u32x4, __builtin_amdgcn_raw_buffer_load_b128(rsrc(w2, DM * FF * 2),
              rr * 2048 + c16, ((7 * tmA + i - 9) * 32 + 8 * e) * 2048 + tnA * 128, UAUX));
      }
    };
    auto u_write = [&](auto I) {
      constexpr int i = decltype(I)::value;
#pragma unroll
      for (int e = 0; e < 4; ++e) {
        const u32x4 v = U[(i + 5) % 7][e];
        *(u32x4*)(smem + L_IM + e * 1024 + rr * 128 + (c16 ^ ((rr & 2) << 5))) = (e & 1) ? (u32x4){v[2], v[3], v[0], v[1]} : v;
      }
    };
    auto u_store = [&](auto I) {
      constexpr int i = decltype(I)::value;
      const u32 sw = ((r & 8) << 3) | ((g & 1) << 3);
      const char* im = smem + L_IM + (8 * g + (r >> 2)) * 128;
      for_n<4>([&](auto E) {
        constexpr int e = decltype(E)::value;
        constexpr bool split = i >= 2 && i < 9 && (2 * (i - 2) + (e >> 1)) % 7 == 3;
        const char* p = im + (((e >> 1) * 64 + (split ? (e & 1) * 32 + (r & 3) * 8 : (r & 3) * 16 + (e & 1) * 8)) ^ sw);
        const u32x2 lo = __builtin_bit_cast(u32x2, __builtin_amdgcn_ds_read_tr16_b64_v4i16((LDS_AS s16x4*)p));
        const u32x2 hi = __builtin_bit_cast(u32x2, __builtin_amdgcn_ds_read_tr16_b64_v4i16((LDS_AS s16x4*)(p + 512)));
        F[e] = (u32x4){lo[0], lo[1], hi[0], hi[1]};
      });
      for_n<4>([&](auto E) {
        constexpr int e = decltype(E)::value;
        if constexpr (i < 2) st16(F[e], OFF_WO + ((tnA ^ (4 * i)) * 32 + 2 * tmA + i) * QS + qoff(e, lane));
        else if constexpr (i < 9) {
          constexpr int f = w1_frag(i, e);
          st16(F[e], OFF_W1 + ((((tnA & 14) + (f >> 4)) * 32 + 2 * tmA + (tnA & 1)) * 4 + ((f & 15) >> 2)) * QS + qoff(f & 3, lane));
        } else st16(F[e], OFF_W2 + (tnA * 112 + (tmA >> 3) * 56 + hpos(tmA, i - 9)) * QS + qoff(e, lane));
      });
    };
    auto u_sums = [&](auto I) {
      constexpr int i = decltype(I)::value;
      constexpr u32 K = w1_frag(i, 0) | (w1_frag(i, 1) << 8) | (w1_frag(i, 2) << 16) | (w1_frag(i, 3) << 24);
      f32x4 cs = {0.f, 0.f, 0.f, 0.f};
#pragma unroll
      for (int e = 0; e < 4; ++e) {
        const u32 o = (r >> 2) == e ? 0x3f803f80u : 0u;
        cs = __builtin_amdgcn_mfma_f32_16x16x32_bf16(__builtin_bit_cast(bf16x8, (u32x4){o, o, o, o}), __builtin_bit_cast(bf16x8, F[e]), cs, 0, 0, 0);
      }
      const u32 f = (K >> (8 * g)) & 255;
      __builtin_amdgcn_raw_buffer_store_b32(__float_as_uint(cs[0]), rs,
          OFF_CS1 + ((((tnA & 14) + (f >> 4)) * 32 + 2 * tmA + (tnA & 1)) * 256 + (f & 15) * 16 + r) * 4, 0, 16);
    };
    u64 tok = 0;
    auto up = [&](int set) { st8(tok, lane == 0, flag(set, tmA, tnA)); };
    f32x4 cv[32], ca[2];
    auto cs_load = [&](u32 src, int p0, int p1) {
#pragma unroll
      for (int p = p0; p < p1; ++p)
        cv[p] = __builtin_bit_cast(f32x4, __builtin_amdgcn_raw_buffer_load_b128(rs, lane * 16 + (p & 3) * 1024, src + (p >> 2) * 4096, 16));
    };
    auto cs_add = [&](int p0, int p1) {
#pragma unroll
      for (int p = p0; p < p1; ++p) {
        asm volatile("" : "+v"(cv[p]));
        if (p < 2) ca[p] = cv[p];
        else {
#pragma unroll
          for (int k = 0; k < 4; ++k) {
            float t = ca[p & 1][k];
            const float u = cv[p][k];
            asm("v_add_f32 %0, %0, %1" : "+v"(t) : "v"(u));
            ca[p & 1][k] = t;
          }
        }
      }
      asm volatile("" : "+v"(ca[0]), "+v"(ca[1]));
    };
    u64 vf = 0;
    const u32 fo = flag(F_WO, r, ((tnB & 14) + (g & 1)) ^ (2 * (g & 2)));
    __builtin_amdgcn_s_sleep(PFW0);
    u_load(IC<0>{});
    u_load(IC<1>{});
    for_n<34>([&](auto S) {
      constexpr int s = decltype(S)::value;
      bar();
      if constexpr (s == TOKB) { tok = token(); asm volatile("" : "+s"(tok)); }
      if constexpr (s >= HL && s < HL + 4) {
        if constexpr ((s - HL) % 2 == 0) u_write(IC<(s - HL) / 2>{}); else u_store(IC<(s - HL) / 2>{});
      }
      if constexpr (s == HL + 3 + FLD) { wait_vm<0>(); up(F_WO); }
      if constexpr (s == G0 - 4) vf = ldf(fo);
      if constexpr (s == G0 - 1) { if (!__all(vf == tok)) poll(fo, tok); }
      if constexpr (s == G0) for_n<4>([&](auto I) { u_load(IC<2 + decltype(I)::value>{}); });
      if constexpr (s == G0 + 1) for_n<3>([&](auto I) { u_load(IC<6 + decltype(I)::value>{}); });
    });
    static_assert(TOKB < HL && HL + 3 + FLD < G0 - 4 && G0 + 1 < 33, "schedule");
    for_n<7>([&](auto I) {
      constexpr int i = 2 + decltype(I)::value;
      u_write(IC<i>{});
      u_store(IC<i>{});
      u_sums(IC<i>{});
    });
    bar(); bar(); bar();
    wait_vm<0>();
    up(F_W1);
#pragma nounroll
    for (int u = 3; u <= G1; ++u) bar();
    for_n<7>([&](auto I) {
      u_load(IC<9 + decltype(I)::value>{});
      if constexpr (decltype(I)::value == 1 || decltype(I)::value == 3) bar();
    });
#pragma nounroll
    for (int u = G1 + 3; u < 16 + H2; ++u) bar();
    for_n<14>([&](auto S) {
      constexpr int s = decltype(S)::value, i = 9 + s / 2;
      bar();
      if constexpr (s % 2 == 0) u_write(IC<i>{}); else u_store(IC<i>{});
    });
    for_n<12>([&](auto S) {
      constexpr int s = decltype(S)::value;
      bar();
      if constexpr (s == 3) { wait_vm<16>(); up(F_W2); }
      if constexpr (s < 6) cs_load(OFF_CS1 + tnA * 32768, s * 16 / 3, (s + 1) * 16 / 3);
      if constexpr (s >= 8) cs_add(8 * (s - 8), 8 * (s - 7));
    });
    static_assert(H2 + 14 + 12 <= 31, "schedule");
    *(f32x4*)(smem + L_CS1 + lane * 16) = ca[0] + ca[1];
    wait_lgkm0();
    QUIT();
  } else {
    FRESH_LANE();
    const int tmh = tmA > 0 ? tmA - 1 : 0;
    u64 tok;
    auto bars = [&](int n) {
#pragma nounroll
      for (int u = 0; u < n; ++u) bar();
    };
    auto ror8 = [](float v) { return __uint_as_float(__builtin_amdgcn_mov_dpp(__float_as_uint(v), 0x128, 0xf, 0xf, false)); };
    auto put = [&](int xi, float mean, float m2, bool on, int row) {
      const float sd = __builtin_amdgcn_sqrtf(bfr(bfr(m2 * (1.f / DM)) + LN_EPS));
      st8(pack2(mean, sd) | (tok << 32), on, flag(F_LN + xi, tmA, tnA) + row * 8);
    };
    auto reduce = [&](int xi) {
      const u32 src = OFF_ST + xi * 524288 + span(tmA, c13) + (tnA >> 2) * 16384 + (tnA & 3) * 2048, tag = (u32)tok;
      u32x4 a, b;
      auto ld = [&]() {
        asm volatile("" ::: "memory");
        a = __builtin_bit_cast(u32x4, __builtin_amdgcn_raw_buffer_load_b128(rs, lane * 16, src, 16));
        b = __builtin_bit_cast(u32x4, __builtin_amdgcn_raw_buffer_load_b128(rs, lane * 16 + 1024, src, 16));
      };
      ld();
      bar(); bar(); bar();
#pragma nounroll
      for (int spin = 0; spin < (1 << 22); ++spin) {
        if (__all((((a[1] ^ tag) | (a[3] ^ tag)) | ((b[1] ^ tag) | (b[3] ^ tag))) == 0)) break;
        __builtin_amdgcn_s_sleep(1);
        ld();
      }
      float m = __uint_as_float(a[0]) + __uint_as_float(b[0]);
      m = xsum(m + ror8(m)) * (1.f / 16.f);
      const float d0 = __uint_as_float(a[0]) - m, d1 = __uint_as_float(b[0]) - m;
      float q = __uint_as_float(a[2]) + __uint_as_float(b[2]) + 64.f * (d0 * d0 + d1 * d1);
      asm volatile("" : "+v"(m), "+v"(q));
      bar();
      asm volatile("" : "+v"(m), "+v"(q));
      put(xi, m, xsum(q + ror8(q)), lane < 8, lane);
    };
    auto gather = [&](auto H, int xi) {
      constexpr int n = 2 + decltype(H)::value;
      const u32 tag = (u32)tok, f0 = flag(F_LN + xi, tmA, lane >> 3) + (lane & 7) * 8;
      const u32 f[3] = {f0, f0 + 1024, flag(F_LN + xi, tmh, 14 + (r >> 3)) + (lane & 7) * 8};
      u32x2 v[3];
      auto ld = [&]() {
#pragma unroll
        for (int h = 0; h < n; ++h) v[h] = __builtin_bit_cast(u32x2, ldf(f[h]));
      };
      ld();
      bar(); bar(); bar();
#pragma nounroll
      for (int spin = 0; spin < (1 << 22); ++spin) {
        if (__all(((v[0][1] ^ tag) | (v[1][1] ^ tag) | (v[n - 1][1] ^ tag)) == 0)) break;
        __builtin_amdgcn_s_sleep(1);
        ld();
      }
#pragma unroll
      for (int h = 0; h < n; ++h) {
        const float iv = __builtin_amdgcn_rcpf(bf_hi(v[h][0]));
        const int o = h < 2 ? 64 + 256 * h + lane * 4 : r * 4;
        *(float*)(smem + L_INV + o) = iv;
        *(float*)(smem + L_MNV + o) = n == 3 ? bf_lo(v[h][0]) : bf_lo(v[h][0]) * iv;
      }
      wait_lgkm0();
    };
    auto guard = [&](u32 f, int K, auto&& lim_, auto&& step) {
      int b = 0, lim = 0;
#pragma nounroll
      for (int spin = 0; spin < (1 << 22); ++spin) {
        const u64 v = ldf(f);
        if (b < lim) { bar(); step(b++); } else __builtin_amdgcn_s_sleep(1);
        if (b < lim) { bar(); step(b++); }
        lim = lim_((u32)__ballot(v == tok));
        if (lim >= 100) break;
        lim = min(K, lim);
      }
#pragma nounroll
      while (b < K) { bar(); step(b++); }
    };
    auto ahead = [](int msk, int spp, int D) {
      return [=](u32 m) { const int n = __builtin_ctz(~(m & (m >> 16))); return n >= 16 ? 100 : (n & msk) * spp - D; };
    };
    const __amdgpu_buffer_rsrc_t rx = rsrc(x, SEQ * DM * 2), rq = rsrc(wqkv, DM * 3 * DM * 2);
    u32 wx, ww;
    {
      const int n = idx * 64 + lane, m = idx * 48 + (lane < 48 ? lane : 47), k = (m * 5462) >> 16, p = m - 12 * k;
      wx = (n & 511) * 2048 + ((n >> 9) & 1) * 128 + ((idx >> 4) * 1024 + (xcd >> 2) * 512) * 2048;
      ww = k * 6144 + (p >> 2) * 2048 + (p & 3) * 128 + (xcd & 3) * 512;
    }
    {
      const u64 ep = ldf(OFF_EP);
      u16 bv[4];
#pragma unroll
      for (int h = 0; h < 4; ++h) {
        const int t = min(lane + 64 * h, 223), f = t >> 4, rho = t & 15, wn = f / 7, j = f % 7;
        bv[h] = b1[tnA * 224 + (j == 6 ? 96 + 16 * wn + rho : (4 * wn + (j >> 1)) * 32 + 8 * (rho >> 2) + 4 * (j & 1) + (rho & 3))];
      }
      DMA(rsrc(b2, DM * 2), (LDS_AS void*)(smem + L_B2), 4, lane * 4, (tnB & 14) * 128, 0, 0);
      pin2(wx, ww);
      __builtin_amdgcn_s_sleep(XW);
      bf16x8 xf[16];
#pragma unroll
      for (int e = 0; e < 16; ++e)
        xf[e] = __builtin_bit_cast(bf16x8, __builtin_amdgcn_raw_buffer_load_b128(rx, (r >> 1) * 2048 + (r & 1) * 64 + g * 16 + e * 128,
            (tmA * 128 + tnA * 8) * 2048, 0));
      auto probe = [&](int tn) { return flag(F_PR, tmA ^ (4 * (idx & 1)), tn); };
      const u32x4 one4 = {0x3f803f80u, 0x3f803f80u, 0x3f803f80u, 0x3f803f80u};
      const bf16x8 ones = __builtin_bit_cast(bf16x8, one4);
      f32x4 sm[2], sq[2];
      u32 tlb;
      u64 vt;
      for_n<LG0>([&](auto S) {
        constexpr int s = decltype(S)::value;
        bar();
        if constexpr (s == TOKB - 1) {
          __builtin_amdgcn_raw_buffer_store_b32(0, rs, blockIdx.x < NVOTE ? probe(tnA ^ 4) + (lane & 31) * 4 : 0x80000000u, 0, 16);
#pragma unroll
          for (int h = 0; h < 4; ++h) ((float*)(smem + L_BS))[lane + 64 * h] = bf_lo(bv[h]);
          tok = ((u64)(u32)__builtin_amdgcn_readfirstlane((u32)ep) | ((u64)(u32)__builtin_amdgcn_readfirstlane((u32)(ep >> 32)) << 32))
                * 6364136223846793005ull + 1442695040888963407ull;
          *(LDS_AS u32x4*)(smem + L_CNT) = (u32x4){0u, 0u, (u32)tok, (u32)(tok >> 32)};
          wait_lgkm0();
          asm volatile("s_load_dword %0, %1, 0x0" : "=s"(tlb) : "s"(ws + OFF_EP + 128) : "memory");
        }
        if constexpr (s >= PF0 && (s - PF0) % PFS == 0 && (s - PF0) / PFS < 8 - PFJ) {
          const int j = (PFJ + (s - PF0) / PFS + rot0) & 7;
          DMA(rx, (LDS_AS void*)(smem + L_PF), 4, wx, j * 256, 0, 0);
          DMA(rq, (LDS_AS void*)(smem + L_PF), 4, ww, j * (128 * 6144), 0, 0);
        }
        if constexpr (s >= XP && s < XP + 4) {
#pragma unroll
          for (int e = 4 * (s - XP); e < 4 * (s - XP) + 4; ++e) {
            const f32x4 z = {0.f, 0.f, 0.f, 0.f};
            asm volatile("" : "+v"(xf[e]));
            sm[e & 1] = __builtin_amdgcn_mfma_f32_16x16x32_bf16(ones, xf[e], e < 2 ? z : sm[e & 1], 0, 0, 0);
            sq[e & 1] = __builtin_amdgcn_mfma_f32_16x16x32_bf16(xf[e], xf[e], e < 2 ? z : sq[e & 1], 0, 0, 0);
          }
        }
        if constexpr (s >= XP && s <= XP + 4) asm volatile("" : "+v"(sm[0]), "+v"(sm[1]), "+v"(sq[0]), "+v"(sq[1]));
        if constexpr (s == XP + 4) {
          const f32x4 q4 = sq[0] + sq[1];
          float q = (r & 2) ? ((r & 1) ? q4[3] : q4[2]) : ((r & 1) ? q4[1] : q4[0]), t = sm[0][0] + sm[1][0];
          q += __uint_as_float(__builtin_amdgcn_mov_dpp(__float_as_uint(q), 0xb1, 0xf, 0xf, false));
          t += __uint_as_float(__builtin_amdgcn_mov_dpp(__float_as_uint(t), 0xb1, 0xf, 0xf, false));
          const float m = t * (1.f / DM);
          put(0, m, fmaxf(__builtin_fmaf(-t, m, q), 0.f), g == (r >> 2) && !(r & 1), r >> 1);
        }
        if constexpr (s == PRB) {
          wait_lgkm0();
          asm volatile("" :: "s"(tlb));
          if (blockIdx.x < NVOTE) {
            u64 t0, t1;
            u32 v;
            asm volatile("s_memtime %0\n\ts_waitcnt lgkmcnt(0)\n\ts_load_dword %2, %3, 0x0\n\ts_waitcnt lgkmcnt(0)\n\t"
                         "s_memtime %1\n\ts_waitcnt lgkmcnt(0)" : "=&s"(t0), "=&s"(t1), "=&s"(v) : "s"(ws + probe(tnA)) : "memory");
            const u32 t = min((u32)t1 - (u32)t0, 4095u);
            st8(tok ^ t, lane == 0, flag(F_XC, 0, blockIdx.x));
          }
        }
        if constexpr (s == LG0 - 4) vt = ldf(flag(F_XC, 0, r));
      });
      static_assert(XP >= TOKB && PRB > XP + 4 && LG0 - 4 > PRB && LG0 + 3 <= 31 && L_CNT % 16 == 0, "schedule");
#pragma nounroll
      for (int spin = 0; spin < (1 << 22); ++spin) {
        if (__all((vt ^ tok) < 4096)) break;
        __builtin_amdgcn_s_sleep(1);
        vt = ldf(flag(F_XC, 0, r));
      }
      const u32 t = (u32)(vt ^ tok);
      const u64 slower = __ballot(t > (u32)__builtin_amdgcn_mov_dpp(t, 0x128, 0xf, 0xf, false));
      const u32 c = __builtin_popcount((u32)slower & 255) > 4 ? 8192 : 0;
      *(volatile LDS_AS u32*)(smem + L_C13) = c;
      gather(IC<1>{}, 0);
      c13 = c;
      bars(34 - CLB - (LG0 + 3));
    }
#pragma nounroll
    for (int pair = 0; pair < 2; ++pair) {
#pragma unroll
      for (int e = 0; e < 16; ++e)
        st16(((u32x4){~0u, ~0u, ~0u, ~0u}), xbox(pair, 0) + (e + (e & 8)) * 1024 + lane * 16);
      bars(CLB);
      wait_vm<0>();
      guard(flag(4 * pair + 1, tmB, (rotB + r) & 15), 16, ahead(15, 1, DN), [](int) {});
      guard(flag(4 * pair + 2, tmA, (rotA + r) & 15), KW1, ahead(14, 2, DW), [](int) {});
      reduce(2 * pair + 1);
      bars(LG - (KW1 + 4));
      gather(IC<0>{}, 2 * pair + 1);
      bars(33 - (LG + 3));
      guard(flag(lane < 8 ? 4 * pair + 3 : lane < 16 ? F_2ND + pair : F_3RD + pair, tmB, 8 * (tnB & 1) + ((lane < 8 ? r ^ (rotA >> 1) : r) & 7)), 13,
            [](u32 m) { const int n = __builtin_ctz(~m | (1u << 24)); return n >= 24 ? 100 : min(n, 16) - 3; },
            [&](int b) { if (b == 0 || b == 3) st8(tok, lane == 0, flag(b ? F_3RD + pair : F_2ND + pair, tmA, tnA)); });
      if (pair) break;
      bars(WQ - 13);
      for_n<29 - WQ>([&](auto S) {
        constexpr int s = decltype(S)::value;
        if constexpr (s < 4) {
          DMA(rq, (LDS_AS void*)(smem + L_PF), 4, ww, ((2 * s + (rotA >> 1)) & 7) * (128 * 6144), 0, 0);
          DMA(rq, (LDS_AS void*)(smem + L_PF), 4, ww, ((2 * s + 1 + (rotA >> 1)) & 7) * (128 * 6144), 0, 0);
        }
        bar();
      });
      guard(flag(4, (g & 1) ? tmh : tmA, (rotA + r) & 15), KW1, ahead(14, 2, DW), [](int) {});
      st8(tok, lane == 0 && blockIdx.x == 0, OFF_EP);
      reduce(2);
      bars(LG - (KW1 + 4));
      gather(IC<1>{}, 2);
      bars(33 - CLB - (LG + 3));
      static_assert(WQ <= 25 && LG + 3 <= 31, "schedule");
    }
    QUIT();
  }
#endif
}

extern "C" void kernel_launch(void* const* d_in, const int* in_sizes, int n_in, void* d_out, int out_size,
                              void* d_ws, size_t ws_size, hipStream_t stream) {
  const u16* x0 = (const u16*)d_in[0];
  const u16* wqkv = (const u16*)d_in[1];
  const u16* wo = (const u16*)d_in[2];
  const u16* w1 = (const u16*)d_in[4];
  const u16* b1 = (const u16*)d_in[5];
  const u16* w2 = (const u16*)d_in[6];
  const u16* b2 = (const u16*)d_in[7];
  (void)hipFuncSetAttribute((const void*)mega_k, hipFuncAttributeMaxDynamicSharedMemorySize, LDS_BYTES);
  mega_k<<<256, 512, LDS_BYTES, stream>>>((char*)d_ws + (ws_size - WS_USED - WS_ALIGN), x0, wqkv, wo, w1, b1, w2, b2, (u16*)d_out);
}
```

```cpp
#include <hip/hip_runtime.h>
#include <stdint.h>
#include <type_traits>
#include <utility>

typedef unsigned short u16;
typedef unsigned int u32;
typedef unsigned long long u64;
typedef __bf16 bf16x8 __attribute__((ext_vector_type(8)));
typedef __bf16 bf16x2 __attribute__((ext_vector_type(2)));
typedef short s16x4 __attribute__((ext_vector_type(4)));
typedef float f32x4 __attribute__((ext_vector_type(4)));
typedef float f32x2 __attribute__((ext_vector_type(2)));
typedef u32 u32x4 __attribute__((ext_vector_type(4)));
typedef u32 u32x2 __attribute__((ext_vector_type(2)));
typedef u32 u32x10 __attribute__((ext_vector_type(10)));
typedef int i32x4 __attribute__((ext_vector_type(4)));
typedef int i32x2 __attribute__((ext_vector_type(2)));
#define LDS_AS __attribute__((address_space(3)))
#define GLB_AS __attribute__((address_space(1)))

constexpr int SEQ = 2048, DM = 1024, FF = 3584;
constexpr float LN_EPS = 1e-5f;

#define DN 4
#define DW 5
#define UAUX 2
#define PFW0 30
#define PF0 4
#define PFS 2
#define PFJ 2
#define CQP 5
#define HL 8
#define FLD 3
#define XW 30
#define TOKB 3
#define XP 8
#define LG0 24
#define LG 25
#define WQ 24
#define G0 27
#define G1 12
#define H2 4
#define PUBJ 2
#define CB 1
#define KW1 11
#define CLB 1
#define SND 12
#define RST 9
#define XB2 1
#define XA2 1
#define XBO 1
#define CW 1
#define PRB 13
#define CQG 22
#define R3W 1
#define LVA 16
#define LVB 6
#define DB 3
#define SNK 31
#define D2 0
#define D2B 6
#define RL2 15
#define RL1 10
#define RLQ 16
#define ROT 7
#ifndef NVOTE
#define NVOTE 16
#endif
constexpr u32 MB = 1u << 20;
constexpr u32 QS = 5120, Q2 = D2 ? 4096 : QS;
constexpr u32 XS = 8 * MB, HS = 32 * MB;
constexpr u32 OFF_WO = 0;
constexpr u32 OFF_W1 = 3 * MB;
constexpr u32 OFF_W2 = 13 * MB;
constexpr u32 OFF_CS1 = 22 * MB;
constexpr u32 OFF_FL = 25 * MB;
constexpr u32 OFF_EP = OFF_FL + 26 * 32768;
constexpr u32 OFF_DBG = 26 * MB;
constexpr u32 OFF_XC = 28 * MB;
constexpr u32 OFF_X = 28 * MB;
constexpr u32 OFF_O = OFF_X + 4 * XS;
constexpr u32 OFF_H = OFF_O + 2 * XS;
constexpr u32 OFF_ST = OFF_H + 2 * HS;
constexpr u32 WS_USED = OFF_ST + 2 * MB, WS_ALIGN = 4 * MB;
#define HAS_ALIGN
constexpr int F_WO = 9, F_W1 = 10, F_W2 = 11, F_2ND = 12, F_3RD = 14, F_XC = 18, F_PR = 19, F_LN = 22;
__device__ __forceinline__ u32 qoff(u32 e, u32 slot) {
  return slot * 16 + (e * 1152 + (e == 3 ? 128 : 0)) + (slot >= (e == 3 ? 56 : 24 - 8 * e) ? 128 : 0);
}
__device__ __forceinline__ u32 hpos(u32 t, u32 j) {
  return (j & 2) == 0 ? 16 * (j & 1) + 2 * (t & 7) + (j >> 2) : 32 + 3 * (t & 7) + (j & 1) + (j >> 2) * 2;
}
__device__ __forceinline__ u32 aq(u32 kg, u32 half) { return kg * 32768 + (kg & 1) * 3072 + half * 16384; }
__device__ __forceinline__ u32 ppair(u32 tm) { return (tm & 3) + (tm >> 3) * 4; }
__device__ __forceinline__ u32 xpan(u32 tm, u32 c13) { return ppair(tm) * MB + (((((tm >> 2) ^ (tm >> 1)) & 1) << 13) ^ c13); }
__device__ __forceinline__ u32 hpan(u32 tm, u32 kh, u32 c13) {
  return ppair(tm) * (4 * MB) + (((kh ^ tm) & 1) << 21) + (((((tm >> 2) ^ kh ^ tm) & 1) << 13) ^ c13);
}
__device__ __forceinline__ u32 span(u32 tm, u32 c13) { return ppair(tm) * 65536 + (((tm & 4) << 11) ^ c13); }

constexpr int SLOT = 24576, NRING = 6, RING = SLOT * NRING;
constexpr int L_INV = RING, L_MNV = L_INV + 576, L_CNT = L_MNV + 576, L_TOK = L_CNT + 8, L_C13 = L_TOK + 8;
constexpr int L_CSQ = RING + 1280, L_CS1 = L_CSQ + 1024, L_BS = L_CS1 + 1024;
constexpr int L_XC = L_BS + 1024;
constexpr int L_IM = L_XC + 6144;
constexpr int L_B2 = L_IM + 4096, L_PF = L_B2 + 256;
constexpr int LDS_BYTES = L_PF + 256;

__device__ __forceinline__ float bf_lo(u32 w) { return __uint_as_float(w << 16); }
__device__ __forceinline__ float bf_hi(u32 w) { return __uint_as_float(w & 0xffff0000u); }
__device__ __forceinline__ u32 pack2(float lo, float hi) {
  f32x2 v = {lo, hi};
  bf16x2 r = __builtin_convertvector(v, bf16x2);
  return __builtin_bit_cast(u32, r);
}
__device__ __forceinline__ float bfr(float x) { return bf_lo(pack2(x, 0.f) & 0xffffu); }
__device__ __forceinline__ u32 gelu_tanh2(f32x2 x) {
  const float c0 = -2.3022081983f, c1 = -0.1029432396f;
  const f32x2 p = x * x * c1 + c0;
  const f32x2 a = x * p;
  const f32x2 d = (f32x2){__builtin_amdgcn_exp2f(a[0]), __builtin_amdgcn_exp2f(a[1])} + 1.f;
  const f32x2 g = x * (f32x2){__builtin_amdgcn_rcpf(d[0]), __builtin_amdgcn_rcpf(d[1])};
  return pack2(g[0], g[1]);
}
template <int N_> __device__ __forceinline__ void wait_vm() { asm volatile("s_waitcnt vmcnt(%0)" ::"n"(N_) : "memory"); }
__device__ __forceinline__ void wait_lgkm0() { asm volatile("s_waitcnt lgkmcnt(0)" ::: "memory"); }
__device__ __forceinline__ void bar() {
  __builtin_amdgcn_sched_barrier(0);
  asm volatile("s_barrier" ::: "memory");
  __builtin_amdgcn_sched_barrier(0);
}
template <int V> using IC = std::integral_constant<int, V>;
template <typename F, int... I>
__device__ __forceinline__ void for_seq(F&& f, std::integer_sequence<int, I...>) { (f(IC<I>{}), ...); }
template <int N, typename F> __device__ __forceinline__ void for_n(F&& f) { for_seq(f, std::make_integer_sequence<int, N>{}); }

__device__ __forceinline__ float xsum(float x) {
#if defined(__HIP_DEVICE_COMPILE__)
  auto a = __builtin_amdgcn_permlane16_swap(__float_as_uint(x), __float_as_uint(x), false, false);
  x = __uint_as_float(a[0]) + __uint_as_float(a[1]);
  auto b = __builtin_amdgcn_permlane32_swap(__float_as_uint(x), __float_as_uint(x), false, false);
  x = __uint_as_float(b[0]) + __uint_as_float(b[1]);
#endif
  return x;
}
__device__ __forceinline__ float xmax(float x) {
#if defined(__HIP_DEVICE_COMPILE__)
  auto a = __builtin_amdgcn_permlane16_swap(__float_as_uint(x), __float_as_uint(x), false, false);
  x = fmaxf(__uint_as_float(a[0]), __uint_as_float(a[1]));
  auto b = __builtin_amdgcn_permlane32_swap(__float_as_uint(x), __float_as_uint(x), false, false);
  x = fmaxf(__uint_as_float(b[0]), __uint_as_float(b[1]));
#endif
  return x;
}
__device__ __forceinline__ f32x2 row_part(const u32x4& c0, const u32x4& c1) {
  float v[16];
#pragma unroll
  for (int k = 0; k < 4; ++k) {
    v[2 * k] = bf_lo(c0[k]); v[2 * k + 1] = bf_hi(c0[k]);
    v[8 + 2 * k] = bf_lo(c1[k]); v[8 + 2 * k + 1] = bf_hi(c1[k]);
  }
  float s = 0.f;
#pragma unroll
  for (int k = 0; k < 16; ++k) s += v[k];
  const float m = xsum(s) * (1.f / 64.f);
  float q = 0.f;
#pragma unroll
  for (int k = 0; k < 16; ++k) { const float d = v[k] - m; q += d * d; }
  return (f32x2){m, xsum(q)};
}

#define st16(v, off) __builtin_amdgcn_raw_buffer_store_b128(__builtin_bit_cast(i32x4, v), rs, (off), 0, 16)
__device__ __forceinline__ float sum8(const u32x4& f) {
  float s = bf_lo(f[0]) + bf_hi(f[0]);
#pragma unroll
  for (int k = 1; k < 4; ++k) { s += bf_lo(f[k]); s += bf_hi(f[k]); }
  return s;
}
__device__ __forceinline__ float fold32(float a, float b) {
#if defined(__HIP_DEVICE_COMPILE__)
  const auto x = __builtin_amdgcn_permlane32_swap(__float_as_uint(a), __float_as_uint(b), false, false);
  a = __uint_as_float(x[0]) + __uint_as_float(x[1]);
#endif
  return a;
}
__device__ __forceinline__ float fold16(float a, float b) {
#if defined(__HIP_DEVICE_COMPILE__)
  const auto x = __builtin_amdgcn_permlane16_swap(__float_as_uint(a), __float_as_uint(b), false, false);
  a = __uint_as_float(x[0]) + __uint_as_float(x[1]);
#endif
  return a;
}

__device__ __forceinline__ int fresh_lane(int tid) { int l = tid & 63; asm volatile("" : "+v"(l)); return l; }
__device__ __forceinline__ f32x4 zero4() {
  f32x4 z = {0.f, 0.f, 0.f, 0.f};
  asm volatile("" : "+v"(z));
  return z;
}
template <int BIG = 0> __device__ __forceinline__ void next_slot(int& so) {
  if constexpr (BIG) so = (so + 32768) & 131071; else { so += SLOT; if (so == RING) so = 0; }
}

#define DMA __builtin_amdgcn_raw_ptr_buffer_load_lds
#define SB() __builtin_amdgcn_sched_barrier(0)
__device__ __forceinline__ void mfma(f32x4& c, const bf16x8& a, const bf16x8& b) {
  asm volatile("v_mfma_f32_16x16x32_bf16 %0, %1, %2, %0" : "+v"(c) : "v"(a), "v"(b));
}
template <int OFF> __device__ __forceinline__ void dsrd(bf16x8& d, u32 addr) {
  asm volatile("ds_read_b128 %0, %1 offset:%2" : "=v"(d) : "v"(addr), "n"(OFF));
}
template <int OFF> __device__ __forceinline__ void dstr(bf16x8& d, u32 addr) {
  u32x2 lo, hi;
  asm volatile("ds_read_b64_tr_b16 %0, %2 offset:%3\n\tds_read_b64_tr_b16 %1, %2 offset:%4"
               : "=&v"(lo), "=&v"(hi) : "v"(addr), "n"(OFF), "n"(OFF + 512));
  const u32x4 v = {lo[0], lo[1], hi[0], hi[1]};
  d = __builtin_bit_cast(bf16x8, v);
}
template <int OFF> __device__ __forceinline__ void dstr1(u32x2& d, u32 addr) {
  asm volatile("ds_read_b64_tr_b16 %0, %1 offset:%2" : "=v"(d) : "v"(addr), "n"(OFF));
}
template <int OFF> __device__ __forceinline__ void dsrd1(float& d, u32 addr) {
  asm volatile("ds_read_b32 %0, %1 offset:%2" : "=v"(d) : "v"(addr), "n"(OFF));
}
template <int O0, int O1> __device__ __forceinline__ void dsrd2(f32x2& d, u32 addr) {
  asm volatile("ds_read2_b32 %0, %1 offset0:%2 offset1:%3" : "=v"(d) : "v"(addr), "n"(O0), "n"(O1));
}
template <int OFF> __device__ __forceinline__ void dsrd4(f32x4& d, u32 addr) {
  asm volatile("ds_read_b128 %0, %1 offset:%2" : "=v"(d) : "v"(addr), "n"(OFF));
}
template <class T> __device__ __forceinline__ void pin(T& a) { asm volatile("" : "+v"(a)); }
template <class T, class... R> __device__ __forceinline__ void pin(T& a, R&... r) { pin(a); pin(r...); }
template <class F> constexpr u64 lanes(F f) {
  u64 m = 0;
  for (int l = 0; l < 64; ++l) m |= (u64)(f(l & 15, l >> 4) ? 1 : 0) << l;
  return m;
}
constexpr u64 M_PAR = lanes([](int r, int) { return r & 1; });
constexpr u64 M_OWN0 = lanes([](int r, int g) { return 4 * g + (r & 1) <= r; });
constexpr u64 M_OWN1 = lanes([](int r, int g) { return 4 * g + (r & 1) + 2 <= r; });
#define LANE_IN(m) __builtin_amdgcn_inverse_ballot_w64(m)
__device__ __forceinline__ void pin1(u32& a) { asm volatile("" : "+v"(a)); }
__device__ __forceinline__ void pin2(u32& a, u32& b) { asm volatile("" : "+v"(a), "+v"(b)); }
constexpr int rd_at(int m, int n, int last) {
  for (int q = 0; q < n; ++q) if (q * last / (n - 1) == m) return q;
  return -1;
}
template <int NSL, int NM, int NR, int ENDB, class AD, class RD, class MM, class HK, class TL>
__device__ __forceinline__ void kloop(int& so, AD&& ad, RD&& rd, MM&& mm, HK&& hook, TL&& tail) {
  constexpr int BIG = NM == 32;
  ad(so, IC<0>{});
  bar();
  for_n<NR>([&](auto Q) { rd(IC<0>{}, Q); });
  next_slot<BIG>(so);
  ad(so, IC<1>{});
  hook(-1);
  auto body = [&](auto S) {
    constexpr int s = decltype(S)::value;
    wait_lgkm0();
    bar();
    for_n<NM>([&](auto M) {
      constexpr int m = decltype(M)::value;
      mm(S, M);
      constexpr int RL = BIG ? RL2 : NR - 1, q = rd_at(m, NR, RL);
      if constexpr (q >= 0) rd(IC<1 - s>{}, IC<q>{});
      if constexpr (m == RL + 1) { next_slot<BIG>(so); asm volatile("" : "+s"(so)); }
      if constexpr (m == RL + 2) ad(so, S);
      SB();
    });
  };
#pragma nounroll
  for (int u = NSL / 2 - 1; u > 0; --u) {
    body(IC<0>{});
    body(IC<1>{});
    hook(NSL / 2 - 1 - u);
  }
  body(IC<0>{});
  wait_lgkm0();
  if constexpr (ENDB) { bar(); so = 0; }
  SB();
  for_n<NM>([&](auto M) { mm(IC<3>{}, M); SB(); tail(M); SB(); });
  asm volatile("s_nop 15");
  SB();
}

template <int NM, int NR, int ENDB, class RD, class MM, class HK, class TL>
__device__ __forceinline__ void kloop6(RD&& rd, MM&& mm, HK&& hook, TL&& tail) {
  bar();
  for_n<NR>([&](auto Q) { rd(IC<0>{}, Q); });
  hook(-1);
  auto body = [&](auto S) {
    constexpr int s = decltype(S)::value;
    wait_lgkm0();
    bar();
    for_n<NM>([&](auto M) {
      constexpr int m = decltype(M)::value;
      mm(IC<s & 1>{}, M);
      constexpr int q = rd_at(m, NR, NR == 11 ? RL1 : RLQ);
      if constexpr (q >= 0) rd(IC<(s + 1) % NRING>{}, IC<q>{});
      SB();
    });
  };
#pragma nounroll
  for (int u = 5; u > 0; --u) for_n<NRING>(body);
  body(IC<0>{});
  wait_lgkm0();
  if constexpr (ENDB) bar();
  SB();
  for_n<NM>([&](auto M) { mm(IC<1>{}, M); SB(); tail(M); SB(); });
  asm volatile("s_nop 15");
  SB();
}

struct Slot { u32 k, a0, l0; };
template <int NSL, int P, int D, int X, class PR, class FI, class BR>
__device__ __forceinline__ void stream(int& so, int u0, PR&& prep, FI&& fire, BR&& bar) {
#pragma nounroll
  for (int u = u0; u < D + X; ++u) {
    const Slot a = prep(u);
    if constexpr ((D + X) * P > 63) wait_vm<63 - P>();
    for_n<P>([&](auto I) { fire(I, a); });
  }
  auto next = [&](int u) {
    Slot a = prep(u);
    asm volatile("" : "+s"(a.k), "+s"(a.a0), "+s"(a.l0), "+s"(so));
    return a;
  };
  auto head = [&](const Slot& a) {
    wait_vm<((D - 1) * P < 59 ? (D - 1) * P : 59)>();
    bar();
    for_n<4>([&](auto I) { fire(I, a); });
    SB();
  };
  auto rest = [&](const Slot& a) {
    if constexpr (D * P > 63) wait_vm<67 - P>();
    for_n<P - 4>([&](auto I) { fire(IC<4 + decltype(I)::value>{}, a); });
  };
  if constexpr ((ROT >> (NSL == 28 ? 0 : NSL == 16 ? 1 : 2)) & 1) {
    Slot a = next(X + D);
    if constexpr (X) { wait_vm<(D * P < 63 ? D * P : 63)>(); bar(); }
    head(a);
#pragma nounroll
    for (int u = X + D + 1; u < NSL; ++u) {
      const Slot b = next(u);
      rest(a);
      head(b);
      a = b;
    }
    rest(a);
  } else {
    if constexpr (X) { wait_vm<(D * P < 63 ? D * P : 63)>(); bar(); }
#pragma nounroll
    for (int u = X + D; u < NSL; ++u) {
      const Slot a = next(u);
      head(a);
      rest(a);
    }
  }
  for_n<D>([&](auto M) { constexpr int n = (D - 1 - decltype(M)::value) * P; wait_vm<(n < 63 ? n : 63)>(); bar(); });
}

#define STAMP(w, i)
#define TS(v)
#define TSW(v, i)
#define TSP 1

constexpr int w1_frag(int i, int e) {
  const int gam = 2 * (i - 2) + (e >> 1), gl = gam % 7;
  return (gam / 7) * 16 + (gl == 3 ? 6 + 7 * (e & 1) : (gl < 3 ? gl * 2 : 7 + (gl - 4) * 2) + (e & 1));
}
__device__ __forceinline__ void warm_code(u32 n, u32 lds) {
#if defined(__HIP_DEVICE_COMPILE__)
  u64 pc;
  u32 back, len;
  asm volatile("s_getpc_b64 %0\n.Lcw%=:\n\ts_mov_b32 %1, .Lcw%=-mega_k\n\ts_mov_b32 %2, .Lcode_end-mega_k" : "=s"(pc), "=s"(back), "=s"(len));
  pc -= back;
  const u32x4 rc = {(u32)pc, (u32)(pc >> 32), len, 0x00020000u};
  asm volatile("s_mov_b32 %0, m0\n\ts_mov_b32 m0, %1\n\ts_nop 0\n\tbuffer_load_dword %2, %3, 0 offen lds\n\ts_mov_b32 m0, %0"
               : "=&s"(back) : "s"(lds), "v"(n * 128), "s"(rc));
#endif
}

template <int N> __device__ __forceinline__ void announce(u32 fl, __amdgpu_buffer_rsrc_t rs) {
  u32x2 tk;
  u32 a, o, f, n;
  asm volatile("s_mov_b64 exec, 1\n\tv_mov_b32 %1, %7\n\tds_read_b64 %0, %1 offset:8\n\tv_mov_b32 %2, 1\n\tv_mov_b32 %3, %5\n\t"
               "s_waitcnt vmcnt(%8)\n\tds_add_rtn_u32 %1, %1, %2\n\ts_waitcnt lgkmcnt(0)\n\t"
               "v_readfirstlane_b32 %4, %1\n\ts_and_b32 %4, %4, 3\n\ts_cmp_eq_u32 %4, 3\n\ts_cbranch_scc0 .Lan%=\n\t"
               "buffer_store_dwordx2 %0, %3, %6, 0 offen sc1\n.Lan%=:\n\ts_mov_b64 exec, -1"
               : "=&v"(tk), "=&v"(a), "=&v"(o), "=&v"(f), "=&s"(n)
               : "s"(fl), "s"(rs), "n"(L_CNT), "n"(N) : "memory", "scc");
  static_assert(L_TOK == L_CNT + 8, "layout");
}

extern "C" __global__ __launch_bounds__(512, 2) void mega_k(char* __restrict__ ws0, const u16* __restrict__ x,
                                                 const u16* __restrict__ wqkv, const u16* __restrict__ wo,
                                                 const u16* __restrict__ w1, const u16* __restrict__ b1,
                                                 const u16* __restrict__ w2, const u16* __restrict__ b2,
                                                 u16* __restrict__ out) {
#if defined(__HIP_DEVICE_COMPILE__)
  extern __shared__ __attribute__((aligned(16))) char smem[];
  const int tid = threadIdx.x;
  int wid = __builtin_amdgcn_readfirstlane(tid >> 6);
#define FRESH_LANE() const int lane = fresh_lane(tid), r = lane & 15, g = lane >> 4; (void)r; (void)g
  const int xcd = blockIdx.x & 7, idx = blockIdx.x >> 3;
  asm volatile(".subsection 1\n.Lcode_end:\n\t.subsection 0");
  if (CW & 1) warm_code(((tid & 63) * 8 + wid) * 32 + idx, L_PF);
  SB();
  char* const ws = ws0 + (-(u32)(uintptr_t)ws0 & (WS_ALIGN - 1));
  const int tmA = (idx >> 4) * 8 + (xcd >> 2) * 4 + ((idx >> 2) & 3), tnA = (xcd & 3) * 4 + (idx & 3);
  const int tmB = (idx >> 4) * 8 + (xcd >> 1) * 2 + ((idx >> 3) & 1), tnB = (idx & 7) * 2 + (xcd & 1);
  const int rotA = 8 - 8 * (xcd & 1), rotB = 4 * ((xcd + 1) & 3), rot0 = xcd;
  const __amdgpu_buffer_rsrc_t rs = __builtin_amdgcn_make_buffer_rsrc((void*)ws, 0, WS_USED, 0x00020000);
  u32 c13 = 0;
  auto side = [&]() { c13 = __builtin_amdgcn_readfirstlane(*(const volatile LDS_AS u32*)(smem + L_C13)); };
  auto flag = [&](int set, int tm, int tn) { return OFF_FL + ((((set * 16 + tm) * 16 + tn) * 128) ^ c13); };
  auto xbox = [&](int pair, int o) { return OFF_XC + pair * (4 * MB) + ((idx * 4 + (xcd & 3)) ^ o) * 32768 + (((((tmB >> 2) ^ (idx >> 4)) & 1) << 13) ^ c13); };
  auto rsrc = [&](const u16* p, u32 bytes) { return __builtin_amdgcn_make_buffer_rsrc((void*)p, 0, bytes, 0x00020000); };
  auto token = [&]() {
    const u32x2 tk = *(const volatile LDS_AS u32x2*)(smem + L_TOK);
    return (u64)(u32)__builtin_amdgcn_readfirstlane(tk[0]) | ((u64)(u32)__builtin_amdgcn_readfirstlane(tk[1]) << 32);
  };
  auto ldf = [&](u32 f) {
    asm volatile("" ::: "memory");
    return __builtin_bit_cast(u64, __builtin_amdgcn_raw_buffer_load_b64(rs, f, 0, 16));
  };
  auto poll = [&](u32 f, u64 tok) {
#pragma nounroll
    for (int spin = 0; spin < (1 << 22); ++spin) {
      if (__all(ldf(f) == tok)) break;
      __builtin_amdgcn_s_sleep(1);
    }
    asm volatile("" ::: "memory");
  };
  auto st8 = [&](u64 v, bool on, u32 f) {
    __builtin_amdgcn_raw_buffer_store_b64(__builtin_bit_cast(i32x2, v), rs, on ? f : 0x80000000u, 0, 16);
  };
  int so = 0;
#define QUIT() __builtin_amdgcn_endpgm()

  auto x_lanes = [&](u32 (&vo)[4]) {
    FRESH_LANE();
#pragma unroll
    for (int e = 0; e < 4; ++e)
      vo[e] = (8 * e + (lane >> 3)) * 2048 + (((lane & 7) ^ (4 * (e & 1) + (lane >> 4))) * 16) - e * 1024;
  };
  auto x_half = [&](auto H, int U, const u32 (&vo)[4]) {
    constexpr int h = decltype(H)::value;
    const __amdgpu_buffer_rsrc_t rx = rsrc(x, SEQ * DM * 2);
    const u32 sx = tmA * 128 * 2048, sh = (tmA > 0 ? tmA * 128 - 16 : 0) * 2048;
    U = (U + 2 * rot0) & 15;
    for_n<8>([&](auto E) {
      constexpr int e = decltype(E)::value;
      DMA(rx, (LDS_AS void*)(smem + so + h * SLOT + (e >> 2) * 4096), 16, vo[e & 3],
          sx + (h * 64 + (e >> 2) * 32) * 2048 + U * 128, (e & 3) * 1024, 0);
    });
    DMA(rx, (LDS_AS void*)(smem + so + SLOT + 21504), 16, vo[h], sh + U * 128, h * 1024, 0);
    if constexpr (h == 1) { so += 2 * SLOT; if (so == RING) so = 0; }
  };
  auto q_lanes = [&](u32 (&vo)[4]) {
    FRESH_LANE();
#pragma unroll
    for (int e = 0; e < 4; ++e)
      vo[e] = (8 * e + (lane >> 3)) * 6144 + (((lane & 7) ^ (2 * ((lane >> 4) & 1) + 4 * (e & 1))) * 16) - e * 1024;
  };
  if (wid == 6) {
    FRESH_LANE();
    DMA(rsrc(wqkv, DM * 3 * DM * 2), (LDS_AS void*)(smem + L_PF), 4,
        lane < 24 && !(xcd & 1) ? ((rot0 * 128 + idx * 8 + (lane & 7)) & 1023) * 6144 + (lane >> 3) * 2048 : 0x80000000u,
        (xcd & 3) * 512 + 384, 0, 0);
  }
  asm volatile("" : "+s"(wid));
  if (wid == 5) {
    u32 vo[4];
    q_lanes(vo);
#pragma nounroll
    for (int u = 0; u < 5; ++u)
      for_n<12>([&](auto I) {
        constexpr int i = decltype(I)::value;
        DMA(rsrc(wqkv, DM * 3 * DM * 2), (LDS_AS void*)(smem + u * SLOT + 9216 + (i >> 2) * 4096), 16, vo[i & 3],
            ((u + 4 * rot0) & 31) * (32 * 6144) + (i >> 2) * 2048 + tnA * 128, (i & 3) * 1024, 0);
      });
  }
  asm volatile("" : "+s"(wid));
  if (wid == 4) {
    u32 vo[4];
    x_lanes(vo);
#pragma nounroll
    for (int U = 0; U < 3; ++U) { x_half(IC<0>{}, U, vo); x_half(IC<1>{}, U, vo); }
  }
  so = 0;
  asm volatile("" : "+s"(wid));

  if (wid < 4) {
    auto publish = [&](auto N, int ph, int tm, int tn) {
      SB();
      announce<decltype(N)::value>(flag(ph + 1, tm, tn), rs);
      SB();
    };
    const float* t_inv = (const float*)(smem + L_INV);
    const float* t_mnv = (const float*)(smem + L_MNV);
    const float* t_cs = (const float*)(smem + L_CS1);
    const float* t_bs = (const float*)(smem + L_BS);
    const int wmi = wid >> 1, wni = wid & 1;

    const int w = 2 * (tnB & 1) + wmi, tn = (tnB & 14) + wni;
    u32x4 c[2][2];
    auto start = [&](f32x4 (&acc)[4][4], const u32x4 (&bb)[2], bool bias) {
#pragma unroll
      for (int i = 0; i < 2; ++i)
#pragma unroll
        for (int j = 0; j < 4; ++j) {
          const u32 c0 = c[i][j >> 1][2 * (j & 1)], c1 = c[i][j >> 1][2 * (j & 1) + 1];
          const u32 b0 = bb[j >> 1][2 * (j & 1)], b1 = bb[j >> 1][2 * (j & 1) + 1];
          f32x4 a = {bf_lo(c0), bf_hi(c0), bf_lo(c1), bf_hi(c1)};
          if (bias) a += (f32x4){bf_lo(b0), bf_hi(b0), bf_lo(b1), bf_hi(b1)};
          asm volatile("" : "+v"(a));
          acc[i][j] = a;
        }
    };
    auto finish = [&](const f32x4 (&acc)[4][4], int xo  , int ph) {
      FRESH_LANE();
#pragma unroll
      for (int i = 0; i < 2; ++i)
#pragma unroll
        for (int gl = 0; gl < 2; ++gl) {
#pragma unroll
          for (int k = 0; k < 4; ++k) {
            const f32x4& a = acc[i][2 * gl + (k >> 1)];
            c[i][gl][k] = pack2(a[2 * (k & 1)], a[2 * (k & 1) + 1]);
          }
          if (xo < 4) st16(c[i][gl], OFF_X + xo * XS + xpan(tmB, c13) + tn * 65536 + aq(gl, w >> 1) + qoff((w & 1) * 2 + i, lane));
          else __builtin_amdgcn_raw_buffer_store_b128(__builtin_bit_cast(i32x4, c[i][gl]), rsrc(out, SEQ * DM * 2),
                   (tmB * 128 + (2 * w + i) * 16 + r) * 2048 + tn * 128 + gl * 64 + g * 16, 0, 16);
        }
      if (xo < 4) {
        SB();
        TS(ta);
        const u32 tag = *(const volatile LDS_AS u32*)(smem + L_TOK);
        const f32x2 s0 = row_part(c[0][0], c[0][1]), s1 = row_part(c[1][0], c[1][1]), s = (g & 1) ? s1 : s0;
        __builtin_amdgcn_raw_buffer_store_b64((i32x2){(int)__float_as_uint((g & 2) ? s[1] : s[0]), (int)tag}, rs,
            OFF_ST + xo * 524288 + span(tmB, c13) + w * 16384 + tn * 128 + (lane & 24) * 256 + (lane & 7) * 16 + (g >> 1) * 8, 0, 16);
        SB();
        TS(tb);
        publish(IC<1>{}, ph, tmB, tnB);
        TS(tc);
      }
      STAMP(0, 4 * ph + 3);
    };

    auto level = [&](int n) {
      f32x4 d[8];
      bf16x8 a[2], b[4];
      u32 p = fresh_lane(tid) * 16;
      pin(p);
      dsrd<0>(a[0], p); dsrd<1024>(a[1], p); dsrd<9216>(b[0], p); dsrd<10240>(b[1], p); dsrd<11264>(b[2], p); dsrd<12288>(b[3], p);
#pragma unroll
      for (int i = 0; i < 8; ++i) d[i] = zero4();
      wait_lgkm0();
#pragma nounroll
      for (int t = 0; t < n; ++t)
#pragma unroll
        for (int i = 0; i < 8; ++i) mfma(d[i], b[i & 3], a[i >> 2]);
      asm volatile("s_nop 15\n\ts_nop 15" ::: "memory");
      pin(d[0], d[1], d[2], d[3], d[4], d[5], d[6], d[7]);
    };

#pragma nounroll
    for (int pair = 0; pair < 2; ++pair) {
      f32x4 aco[4][4];
      {
        f32x4 acc[4][6], acch[2];
        f32x4 muv[4], muh, cs[4];
        f32x2 mu2[2], ivk[2][2], fk[2][2], csv;
        u32 sel[4], oa[2];
        {
        FRESH_LANE();
        STAMP(0, 16 * pair);
        const u32 lB = 9216 + (8 * g + (r >> 2)) * 128 + (((2 * wni) ^ ((r >> 3) & 1) ^ ((g & 1) << 1)) * 32) + (r & 3) * 8;
        const u32 lA = r * 128 + ((g ^ (r >> 1)) * 16);
        auto loop = [&](auto WM, auto RAW) {
          constexpr int wm = decltype(WM)::value, raw = decltype(RAW)::value;
#pragma unroll
          for (int i = 0; i < 4; ++i)
#pragma unroll
            for (int j = 0; j < 6; ++j) acc[i][j] = zero4();
          acch[0] = zero4(); acch[1] = zero4();
          bf16x8 fA[2][4], fH[2];
          u32x2 hB[2][12];
          u32 pA[3][2], pB[3][2], pH[3][2];
          f32x4 accq;
          if (raw) { accq = zero4(); *(u32*)(smem + L_XC + lane * 4) = 0x3f803f80u; }
#pragma unroll
          for (int k = 0; k < 3; ++k) {
            pA[k][0] = (raw ? lA : lane * 16) + 2 * k * SLOT; pA[k][1] = (lA ^ 64) + 2 * k * SLOT;
            pB[k][0] = lB + 2 * k * SLOT; pB[k][1] = (lB ^ 32) + 2 * k * SLOT;
            pin1(pA[k][0]); pin2(pB[k][0], pB[k][1]);
            if (raw) {
              pH[k][0] = r ? pA[k][0] : L_XC - (SLOT + 21504) + lA; pH[k][1] = r ? pA[k][1] : L_XC - (SLOT + 21504) + (lA ^ 64);
              pin1(pA[k][1]); pin2(pH[k][0], pH[k][1]);
            }
          }
          int ln;
          u32 aR, aG, aK[2];
          f32x2 ivq[2];
          float mh, cv;
          float fq[2][4];
          auto fB = [&](int set, int j) {
            return __builtin_bit_cast(bf16x8, (u32x4){hB[set][2 * j][0], hB[set][2 * j][1], hB[set][2 * j + 1][0], hB[set][2 * j + 1][1]});
          };
          kloop6<26 + raw, 17, 0>(
            [&](auto SL, auto Q) {
              constexpr int set = decltype(SL)::value & 1, k = decltype(SL)::value >> 1, q = decltype(Q)::value, h = q - 5;
              if constexpr (q < 4) dsrd<raw ? wm * SLOT + q * 2048 : set * SLOT + wm * 4096 + q * 1024>(fA[set][q], pA[k][raw & set]);
              else if constexpr (q == 4) dsrd<raw ? SLOT + 21504 : set * SLOT + 8192>(fH[set], raw ? pH[k][set] : pA[k][0]);
              else dstr1<set * SLOT + (h >> 2) * 4096 + (h & 1) * 512>(hB[set][h], pB[k][(h >> 1) & 1]);
            },
            [&](auto SET, auto M) {
              constexpr int set = decltype(SET)::value, m = decltype(M)::value, j = m >> 2, i = (SNK & 2) && (j & 1) ? 3 - (m & 3) : m & 3;
              if constexpr (m < 16) mfma(acc[i][j], fB(set, j), fA[set][i]);
              else if constexpr (m < 24) mfma(acc[i][j], fA[set][i], fB(set, j));
              else if constexpr (m == 26) mfma(accq, fB(set, wm), fH[set]);
              else if constexpr (wm == 0) mfma(acch[i], fB(set, 2 + i), fH[set]);
              else mfma(acch[i], fH[set], fB(set, 4 + i));
            }, [&](int t) { if (t < 0) STAMP(0, 16 * pair + 1); },
            [&](auto M) {
              constexpr int m = decltype(M)::value;
              if constexpr (m == 0) {
                ln = fresh_lane(tid);
                aR = L_INV + wm * 256 + (ln & 15) * 4; aG = L_INV + wm * 256 + (ln & 48);
                pin(aR, aG);
              }
              if constexpr (m == 1) { dsrd2<16, 32>(ivq[0], aR); dsrd2<48, 64>(ivq[1], aR); dsrd4<640 + 192>(muv[3], aG); }
              if constexpr (m == 2) { aK[0] = aG + (ln & 1) * 4; aK[1] = aK[0] + 64; pin(aK[0], aK[1]); }
              if constexpr (m == 3) {
                const u32 a0 = LANE_IN(M_OWN0) ? aK[1] : aK[0], a1 = LANE_IN(M_OWN1) ? aK[1] : aK[0];
                aK[0] = a0; aK[1] = a1;
                pin(aK[0], aK[1]);
              }
              if constexpr (m == 4) { dsrd2<0, 16>(ivk[0][0], aK[0]); dsrd2<32, 48>(ivk[0][1], aK[0]); dsrd2<2, 18>(ivk[1][0], aK[1]); }
              if constexpr (m == 5) { dsrd2<34, 50>(ivk[1][1], aK[1]); dsrd2<160, 176>(mu2[0], aR); dsrd2<192, 208>(mu2[1], aR); }
              if constexpr (m == 6) { dsrd4<640>(muv[0], aG); dsrd4<640 + 64>(muv[1], aG); dsrd4<640 + 128>(muv[2], aG); }
              if constexpr (m == 7) {
                if constexpr (wm) dsrd4<576 - 256>(muh, aG); else dsrd1<576>(mh, aR);
                aK[0] = L_C13;
                pin(aK[0]);
                dsrd1<0>(cv, aK[0]);
              }
              if constexpr (m == 8) { aR += L_CSQ - L_INV - wm * 256 + wni * 384; aG += L_CSQ - L_INV - wm * 256 + wni * 384; pin(aR, aG); }
              if constexpr (!raw) {
                if constexpr (m == 9) { dsrd4<128>(cs[2], aG); dsrd4<192>(cs[3], aG); dsrd2<64, 80>(csv, aR); }
                if constexpr (m == 10) { dsrd4<0>(cs[0], aG); dsrd4<64>(cs[1], aG); }
              }
              if constexpr (m == 11) { sel[0] = LANE_IN(M_PAR) ? 0x01000c0cu : 0x0c0c0100u; pin(sel[0]); }
              if constexpr (m == 12) { sel[1] = LANE_IN(M_PAR) ? 0x03020c0cu : 0x0c0c0302u; pin(sel[1]); }
              if constexpr (m == 13) {
                sel[2] = LANE_IN(M_OWN0) ? sel[0] : 0x0c0c0c0cu; sel[3] = LANE_IN(M_OWN1) ? sel[1] : 0x0c0c0c0cu;
                pin(sel[2], sel[3]);
              }
              if constexpr (m == 14) {
                sel[0] = LANE_IN(M_OWN0) ? 0x0c0c0c0cu : sel[0]; sel[1] = LANE_IN(M_OWN1) ? 0x0c0c0c0cu : sel[1];
                pin(sel[0], sel[1]);
              }
              if constexpr (m == 16) {
                wait_lgkm0();
                pin(ivq[0], ivq[1], muv[0], muv[1], muv[2], muv[3], ivk[0][0], ivk[0][1], ivk[1][0], ivk[1][1]);
                pin(mu2[0], mu2[1], cv);
                if constexpr (wm) pin(muh); else { pin(mh); muh = (f32x4){mh, mh, mh, mh}; }
                if constexpr (!raw) pin(cs[0], cs[1], cs[2], cs[3], csv);
              }
              if constexpr (m >= 17 && m < 21) {
                constexpr int i = m - 17;
                float cq = ivq[i >> 1][i & 1] * (0.17677669529663687f * 1.4426950408889634f);
                pin(cq);
                fq[0][i] = ivk[0][i >> 1][i & 1] * cq; fq[1][i] = ivk[1][i >> 1][i & 1] * cq;
                pin(fq[0][i], fq[1][i]);
                if constexpr (i & 1) { fk[0][i >> 1] = (f32x2){fq[0][i - 1], fq[0][i]}; fk[1][i >> 1] = (f32x2){fq[1][i - 1], fq[1][i]}; }
              }
              if constexpr (m == 21) {
                oa[0] = ((OFF_O + pair * XS + xpan(tmA, 0) + tnA * 65536 + aq(wni, wm)) ^ __float_as_uint(cv)) + ln * 16;
                oa[1] = oa[0] + 128;
                pin(oa[0], oa[1]);
              }
              if constexpr (m == 22) c13 = __builtin_amdgcn_readfirstlane(__float_as_uint(cv));
            });
          if constexpr (raw) {
            FRESH_LANE();
            if (LANE_IN(0x0001000100010001ull)) {
              *(f32x4*)(smem + aG + wm * 64) = accq;
              if (wm == 0) { *(f32x4*)(smem + aG + 128) = acch[0]; *(f32x4*)(smem + aG + 192) = acch[1]; }
            }
            if (wm == 1 && LANE_IN(0xffffull)) { *(float*)(smem + aR + 256) = acch[0][0]; *(float*)(smem + aR + 320) = acch[1][0]; }
            wait_lgkm0();
            bar();
            dsrd4<128>(cs[2], aG); dsrd4<192>(cs[3], aG); dsrd2<64, 80>(csv, aR); dsrd4<0>(cs[0], aG); dsrd4<64>(cs[1], aG);
#pragma unroll
            for (int i = 0; i < 2; ++i)
#pragma unroll
              for (int gl = 0; gl < 2; ++gl)
                c[i][gl] = __builtin_bit_cast(u32x4, __builtin_amdgcn_raw_buffer_load_b128(rsrc(x, SEQ * DM * 2),
                    (16 * i + r) * 2048 + g * 16, (tmB * 128 + w * 32) * 2048 + tn * 128 + gl * 64, 0));
            wait_lgkm0();
            pin(cs[0], cs[1], cs[2], cs[3], csv);
          }
        };
        if (pair == 0) { if (wmi == 0) loop(IC<0>{}, IC<1>{}); else loop(IC<1>{}, IC<1>{}); }
        else { if (wmi == 0) loop(IC<0>{}, IC<0>{}); else loop(IC<1>{}, IC<0>{}); }
        }
        FRESH_LANE();
        STAMP(0, 16 * pair + 2);
        auto fix_qk = [&](const f32x4& a0, const f32x4& a1, float mu, int f) {
          const f32x4 y0 = a0 - cs[f] * mu, y1 = a1 - cs[f + 1] * mu;
          u32x4 v = {pack2(y0[0], y0[1]), pack2(y0[2], y0[3]), pack2(y1[0], y1[1]), pack2(y1[2], y1[3])};
          pin(v);
          return v;
        };
        auto fix_v = [&](const f32x4& a, const f32x4& mu, float cw) {
          const f32x4 y = a - mu * cw;
          u32x2 v = {pack2(y[0], y[1]), pack2(y[2], y[3])};
          pin(v);
          return v;
        };
        u32x4 Qf[4], Kf[5];
        u32x2 Vf[5][2];
        char* xc = smem + L_XC + wni * 3072;
        Kf[4] = fix_qk(acc[3][2], acc[3][3], mu2[1][1], 2);
        Vf[4][0] = fix_v(acc[3][4], muv[3], csv[0]); Vf[4][1] = fix_v(acc[3][5], muv[3], csv[1]);
        if (wmi == 0) {
          *(u32x4*)(xc + lane * 16) = Kf[4];
          *(u32x2*)(xc + 1024 + lane * 8) = Vf[4][0]; *(u32x2*)(xc + 1536 + lane * 8) = Vf[4][1];
          Kf[0] = fix_qk(acch[0], acch[1], muh[0], 2);
        } else {
          Kf[0] = Kf[4];
          *(u32x2*)(xc + 2048 + lane * 8) = fix_v(acch[0], muh, csv[0]);
          *(u32x2*)(xc + 2560 + lane * 8) = fix_v(acch[1], muh, csv[1]);
        }
#pragma unroll
        for (int i = 0; i < 3; ++i) Kf[i + 1] = fix_qk(acc[i][2], acc[i][3], mu2[i >> 1][i & 1], 2);
        wait_lgkm0();
        bar();
        TS(td);
        if (wmi == 1) Kf[0] = *(const u32x4*)(xc + lane * 16);
        Vf[0][0] = *(const u32x2*)(xc + (wmi == 0 ? 2048 : 1024) + lane * 8);
        Vf[0][1] = *(const u32x2*)(xc + (wmi == 0 ? 2560 : 1536) + lane * 8);
        const f32x4 z = {0.f, 0.f, 0.f, 0.f};
        f32x4 S[4][2];
#pragma unroll
        for (int i = 3; i >= 0; --i) {
          Qf[i] = fix_qk(acc[i][0], acc[i][1], mu2[i >> 1][i & 1], 0);
          S[i][1] = __builtin_amdgcn_mfma_f32_16x16x32_bf16(__builtin_bit_cast(bf16x8, Kf[i + 1]), __builtin_bit_cast(bf16x8, Qf[i]), z, 0, 0, 0);
          S[i][0] = __builtin_amdgcn_mfma_f32_16x16x32_bf16(__builtin_bit_cast(bf16x8, Kf[i]), __builtin_bit_cast(bf16x8, Qf[i]), z, 0, 0, 0);
        }
#pragma unroll
        for (int i = 0; i < 3; ++i) { Vf[i + 1][0] = fix_v(acc[i][4], muv[i], csv[0]); Vf[i + 1][1] = fix_v(acc[i][5], muv[i], csv[1]); }
        const bool par = LANE_IN(M_PAR), own0 = LANE_IN(M_OWN0), own1 = LANE_IN(M_OWN1);
        f32x2 l[2][2];
#pragma unroll
        for (int i = 3; i >= 0; --i) {
          auto pick = [](bool c, float a, float b) { return c ? a : b; };
          l[0][i >> 1][i & 1] = pick(own0, pick(par, S[i][1][1], S[i][1][0]), pick(par, S[i][0][1], S[i][0][0]));
          l[1][i >> 1][i & 1] = pick(own1, pick(par, S[i][1][3], S[i][1][2]), pick(par, S[i][0][3], S[i][0][2]));
        }
#pragma unroll
        for (int i = 0; i < 4; ++i) l[i >> 1][i & 1] *= fk[i >> 1][i & 1];
        {
          const u64 first = tmA == 0 && wmi == 0 ? ~0ull : 0ull;
          if (LANE_IN(first & ~M_OWN0)) l[0][0][0] = -1.4426950e9f;
          if (LANE_IN(first & ~M_OWN1)) l[1][0][0] = -1.4426950e9f;
        }
        auto all4 = [](f32x2 (&v)[2], auto f, auto mid) {
          auto U = [](float a) { return __float_as_uint(a); };
          auto F = [](u32 a) { return __uint_as_float(a); };
          const auto a = __builtin_amdgcn_permlane32_swap(U(v[0][0]), U(v[1][0]), false, false);
          const auto b = __builtin_amdgcn_permlane32_swap(U(v[0][1]), U(v[1][1]), false, false);
          const auto c = __builtin_amdgcn_permlane16_swap(U(f(F(a[0]), F(a[1]))), U(f(F(b[0]), F(b[1]))), false, false);
          const u32 t = U(mid(f(F(c[0]), F(c[1]))));
          const auto d = __builtin_amdgcn_permlane16_swap(t, t, false, false);
          const auto e0 = __builtin_amdgcn_permlane32_swap(d[0], d[0], false, false);
          const auto e1 = __builtin_amdgcn_permlane32_swap(d[1], d[1], false, false);
          v[0] = (f32x2){F(e0[0]), F(e1[0])}; v[1] = (f32x2){F(e0[1]), F(e1[1])};
        };
        f32x2 t[2] = {__builtin_elementwise_maximum(l[0][0], l[1][0]), __builtin_elementwise_maximum(l[0][1], l[1][1])};
        all4(t, [](float a, float b) { return __builtin_elementwise_maximum(a, b); }, [](float a) { return a; });
#pragma unroll
        for (int i = 0; i < 4; ++i) {
          const f32x2 d = l[i >> 1][i & 1] - t[i & 1];
          l[i >> 1][i & 1] = (f32x2){__builtin_amdgcn_exp2f(d[0]), __builtin_amdgcn_exp2f(d[1])};
        }
        t[0] = l[0][0] + l[1][0]; t[1] = l[0][1] + l[1][1];
        all4(t, [](float a, float b) { return a + b; }, [](float a) { return __builtin_amdgcn_rcpf(a); });
#pragma unroll
        for (int i = 0; i < 4; ++i) l[i >> 1][i & 1] = l[i >> 1][i & 1] * ivk[i >> 1][i & 1] * t[i & 1];
        u32x10 V[2];
#pragma unroll
        for (int i = 0; i < 10; ++i) { V[0][i] = Vf[i >> 1][0][i & 1]; V[1][i] = Vf[i >> 1][1][i & 1]; }
        pin(V[0], V[1]);
        for_n<4>([&](auto I) {
          constexpr int i = decltype(I)::value;
          const u32 pw = pack2(l[0][i >> 1][i & 1], l[1][i >> 1][i & 1]);
          const u32x4 P = {__builtin_amdgcn_perm(pw, pw, sel[0]), __builtin_amdgcn_perm(pw, pw, sel[1]),
                           __builtin_amdgcn_perm(pw, pw, sel[2]), __builtin_amdgcn_perm(pw, pw, sel[3])};
          const u32x4 V0 = __builtin_shufflevector(V[0], V[0], 2 * i, 2 * i + 1, 2 * i + 2, 2 * i + 3);
          const u32x4 V1 = __builtin_shufflevector(V[1], V[1], 2 * i, 2 * i + 1, 2 * i + 2, 2 * i + 3);
          const f32x4 O0 = __builtin_amdgcn_mfma_f32_16x16x32_bf16(__builtin_bit_cast(bf16x8, V0), __builtin_bit_cast(bf16x8, P), z, 0, 0, 0);
          const f32x4 O1 = __builtin_amdgcn_mfma_f32_16x16x32_bf16(__builtin_bit_cast(bf16x8, V1), __builtin_bit_cast(bf16x8, P), z, 0, 0, 0);
          const u32x4 o = {pack2(O0[0], O0[1]), pack2(O0[2], O0[3]), pack2(O1[0], O1[1]), pack2(O1[2], O1[3])};
          st16(o, (LANE_IN(~0ull << (i == 3 ? 56 : 24 - 8 * i)) ? oa[1] : oa[0]) + qoff(i, 0));
        });
        SB();
        TS(te);
        start(aco, c[0], false);
        publish(IC<0>{}, 4 * pair, tmA, tnA);
        if (pair == 0) level(LVA);
        TSW(td, 35); TSW(te, 34);
        STAMP(0, 16 * pair + 3);
      }
      {
        FRESH_LANE();
        bf16x8 fA[2][2][2], fB[2][2][4];
        u32 pA, pB;
        so = 2 * SLOT;
        kloop<16, 16, 12, 0>(so,
          [&](int s_, auto) { pA = lane * 16 + wmi * 2048 + s_; pB = lane * 16 + wni * 4096 + s_; pin2(pA, pB); },
          [&](auto SET, auto Q) {
            constexpr int set = decltype(SET)::value, q = decltype(Q)::value, s = q / 6, m = q % 6;
            if constexpr (m < 2) dsrd<s * 4096 + m * 1024>(fA[set][s][m], pA);
            else dsrd<8192 + s * 8192 + (m - 2) * 1024>(fB[set][s][m - 2], pB);
          },
          [&](auto SET, auto M) {
            constexpr int set = decltype(SET)::value & 1, m = decltype(M)::value;
            constexpr int s = (SNK & 4) ? (m ^ (m >> 1)) & 1 : m >> 3, j = (SNK & 4) ? m >> 2 : (m >> 1) & 3, i = (SNK & 4) ? ((m >> 1) ^ (m >> 2)) & 1 : m & 1;
            mfma(aco[i][j], fB[set][s][j], fA[set][s][i]);
          },
          [&](int t) { if (t < 0) STAMP(0, 16 * pair + 5); }, [](auto) {});
        STAMP(0, 16 * pair + 6);
        finish(aco, 2 * pair + 1, 4 * pair + 1);
        if (pair == 0) level(LVB);
      }
      {
        f32x4 acc[4][7];
        {
        FRESH_LANE();
#pragma unroll
        for (int i = 0; i < 4; ++i)
#pragma unroll
          for (int j = 0; j < 7; ++j) acc[i][j] = zero4();
        bf16x8 fA[2][4], fB[2][7];
        u32 pA[3], pB[3];
#pragma unroll
        for (int k = 0; k < 3; ++k) {
          pA[k] = lane * 16 + wmi * 4096 + 2 * k * SLOT; pB[k] = lane * 16 + wni * 7168 + 2 * k * SLOT;
          pin2(pA[k], pB[k]);
        }
        kloop6<28, 11, 1>(
          [&](auto SL, auto Q) {
            constexpr int set = decltype(SL)::value & 1, k = decltype(SL)::value >> 1, q = decltype(Q)::value;
            if constexpr (q < 4) dsrd<set * SLOT + q * 1024>(fA[set][q], pA[k]);
            else dsrd<set * SLOT + 8192 + (q - 4) * 1024>(fB[set][q - 4], pB[k]);
          },
          [&](auto SET, auto M) {
            constexpr int set = decltype(SET)::value, m = decltype(M)::value, j0 = m >> 2, i0 = (SNK & 1) && (j0 & 1) ? 3 - (m & 3) : m & 3;
            constexpr int i = i0, j = j0;
            mfma(acc[i][j], fB[set][j], fA[set][i]);
          }, [&](int t) { if (t < 0) STAMP(0, 16 * pair + 9); }, [](auto) {});
        }
        FRESH_LANE();
        STAMP(0, 16 * pair + 10);
        float iv[4], mv[4];
#pragma unroll
        for (int i = 0; i < 4; ++i) { iv[i] = t_inv[16 + wmi * 64 + 16 * i + r]; mv[i] = t_mnv[16 + wmi * 64 + 16 * i + r]; }
        const u32 hbase = OFF_H + pair * HS + hpan(tmA, tnA >> 3, c13);
        auto act = [&](const f32x4& a, const f32x4& cs, const f32x4& bs, int i) {
          const f32x4 y = a * iv[i] + (bs - cs * mv[i]);
          return (u32x2){gelu_tanh2((f32x2){y[0], y[1]}), gelu_tanh2((f32x2){y[2], y[3]})};
        };
#pragma unroll
        for (int jp = 0; jp < 3; ++jp) {
          const int t0 = (wni * 7 + 2 * jp) * 16 + 4 * g;
          const f32x4 cs0 = *(const f32x4*)(t_cs + t0), cs1 = *(const f32x4*)(t_cs + t0 + 16);
          const f32x4 bs0 = *(const f32x4*)(t_bs + t0), bs1 = *(const f32x4*)(t_bs + t0 + 16);
#pragma unroll
          for (int i = 0; i < 4; ++i) {
            const u32x2 a = act(acc[i][2 * jp], cs0, bs0, i), b = act(acc[i][2 * jp + 1], cs1, bs1, i);
            const u32x4 v = {a[0], a[1], b[0], b[1]};
            st16(v, hbase + aq(hpos(tnA, wni * 4 + jp), wmi) + qoff(i, lane));
            if (4 * jp + i == 3 + PUBJ) publish(IC<PUBJ>{}, 4 * pair + 2, tmA, tnA);
          }
          if (jp < 2) SB();
        }
        {
          const int t0 = (wni * 7 + 6) * 16 + 4 * g;
          const f32x4 cs0 = *(const f32x4*)(t_cs + t0), bs0 = *(const f32x4*)(t_bs + t0);
#pragma unroll
          for (int i = 0; i < 4; i += 2) {
            const u32x2 a = act(acc[i][6], cs0, bs0, i), b = act(acc[i + 1][6], cs0, bs0, i + 1);
            const auto s0 = __builtin_amdgcn_permlane16_swap(a[0], b[0], false, false);
            const auto s1 = __builtin_amdgcn_permlane16_swap(a[1], b[1], false, false);
            const u32x4 v = {s0[0], s1[0], s0[1], s1[1]};
            st16(v, hbase + aq(hpos(tnA, 3), wmi) + qoff(i + (g & 1), (2 * wni + (g >> 1)) * 16 + r));
          }
        }
        STAMP(0, 16 * pair + 11);
      }
      {
        FRESH_LANE();
        f32x4 acc[4][4];
        SB();
        const u32x4 bb[2] = {*(const u32x4*)(smem + L_B2 + wni * 128 + g * 16), *(const u32x4*)(smem + L_B2 + wni * 128 + 64 + g * 16)};
#pragma unroll
        for (int i = 2; i < 4; ++i)
#pragma unroll
          for (int j = 0; j < 4; ++j) acc[i][j] = zero4();
        start(acc, bb, true);
        bf16x8 fA[2][2][4], fB[2][2][4];
        u32 pA, pB;
        const u32 xc = wmi * 16384 + wni * 4096;
        wait_vm<6>();
        so = 0;
        kloop<28, 32, 16, 1>(so,
          [&](int s_, auto) { pA = lane * 16 + wmi * 2048 + s_; pB = lane * 16 + wni * 4096 + s_; pin2(pA, pB); },
          [&](auto SET, auto Q) {
            constexpr int set = decltype(SET)::value, q = decltype(Q)::value, s = q >> 3, m = q & 7;
            if constexpr (m < 4) dsrd<s * 8192 + (m >> 1) * 4096 + (m & 1) * 1024>(fA[set][s][m], pA);
            else dsrd<16384 + s * 8192 + (m - 4) * 1024>(fB[set][s][m - 4], pB);
          },
          [&](auto SET, auto M) {
            constexpr int set = decltype(SET)::value & 1, m = decltype(M)::value, o = decltype(SET)::value > 1 ? 0 : SNK >> 3;
            constexpr int s = o == 0 ? (m >> 3) & 1 : o == 1 ? m >> 4 : o == 2 ? m & 1 : (m ^ (m >> 1)) & 1;
            constexpr int j = o == 0 ? (m >> 1) & 3 : o == 1 ? (m >> 2) & 3 : m >> 3;
            constexpr int i = o == 0 ? (m & 1) + (m < 16 ? 2 : 0) : o == 1 ? ((m & 4) ? 3 - (m & 3) : m & 3) : o == 3 && (m & 8) ? 3 - ((m >> 1) & 3) : (m >> 1) & 3;
            mfma(acc[i][j], fB[set][s][j], fA[set][s][i]);
          },
          [&](int t) { if (t < 0) { STAMP(0, 16 * pair + 13); } else wait_vm<0>(); },
          [&](auto M) {
            constexpr int k = decltype(M)::value - SND - 1;
            if constexpr (k >= 0 && k < 8 && k % 2 == 0) {
              const f32x4 a = acc[2][k >> 1], b = acc[3][k >> 1];
              const u32x4 v = {pack2(a[0], a[1]), pack2(a[2], a[3]), pack2(b[0], b[1]), pack2(b[2], b[3])};
              st16(__builtin_elementwise_min(v, (u32x4){~1u, ~1u, ~1u, ~1u}), xbox(pair, 0) + xc + lane * 16 + k * 512);
            }
          });
        STAMP(0, 16 * pair + 14);
        u32x4 xv[4];
#pragma nounroll
        for (int spin = 0; spin < (1 << 22); ++spin) {
#pragma unroll
          for (int k = 0; k < 4; ++k)
            xv[k] = __builtin_bit_cast(u32x4, __builtin_amdgcn_raw_buffer_load_b128(rs, lane * 16 + k * 1024, xbox(pair, 1) + xc, 16));
          const u32x4 o = __builtin_elementwise_max(__builtin_elementwise_max(xv[0], xv[1]), __builtin_elementwise_max(xv[2], xv[3]));
          if (!__any(max(max(o[0], o[1]), max(o[2], o[3])) == ~0u)) break;
          asm volatile("" ::: "memory");
        }
#pragma unroll
        for (int k = 0; k < 4; ++k) {
          acc[0][k] += (f32x4){bf_lo(xv[k][0]), bf_hi(xv[k][0]), bf_lo(xv[k][1]), bf_hi(xv[k][1])};
          acc[1][k] += (f32x4){bf_lo(xv[k][2]), bf_hi(xv[k][2]), bf_lo(xv[k][3]), bf_hi(xv[k][3])};
        }
        finish(acc, pair == 0 ? 2 : 4, 4 * pair + 3);
      }
    }
  } else if (wid == 4) {
    {
      u32 vo[4];
      x_lanes(vo);
      wait_vm<36>();
      bar();
      bar();
#pragma nounroll
      for (int U = 3; U < 16; ++U) {
        wait_vm<18>();
        bar();
        x_half(IC<0>{}, U, vo);
        bar();
        x_half(IC<1>{}, U, vo);
      }
      wait_vm<18>(); bar(); bar();
      wait_vm<0>(); bar(); bar(); bar();
    }
    auto phase = [&](auto NSL_, auto HALO_, u32 src, u32 hsrc, int ph  , int tm, int tmh) {
      constexpr int NSL = decltype(NSL_)::value, HALO = decltype(HALO_)::value;
      constexpr int BIG = NSL == 28, PA = BIG ? 16 : 8, PT = PA + HALO, D = BIG ? DB : NSL == 16 ? DN : DW, X = BIG ? XA2 : 0;
      FRESH_LANE();
      u32 vo[4];
#pragma unroll
      for (int e = 0; e < 4; ++e) vo[e] = qoff(e, lane) - e * 1024;
      poll(flag(ph + 1, (lane & 16) ? tmh : tm, BIG ? 8 * (tnB & 1) + ((rotA >> 1) + (r < D + X ? r : 0))
                                               : NSL == 16 ? (rotB + (r < DN ? r : 0)) & 15 : rotA + (r & (DW > 4 ? 3 : 1))), token());
      STAMP(1, 4 * (ph + 1));
      const u32 flip = BIG ? (tnB & 1) * 16384 : 0;
      stream<NSL, PT, D, X>(so, 0,
        [&](int u) {
          u = BIG ? (u < 8 ? u ^ (rotA >> 1) : u) : NSL == 16 ? (u + rotB) & 15 : u ^ (2 * rotA);
          const u32 ko = aq(NSL == 32 ? u : 2 * u, 0), l = so;
          next_slot<BIG>(so);
          return Slot{ko, src + ko + flip, l};
        },
        [&](auto E, const Slot& a) {
          constexpr int e = decltype(E)::value;
          if constexpr (e == PA) DMA(rs, (LDS_AS void*)(a.l0 + 8192), 16, vo[3], hsrc + a.k, 0, 16);
          else DMA(rs, (LDS_AS void*)(a.l0 + (e >> 2) * 4096), 16, vo[e & 3],
                   e < 4 ? a.a0 : src + a.k + (NSL == 16 ? aq(e >> 2, 0) : aq(e >> 3, (e >> 2) & 1) + ((e & 4) ? -flip : flip)),
                   (e & 3) * 1024, 16);
        }, [&]() { bar(); });
    };
#pragma nounroll
    for (int pair = 0; pair < 2; ++pair) {
      const int tmh = tmA > 0 ? tmA - 1 : 0;
      const u32 xq = OFF_X + 2 * pair * XS;
      if (pair) phase(IC<32>{}, IC<1>{}, xq + xpan(tmA, c13), xq + xpan(tmh, c13) + 16384 + 3072, 3, tmA, tmh);
      side();
      bar();
      phase(IC<16>{}, IC<0>{}, OFF_O + pair * XS + xpan(tmB, c13) + aq(0, tnB & 1), 0, 4 * pair, tmB, tmB);
      phase(IC<32>{}, IC<0>{}, xq + XS + xpan(tmA, c13), 0, 4 * pair + 1, tmA, tmA);
      bar();
      so = 0;
      phase(IC<28>{}, IC<0>{}, OFF_H + pair * HS + hpan(tmB, tnB & 1, c13), 0, 4 * pair + 2, tmB, tmB);
      bar();
      so = 0;
    }
  } else if (wid == 5) {
    FRESH_LANE();
    auto phase = [&](auto NSL_, auto PB_, auto AUX_, int u0, int add, int rot, __amdgpu_buffer_rsrc_t rb, const u32 (&vo)[4], u32 boff,
                     u32 src, u32 step, auto&& q) {
      constexpr int NSL = decltype(NSL_)::value, PB = decltype(PB_)::value;
      stream<NSL, PB, (NSL == 28 ? DB : 5), (NSL == 28 ? XB2 : NSL == 16 ? XBO : 0)>(so, u0,
        [&](int u) {
          u = NSL == 28 ? (u < 8 ? u ^ rot : u) : ((u + add) & (NSL - 1)) ^ rot;
          const u32 l = so + boff;
          next_slot<NSL == 28>(so);
          return Slot{0, src + u * step, l};
        },
        [&](auto I, const Slot& a) {
          constexpr int i = decltype(I)::value;
          DMA(rb, (LDS_AS void*)(a.l0 + (i >> 2) * 4096), 16, vo[i & 3], a.a0 + q(i >> 2), (i & 3) * 1024, decltype(AUX_)::value);
        }, [&]() { bar(); });
    };
    auto tiles = [&](auto NSL_, auto PB_, u32 boff, u32 src) {
      constexpr int NSL = decltype(NSL_)::value, PB = decltype(PB_)::value;
      u32 vo[4];
#pragma unroll
      for (int e = 0; e < 4; ++e) vo[e] = NSL == 28 && D2 ? lane * 16 : qoff(e, lane) - e * 1024;
      constexpr u32 Q = NSL == 28 ? Q2 : QS;
      phase(NSL_, PB_, IC<16>{}, 0, NSL == 16 ? rotB : 0, NSL == 28 ? rotA >> 1 : NSL == 16 ? 0 : 2 * rotA, rs, vo, boff, src,
            PB == 16 ? 2 * Q : 4 * Q, [](int i) { return PB == 16 ? (i >> 1) * Q + (i & 1) * ((NSL == 28 ? 112 : 32) * Q) : i * Q; });
    };
    so = 5 * SLOT;
#pragma nounroll
    for (int pair = 0; pair < 2; ++pair) {
      {
        u32 vo[4];
        q_lanes(vo);
        phase(IC<32>{}, IC<12>{}, IC<0>{}, pair ? 0 : 5, pair ? 0 : 4 * rot0, pair ? 2 * rotA : 0, rsrc(wqkv, DM * 3 * DM * 2), vo, 9216,
              tnA * 128, 32 * 6144, [](int i) { return i * 2048; });
      }
      if (pair == 0) bar();
      bar();
      tiles(IC<16>{}, IC<16>{}, 8192, OFF_WO + (tnB & 14) * (32 * QS));
      if (pair == 0) poll(flag(F_W1, r, (tnA & 14) + (g & 1)), token());
      tiles(IC<32>{}, IC<14>{}, 8192, OFF_W1 + tnA * (32 * 4 * QS));
      bar();
      so = 0;
      if (pair == 0) poll(flag(F_W2, 8 * (tnB & 1) + (r & 7), (tnB & 14) + (r >> 3)), token());
      tiles(IC<28>{}, IC<16>{}, 16384, OFF_W2 + ((tnB & 14) * 112 + (tnB & 1) * 56) * Q2);
      bar();
      so = 0;
    }
  } else if (wid == 6) {
    FRESH_LANE();
    const int rr = lane >> 3, c16 = (lane & 7) * 16;
    u32x4 U[7][4], F[4];
    auto u_load = [&](auto I) {
      constexpr int i = decltype(I)::value;
#pragma unroll
      for (int e = 0; e < 4; ++e) {
        if constexpr (i < 2)
          U[(i + 5) % 7][e] = __builtin_bit_cast(u32x4, __builtin_amdgcn_raw_buffer_load_b128(rsrc(wo, DM * DM * 2),
              ((rr & 3) + 16 * (rr >> 2)) * 2048 + c16, ((2 * tmA + i) * 32 + 4 * e) * 2048 + (tnA ^ (4 * i)) * 128, UAUX));
        else if constexpr (i < 9)
          U[(i + 5) % 7][e] = __builtin_bit_cast(u32x4, __builtin_amdgcn_raw_buffer_load_b128(rsrc(w1, DM * FF * 2),
              rr * 7168 + c16, ((2 * tmA + (tnA & 1)) * 32 + 8 * e) * 7168 + (7 * (tnA >> 1) + i - 2) * 128, UAUX));
        else
          U[(i + 5) % 7][e] = __builtin_bit_cast(u32x4, __builtin_amdgcn_raw_buffer_load_b128(rsrc(w2, DM * FF * 2),
              rr * 2048 + c16, ((7 * tmA + i - 9) * 32 + 8 * e) * 2048 + tnA * 128, UAUX));
      }
    };
    auto u_write = [&](auto I) {
      constexpr int i = decltype(I)::value;
#pragma unroll
      for (int e = 0; e < 4; ++e) {
        const u32x4 v = U[(i + 5) % 7][e];
        *(u32x4*)(smem + L_IM + e * 1024 + rr * 128 + (c16 ^ ((rr & 2) << 5))) = (e & 1) ? (u32x4){v[2], v[3], v[0], v[1]} : v;
      }
    };
    auto u_store = [&](auto I) {
      constexpr int i = decltype(I)::value;
      const u32 sw = ((r & 8) << 3) | ((g & 1) << 3);
      const char* im = smem + L_IM + (8 * g + (r >> 2)) * 128;
      for_n<4>([&](auto E) {
        constexpr int e = decltype(E)::value;
        constexpr bool split = i >= 2 && i < 9 && (2 * (i - 2) + (e >> 1)) % 7 == 3;
        const char* p = im + (((e >> 1) * 64 + (split ? (e & 1) * 32 + (r & 3) * 8 : (r & 3) * 16 + (e & 1) * 8)) ^ sw);
        const u32x2 lo = __builtin_bit_cast(u32x2, __builtin_amdgcn_ds_read_tr16_b64_v4i16((LDS_AS s16x4*)p));
        const u32x2 hi = __builtin_bit_cast(u32x2, __builtin_amdgcn_ds_read_tr16_b64_v4i16((LDS_AS s16x4*)(p + 512)));
        F[e] = (u32x4){lo[0], lo[1], hi[0], hi[1]};
      });
      for_n<4>([&](auto E) {
        constexpr int e = decltype(E)::value;
        if constexpr (i < 2) st16(F[e], OFF_WO + ((tnA ^ (4 * i)) * 32 + 2 * tmA + i) * QS + qoff(e, lane));
        else if constexpr (i < 9) {
          constexpr int f = w1_frag(i, e);
          st16(F[e], OFF_W1 + ((((tnA & 14) + (f >> 4)) * 32 + 2 * tmA + (tnA & 1)) * 4 + ((f & 15) >> 2)) * QS + qoff(f & 3, lane));
        } else st16(F[e], OFF_W2 + (tnA * 112 + (tmA >> 3) * 56 + hpos(tmA, i - 9)) * Q2 + (D2 ? e * 1024 + lane * 16 : qoff(e, lane)));
      });
    };
    auto u_sums = [&](auto I) {
      constexpr int i = decltype(I)::value;
      constexpr u32 K = w1_frag(i, 0) | (w1_frag(i, 1) << 8) | (w1_frag(i, 2) << 16) | (w1_frag(i, 3) << 24);
      f32x4 cs = {0.f, 0.f, 0.f, 0.f};
#pragma unroll
      for (int e = 0; e < 4; ++e) {
        const u32 o = (r >> 2) == e ? 0x3f803f80u : 0u;
        cs = __builtin_amdgcn_mfma_f32_16x16x32_bf16(__builtin_bit_cast(bf16x8, (u32x4){o, o, o, o}), __builtin_bit_cast(bf16x8, F[e]), cs, 0, 0, 0);
      }
      const u32 f = (K >> (8 * g)) & 255;
      __builtin_amdgcn_raw_buffer_store_b32(__float_as_uint(cs[0]), rs,
          OFF_CS1 + ((((tnA & 14) + (f >> 4)) * 32 + 2 * tmA + (tnA & 1)) * 256 + (f & 15) * 16 + r) * 4, 0, 16);
    };
    u64 tok = 0;
    auto up = [&](int set) { st8(tok, lane == 0, flag(set, tmA, tnA)); };
    f32x4 cv[32], ca[2];
    auto cs_load = [&](u32 src, int p0, int p1) {
#pragma unroll
      for (int p = p0; p < p1; ++p)
        cv[p] = __builtin_bit_cast(f32x4, __builtin_amdgcn_raw_buffer_load_b128(rs, lane * 16 + (p & 3) * 1024, src + (p >> 2) * 4096, 16));
    };
    auto cs_add = [&](int p0, int p1) {
#pragma unroll
      for (int p = p0; p < p1; ++p) {
        asm volatile("" : "+v"(cv[p]));
        if (p < 2) ca[p] = cv[p];
        else {
#pragma unroll
          for (int k = 0; k < 4; ++k) {
            float t = ca[p & 1][k];
            const float u = cv[p][k];
            asm("v_add_f32 %0, %0, %1" : "+v"(t) : "v"(u));
            ca[p & 1][k] = t;
          }
        }
      }
      asm volatile("" : "+v"(ca[0]), "+v"(ca[1]));
    };
    u64 vf = 0;
    const u32 fo = flag(F_WO, r, ((tnB & 14) + (g & 1)) ^ (2 * (g & 2)));
    __builtin_amdgcn_s_sleep(PFW0);
    u_load(IC<0>{});
    u_load(IC<1>{});
    for_n<34>([&](auto S) {
      constexpr int s = decltype(S)::value;
      bar();
      if constexpr (s == TOKB) { tok = token(); asm volatile("" : "+s"(tok)); }
      if constexpr (s >= HL && s < HL + 4) {
        if constexpr ((s - HL) % 2 == 0) u_write(IC<(s - HL) / 2>{}); else u_store(IC<(s - HL) / 2>{});
      }
      if constexpr (s == HL + 3 + FLD) { wait_vm<0>(); up(F_WO); }
      if constexpr (s == G0 - 4) vf = ldf(fo);
      if constexpr (s == G0 - 1) { if (!__all(vf == tok)) poll(fo, tok); }
      if constexpr (s == G0) for_n<4>([&](auto I) { u_load(IC<2 + decltype(I)::value>{}); });
      if constexpr (s == G0 + 1) for_n<3>([&](auto I) { u_load(IC<6 + decltype(I)::value>{}); });
    });
    static_assert(TOKB < HL && HL + 3 + FLD < G0 - 4 && G0 + 1 < 33, "schedule");
    for_n<7>([&](auto I) {
      constexpr int i = 2 + decltype(I)::value;
      u_write(IC<i>{});
      u_store(IC<i>{});
      u_sums(IC<i>{});
    });
    bar(); bar(); bar();
    wait_vm<0>();
    up(F_W1);
#pragma nounroll
    for (int u = 3; u <= G1; ++u) bar();
    for_n<7>([&](auto I) {
      u_load(IC<9 + decltype(I)::value>{});
      if constexpr (decltype(I)::value == 1 || decltype(I)::value == 3) bar();
    });
#pragma nounroll
    for (int u = G1 + 3; u < 16 + H2; ++u) bar();
    for_n<14>([&](auto S) {
      constexpr int s = decltype(S)::value, i = 9 + s / 2;
      bar();
      if constexpr (s % 2 == 0) u_write(IC<i>{}); else u_store(IC<i>{});
    });
    for_n<12>([&](auto S) {
      constexpr int s = decltype(S)::value;
      bar();
      if constexpr (s == 3) { wait_vm<16>(); up(F_W2); }
      if constexpr (s < 6) cs_load(OFF_CS1 + tnA * 32768, s * 16 / 3, (s + 1) * 16 / 3);
      if constexpr (s >= 8) cs_add(8 * (s - 8), 8 * (s - 7));
    });
    static_assert(H2 + 14 + 12 <= 31, "schedule");
    *(f32x4*)(smem + L_CS1 + lane * 16) = ca[0] + ca[1];
    wait_lgkm0();
    QUIT();
  } else {
    FRESH_LANE();
    const int tmh = tmA > 0 ? tmA - 1 : 0;
    u64 tok;
    auto bars = [&](int n) {
#pragma nounroll
      for (int u = 0; u < n; ++u) bar();
    };
    auto ror8 = [](float v) { return __uint_as_float(__builtin_amdgcn_mov_dpp(__float_as_uint(v), 0x128, 0xf, 0xf, false)); };
    auto put = [&](int xi, float mean, float m2, bool on, int row) {
      const float sd = __builtin_amdgcn_sqrtf(bfr(bfr(m2 * (1.f / DM)) + LN_EPS));
      st8(pack2(mean, sd) | (tok << 32), on, flag(F_LN + xi, tmA, tnA) + row * 8);
    };
    auto reduce = [&](int xi) {
      const u32 src = OFF_ST + xi * 524288 + span(tmA, c13) + (tnA >> 2) * 16384 + (tnA & 3) * 2048, tag = (u32)tok;
      u32x4 a, b;
      auto ld = [&]() {
        asm volatile("" ::: "memory");
        a = __builtin_bit_cast(u32x4, __builtin_amdgcn_raw_buffer_load_b128(rs, lane * 16, src, 16));
        b = __builtin_bit_cast(u32x4, __builtin_amdgcn_raw_buffer_load_b128(rs, lane * 16 + 1024, src, 16));
      };
      ld();
      bar(); bar(); bar();
#pragma nounroll
      for (int spin = 0; spin < (1 << 22); ++spin) {
        if (__all((((a[1] ^ tag) | (a[3] ^ tag)) | ((b[1] ^ tag) | (b[3] ^ tag))) == 0)) break;
        __builtin_amdgcn_s_sleep(1);
        ld();
      }
      float m = __uint_as_float(a[0]) + __uint_as_float(b[0]);
      m = xsum(m + ror8(m)) * (1.f / 16.f);
      const float d0 = __uint_as_float(a[0]) - m, d1 = __uint_as_float(b[0]) - m;
      float q = __uint_as_float(a[2]) + __uint_as_float(b[2]) + 64.f * (d0 * d0 + d1 * d1);
      asm volatile("" : "+v"(m), "+v"(q));
      bar();
      asm volatile("" : "+v"(m), "+v"(q));
      put(xi, m, xsum(q + ror8(q)), lane < 8, lane);
    };
    auto gather = [&](auto H, int xi) {
      constexpr int n = 2 + decltype(H)::value;
      const u32 tag = (u32)tok, f0 = flag(F_LN + xi, tmA, lane >> 3) + (lane & 7) * 8;
      const u32 f[3] = {f0, f0 + 1024, flag(F_LN + xi, tmh, 14 + (r >> 3)) + (lane & 7) * 8};
      u32x2 v[3];
      auto ld = [&]() {
#pragma unroll
        for (int h = 0; h < n; ++h) v[h] = __builtin_bit_cast(u32x2, ldf(f[h]));
      };
      ld();
      bar(); bar(); bar();
#pragma nounroll
      for (int spin = 0; spin < (1 << 22); ++spin) {
        if (__all(((v[0][1] ^ tag) | (v[1][1] ^ tag) | (v[n - 1][1] ^ tag)) == 0)) break;
        __builtin_amdgcn_s_sleep(1);
        ld();
      }
#pragma unroll
      for (int h = 0; h < n; ++h) {
        const float iv = __builtin_amdgcn_rcpf(bf_hi(v[h][0]));
        const int o = h < 2 ? 64 + 256 * h + lane * 4 : r * 4;
        *(float*)(smem + L_INV + o) = iv;
        *(float*)(smem + L_MNV + o) = n == 3 ? bf_lo(v[h][0]) : bf_lo(v[h][0]) * iv;
      }
      wait_lgkm0();
    };
    auto guard = [&](u32 f, int K, auto&& lim_, auto&& step) {
      int b = 0, lim = 0;
#pragma nounroll
      for (int spin = 0; spin < (1 << 22); ++spin) {
        const u64 v = ldf(f);
        if (b < lim) { bar(); step(b++); } else __builtin_amdgcn_s_sleep(1);
        if (b < lim) { bar(); step(b++); }
        lim = lim_((u32)__ballot(v == tok));
        if (lim >= 100) break;
        lim = min(K, lim);
      }
#pragma nounroll
      while (b < K) { bar(); step(b++); }
    };
    auto ahead = [](int msk, int spp, int D) {
      return [=](u32 m) { const int n = __builtin_ctz(~(m & (m >> 16))); return n >= 16 ? 100 : (n & msk) * spp - D; };
    };
    const __amdgpu_buffer_rsrc_t rx = rsrc(x, SEQ * DM * 2), rq = rsrc(wqkv, DM * 3 * DM * 2);
    u32 wx, ww;
    {
      const int n = idx * 64 + lane, m = idx * 48 + (lane < 48 ? lane : 47), k = (m * 5462) >> 16, p = m - 12 * k;
      wx = (n & 511) * 2048 + ((n >> 9) & 1) * 128 + ((idx >> 4) * 1024 + (xcd >> 2) * 512) * 2048;
      ww = k * 6144 + (p >> 2) * 2048 + (p & 3) * 128 + (xcd & 3) * 512;
    }
    {
      const u64 ep = ldf(OFF_EP);
      u16 bv[4];
#pragma unroll
      for (int h = 0; h < 4; ++h) {
        const int t = min(lane + 64 * h, 223), f = t >> 4, rho = t & 15, wn = f / 7, j = f % 7;
        bv[h] = b1[tnA * 224 + (j == 6 ? 96 + 16 * wn + rho : (4 * wn + (j >> 1)) * 32 + 8 * (rho >> 2) + 4 * (j & 1) + (rho & 3))];
      }
      DMA(rsrc(b2, DM * 2), (LDS_AS void*)(smem + L_B2), 4, lane * 4, (tnB & 14) * 128, 0, 0);
      pin2(wx, ww);
      __builtin_amdgcn_s_sleep(XW);
      bf16x8 xf[16];
#pragma unroll
      for (int e = 0; e < 16; ++e)
        xf[e] = __builtin_bit_cast(bf16x8, __builtin_amdgcn_raw_buffer_load_b128(rx, (r >> 1) * 2048 + (r & 1) * 64 + g * 16 + e * 128,
            (tmA * 128 + tnA * 8) * 2048, 0));
      auto probe = [&](int tn) { return flag(F_PR, tmA ^ (4 * (idx & 1)), tn); };
      const u32x4 one4 = {0x3f803f80u, 0x3f803f80u, 0x3f803f80u, 0x3f803f80u};
      const bf16x8 ones = __builtin_bit_cast(bf16x8, one4);
      f32x4 sm[2], sq[2];
      u32 tlb;
      u64 vt;
      for_n<LG0>([&](auto S) {
        constexpr int s = decltype(S)::value;
        bar();
        if constexpr (s == TOKB - 1) {
          __builtin_amdgcn_raw_buffer_store_b32(0, rs, blockIdx.x < NVOTE ? probe(tnA ^ 4) + (lane & 31) * 4 : 0x80000000u, 0, 16);
#pragma unroll
          for (int h = 0; h < 4; ++h) ((float*)(smem + L_BS))[lane + 64 * h] = bf_lo(bv[h]);
          tok = ((u64)(u32)__builtin_amdgcn_readfirstlane((u32)ep) | ((u64)(u32)__builtin_amdgcn_readfirstlane((u32)(ep >> 32)) << 32))
                * 6364136223846793005ull + 1442695040888963407ull;
          *(LDS_AS u32x4*)(smem + L_CNT) = (u32x4){0u, 0u, (u32)tok, (u32)(tok >> 32)};
          wait_lgkm0();
          asm volatile("s_load_dword %0, %1, 0x0" : "=s"(tlb) : "s"(ws + OFF_EP + 128) : "memory");
        }
        if constexpr (s >= PF0 && (s - PF0) % PFS == 0 && (s - PF0) / PFS < 8 - PFJ) {
          const int j = (PFJ + (s - PF0) / PFS + rot0) & 7;
          DMA(rx, (LDS_AS void*)(smem + L_PF), 4, wx, j * 256, 0, 0);
          DMA(rq, (LDS_AS void*)(smem + L_PF), 4, ww, j * (128 * 6144), 0, 0);
        }
        if constexpr (s >= XP && s < XP + 4) {
#pragma unroll
          for (int e = 4 * (s - XP); e < 4 * (s - XP) + 4; ++e) {
            const f32x4 z = {0.f, 0.f, 0.f, 0.f};
            asm volatile("" : "+v"(xf[e]));
            sm[e & 1] = __builtin_amdgcn_mfma_f32_16x16x32_bf16(ones, xf[e], e < 2 ? z : sm[e & 1], 0, 0, 0);
            sq[e & 1] = __builtin_amdgcn_mfma_f32_16x16x32_bf16(xf[e], xf[e], e < 2 ? z : sq[e & 1], 0, 0, 0);
          }
        }
        if constexpr (s >= XP && s <= XP + 4) asm volatile("" : "+v"(sm[0]), "+v"(sm[1]), "+v"(sq[0]), "+v"(sq[1]));
        if constexpr (s == XP + 4) {
          const f32x4 q4 = sq[0] + sq[1];
          float q = (r & 2) ? ((r & 1) ? q4[3] : q4[2]) : ((r & 1) ? q4[1] : q4[0]), t = sm[0][0] + sm[1][0];
          q += __uint_as_float(__builtin_amdgcn_mov_dpp(__float_as_uint(q), 0xb1, 0xf, 0xf, false));
          t += __uint_as_float(__builtin_amdgcn_mov_dpp(__float_as_uint(t), 0xb1, 0xf, 0xf, false));
          const float m = t * (1.f / DM);
          put(0, m, fmaxf(__builtin_fmaf(-t, m, q), 0.f), g == (r >> 2) && !(r & 1), r >> 1);
        }
        if constexpr (s == PRB) {
          wait_lgkm0();
          asm volatile("" :: "s"(tlb));
          if (blockIdx.x < NVOTE) {
            u64 t0, t1;
            u32 v;
            asm volatile("s_memtime %0\n\ts_waitcnt lgkmcnt(0)\n\ts_load_dword %2, %3, 0x0\n\ts_waitcnt lgkmcnt(0)\n\t"
                         "s_memtime %1\n\ts_waitcnt lgkmcnt(0)" : "=&s"(t0), "=&s"(t1), "=&s"(v) : "s"(ws + probe(tnA)) : "memory");
            const u32 t = min((u32)t1 - (u32)t0, 4095u);
            st8(tok ^ t, lane == 0, flag(F_XC, 0, blockIdx.x));
          }
        }
        if constexpr (s == LG0 - 4) vt = ldf(flag(F_XC, 0, r));
      });
      static_assert(XP >= TOKB && PRB > XP + 4 && LG0 - 4 > PRB && LG0 + 3 <= 31 && L_CNT % 16 == 0, "schedule");
#pragma nounroll
      for (int spin = 0; spin < (1 << 22); ++spin) {
        if (__all((vt ^ tok) < 4096)) break;
        __builtin_amdgcn_s_sleep(1);
        vt = ldf(flag(F_XC, 0, r));
      }
      const u32 t = (u32)(vt ^ tok);
      const u64 slower = __ballot(t > (u32)__builtin_amdgcn_mov_dpp(t, 0x128, 0xf, 0xf, false));
      const u32 c = __builtin_popcount((u32)slower & 255) > 4 ? 8192 : 0;
      *(volatile LDS_AS u32*)(smem + L_C13) = c;
      gather(IC<1>{}, 0);
      c13 = c;
      bars(34 - CLB - (LG0 + 3));
    }
#pragma nounroll
    for (int pair = 0; pair < 2; ++pair) {
#pragma unroll
      for (int e = 0; e < 16; ++e)
        st16(((u32x4){~0u, ~0u, ~0u, ~0u}), xbox(pair, 0) + (e + (e & 8)) * 1024 + lane * 16);
      bars(CLB);
      wait_vm<0>();
      guard(flag(4 * pair + 1, tmB, (rotB + r) & 15), 16, ahead(15, 1, DN), [](int) {});
      guard(flag(4 * pair + 2, tmA, (rotA + r) & 15), KW1, ahead(14, 2, DW), [](int) {});
      reduce(2 * pair + 1);
      bars(LG - (KW1 + 4));
      gather(IC<0>{}, 2 * pair + 1);
      bars(33 - (LG + 3));
      const u32 w3 = lane < 56 ? OFF_W2 + ((idx >> 1) * 112 + (tnB & 1) * 56 + (idx & 1) * 14) * 4096 + lane * 1024 + 384 : 0x80000000u;
      if (D2 & 2) {
        if (pair == 0) poll(flag(F_W2, 8 * (tnB & 1) + (r & 7), idx >> 1), tok);
        DMA(rs, (LDS_AS void*)(smem + L_PF), 4, w3, 0, 0, 16);
      }
      guard(flag(lane < 8 ? 4 * pair + 3 : lane < 16 ? F_2ND + pair : F_3RD + pair, tmB, 8 * (tnB & 1) + ((lane < 8 ? r ^ (rotA >> 1) : r) & 7)), 13,
            [](u32 m) { const int n = __builtin_ctz(~m | (1u << 24)); return n >= 24 ? 100 : min(n, 16) - 3; },
            [&](int b) {
              if (b == 0 || b == 3) st8(tok, lane == 0, flag(b ? F_3RD + pair : F_2ND + pair, tmA, tnA));
              if ((D2 & 2) && b == D2B) DMA(rs, (LDS_AS void*)(smem + L_PF), 4, w3, 28 * 4096, 0, 16);
            });
      if (pair) break;
      bars(WQ - 13);
      for_n<29 - WQ>([&](auto S) {
        constexpr int s = decltype(S)::value;
        if constexpr (s < 4) {
          DMA(rq, (LDS_AS void*)(smem + L_PF), 4, ww, ((2 * s + (rotA >> 1)) & 7) * (128 * 6144), 0, 0);
          DMA(rq, (LDS_AS void*)(smem + L_PF), 4, ww, ((2 * s + 1 + (rotA >> 1)) & 7) * (128 * 6144), 0, 0);
        }
        bar();
      });
      guard(flag(4, (g & 1) ? tmh : tmA, (rotA + r) & 15), KW1, ahead(14, 2, DW), [](int) {});
      st8(tok, lane == 0 && blockIdx.x == 0, OFF_EP);
      reduce(2);
      bars(LG - (KW1 + 4));
      gather(IC<1>{}, 2);
      bars(33 - CLB - (LG + 3));
      static_assert(WQ <= 25 && LG + 3 <= 31, "schedule");
    }
    QUIT();
  }
#endif
}

extern "C" void kernel_launch(void* const* d_in, const int* in_sizes, int n_in, void* d_out, int out_size,
                              void* d_ws, size_t ws_size, hipStream_t stream) {
  const u16* x0 = (const u16*)d_in[0];
  const u16* wqkv = (const u16*)d_in[1];
  const u16* wo = (const u16*)d_in[2];
  const u16* w1 = (const u16*)d_in[4];
  const u16* b1 = (const u16*)d_in[5];
  const u16* w2 = (const u16*)d_in[6];
  const u16* b2 = (const u16*)d_in[7];
  (void)hipFuncSetAttribute((const void*)mega_k, hipFuncAttributeMaxDynamicSharedMemorySize, LDS_BYTES);
  mega_k<<<256, 512, LDS_BYTES, stream>>>((char*)d_ws + (ws_size - WS_USED - WS_ALIGN), x0, wqkv, wo, w1, b1, w2, b2, (u16*)d_out);
}
```

```cpp
#include <hip/hip_runtime.h>
#include <stdint.h>
#include <type_traits>
#include <utility>

typedef unsigned short u16;
typedef unsigned int u32;
typedef unsigned long long u64;
typedef __bf16 bf16x8 __attribute__((ext_vector_type(8)));
typedef __bf16 bf16x2 __attribute__((ext_vector_type(2)));
typedef short s16x4 __attribute__((ext_vector_type(4)));
typedef float f32x4 __attribute__((ext_vector_type(4)));
typedef float f32x2 __attribute__((ext_vector_type(2)));
typedef u32 u32x4 __attribute__((ext_vector_type(4)));
typedef u32 u32x2 __attribute__((ext_vector_type(2)));
typedef u32 u32x10 __attribute__((ext_vector_type(10)));
typedef int i32x4 __attribute__((ext_vector_type(4)));
typedef int i32x2 __attribute__((ext_vector_type(2)));
#define LDS_AS __attribute__((address_space(3)))
#define GLB_AS __attribute__((address_space(1)))

constexpr int SEQ = 2048, DM = 1024, FF = 3584;
constexpr float LN_EPS = 1e-5f;

#define DN 4
#define DW 5
#define UAUX 2
#define PFW0 30
#define PF0 4
#define PFS 2
#define PFJ 2
#define CQP 5
#define HL 8
#define FLD 3
#define XW 30
#define TOKB 3
#define XP 8
#define LG0 24
#define LG 25
#define WQ 24
#define G0 27
#define G1 12
#define H2 4
#define PUBJ 3
#define CB 1
#define KW1 11
#define CLB 1
#define SND 12
#define RST 9
#define XB2 1
#define XA2 1
#define XBO 1
#define CW 1
#define PRB 13
#define CQG 22
#define R3W 1
#define LVA 16
#define LVB 6
#define DB 3
#define SNK 31
#define D2 0
#define D2B 6
#define RL2 15
#define RL1 10
#define RLQ 16
#define ROT 7
#ifndef NVOTE
#define NVOTE 16
#endif
#define NP 1
#define ZO 25
#define Z1 25
#define Z2 0
#define ZQ 50
#define TW2 1
#define OA4 0
#define LK 42
#define ZG 0
#define PS 2
constexpr u32 MB = 1u << 20;
constexpr u32 QS = 5120, Q2 = D2 ? 4096 : QS;
constexpr u32 XS = 8 * MB, HS = 32 * MB;
constexpr u32 OFF_WO = 0;
constexpr u32 OFF_W1 = 3 * MB;
constexpr u32 OFF_W2 = 13 * MB;
constexpr u32 OFF_CS1 = 22 * MB;
constexpr u32 OFF_FL = 25 * MB;
constexpr u32 OFF_EP = OFF_FL + 26 * 32768;
constexpr u32 OFF_DBG = 26 * MB;
constexpr u32 OFF_XC = 28 * MB;
constexpr u32 OFF_X = 28 * MB;
constexpr u32 OFF_O = OFF_X + 4 * XS;
constexpr u32 OFF_H = OFF_O + 2 * XS;
constexpr u32 OFF_ST = OFF_H + 2 * HS;
constexpr u32 WS_USED = OFF_ST + 2 * MB, WS_ALIGN = 4 * MB;
#define HAS_ALIGN
constexpr int F_WO = 9, F_W1 = 10, F_W2 = 11, F_2ND = 12, F_3RD = 14, F_XC = 18, F_PR = 19, F_LN = 22;
__device__ __forceinline__ u32 qoff(u32 e, u32 slot) {
  return slot * 16 + (e * 1152 + (e == 3 ? 128 : 0)) + (slot >= (e == 3 ? 56 : 24 - 8 * e) ? 128 : 0);
}
__device__ __forceinline__ u32 hpos(u32 t, u32 j) {
  return (j & 2) == 0 ? 16 * (j & 1) + 2 * (t & 7) + (j >> 2) : 32 + 3 * (t & 7) + (j & 1) + (j >> 2) * 2;
}
__device__ __forceinline__ u32 aq(u32 kg, u32 half) { return kg * 32768 + (kg & 1) * 3072 + half * 16384; }
__device__ __forceinline__ u32 ppair(u32 tm) { return (tm & 3) + (tm >> 3) * 4; }
__device__ __forceinline__ u32 xpan(u32 tm, u32 c13) { return ppair(tm) * MB + (((((tm >> 2) ^ (tm >> 1)) & 1) << 13) ^ c13); }
__device__ __forceinline__ u32 hpan(u32 tm, u32 kh, u32 c13) {
  return ppair(tm) * (4 * MB) + (((kh ^ tm) & 1) << 21) + (((((tm >> 2) ^ kh ^ tm) & 1) << 13) ^ c13);
}
__device__ __forceinline__ u32 span(u32 tm, u32 c13) { return ppair(tm) * 65536 + (((tm & 4) << 11) ^ c13); }

constexpr int SLOT = 24576, NRING = 6, RING = SLOT * NRING;
constexpr int L_INV = RING, L_MNV = L_INV + 576, L_CNT = L_MNV + 576, L_TOK = L_CNT + 8, L_C13 = L_TOK + 8;
constexpr int L_CSQ = RING + 1280, L_CS1 = L_CSQ + 1024, L_BS = L_CS1 + 1024;
constexpr int L_XC = L_BS + 1024;
constexpr int L_IM = L_XC + 6144;
constexpr int L_B2 = L_IM + 4096, L_PF = L_B2 + 256;
constexpr int LDS_BYTES = L_PF + 256;

__device__ __forceinline__ float bf_lo(u32 w) { return __uint_as_float(w << 16); }
__device__ __forceinline__ float bf_hi(u32 w) { return __uint_as_float(w & 0xffff0000u); }
__device__ __forceinline__ u32 pack2(float lo, float hi) {
  f32x2 v = {lo, hi};
  bf16x2 r = __builtin_convertvector(v, bf16x2);
  return __builtin_bit_cast(u32, r);
}
__device__ __forceinline__ float bfr(float x) { return bf_lo(pack2(x, 0.f) & 0xffffu); }
__device__ __forceinline__ u32x4 gelu_tanh8(const f32x2 (&x)[4]) {
  const float c0 = -2.3022081983f, c1 = -0.1029432396f;
  f32x2 t[4];
#define STEP(e) do { _Pragma("unroll") for (int k = 0; k < 4; ++k) t[k] = e; __builtin_amdgcn_sched_barrier(0); } while (0)
  STEP(x[k] * x[k]);
  STEP(t[k] * c1 + c0);
  STEP(x[k] * t[k]);
  STEP(((f32x2){__builtin_amdgcn_exp2f(t[k][0]), __builtin_amdgcn_exp2f(t[k][1])}));
  STEP(t[k] + 1.f);
  STEP(((f32x2){__builtin_amdgcn_rcpf(t[k][0]), __builtin_amdgcn_rcpf(t[k][1])}));
  STEP(x[k] * t[k]);
#undef STEP
  return (u32x4){pack2(t[0][0], t[0][1]), pack2(t[1][0], t[1][1]), pack2(t[2][0], t[2][1]), pack2(t[3][0], t[3][1])};
}
template <int N_> __device__ __forceinline__ void wait_vm() { asm volatile("s_waitcnt vmcnt(%0)" ::"n"(N_) : "memory"); }
__device__ __forceinline__ void wait_lgkm0() { asm volatile("s_waitcnt lgkmcnt(0)" ::: "memory"); }
__device__ __forceinline__ void bar() {
  __builtin_amdgcn_sched_barrier(0);
  asm volatile("s_barrier" ::: "memory");
  __builtin_amdgcn_sched_barrier(0);
}
template <int V> using IC = std::integral_constant<int, V>;
template <typename F, int... I>
__device__ __forceinline__ void for_seq(F&& f, std::integer_sequence<int, I...>) { (f(IC<I>{}), ...); }
template <int N, typename F> __device__ __forceinline__ void for_n(F&& f) { for_seq(f, std::make_integer_sequence<int, N>{}); }

__device__ __forceinline__ float xsum(float x) {
#if defined(__HIP_DEVICE_COMPILE__)
  auto a = __builtin_amdgcn_permlane16_swap(__float_as_uint(x), __float_as_uint(x), false, false);
  x = __uint_as_float(a[0]) + __uint_as_float(a[1]);
  auto b = __builtin_amdgcn_permlane32_swap(__float_as_uint(x), __float_as_uint(x), false, false);
  x = __uint_as_float(b[0]) + __uint_as_float(b[1]);
#endif
  return x;
}
__device__ __forceinline__ float xmax(float x) {
#if defined(__HIP_DEVICE_COMPILE__)
  auto a = __builtin_amdgcn_permlane16_swap(__float_as_uint(x), __float_as_uint(x), false, false);
  x = fmaxf(__uint_as_float(a[0]), __uint_as_float(a[1]));
  auto b = __builtin_amdgcn_permlane32_swap(__float_as_uint(x), __float_as_uint(x), false, false);
  x = fmaxf(__uint_as_float(b[0]), __uint_as_float(b[1]));
#endif
  return x;
}
#define st16(v, off) __builtin_amdgcn_raw_buffer_store_b128(__builtin_bit_cast(i32x4, v), rs, (off), 0, 16)
__device__ __forceinline__ float sum8(const u32x4& f) {
  float s = bf_lo(f[0]) + bf_hi(f[0]);
#pragma unroll
  for (int k = 1; k < 4; ++k) { s += bf_lo(f[k]); s += bf_hi(f[k]); }
  return s;
}
__device__ __forceinline__ float fold32(float a, float b) {
#if defined(__HIP_DEVICE_COMPILE__)
  const auto x = __builtin_amdgcn_permlane32_swap(__float_as_uint(a), __float_as_uint(b), false, false);
  a = __uint_as_float(x[0]) + __uint_as_float(x[1]);
#endif
  return a;
}
__device__ __forceinline__ float fold16(float a, float b) {
#if defined(__HIP_DEVICE_COMPILE__)
  const auto x = __builtin_amdgcn_permlane16_swap(__float_as_uint(a), __float_as_uint(b), false, false);
  a = __uint_as_float(x[0]) + __uint_as_float(x[1]);
#endif
  return a;
}

__device__ __forceinline__ float row_parts(const u32x4 (&c)[2][2]) {
  float res = 0.f;
#if defined(__HIP_DEVICE_COMPILE__)
#define SB_() __builtin_amdgcn_sched_barrier(0)
  f32x2 v[2][8], t[2][4];
  float m[2], q[2];
#pragma unroll
  for (int k = 0; k < 16; ++k) v[k >> 3][k & 7] = (f32x2){bf_lo(c[k >> 3][(k >> 2) & 1][k & 3]), bf_hi(c[k >> 3][(k >> 2) & 1][k & 3])};
  SB_();
#pragma unroll
  for (int k = 0; k < 8; ++k) t[k >> 2][k & 3] = v[k >> 2][k & 3] + v[k >> 2][4 + (k & 3)];
  SB_();
#pragma unroll
  for (int k = 0; k < 4; ++k) t[k >> 1][k & 1] += t[k >> 1][2 + (k & 1)];
  SB_();
  t[0][0] += t[0][1]; t[1][0] += t[1][1];
  SB_();
  m[0] = t[0][0][0] + t[0][0][1]; m[1] = t[1][0][0] + t[1][0][1];
  SB_();
  const f32x2 mm = (f32x2){m[0], m[1]} * (1.f / 16.f);
  SB_();
#pragma unroll
  for (int k = 0; k < 16; ++k) v[k >> 3][k & 7] -= mm[k >> 3];
  const auto x = __builtin_amdgcn_permlane16_swap(__float_as_uint(mm[0]), __float_as_uint(mm[1]), false, false);
  SB_();
#pragma unroll
  for (int k = 0; k < 8; ++k) t[k >> 2][k & 3] = v[k >> 2][k & 3] * v[k >> 2][k & 3];
  const float d = __uint_as_float(x[0]) - __uint_as_float(x[1]);
  float h = __uint_as_float(x[0]) + __uint_as_float(x[1]), h2 = h;
  asm volatile("" : "+v"(h2));
  SB_();
#pragma unroll
  for (int k = 0; k < 8; ++k) t[k >> 2][k & 3] = v[k >> 2][4 + (k & 3)] * v[k >> 2][4 + (k & 3)] + t[k >> 2][k & 3];
  const auto y = __builtin_amdgcn_permlane32_swap(__float_as_uint(h), __float_as_uint(h2), false, false);
  SB_();
#pragma unroll
  for (int k = 0; k < 4; ++k) t[k >> 1][k & 1] += t[k >> 1][2 + (k & 1)];
  const float e = __uint_as_float(y[0]) - __uint_as_float(y[1]), d8 = 8.f * d;
  SB_();
  t[0][0] += t[0][1]; t[1][0] += t[1][1];
  const float mean = (__uint_as_float(y[0]) + __uint_as_float(y[1])) * 0.125f, e2 = e + e;
  SB_();
  q[0] = t[0][0][0] + t[0][0][1]; q[1] = t[1][0][0] + t[1][0][1];
  const float w = __builtin_fmaf(d8, d, e2 * e);
  SB_();
  const auto z = __builtin_amdgcn_permlane16_swap(__float_as_uint(q[0]), __float_as_uint(q[1]), false, false);
  res = fold32(mean, __uint_as_float(z[0]) + __uint_as_float(z[1]) + w);
#undef SB_
#endif
  return res;
}

__device__ __forceinline__ int fresh_lane(int tid) { int l = tid & 63; asm volatile("" : "+v"(l)); return l; }
__device__ __forceinline__ f32x4 zero4() {
  f32x4 z = {0.f, 0.f, 0.f, 0.f};
  asm volatile("" : "+v"(z));
  return z;
}
template <int BIG = 0> __device__ __forceinline__ void next_slot(int& so) {
  if constexpr (BIG) so = (so + 32768) & 131071; else { so += SLOT; if (so == RING) so = 0; }
}

#define DMA __builtin_amdgcn_raw_ptr_buffer_load_lds
#define SB() __builtin_amdgcn_sched_barrier(0)
__device__ __forceinline__ void mfma(f32x4& c, const bf16x8& a, const bf16x8& b) {
  asm volatile("v_mfma_f32_16x16x32_bf16 %0, %1, %2, %0" : "+v"(c) : "v"(a), "v"(b));
}
template <int OFF> __device__ __forceinline__ void dsrd(bf16x8& d, u32 addr) {
  asm volatile("ds_read_b128 %0, %1 offset:%2" : "=v"(d) : "v"(addr), "n"(OFF));
}
template <int OFF> __device__ __forceinline__ void dstr(bf16x8& d, u32 addr) {
  u32x2 lo, hi;
  asm volatile("ds_read_b64_tr_b16 %0, %2 offset:%3\n\tds_read_b64_tr_b16 %1, %2 offset:%4"
               : "=&v"(lo), "=&v"(hi) : "v"(addr), "n"(OFF), "n"(OFF + 512));
  const u32x4 v = {lo[0], lo[1], hi[0], hi[1]};
  d = __builtin_bit_cast(bf16x8, v);
}
template <int OFF> __device__ __forceinline__ void dstr1(u32x2& d, u32 addr) {
  asm volatile("ds_read_b64_tr_b16 %0, %1 offset:%2" : "=v"(d) : "v"(addr), "n"(OFF));
}
template <int OFF> __device__ __forceinline__ void dsrd1(float& d, u32 addr) {
  asm volatile("ds_read_b32 %0, %1 offset:%2" : "=v"(d) : "v"(addr), "n"(OFF));
}
template <int O0, int O1> __device__ __forceinline__ void dsrd2(f32x2& d, u32 addr) {
  asm volatile("ds_read2_b32 %0, %1 offset0:%2 offset1:%3" : "=v"(d) : "v"(addr), "n"(O0), "n"(O1));
}
template <int OFF> __device__ __forceinline__ void dsrd4(f32x4& d, u32 addr) {
  asm volatile("ds_read_b128 %0, %1 offset:%2" : "=v"(d) : "v"(addr), "n"(OFF));
}
template <class T> __device__ __forceinline__ void pin(T& a) { asm volatile("" : "+v"(a)); }
template <class T, class... R> __device__ __forceinline__ void pin(T& a, R&... r) { pin(a); pin(r...); }
template <class F> constexpr u64 lanes(F f) {
  u64 m = 0;
  for (int l = 0; l < 64; ++l) m |= (u64)(f(l & 15, l >> 4) ? 1 : 0) << l;
  return m;
}
constexpr u64 M_PAR = lanes([](int r, int) { return r & 1; });
constexpr u64 M_OWN0 = lanes([](int r, int g) { return 4 * g + (r & 1) <= r; });
constexpr u64 M_OWN1 = lanes([](int r, int g) { return 4 * g + (r & 1) + 2 <= r; });
#define LANE_IN(m) __builtin_amdgcn_inverse_ballot_w64(m)
__device__ __forceinline__ void pin1(u32& a) { asm volatile("" : "+v"(a)); }
__device__ __forceinline__ void pin2(u32& a, u32& b) { asm volatile("" : "+v"(a), "+v"(b)); }
constexpr int rd_at(int m, int n, int last) {
  for (int q = 0; q < n; ++q) if (q * last / (n - 1) == m) return q;
  return -1;
}
template <int NSL, int NM, int NR, int ENDB, class AD, class RD, class MM, class HK, class TL>
__device__ __forceinline__ void kloop(int& so, AD&& ad, RD&& rd, MM&& mm, HK&& hook, TL&& tail) {
  constexpr int BIG = NM == 32;
  ad(so, IC<0>{});
  bar();
  for_n<NR>([&](auto Q) { rd(IC<0>{}, Q); });
  next_slot<BIG>(so);
  ad(so, IC<1>{});
  hook(-1);
  auto body = [&](auto S) {
    constexpr int s = decltype(S)::value;
    wait_lgkm0();
    bar();
    for_n<NM>([&](auto M) {
      constexpr int m = decltype(M)::value;
      mm(S, M);
      constexpr int RL = BIG ? RL2 : NR - 1, q = rd_at(m, NR, RL);
      if constexpr (q >= 0) rd(IC<1 - s>{}, IC<q>{});
      if constexpr (m == RL + 1) { next_slot<BIG>(so); asm volatile("" : "+s"(so)); }
      if constexpr (m == RL + 2) ad(so, S);
      SB();
    });
  };
#pragma nounroll
  for (int u = NSL / 2 - 1; u > 0; --u) {
    body(IC<0>{});
    body(IC<1>{});
    hook(NSL / 2 - 1 - u);
  }
  if constexpr (BIG && TW2) {
    wait_lgkm0();
    bar();
    for_n<16>([&](auto M) {
      constexpr int m = decltype(M)::value;
      mm(IC<2>{}, M);
      rd(IC<1>{}, IC<(m < 6 ? m + 2 : m < 12 ? m + 4 : m < 14 ? m - 12 : m - 6)>{});
      SB();
    });
    asm volatile("s_waitcnt lgkmcnt(10)" ::: "memory");
    SB();
    for_n<48>([&](auto T) {
      constexpr int t = decltype(T)::value;
      if constexpr (t == 8) { wait_lgkm0(); bar(); so = 0; }
      mm(IC<(t >> 4 == 1 ? 2 : 3)>{}, IC<(t < 32 ? t : t - 16)>{});
      SB(); tail(T); SB();
    });
  } else {
    body(IC<0>{});
    wait_lgkm0();
    if constexpr (ENDB) { bar(); so = 0; }
    SB();
    for_n<NM>([&](auto M) { mm(IC<3>{}, M); SB(); tail(M); SB(); });
  }
  asm volatile("s_nop 15");
  SB();
}

template <int NM, int NR, int ENDB, class RD, class MM, class HK, class TL>
__device__ __forceinline__ void kloop6(RD&& rd, MM&& mm, HK&& hook, TL&& tail) {
  bar();
  for_n<NR>([&](auto Q) { rd(IC<0>{}, Q); });
  hook(-1);
  auto body = [&](auto S) {
    constexpr int s = decltype(S)::value;
    wait_lgkm0();
    bar();
    for_n<NM>([&](auto M) {
      constexpr int m = decltype(M)::value;
      mm(IC<s & 1>{}, M);
      constexpr int q = rd_at(m, NR, NR == 11 ? RL1 : RLQ);
      if constexpr (q >= 0) rd(IC<(s + 1) % NRING>{}, IC<q>{});
      SB();
    });
  };
#pragma nounroll
  for (int u = 5; u > 0; --u) for_n<NRING>(body);
  body(IC<0>{});
  wait_lgkm0();
  if constexpr (ENDB) bar();
  SB();
  for_n<NM>([&](auto M) { mm(IC<1>{}, M); SB(); tail(M); SB(); });
  asm volatile("s_nop 15");
  SB();
}

struct Slot { u32 k, a0, l0; };
template <int NSL, int P, int D, int X, class PR, class FI, class BR>
__device__ __forceinline__ void stream(int& so, int u0, PR&& prep, FI&& fire, BR&& bar) {
#pragma nounroll
  for (int u = u0; u < D + X; ++u) {
    const Slot a = prep(u);
    if constexpr ((D + X) * P > 63) wait_vm<63 - P>();
    for_n<P>([&](auto I) { fire(I, a); });
  }
  auto next = [&](int u) {
    Slot a = prep(u);
    asm volatile("" : "+s"(a.k), "+s"(a.a0), "+s"(a.l0), "+s"(so));
    return a;
  };
  auto head = [&](const Slot& a) {
    wait_vm<((D - 1) * P < 59 ? (D - 1) * P : 59)>();
    bar();
    for_n<4>([&](auto I) { fire(I, a); });
    SB();
  };
  auto rest = [&](const Slot& a) {
    if constexpr (D * P > 63) wait_vm<67 - P>();
    for_n<P - 4>([&](auto I) { fire(IC<4 + decltype(I)::value>{}, a); });
  };
  if constexpr ((ROT >> (NSL == 28 ? 0 : NSL == 16 ? 1 : 2)) & 1) {
    Slot a = next(X + D);
    if constexpr (X) { wait_vm<(D * P < 63 ? D * P : 63)>(); bar(); }
    head(a);
#pragma nounroll
    for (int u = X + D + 1; u < NSL; ++u) {
      const Slot b = next(u);
      rest(a);
      head(b);
      a = b;
    }
    rest(a);
  } else {
    if constexpr (X) { wait_vm<(D * P < 63 ? D * P : 63)>(); bar(); }
#pragma nounroll
    for (int u = X + D; u < NSL; ++u) {
      const Slot a = next(u);
      head(a);
      rest(a);
    }
  }
  for_n<D>([&](auto M) { constexpr int n = (D - 1 - decltype(M)::value) * P; wait_vm<(n < 63 ? n : 63)>(); bar(); });
}

#define STAMP(w, i)
#define TS(v)
#define TSW(v, i)
#define TSP 1

constexpr int w1_frag(int i, int e) {
  const int gam = 2 * (i - 2) + (e >> 1), gl = gam % 7;
  return (gam / 7) * 16 + (gl == 3 ? 6 + 7 * (e & 1) : (gl < 3 ? gl * 2 : 7 + (gl - 4) * 2) + (e & 1));
}
__device__ __forceinline__ void warm_code(u32 n, u32 lds) {
#if defined(__HIP_DEVICE_COMPILE__)
  u64 pc;
  u32 back, len;
  asm volatile("s_getpc_b64 %0\n.Lcw%=:\n\ts_mov_b32 %1, .Lcw%=-mega_k\n\ts_mov_b32 %2, .Lcode_end-mega_k" : "=s"(pc), "=s"(back), "=s"(len));
  pc -= back;
  const u32x4 rc = {(u32)pc, (u32)(pc >> 32), len, 0x00020000u};
  asm volatile("s_mov_b32 %0, m0\n\ts_mov_b32 m0, %1\n\ts_nop 0\n\tbuffer_load_dword %2, %3, 0 offen lds\n\ts_mov_b32 m0, %0"
               : "=&s"(back) : "s"(lds), "v"(n * 128), "s"(rc));
#endif
}

template <int N> __device__ __forceinline__ void announce(u32 fl, __amdgpu_buffer_rsrc_t rs) {
  u32x2 tk;
  u32 a, o, f, n;
  asm volatile("s_mov_b64 exec, 1\n\tv_mov_b32 %1, %7\n\tds_read_b64 %0, %1 offset:8\n\tv_mov_b32 %2, 1\n\tv_mov_b32 %3, %5\n\t"
               "s_waitcnt vmcnt(%8)\n\tds_add_rtn_u32 %1, %1, %2\n\ts_waitcnt lgkmcnt(0)\n\t"
               "v_readfirstlane_b32 %4, %1\n\ts_and_b32 %4, %4, 3\n\ts_cmp_eq_u32 %4, 3\n\ts_cbranch_scc0 .Lan%=\n\t"
               "buffer_store_dwordx2 %0, %3, %6, 0 offen sc1\n.Lan%=:\n\ts_mov_b64 exec, -1"
               : "=&v"(tk), "=&v"(a), "=&v"(o), "=&v"(f), "=&s"(n)
               : "s"(fl), "s"(rs), "n"(L_CNT), "n"(N) : "memory", "scc");
  static_assert(L_TOK == L_CNT + 8, "layout");
}

extern "C" __global__ __launch_bounds__(512, 2) void mega_k(char* __restrict__ ws0, const u16* __restrict__ x,
                                                 const u16* __restrict__ wqkv, const u16* __restrict__ wo,
                                                 const u16* __restrict__ w1, const u16* __restrict__ b1,
                                                 const u16* __restrict__ w2, const u16* __restrict__ b2,
                                                 u16* __restrict__ out) {
#if defined(__HIP_DEVICE_COMPILE__)
  extern __shared__ __attribute__((aligned(16))) char smem[];
  const int tid = threadIdx.x;
  int wid = __builtin_amdgcn_readfirstlane(tid >> 6);
#define FRESH_LANE() const int lane = fresh_lane(tid), r = lane & 15, g = lane >> 4; (void)r; (void)g
  const int xcd = blockIdx.x & 7, idx = blockIdx.x >> 3;
  asm volatile(".subsection 1\n.Lcode_end:\n\t.subsection 0");
  if (CW & 1) warm_code(((tid & 63) * 8 + wid) * 32 + idx, L_PF);
  SB();
  char* const ws = ws0 + (-(u32)(uintptr_t)ws0 & (WS_ALIGN - 1));
  const int tmA = (idx >> 4) * 8 + (xcd >> 2) * 4 + ((idx >> 2) & 3), tnA = (xcd & 3) * 4 + (idx & 3);
  const int tmB = (idx >> 4) * 8 + (xcd >> 1) * 2 + ((idx >> 3) & 1), tnB = (idx & 7) * 2 + (xcd & 1);
  const int rotA = 8 - 8 * (xcd & 1), rotB = 4 * ((xcd + 1) & 3), rot0 = xcd;
  const __amdgpu_buffer_rsrc_t rs = __builtin_amdgcn_make_buffer_rsrc((void*)ws, 0, WS_USED, 0x00020000);
  u32 c13 = 0;
  auto side = [&]() { c13 = __builtin_amdgcn_readfirstlane(*(const volatile LDS_AS u32*)(smem + L_C13)); };
  auto flag = [&](int set, int tm, int tn) { return OFF_FL + ((((set * 16 + tm) * 16 + tn) * 128) ^ c13); };
  auto xbox = [&](int pair, int o) { return OFF_XC + pair * (4 * MB) + ((idx * 4 + (xcd & 3)) ^ o) * 32768 + (((((tmB >> 2) ^ (idx >> 4)) & 1) << 13) ^ c13); };
  auto rsrc = [&](const u16* p, u32 bytes) { return __builtin_amdgcn_make_buffer_rsrc((void*)p, 0, bytes, 0x00020000); };
  auto token = [&]() {
    const u32x2 tk = *(const volatile LDS_AS u32x2*)(smem + L_TOK);
    return (u64)(u32)__builtin_amdgcn_readfirstlane(tk[0]) | ((u64)(u32)__builtin_amdgcn_readfirstlane(tk[1]) << 32);
  };
  auto ldf = [&](u32 f) {
    asm volatile("" ::: "memory");
    return __builtin_bit_cast(u64, __builtin_amdgcn_raw_buffer_load_b64(rs, f, 0, 16));
  };
  auto poll = [&](u32 f, u64 tok) {
#pragma nounroll
    for (int spin = 0; spin < (1 << 22); ++spin) {
      if (__all(ldf(f) == tok)) break;
      __builtin_amdgcn_s_sleep(1);
    }
    asm volatile("" ::: "memory");
  };
  auto poll_n = [&](u32 f, u64 tok, u64 (&v)[NP]) {
    auto look = [&](u64& d) { asm volatile("buffer_load_dwordx2 %0, %1, %2, 0 offen sc1" : "=v"(d) : "v"(f), "s"(rs) : "memory"); };
#pragma unroll
    for (int i = 0; i < NP; ++i) { if (i) __builtin_amdgcn_s_sleep(PS); look(v[i]); }
#pragma nounroll
    for (int spin = 0; spin < (1 << 21); ++spin) {
      bool up = false;
#pragma unroll
      for (int i = 0; i < NP; ++i) {
        asm volatile("s_waitcnt vmcnt(%1)" : "+v"(v[i]) : "n"(NP - 1) : "memory");
        up = __all(v[i] == tok);
        if (up) break;
        look(v[i]);
      }
      if (up) break;
    }
    asm volatile("" ::: "memory");
  };
  auto st8 = [&](u64 v, bool on, u32 f) {
    __builtin_amdgcn_raw_buffer_store_b64(__builtin_bit_cast(i32x2, v), rs, on ? f : 0x80000000u, 0, 16);
  };
  int so = 0;
#define QUIT() __builtin_amdgcn_endpgm()

  auto x_lanes = [&](u32 (&vo)[4]) {
    FRESH_LANE();
#pragma unroll
    for (int e = 0; e < 4; ++e)
      vo[e] = (8 * e + (lane >> 3)) * 2048 + (((lane & 7) ^ (4 * (e & 1) + (lane >> 4))) * 16) - e * 1024;
  };
  auto x_half = [&](auto H, int U, const u32 (&vo)[4]) {
    constexpr int h = decltype(H)::value;
    const __amdgpu_buffer_rsrc_t rx = rsrc(x, SEQ * DM * 2);
    const u32 sx = tmA * 128 * 2048, sh = (tmA > 0 ? tmA * 128 - 16 : 0) * 2048;
    U = (U + 2 * rot0) & 15;
    for_n<8>([&](auto E) {
      constexpr int e = decltype(E)::value;
      DMA(rx, (LDS_AS void*)(smem + so + h * SLOT + (e >> 2) * 4096), 16, vo[e & 3],
          sx + (h * 64 + (e >> 2) * 32) * 2048 + U * 128, (e & 3) * 1024, 0);
    });
    DMA(rx, (LDS_AS void*)(smem + so + SLOT + 21504), 16, vo[h], sh + U * 128, h * 1024, 0);
    if constexpr (h == 1) { so += 2 * SLOT; if (so == RING) so = 0; }
  };
  auto q_lanes = [&](u32 (&vo)[4]) {
    FRESH_LANE();
#pragma unroll
    for (int e = 0; e < 4; ++e)
      vo[e] = (8 * e + (lane >> 3)) * 6144 + (((lane & 7) ^ (2 * ((lane >> 4) & 1) + 4 * (e & 1))) * 16) - e * 1024;
  };
  if (wid == 6) {
    FRESH_LANE();
    DMA(rsrc(wqkv, DM * 3 * DM * 2), (LDS_AS void*)(smem + L_PF), 4,
        lane < 24 && !(xcd & 1) ? ((rot0 * 128 + idx * 8 + (lane & 7)) & 1023) * 6144 + (lane >> 3) * 2048 : 0x80000000u,
        (xcd & 3) * 512 + 384, 0, 0);
  }
  asm volatile("" : "+s"(wid));
  if (wid == 5) {
    u32 vo[4];
    q_lanes(vo);
#pragma nounroll
    for (int u = 0; u < 5; ++u)
      for_n<12>([&](auto I) {
        constexpr int i = decltype(I)::value;
        DMA(rsrc(wqkv, DM * 3 * DM * 2), (LDS_AS void*)(smem + u * SLOT + 9216 + (i >> 2) * 4096), 16, vo[i & 3],
            ((u + 4 * rot0) & 31) * (32 * 6144) + (i >> 2) * 2048 + tnA * 128, (i & 3) * 1024, 0);
      });
  }
  asm volatile("" : "+s"(wid));
  if (wid == 4) {
    u32 vo[4];
    x_lanes(vo);
#pragma nounroll
    for (int U = 0; U < 3; ++U) { x_half(IC<0>{}, U, vo); x_half(IC<1>{}, U, vo); }
  }
  so = 0;
  asm volatile("" : "+s"(wid));

  if (wid < 4) {
    auto publish = [&](auto N, int ph, int tm, int tn) {
      SB();
      announce<decltype(N)::value>(flag(ph + 1, tm, tn), rs);
      SB();
    };
    const int wmi = wid >> 1, wni = wid & 1;

    const int w = 2 * (tnB & 1) + wmi, tn = (tnB & 14) + wni;
    u32x4 c[2][2];
    auto start = [&](f32x4 (&acc)[4][4], const u32x4 (&bb)[2], bool bias) {
#pragma unroll
      for (int i = 0; i < 2; ++i)
#pragma unroll
        for (int j = 0; j < 4; ++j) {
          const u32 c0 = c[i][j >> 1][2 * (j & 1)], c1 = c[i][j >> 1][2 * (j & 1) + 1];
          const u32 b0 = bb[j >> 1][2 * (j & 1)], b1 = bb[j >> 1][2 * (j & 1) + 1];
          f32x4 a = {bf_lo(c0), bf_hi(c0), bf_lo(c1), bf_hi(c1)};
          if (bias) a += (f32x4){bf_lo(b0), bf_hi(b0), bf_lo(b1), bf_hi(b1)};
          asm volatile("" : "+v"(a));
          acc[i][j] = a;
        }
    };
    u32 fo[4];
    auto dest = [&](auto E, int xo) {
      constexpr int e = decltype(E)::value, i = e >> 1, gl = e & 1;
      FRESH_LANE();
      const u32 a = OFF_X + xo * XS + xpan(tmB, c13) + tn * 65536 + aq(gl, w >> 1) + qoff((w & 1) * 2 + i, lane);
      const u32 b = (tmB * 128 + (2 * w + i) * 16 + r) * 2048 + tn * 128 + gl * 64 + g * 16;
      fo[e] = xo < 4 ? a : b;
      pin(fo[e]);
    };
    auto finish = [&](const f32x4 (&acc)[4][4], int xo, int ph) {
      FRESH_LANE();
      const __amdgpu_buffer_rsrc_t rd = __builtin_amdgcn_make_buffer_rsrc(xo < 4 ? (void*)ws : (void*)out, 0, xo < 4 ? WS_USED : SEQ * DM * 2, 0x00020000);
#pragma unroll
      for (int i = 0; i < 2; ++i)
#pragma unroll
        for (int gl = 0; gl < 2; ++gl) {
#pragma unroll
          for (int k = 0; k < 4; ++k) {
            const f32x4& a = acc[i][2 * gl + (k >> 1)];
            c[i][gl][k] = pack2(a[2 * (k & 1)], a[2 * (k & 1) + 1]);
          }
          __builtin_amdgcn_raw_buffer_store_b128(__builtin_bit_cast(i32x4, c[i][gl]), rd, fo[2 * i + gl], 0, 16);
        }
      if (xo < 4) {
        SB();
        TS(ta);
        const u32 tag = *(const volatile LDS_AS u32*)(smem + L_TOK);
        __builtin_amdgcn_raw_buffer_store_b64((i32x2){(int)__float_as_uint(row_parts(c)), (int)tag}, rs,
            OFF_ST + xo * 524288 + span(tmB, c13) + w * 16384 + tn * 128 + (lane & 24) * 256 + (lane & 7) * 16 + (g >> 1) * 8, 0, 16);
        SB();
        TS(tb);
        publish(IC<1>{}, ph, tmB, tnB);
        TS(tc);
      }
      STAMP(0, 4 * ph + 3);
    };

    auto level = [&](int n) {
      f32x4 d[8];
      bf16x8 a[2], b[4];
      u32 p = fresh_lane(tid) * 16;
      pin(p);
      dsrd<0>(a[0], p); dsrd<1024>(a[1], p); dsrd<9216>(b[0], p); dsrd<10240>(b[1], p); dsrd<11264>(b[2], p); dsrd<12288>(b[3], p);
#pragma unroll
      for (int i = 0; i < 8; ++i) d[i] = zero4();
      wait_lgkm0();
#pragma nounroll
      for (int t = 0; t < n; ++t)
#pragma unroll
        for (int i = 0; i < 8; ++i) mfma(d[i], b[i & 3], a[i >> 2]);
      asm volatile("s_nop 15\n\ts_nop 15" ::: "memory");
      pin(d[0], d[1], d[2], d[3], d[4], d[5], d[6], d[7]);
    };

#pragma nounroll
    for (int pair = 0; pair < 2; ++pair) {
      f32x4 aco[4][4];
      {
        f32x4 acc[4][6], acch[2];
        f32x4 muv[4], muh, cs[4];
        f32x2 mu2[2], ivk[2][2], fk[2][2], csv;
        u32 sel[4], oa[4];
        {
        FRESH_LANE();
        STAMP(0, 16 * pair);
        const u32 lB = 9216 + (8 * g + (r >> 2)) * 128 + (((2 * wni) ^ ((r >> 3) & 1) ^ ((g & 1) << 1)) * 32) + (r & 3) * 8;
        const u32 lA = r * 128 + ((g ^ (r >> 1)) * 16);
        auto loop = [&](auto WM, auto RAW) {
          constexpr int wm = decltype(WM)::value, raw = decltype(RAW)::value;
#pragma unroll
          for (int i = 0; i < 4; ++i)
#pragma unroll
            for (int j = 0; j < 6; ++j) acc[i][j] = zero4();
          acch[0] = zero4(); acch[1] = zero4();
          bf16x8 fA[2][4], fH[2];
          u32x2 hB[2][12];
          u32 pA[3][2], pB[3][2], pH[3][2];
          f32x4 accq;
          if (raw) { accq = zero4(); *(u32*)(smem + L_XC + lane * 4) = 0x3f803f80u; }
#pragma unroll
          for (int k = 0; k < 3; ++k) {
            pA[k][0] = (raw ? lA : lane * 16) + 2 * k * SLOT; pA[k][1] = (lA ^ 64) + 2 * k * SLOT;
            pB[k][0] = lB + 2 * k * SLOT; pB[k][1] = (lB ^ 32) + 2 * k * SLOT;
            pin1(pA[k][0]); pin2(pB[k][0], pB[k][1]);
            if (raw) {
              pH[k][0] = r ? pA[k][0] : L_XC - (SLOT + 21504) + lA; pH[k][1] = r ? pA[k][1] : L_XC - (SLOT + 21504) + (lA ^ 64);
              pin1(pA[k][1]); pin2(pH[k][0], pH[k][1]);
            }
          }
          int ln;
          u32 aR, aG, aK[2];
          f32x2 ivq[2];
          float mh, cv;
          float fq[2][4];
          auto fB = [&](int set, int j) {
            return __builtin_bit_cast(bf16x8, (u32x4){hB[set][2 * j][0], hB[set][2 * j][1], hB[set][2 * j + 1][0], hB[set][2 * j + 1][1]});
          };
          kloop6<26 + raw, 17, 0>(
            [&](auto SL, auto Q) {
              constexpr int set = decltype(SL)::value & 1, k = decltype(SL)::value >> 1, q = decltype(Q)::value, h = q - 5;
              if constexpr (q < 4) dsrd<raw ? wm * SLOT + q * 2048 : set * SLOT + wm * 4096 + q * 1024>(fA[set][q], pA[k][raw & set]);
              else if constexpr (q == 4) dsrd<raw ? SLOT + 21504 : set * SLOT + 8192>(fH[set], raw ? pH[k][set] : pA[k][0]);
              else dstr1<set * SLOT + (h >> 2) * 4096 + (h & 1) * 512>(hB[set][h], pB[k][(h >> 1) & 1]);
            },
            [&](auto SET, auto M) {
              constexpr int set = decltype(SET)::value, m = decltype(M)::value, j = m >> 2, i = (SNK & 2) && (j & 1) ? 3 - (m & 3) : m & 3;
              if constexpr (m < 16) mfma(acc[i][j], fB(set, j), fA[set][i]);
              else if constexpr (m < 24) mfma(acc[i][j], fA[set][i], fB(set, j));
              else if constexpr (m == 26) mfma(accq, fB(set, wm), fH[set]);
              else if constexpr (wm == 0) mfma(acch[i], fB(set, 2 + i), fH[set]);
              else mfma(acch[i], fH[set], fB(set, 4 + i));
            }, [&](int t) { if (t < 0) STAMP(0, 16 * pair + 1); },
            [&](auto M) {
              constexpr int m = decltype(M)::value;
              if constexpr (m == 0) {
                ln = fresh_lane(tid);
                aR = L_INV + wm * 256 + (ln & 15) * 4; aG = L_INV + wm * 256 + (ln & 48);
                pin(aR, aG);
              }
              if constexpr (m == 1) { dsrd2<16, 32>(ivq[0], aR); dsrd2<48, 64>(ivq[1], aR); dsrd4<640 + 192>(muv[3], aG); }
              if constexpr (m == 2) { aK[0] = aG + (ln & 1) * 4; aK[1] = aK[0] + 64; pin(aK[0], aK[1]); }
              if constexpr (m == 3) {
                const u32 a0 = LANE_IN(M_OWN0) ? aK[1] : aK[0], a1 = LANE_IN(M_OWN1) ? aK[1] : aK[0];
                aK[0] = a0; aK[1] = a1;
                pin(aK[0], aK[1]);
              }
              if constexpr (m == 4) { dsrd2<0, 16>(ivk[0][0], aK[0]); dsrd2<32, 48>(ivk[0][1], aK[0]); dsrd2<2, 18>(ivk[1][0], aK[1]); }
              if constexpr (m == 5) { dsrd2<34, 50>(ivk[1][1], aK[1]); dsrd2<160, 176>(mu2[0], aR); dsrd2<192, 208>(mu2[1], aR); }
              if constexpr (m == 6) { dsrd4<640>(muv[0], aG); dsrd4<640 + 64>(muv[1], aG); dsrd4<640 + 128>(muv[2], aG); }
              if constexpr (m == 7) {
                if constexpr (wm) dsrd4<576 - 256>(muh, aG); else dsrd1<576>(mh, aR);
                aK[0] = L_C13;
                pin(aK[0]);
                dsrd1<0>(cv, aK[0]);
              }
              if constexpr (m == 8) { aR += L_CSQ - L_INV - wm * 256 + wni * 384; aG += L_CSQ - L_INV - wm * 256 + wni * 384; pin(aR, aG); }
              if constexpr (!raw) {
                if constexpr (m == 9) { dsrd4<128>(cs[2], aG); dsrd4<192>(cs[3], aG); dsrd2<64, 80>(csv, aR); }
                if constexpr (m == 10) { dsrd4<0>(cs[0], aG); dsrd4<64>(cs[1], aG); }
              }
              if constexpr (m == 11) { sel[0] = LANE_IN(M_PAR) ? 0x01000c0cu : 0x0c0c0100u; pin(sel[0]); }
              if constexpr (m == 12) { sel[1] = LANE_IN(M_PAR) ? 0x03020c0cu : 0x0c0c0302u; pin(sel[1]); }
              if constexpr (m == 13) {
                sel[2] = LANE_IN(M_OWN0) ? sel[0] : 0x0c0c0c0cu; sel[3] = LANE_IN(M_OWN1) ? sel[1] : 0x0c0c0c0cu;
                pin(sel[2], sel[3]);
              }
              if constexpr (m == 14) {
                sel[0] = LANE_IN(M_OWN0) ? 0x0c0c0c0cu : sel[0]; sel[1] = LANE_IN(M_OWN1) ? 0x0c0c0c0cu : sel[1];
                pin(sel[0], sel[1]);
              }
              if constexpr (m == 16) {
                wait_lgkm0();
                pin(ivq[0], ivq[1], muv[0], muv[1], muv[2], muv[3], ivk[0][0], ivk[0][1], ivk[1][0], ivk[1][1]);
                pin(mu2[0], mu2[1], cv);
                if constexpr (wm) pin(muh); else { pin(mh); muh = (f32x4){mh, mh, mh, mh}; }
                if constexpr (!raw) pin(cs[0], cs[1], cs[2], cs[3], csv);
              }
              if constexpr (m >= 17 && m < 21) {
                constexpr int i = m - 17;
                float cq = ivq[i >> 1][i & 1] * (0.17677669529663687f * 1.4426950408889634f);
                pin(cq);
                fq[0][i] = ivk[0][i >> 1][i & 1] * cq; fq[1][i] = ivk[1][i >> 1][i & 1] * cq;
                pin(fq[0][i], fq[1][i]);
                if constexpr (i & 1) { fk[0][i >> 1] = (f32x2){fq[0][i - 1], fq[0][i]}; fk[1][i >> 1] = (f32x2){fq[1][i - 1], fq[1][i]}; }
              }
              if constexpr (m == 21) {
                oa[0] = ((OFF_O + pair * XS + xpan(tmA, 0) + tnA * 65536 + aq(wni, wm)) ^ __float_as_uint(cv)) + ln * 16;
                oa[1] = oa[0] + 128;
                pin(oa[0], oa[1]);
              }
              if constexpr (m == 23 && OA4) {
                oa[2] = LANE_IN(~0ull << 8) ? oa[1] : oa[0]; oa[3] = LANE_IN(~0ull << 56) ? oa[1] : oa[0];
                pin(oa[2], oa[3]);
              }
              if constexpr (m == 24 && OA4) {
                const u32 a0 = LANE_IN(~0ull << 24) ? oa[1] : oa[0], a1 = LANE_IN(~0ull << 16) ? oa[1] : oa[0];
                oa[0] = a0; oa[1] = a1;
                pin(oa[0], oa[1]);
              }
              if constexpr (m == 22) c13 = __builtin_amdgcn_readfirstlane(__float_as_uint(cv));
            });
          if constexpr (raw) {
            FRESH_LANE();
            if (LANE_IN(0x0001000100010001ull)) {
              *(f32x4*)(smem + aG + wm * 64) = accq;
              if (wm == 0) { *(f32x4*)(smem + aG + 128) = acch[0]; *(f32x4*)(smem + aG + 192) = acch[1]; }
            }
            if (wm == 1 && LANE_IN(0xffffull)) { *(float*)(smem + aR + 256) = acch[0][0]; *(float*)(smem + aR + 320) = acch[1][0]; }
            wait_lgkm0();
            bar();
            dsrd4<128>(cs[2], aG); dsrd4<192>(cs[3], aG); dsrd2<64, 80>(csv, aR); dsrd4<0>(cs[0], aG); dsrd4<64>(cs[1], aG);
#pragma unroll
            for (int i = 0; i < 2; ++i)
#pragma unroll
              for (int gl = 0; gl < 2; ++gl)
                c[i][gl] = __builtin_bit_cast(u32x4, __builtin_amdgcn_raw_buffer_load_b128(rsrc(x, SEQ * DM * 2),
                    (16 * i + r) * 2048 + g * 16, (tmB * 128 + w * 32) * 2048 + tn * 128 + gl * 64, 0));
            wait_lgkm0();
            pin(cs[0], cs[1], cs[2], cs[3], csv);
          }
        };
        if (pair == 0) { if (wmi == 0) loop(IC<0>{}, IC<1>{}); else loop(IC<1>{}, IC<1>{}); }
        else { if (wmi == 0) loop(IC<0>{}, IC<0>{}); else loop(IC<1>{}, IC<0>{}); }
        }
        FRESH_LANE();
        STAMP(0, 16 * pair + 2);
        auto fix_qk = [&](const f32x4& a0, const f32x4& a1, float mu, int f) {
          const f32x4 y0 = cs[f] * mu + a0, y1 = cs[f + 1] * mu + a1;
          u32x4 v = {pack2(y0[0], y0[1]), pack2(y0[2], y0[3]), pack2(y1[0], y1[1]), pack2(y1[2], y1[3])};
          pin(v);
          return v;
        };
        auto fix_v = [&](const f32x4& a, const f32x4& mu, float cw) {
          const f32x4 y = mu * cw + a;
          u32x2 v = {pack2(y[0], y[1]), pack2(y[2], y[3])};
          pin(v);
          return v;
        };
        u32x4 Qf[4], Kf[5];
        u32x2 Vf[5][2];
        char* xc = smem + L_XC + wni * 3072;
        Kf[4] = fix_qk(acc[3][2], acc[3][3], mu2[1][1], 2);
        Vf[4][0] = fix_v(acc[3][4], muv[3], csv[0]); Vf[4][1] = fix_v(acc[3][5], muv[3], csv[1]);
        if (wmi == 0) {
          *(u32x4*)(xc + lane * 16) = Kf[4];
          *(u32x2*)(xc + 1024 + lane * 8) = Vf[4][0]; *(u32x2*)(xc + 1536 + lane * 8) = Vf[4][1];
          Kf[0] = fix_qk(acch[0], acch[1], muh[0], 2);
        } else {
          asm volatile("" : "=v"(Kf[0]));
          f32x2 cl = csv;
          pin(cl);
          const f32x4 y0 = muh * cl[0] + acch[0], y1 = muh * cl[1] + acch[1];
          SB();
          *(u32x2*)(xc + 2048 + lane * 8) = (u32x2){pack2(y0[0], y0[1]), pack2(y0[2], y0[3])};
          *(u32x2*)(xc + 2560 + lane * 8) = (u32x2){pack2(y1[0], y1[1]), pack2(y1[2], y1[3])};
        }
        pin(mu2[0], mu2[1]);
#pragma unroll
        for (int i = 0; i < 3; ++i) Kf[i + 1] = fix_qk(acc[i][2], acc[i][3], mu2[i >> 1][i & 1], 2);
        wait_lgkm0();
        bar();
        TS(td);
        if (wmi == 1) Kf[0] = *(const u32x4*)(xc + lane * 16);
        Vf[0][0] = *(const u32x2*)(xc + (wmi == 0 ? 2048 : 1024) + lane * 8);
        Vf[0][1] = *(const u32x2*)(xc + (wmi == 0 ? 2560 : 1536) + lane * 8);
        const f32x4 z = {0.f, 0.f, 0.f, 0.f};
        f32x4 S[4][2];
        pin(mu2[0], mu2[1], csv);
        auto qk = [&](int i, int k) {
          SB();
          S[i][k] = __builtin_amdgcn_mfma_f32_16x16x32_bf16(__builtin_bit_cast(bf16x8, Kf[i + k]), __builtin_bit_cast(bf16x8, Qf[i]), z, 0, 0, 0);
          SB();
        };
#pragma unroll
        for (int i = 3; i >= 0; --i) {
          f32x4 y0 = cs[0] * mu2[i >> 1][i & 1] + acc[i][0], y1 = cs[1] * mu2[i >> 1][i & 1] + acc[i][1];
          SB();
          Qf[i] = (u32x4){pack2(y0[0], y0[1]), pack2(y0[2], y0[3]), pack2(y1[0], y1[1]), pack2(y1[2], y1[3])};
          pin(Qf[i]);
          SB();
        }
        f32x2 yv[4];
        auto fill = [&](auto K0, auto N) {
          for_n<decltype(N)::value>([&](auto K) {
            constexpr int k = decltype(K0)::value + decltype(K)::value, i = k >> 3, h = (k >> 1) & 1, c = k & 1;
            SB();
            if constexpr (k & 4) { Vf[i + 1][h][c] = pack2(yv[k & 3][0], yv[k & 3][1]); }
            else {
              yv[k & 3] = (f32x2){muv[i][2 * c], muv[i][2 * c + 1]} * csv[h] + (f32x2){acc[i][4 + h][2 * c], acc[i][4 + h][2 * c + 1]};
            }
            if constexpr (k > 8 && k < 12) { SB(); pin(yv[(k - 1) & 3]); }
          });
          SB();
        };
        const bool par = LANE_IN(M_PAR), own0 = LANE_IN(M_OWN0), own1 = LANE_IN(M_OWN1);
        f32x2 l[2][2];
        {
          auto pick = [](bool c, float a, float b) { return c ? a : b; };
          float sp[4][4];
          auto half = [&](int i) {
#pragma unroll
            for (int k = 0; k < 4; ++k) sp[i][k] = pick(par, S[i][k >> 1][2 * (k & 1) + 1], S[i][k >> 1][2 * (k & 1)]);
            SB();
          };
          auto tile = [&](int i) {
            l[0][i >> 1][i & 1] = pick(own0, sp[i][2], sp[i][0]);
            l[1][i >> 1][i & 1] = pick(own1, sp[i][3], sp[i][1]);
            SB();
          };
          qk(3, 1); qk(3, 0); qk(2, 1); qk(2, 0); qk(1, 1); qk(1, 0); qk(0, 1); qk(0, 0);
          half(3); half(2); half(1); tile(3); tile(2); half(0); tile(1); tile(0);
        }
#pragma unroll
        for (int i = 3; i >= 0; --i) { l[i & 1][i >> 1] *= fk[i & 1][i >> 1]; SB(); }
        f32x2 t[2];
        t[1] = __builtin_elementwise_maximum(l[0][1], l[1][1]);
        SB();
        {
          const u64 first = tmA == 0 && wmi == 0 ? ~0ull : 0ull;
          if (LANE_IN(first & ~M_OWN1)) l[1][0][0] = -1.4426950e9f;
          if (LANE_IN(first & ~M_OWN0)) l[0][0][0] = -1.4426950e9f;
        }
        SB();
        t[0][1] = __builtin_elementwise_maximum(l[0][0][1], l[1][0][1]);
        SB();
        t[0][0] = __builtin_elementwise_maximum(l[0][0][0], l[1][0][0]);
        auto all4 = [&](auto K0, f32x2 (&v)[2], auto f, auto mid, auto&& more) {
          constexpr int k0 = decltype(K0)::value;
          auto U = [](float a) { return __float_as_uint(a); };
          auto F = [](u32 a) { return __uint_as_float(a); };
          fill(IC<k0>{}, IC<1>{});
          const auto b = __builtin_amdgcn_permlane32_swap(U(v[0][1]), U(v[1][1]), false, false);
          SB();
          const auto a = __builtin_amdgcn_permlane32_swap(U(v[0][0]), U(v[1][0]), false, false);
          fill(IC<k0 + 1>{}, IC<1>{});
          const float fb = f(F(b[0]), F(b[1]));
          SB();
          const float fa = f(F(a[0]), F(a[1]));
          fill(IC<k0 + 2>{}, IC<2>{});
          const auto c = __builtin_amdgcn_permlane16_swap(U(fa), U(fb), false, false);
          fill(IC<k0 + 4>{}, IC<1>{});
          float t0 = f(F(c[0]), F(c[1])), t1 = f(F(c[1]), F(c[0]));
          pin(t0, t1);
          more(IC<0>{});
          t0 = mid(t0); t1 = mid(t1);
          fill(IC<k0 + 5>{}, IC<2>{});
          const auto d = __builtin_amdgcn_permlane16_swap(U(t0), U(t1), false, false);
          fill(IC<k0 + 7>{}, IC<1>{});
          u32 d0 = d[0], d1 = d[1], d2 = d[0], d3 = d[1];
          pin(d2, d3);
          fill(IC<k0 + 8>{}, IC<2>{});
          const auto e0 = __builtin_amdgcn_permlane32_swap(d0, d2, false, false);
          const auto e1 = __builtin_amdgcn_permlane32_swap(d1, d3, false, false);
          fill(IC<k0 + 10>{}, IC<2>{});
          v[0] = (f32x2){F(e0[0]), F(e1[0])}; v[1] = (f32x2){F(e0[1]), F(e1[1])};
        };
        all4(IC<0>{}, t, [](float a, float b) { return __builtin_elementwise_maximum(a, b); }, [](float a) { return a; }, [](auto) {});
#pragma unroll
        for (int i = 0; i < 4; ++i) { l[i & 1][i >> 1] -= t[i >> 1]; SB(); if (i == 0) pin(yv[3]); }
#pragma unroll
        for (int i = 0; i < 4; ++i) {
          l[i & 1][i >> 1] = (f32x2){__builtin_amdgcn_exp2f(l[i & 1][i >> 1][0]), __builtin_amdgcn_exp2f(l[i & 1][i >> 1][1])};
          SB();
        }
        t[0] = l[0][0] + l[1][0];
        l[0][0] *= ivk[0][0];
        SB();
        t[1] = l[0][1] + l[1][1];
        l[1][0] *= ivk[1][0];
        all4(IC<12>{}, t, [](float a, float b) { return a + b; }, [](float a) { return __builtin_amdgcn_rcpf(a); },
             [&](auto) { SB(); l[0][1] *= ivk[0][1]; l[1][1] *= ivk[1][1]; SB(); });
#pragma unroll
        for (int i = 0; i < 4; ++i) { l[i & 1][i >> 1] *= t[i >> 1]; SB(); }
        u32 pw[4];
#pragma unroll
        for (int i = 0; i < 4; ++i) pw[i] = pack2(l[0][i >> 1][i & 1], l[1][i >> 1][i & 1]);
        u32x10 V[2];
#pragma unroll
        for (int i = 0; i < 10; ++i) { V[0][i] = Vf[i >> 1][0][i & 1]; V[1][i] = Vf[i >> 1][1][i & 1]; }
        pin(V[0], V[1]);
        u32x4 P[4];
        f32x4 O[4][2];
        for_n<7>([&](auto I) {
          constexpr int i = decltype(I)::value, m = i - 1, e = i - 3;
          u32x4 o;
          auto pv = [&](auto H) {
            constexpr int h = decltype(H)::value;
            SB();
            if constexpr (i < 4) {
              P[i][2 * h] = __builtin_amdgcn_perm(pw[i], pw[i], sel[2 * h]);
              P[i][2 * h + 1] = __builtin_amdgcn_perm(pw[i], pw[i], sel[2 * h + 1]);
            }
            if constexpr (e >= 0) { o[2 * h] = pack2(O[e][h][0], O[e][h][1]); o[2 * h + 1] = pack2(O[e][h][2], O[e][h][3]); }
            SB();
            if constexpr (m >= 0 && m < 4)
              O[m][h] = __builtin_amdgcn_mfma_f32_16x16x32_bf16(__builtin_bit_cast(bf16x8, __builtin_shufflevector(V[h], V[h], 2 * m, 2 * m + 1, 2 * m + 2, 2 * m + 3)),
                                                                __builtin_bit_cast(bf16x8, P[m]), z, 0, 0, 0);
            SB();
          };
          pv(IC<0>{}); pv(IC<1>{});
          if constexpr (e >= 0) st16(o, (OA4 ? oa[e] : LANE_IN(~0ull << (e == 3 ? 56 : 24 - 8 * e)) ? oa[1] : oa[0]) + qoff(e, 0));
        });
        SB();
        TS(te);
        start(aco, c[0], false);
        publish(IC<0>{}, 4 * pair, tmA, tnA);
        if (pair == 0) level(LVA);
        TSW(td, 35); TSW(te, 34);
        STAMP(0, 16 * pair + 3);
      }
      {
        FRESH_LANE();
        bf16x8 fA[2][2][2], fB[2][2][4];
        u32 pA, pB;
        so = 2 * SLOT;
        for_n<4>([&](auto E) { dest(E, 2 * pair + 1); });
        kloop<16, 16, 12, 0>(so,
          [&](int s_, auto) { pA = lane * 16 + wmi * 2048 + s_; pB = lane * 16 + wni * 4096 + s_; pin2(pA, pB); },
          [&](auto SET, auto Q) {
            constexpr int set = decltype(SET)::value, q = decltype(Q)::value, s = q / 6, m = q % 6;
            if constexpr (m < 2) dsrd<s * 4096 + m * 1024>(fA[set][s][m], pA);
            else dsrd<8192 + s * 8192 + (m - 2) * 1024>(fB[set][s][m - 2], pB);
          },
          [&](auto SET, auto M) {
            constexpr int set = decltype(SET)::value & 1, m = decltype(M)::value;
            constexpr int s = (SNK & 4) ? (m ^ (m >> 1)) & 1 : m >> 3, j = (SNK & 4) ? m >> 2 : (m >> 1) & 3, i = (SNK & 4) ? ((m >> 1) ^ (m >> 2)) & 1 : m & 1;
            mfma(aco[i][j], fB[set][s][j], fA[set][s][i]);
          },
          [&](int t) { if (t < 0) STAMP(0, 16 * pair + 5); }, [](auto) {});
        STAMP(0, 16 * pair + 6);
        finish(aco, 2 * pair + 1, 4 * pair + 1);
        if (pair == 0) level(LVB);
      }
      {
        f32x4 acc[4][7];
        f32x2 iv[2], mv[2];
        f32x4 tb[2][4];
        u32 aT, ho[4];
        auto tables = [&](auto J) {
          constexpr int j = decltype(J)::value;
          dsrd4<128 * j>(tb[j & 1][0], aT); dsrd4<128 * j + L_BS - L_CS1>(tb[j & 1][1], aT);
          if constexpr (j < 3) { dsrd4<128 * j + 64>(tb[j & 1][2], aT); dsrd4<128 * j + 64 + L_BS - L_CS1>(tb[j & 1][3], aT); }
        };
        {
        FRESH_LANE();
#pragma unroll
        for (int i = 0; i < 4; ++i)
#pragma unroll
          for (int j = 0; j < 7; ++j) acc[i][j] = zero4();
        bf16x8 fA[2][4], fB[2][7];
        u32 pA[3], pB[3];
#pragma unroll
        for (int k = 0; k < 3; ++k) {
          pA[k] = lane * 16 + wmi * 4096 + 2 * k * SLOT; pB[k] = lane * 16 + wni * 7168 + 2 * k * SLOT;
          pin2(pA[k], pB[k]);
        }
        kloop6<28, 11, 1>(
          [&](auto SL, auto Q) {
            constexpr int set = decltype(SL)::value & 1, k = decltype(SL)::value >> 1, q = decltype(Q)::value;
            if constexpr (q < 4) dsrd<set * SLOT + q * 1024>(fA[set][q], pA[k]);
            else dsrd<set * SLOT + 8192 + (q - 4) * 1024>(fB[set][q - 4], pB[k]);
          },
          [&](auto SET, auto M) {
            constexpr int set = decltype(SET)::value, m = decltype(M)::value, j0 = m >> 2, i0 = (SNK & 1) && (j0 & 1) ? 3 - (m & 3) : m & 3;
            constexpr int i = i0, j = j0;
            mfma(acc[i][j], fB[set][j], fA[set][i]);
          }, [&](int t) { if (t < 0) STAMP(0, 16 * pair + 9); },
          [&](auto M) {
            constexpr int m = decltype(M)::value;
            if constexpr (m == 0) { aT = L_INV + 64 + wmi * 256 + (fresh_lane(tid) & 15) * 4; pin(aT); }
            if constexpr (m == 1) { dsrd2<0, 16>(iv[0], aT); dsrd2<32, 48>(iv[1], aT); dsrd2<144, 160>(mv[0], aT); dsrd2<176, 192>(mv[1], aT); }
            if constexpr (m == 2) { aT = L_CS1 + wni * 448 + (fresh_lane(tid) & 48); pin(aT); }
            if constexpr (m == 3) tables(IC<0>{});
            if constexpr (m >= 4 && m < 12 && m % 2 == 0) { ho[m / 2 - 2] = qoff(m / 2 - 2, fresh_lane(tid)); pin(ho[m / 2 - 2]); }
          });
        }
        FRESH_LANE();
        STAMP(0, 16 * pair + 10);
        const u32 hbase = OFF_H + pair * HS + hpan(tmA, tnA >> 3, c13);
        auto act = [&](const f32x4& a0, const f32x4& a1, const f32x4& cs0, const f32x4& bs0, const f32x4& cs1, const f32x4& bs1, int i0, int i1) {
          f32x4 y0 = cs0 * mv[i0 >> 1][i0 & 1] + bs0, y1 = cs1 * mv[i1 >> 1][i1 & 1] + bs1;
          SB();
          y0 = a0 * iv[i0 >> 1][i0 & 1] + y0; y1 = a1 * iv[i1 >> 1][i1 & 1] + y1;
          SB();
          const f32x2 y[4] = {{y0[0], y0[1]}, {y0[2], y0[3]}, {y1[0], y1[1]}, {y1[2], y1[3]}};
          return gelu_tanh8(y);
        };
        for_n<3>([&](auto JP) {
          constexpr int jp = decltype(JP)::value;
          wait_lgkm0();
          f32x4 (&t)[4] = tb[jp & 1];
          pin(t[0], t[1], t[2], t[3]);
          if constexpr (jp == 0) pin(iv[0], iv[1], mv[0], mv[1]);
          tables(IC<jp + 1>{});
          u32 hb = hbase + aq(hpos(tnA, wni * 4 + jp), wmi);
          asm volatile("" : "+s"(hb));
#pragma unroll
          for (int i = 0; i < 4; ++i) {
            u32 a = hb + ho[i];
            pin(a);
            const u32x4 v = act(acc[i][2 * jp], acc[i][2 * jp + 1], t[0], t[1], t[2], t[3], i, i);
            st16(v, a);
            SB();
            if (4 * jp + i == 3 + PUBJ) publish(IC<PUBJ>{}, 4 * pair + 2, tmA, tnA);
          }
        });
        {
          wait_lgkm0();
          pin(tb[1][0], tb[1][1]);
#pragma unroll
          for (int i = 0; i < 4; i += 2) {
            u32 a = hbase + aq(hpos(tnA, 3), wmi) + qoff(i + (g & 1), (2 * wni + (g >> 1)) * 16 + r);
            pin(a);
            const u32x4 u = act(acc[i][6], acc[i + 1][6], tb[1][0], tb[1][1], tb[1][0], tb[1][1], i, i + 1);
            const auto s0 = __builtin_amdgcn_permlane16_swap(u[0], u[2], false, false);
            const auto s1 = __builtin_amdgcn_permlane16_swap(u[1], u[3], false, false);
            const u32x4 v = {s0[0], s1[0], s0[1], s1[1]};
            st16(v, a);
            SB();
          }
        }
        STAMP(0, 16 * pair + 11);
      }
      {
        FRESH_LANE();
        f32x4 acc[4][4];
        SB();
        const u32x4 bb[2] = {*(const u32x4*)(smem + L_B2 + wni * 128 + g * 16), *(const u32x4*)(smem + L_B2 + wni * 128 + 64 + g * 16)};
#pragma unroll
        for (int i = 2; i < 4; ++i)
#pragma unroll
          for (int j = 0; j < 4; ++j) acc[i][j] = zero4();
        start(acc, bb, true);
        bf16x8 fA[2][2][4], fB[2][2][4];
        u32 pA, pB;
        const u32 xc = wmi * 16384 + wni * 4096;
        wait_vm<6>();
        so = 0;
        u32x4 xv[4];
        auto look = [&]() {
          asm volatile("buffer_load_dwordx4 %0, %4, %5, %6 offen sc1\n\tbuffer_load_dwordx4 %1, %4, %5, %6 offen offset:1024 sc1\n\t"
                       "buffer_load_dwordx4 %2, %4, %5, %6 offen offset:2048 sc1\n\tbuffer_load_dwordx4 %3, %4, %5, %6 offen offset:3072 sc1"
                       : "=&v"(xv[0]), "=&v"(xv[1]), "=&v"(xv[2]), "=&v"(xv[3]) : "v"(lane * 16), "s"(rs), "s"(xbox(pair, 1) + xc) : "memory");
        };
        kloop<28, 32, 16, 1>(so,
          [&](int s_, auto) { pA = lane * 16 + wmi * 2048 + s_; pB = lane * 16 + wni * 4096 + s_; pin2(pA, pB); },
          [&](auto SET, auto Q) {
            constexpr int set = decltype(SET)::value, q = decltype(Q)::value, s = q >> 3, m = q & 7;
            if constexpr (m < 4) dsrd<s * 8192 + (m >> 1) * 4096 + (m & 1) * 1024>(fA[set][s][m], pA);
            else dsrd<16384 + s * 8192 + (m - 4) * 1024>(fB[set][s][m - 4], pB);
          },
          [&](auto SET, auto M) {
            constexpr int set = decltype(SET)::value & 1, m = decltype(M)::value, o = decltype(SET)::value > 1 ? 0 : SNK >> 3;
            constexpr int s = o == 0 ? (m >> 3) & 1 : o == 1 ? m >> 4 : o == 2 ? m & 1 : (m ^ (m >> 1)) & 1;
            constexpr int j = o == 0 ? (m >> 1) & 3 : o == 1 ? (m >> 2) & 3 : m >> 3;
            constexpr int i = o == 0 ? (m & 1) + (m < 16 ? 2 : 0) : o == 1 ? ((m & 4) ? 3 - (m & 3) : m & 3) : o == 3 && (m & 8) ? 3 - ((m >> 1) & 3) : (m >> 1) & 3;
            mfma(acc[i][j], fB[set][s][j], fA[set][s][i]);
          },
          [&](int t) { if (t < 0) { STAMP(0, 16 * pair + 13); } else wait_vm<0>(); },
          [&](auto M) {
            constexpr int k = decltype(M)::value - SND - 1;
            if constexpr (k >= 0 && k < 8 && k % 2 == 0) {
              const f32x4 a = acc[2][k >> 1], b = acc[3][k >> 1];
              const u32x4 v = {pack2(a[0], a[1]), pack2(a[2], a[3]), pack2(b[0], b[1]), pack2(b[2], b[3])};
              st16(__builtin_elementwise_min(v, (u32x4){~1u, ~1u, ~1u, ~1u}), xbox(pair, 0) + xc + lane * 16 + k * 512);
            }
            if constexpr (decltype(M)::value == (TW2 ? LK : 31)) look();
          });
        STAMP(0, 16 * pair + 14);
        for_n<4>([&](auto E) { dest(E, pair == 0 ? 2 : 4); });
#pragma nounroll
        for (int spin = 0; spin < (1 << 22); ++spin) {
          asm volatile("s_waitcnt vmcnt(0)" : "+v"(xv[0]), "+v"(xv[1]), "+v"(xv[2]), "+v"(xv[3]) :: "memory");
          const u32x4 o = __builtin_elementwise_max(__builtin_elementwise_max(xv[0], xv[1]), __builtin_elementwise_max(xv[2], xv[3]));
          if (!__any(max(max(o[0], o[1]), max(o[2], o[3])) == ~0u)) break;
          look();
        }
#pragma unroll
        for (int k = 0; k < 4; ++k) {
          acc[0][k] += (f32x4){bf_lo(xv[k][0]), bf_hi(xv[k][0]), bf_lo(xv[k][1]), bf_hi(xv[k][1])};
          acc[1][k] += (f32x4){bf_lo(xv[k][2]), bf_hi(xv[k][2]), bf_lo(xv[k][3]), bf_hi(xv[k][3])};
        }
        finish(acc, pair == 0 ? 2 : 4, 4 * pair + 3);
      }
    }
  } else if (wid == 4) {
    {
      u32 vo[4];
      x_lanes(vo);
      wait_vm<36>();
      bar();
      bar();
#pragma nounroll
      for (int U = 3; U < 16; ++U) {
        wait_vm<18>();
        bar();
        x_half(IC<0>{}, U, vo);
        bar();
        x_half(IC<1>{}, U, vo);
      }
      wait_vm<18>(); bar(); bar();
      wait_vm<0>(); bar(); bar(); bar();
    }
    auto phase = [&](auto NSL_, auto HALO_, u32 src, u32 hsrc, int ph  , int tm, int tmh) {
      constexpr int NSL = decltype(NSL_)::value, HALO = decltype(HALO_)::value;
      constexpr int BIG = NSL == 28, PA = BIG ? 16 : 8, PT = PA + HALO, D = BIG ? DB : NSL == 16 ? DN : DW, X = BIG ? XA2 : 0;
      FRESH_LANE();
      u32 vo[4];
#pragma unroll
      for (int e = 0; e < 4; ++e) vo[e] = qoff(e, lane) - e * 1024;
      u64 pv[NP];
      pin(vo[0], vo[1], vo[2], vo[3]);
      asm volatile("" : "+s"(src));
      { constexpr int z = BIG ? Z2 : NSL == 16 ? ZO : HALO ? ZQ : Z1; if constexpr (z > 0) __builtin_amdgcn_s_sleep(z); }
      poll_n(flag(ph + 1, (lane & 16) ? tmh : tm, BIG ? 8 * (tnB & 1) + ((rotA >> 1) + (r < D + X ? r : 0))
                                                 : NSL == 16 ? (rotB + (r < DN ? r : 0)) & 15 : rotA + (r & (DW > 4 ? 3 : 1))), token(), pv);
      STAMP(1, 4 * (ph + 1));
      const u32 flip = BIG ? (tnB & 1) * 16384 : 0;
      stream<NSL, PT, D, X>(so, 0,
        [&](int u) {
          u = BIG ? (u < 8 ? u ^ (rotA >> 1) : u) : NSL == 16 ? (u + rotB) & 15 : u ^ (2 * rotA);
          const u32 ko = aq(NSL == 32 ? u : 2 * u, 0), l = so;
          next_slot<BIG>(so);
          return Slot{ko, src + ko + flip, l};
        },
        [&](auto E, const Slot& a) {
          constexpr int e = decltype(E)::value;
          if constexpr (e == PA) DMA(rs, (LDS_AS void*)(a.l0 + 8192), 16, vo[3], hsrc + a.k, 0, 16);
          else DMA(rs, (LDS_AS void*)(a.l0 + (e >> 2) * 4096), 16, vo[e & 3],
                   e < 4 ? a.a0 : src + a.k + (NSL == 16 ? aq(e >> 2, 0) : aq(e >> 3, (e >> 2) & 1) + ((e & 4) ? -flip : flip)),
                   (e & 3) * 1024, 16);
        }, [&]() { bar(); });
#pragma unroll
      for (int i = 0; i < NP; ++i) asm volatile("" :: "v"(pv[i]));
    };
#pragma nounroll
    for (int pair = 0; pair < 2; ++pair) {
      const int tmh = tmA > 0 ? tmA - 1 : 0;
      const u32 xq = OFF_X + 2 * pair * XS;
      if (pair) phase(IC<32>{}, IC<1>{}, xq + xpan(tmA, c13), xq + xpan(tmh, c13) + 16384 + 3072, 3, tmA, tmh);
      side();
      bar();
      phase(IC<16>{}, IC<0>{}, OFF_O + pair * XS + xpan(tmB, c13) + aq(0, tnB & 1), 0, 4 * pair, tmB, tmB);
      phase(IC<32>{}, IC<0>{}, xq + XS + xpan(tmA, c13), 0, 4 * pair + 1, tmA, tmA);
      bar();
      so = 0;
      phase(IC<28>{}, IC<0>{}, OFF_H + pair * HS + hpan(tmB, tnB & 1, c13), 0, 4 * pair + 2, tmB, tmB);
      bar();
      so = 0;
    }
  } else if (wid == 5) {
    FRESH_LANE();
    auto phase = [&](auto NSL_, auto PB_, auto AUX_, int u0, int add, int rot, __amdgpu_buffer_rsrc_t rb, const u32 (&vo)[4], u32 boff,
                     u32 src, u32 step, auto&& q) {
      constexpr int NSL = decltype(NSL_)::value, PB = decltype(PB_)::value;
      stream<NSL, PB, (NSL == 28 ? DB : 5), (NSL == 28 ? XB2 : NSL == 16 ? XBO : 0)>(so, u0,
        [&](int u) {
          u = NSL == 28 ? (u < 8 ? u ^ rot : u) : ((u + add) & (NSL - 1)) ^ rot;
          const u32 l = so + boff;
          next_slot<NSL == 28>(so);
          return Slot{0, src + u * step, l};
        },
        [&](auto I, const Slot& a) {
          constexpr int i = decltype(I)::value;
          DMA(rb, (LDS_AS void*)(a.l0 + (i >> 2) * 4096), 16, vo[i & 3], a.a0 + q(i >> 2), (i & 3) * 1024, decltype(AUX_)::value);
        }, [&]() { bar(); });
    };
    auto tiles = [&](auto NSL_, auto PB_, u32 boff, u32 src) {
      constexpr int NSL = decltype(NSL_)::value, PB = decltype(PB_)::value;
      u32 vo[4];
#pragma unroll
      for (int e = 0; e < 4; ++e) vo[e] = NSL == 28 && D2 ? lane * 16 : qoff(e, lane) - e * 1024;
      constexpr u32 Q = NSL == 28 ? Q2 : QS;
      phase(NSL_, PB_, IC<16>{}, 0, NSL == 16 ? rotB : 0, NSL == 28 ? rotA >> 1 : NSL == 16 ? 0 : 2 * rotA, rs, vo, boff, src,
            PB == 16 ? 2 * Q : 4 * Q, [](int i) { return PB == 16 ? (i >> 1) * Q + (i & 1) * ((NSL == 28 ? 112 : 32) * Q) : i * Q; });
    };
    so = 5 * SLOT;
#pragma nounroll
    for (int pair = 0; pair < 2; ++pair) {
      {
        u32 vo[4];
        q_lanes(vo);
        phase(IC<32>{}, IC<12>{}, IC<0>{}, pair ? 0 : 5, pair ? 0 : 4 * rot0, pair ? 2 * rotA : 0, rsrc(wqkv, DM * 3 * DM * 2), vo, 9216,
              tnA * 128, 32 * 6144, [](int i) { return i * 2048; });
      }
      if (pair == 0) bar();
      bar();
      tiles(IC<16>{}, IC<16>{}, 8192, OFF_WO + (tnB & 14) * (32 * QS));
      if (pair == 0) poll(flag(F_W1, r, (tnA & 14) + (g & 1)), token());
      tiles(IC<32>{}, IC<14>{}, 8192, OFF_W1 + tnA * (32 * 4 * QS));
      bar();
      so = 0;
      if (pair == 0) poll(flag(F_W2, 8 * (tnB & 1) + (r & 7), (tnB & 14) + (r >> 3)), token());
      tiles(IC<28>{}, IC<16>{}, 16384, OFF_W2 + ((tnB & 14) * 112 + (tnB & 1) * 56) * Q2);
      bar();
      so = 0;
    }
  } else if (wid == 6) {
    FRESH_LANE();
    const int rr = lane >> 3, c16 = (lane & 7) * 16;
    u32x4 U[7][4], F[4];
    auto u_load = [&](auto I) {
      constexpr int i = decltype(I)::value;
#pragma unroll
      for (int e = 0; e < 4; ++e) {
        if constexpr (i < 2)
          U[(i + 5) % 7][e] = __builtin_bit_cast(u32x4, __builtin_amdgcn_raw_buffer_load_b128(rsrc(wo, DM * DM * 2),
              ((rr & 3) + 16 * (rr >> 2)) * 2048 + c16, ((2 * tmA + i) * 32 + 4 * e) * 2048 + (tnA ^ (4 * i)) * 128, UAUX));
        else if constexpr (i < 9)
          U[(i + 5) % 7][e] = __builtin_bit_cast(u32x4, __builtin_amdgcn_raw_buffer_load_b128(rsrc(w1, DM * FF * 2),
              rr * 7168 + c16, ((2 * tmA + (tnA & 1)) * 32 + 8 * e) * 7168 + (7 * (tnA >> 1) + i - 2) * 128, UAUX));
        else
          U[(i + 5) % 7][e] = __builtin_bit_cast(u32x4, __builtin_amdgcn_raw_buffer_load_b128(rsrc(w2, DM * FF * 2),
              rr * 2048 + c16, ((7 * tmA + i - 9) * 32 + 8 * e) * 2048 + tnA * 128, UAUX));
      }
    };
    auto u_write = [&](auto I) {
      constexpr int i = decltype(I)::value;
#pragma unroll
      for (int e = 0; e < 4; ++e) {
        const u32x4 v = U[(i + 5) % 7][e];
        *(u32x4*)(smem + L_IM + e * 1024 + rr * 128 + (c16 ^ ((rr & 2) << 5))) = (e & 1) ? (u32x4){v[2], v[3], v[0], v[1]} : v;
      }
    };
    auto u_store = [&](auto I) {
      constexpr int i = decltype(I)::value;
      const u32 sw = ((r & 8) << 3) | ((g & 1) << 3);
      const char* im = smem + L_IM + (8 * g + (r >> 2)) * 128;
      for_n<4>([&](auto E) {
        constexpr int e = decltype(E)::value;
        constexpr bool split = i >= 2 && i < 9 && (2 * (i - 2) + (e >> 1)) % 7 == 3;
        const char* p = im + (((e >> 1) * 64 + (split ? (e & 1) * 32 + (r & 3) * 8 : (r & 3) * 16 + (e & 1) * 8)) ^ sw);
        const u32x2 lo = __builtin_bit_cast(u32x2, __builtin_amdgcn_ds_read_tr16_b64_v4i16((LDS_AS s16x4*)p));
        const u32x2 hi = __builtin_bit_cast(u32x2, __builtin_amdgcn_ds_read_tr16_b64_v4i16((LDS_AS s16x4*)(p + 512)));
        F[e] = (u32x4){lo[0], lo[1], hi[0], hi[1]};
      });
      for_n<4>([&](auto E) {
        constexpr int e = decltype(E)::value;
        if constexpr (i < 2) st16(F[e], OFF_WO + ((tnA ^ (4 * i)) * 32 + 2 * tmA + i) * QS + qoff(e, lane));
        else if constexpr (i < 9) {
          constexpr int f = w1_frag(i, e);
          st16(F[e], OFF_W1 + ((((tnA & 14) + (f >> 4)) * 32 + 2 * tmA + (tnA & 1)) * 4 + ((f & 15) >> 2)) * QS + qoff(f & 3, lane));
        } else st16(F[e], OFF_W2 + (tnA * 112 + (tmA >> 3) * 56 + hpos(tmA, i - 9)) * Q2 + (D2 ? e * 1024 + lane * 16 : qoff(e, lane)));
      });
    };
    auto u_sums = [&](auto I) {
      constexpr int i = decltype(I)::value;
      constexpr u32 K = w1_frag(i, 0) | (w1_frag(i, 1) << 8) | (w1_frag(i, 2) << 16) | (w1_frag(i, 3) << 24);
      f32x4 cs = {0.f, 0.f, 0.f, 0.f};
#pragma unroll
      for (int e = 0; e < 4; ++e) {
        const u32 o = (r >> 2) == e ? 0x3f803f80u : 0u;
        cs = __builtin_amdgcn_mfma_f32_16x16x32_bf16(__builtin_bit_cast(bf16x8, (u32x4){o, o, o, o}), __builtin_bit_cast(bf16x8, F[e]), cs, 0, 0, 0);
      }
      const u32 f = (K >> (8 * g)) & 255;
      __builtin_amdgcn_raw_buffer_store_b32(__float_as_uint(cs[0]), rs,
          OFF_CS1 + ((((tnA & 14) + (f >> 4)) * 32 + 2 * tmA + (tnA & 1)) * 256 + (f & 15) * 16 + r) * 4, 0, 16);
    };
    u64 tok = 0;
    auto up = [&](int set) { st8(tok, lane == 0, flag(set, tmA, tnA)); };
    f32x4 cv[32], ca[2];
    auto cs_load = [&](u32 src, int p0, int p1) {
#pragma unroll
      for (int p = p0; p < p1; ++p)
        cv[p] = __builtin_bit_cast(f32x4, __builtin_amdgcn_raw_buffer_load_b128(rs, lane * 16 + (p & 3) * 1024, src + (p >> 2) * 4096, 16));
    };
    auto cs_add = [&](int p0, int p1) {
#pragma unroll
      for (int p = p0; p < p1; ++p) {
        asm volatile("" : "+v"(cv[p]));
        if (p < 2) ca[p] = cv[p];
        else {
#pragma unroll
          for (int k = 0; k < 4; ++k) {
            float t = ca[p & 1][k];
            const float u = cv[p][k];
            asm("v_add_f32 %0, %0, %1" : "+v"(t) : "v"(u));
            ca[p & 1][k] = t;
          }
        }
      }
      asm volatile("" : "+v"(ca[0]), "+v"(ca[1]));
    };
    u64 vf = 0;
    const u32 fo = flag(F_WO, r, ((tnB & 14) + (g & 1)) ^ (2 * (g & 2)));
    __builtin_amdgcn_s_sleep(PFW0);
    u_load(IC<0>{});
    u_load(IC<1>{});
    for_n<34>([&](auto S) {
      constexpr int s = decltype(S)::value;
      bar();
      if constexpr (s == TOKB) { tok = token(); asm volatile("" : "+s"(tok)); }
      if constexpr (s >= HL && s < HL + 4) {
        if constexpr ((s - HL) % 2 == 0) u_write(IC<(s - HL) / 2>{}); else u_store(IC<(s - HL) / 2>{});
      }
      if constexpr (s == HL + 3 + FLD) { wait_vm<0>(); up(F_WO); }
      if constexpr (s == G0 - 4) vf = ldf(fo);
      if constexpr (s == G0 - 1) { if (!__all(vf == tok)) poll(fo, tok); }
      if constexpr (s == G0) for_n<4>([&](auto I) { u_load(IC<2 + decltype(I)::value>{}); });
      if constexpr (s == G0 + 1) for_n<3>([&](auto I) { u_load(IC<6 + decltype(I)::value>{}); });
    });
    static_assert(TOKB < HL && HL + 3 + FLD < G0 - 4 && G0 + 1 < 33, "schedule");
    for_n<7>([&](auto I) {
      constexpr int i = 2 + decltype(I)::value;
      u_write(IC<i>{});
      u_store(IC<i>{});
      u_sums(IC<i>{});
    });
    bar(); bar(); bar();
    wait_vm<0>();
    up(F_W1);
#pragma nounroll
    for (int u = 3; u <= G1; ++u) bar();
    for_n<7>([&](auto I) {
      u_load(IC<9 + decltype(I)::value>{});
      if constexpr (decltype(I)::value == 1 || decltype(I)::value == 3) bar();
    });
#pragma nounroll
    for (int u = G1 + 3; u < 16 + H2; ++u) bar();
    for_n<14>([&](auto S) {
      constexpr int s = decltype(S)::value, i = 9 + s / 2;
      bar();
      if constexpr (s % 2 == 0) u_write(IC<i>{}); else u_store(IC<i>{});
    });
    for_n<12>([&](auto S) {
      constexpr int s = decltype(S)::value;
      bar();
      if constexpr (s == 3) { wait_vm<16>(); up(F_W2); }
      if constexpr (s < 6) cs_load(OFF_CS1 + tnA * 32768, s * 16 / 3, (s + 1) * 16 / 3);
      if constexpr (s >= 8) cs_add(8 * (s - 8), 8 * (s - 7));
    });
    static_assert(H2 + 14 + 12 <= 31, "schedule");
    *(f32x4*)(smem + L_CS1 + lane * 16) = ca[0] + ca[1];
    wait_lgkm0();
    QUIT();
  } else {
    FRESH_LANE();
    const int tmh = tmA > 0 ? tmA - 1 : 0;
    u64 tok;
    auto bars = [&](int n) {
#pragma nounroll
      for (int u = 0; u < n; ++u) bar();
    };
    auto ror8 = [](float v) { return __uint_as_float(__builtin_amdgcn_mov_dpp(__float_as_uint(v), 0x128, 0xf, 0xf, false)); };
    auto put = [&](int xi, float mean, float m2, bool on, int row) {
      const float sd = __builtin_amdgcn_sqrtf(bfr(bfr(m2 * (1.f / DM)) + LN_EPS));
      st8(pack2(mean, sd) | (tok << 32), on, flag(F_LN + xi, tmA, tnA) + row * 8);
    };
    auto reduce = [&](int xi) {
      const u32 src = OFF_ST + xi * 524288 + span(tmA, c13) + (tnA >> 2) * 16384 + (tnA & 3) * 2048, tag = (u32)tok;
      u32x4 a, b;
      auto ld = [&]() {
        asm volatile("" ::: "memory");
        a = __builtin_bit_cast(u32x4, __builtin_amdgcn_raw_buffer_load_b128(rs, lane * 16, src, 16));
        b = __builtin_bit_cast(u32x4, __builtin_amdgcn_raw_buffer_load_b128(rs, lane * 16 + 1024, src, 16));
      };
      ld();
      bar(); bar(); bar();
#pragma nounroll
      for (int spin = 0; spin < (1 << 22); ++spin) {
        if (__all((((a[1] ^ tag) | (a[3] ^ tag)) | ((b[1] ^ tag) | (b[3] ^ tag))) == 0)) break;
        __builtin_amdgcn_s_sleep(1);
        ld();
      }
      float m = __uint_as_float(a[0]) + __uint_as_float(b[0]);
      m = xsum(m + ror8(m)) * (1.f / 16.f);
      const float d0 = __uint_as_float(a[0]) - m, d1 = __uint_as_float(b[0]) - m;
      float q = __uint_as_float(a[2]) + __uint_as_float(b[2]) + 64.f * (d0 * d0 + d1 * d1);
      asm volatile("" : "+v"(m), "+v"(q));
      bar();
      asm volatile("" : "+v"(m), "+v"(q));
      put(xi, m, xsum(q + ror8(q)), lane < 8, lane);
    };
    auto gather = [&](auto H, int xi) {
      constexpr int n = 2 + decltype(H)::value;
      const u32 tag = (u32)tok, f0 = flag(F_LN + xi, tmA, lane >> 3) + (lane & 7) * 8;
      const u32 f[3] = {f0, f0 + 1024, flag(F_LN + xi, tmh, 14 + (r >> 3)) + (lane & 7) * 8};
      u32x2 v[3];
      auto ld = [&]() {
#pragma unroll
        for (int h = 0; h < n; ++h) v[h] = __builtin_bit_cast(u32x2, ldf(f[h]));
      };
      ld();
      bar(); bar(); bar();
#pragma nounroll
      for (int spin = 0; spin < (1 << 22); ++spin) {
        if (__all(((v[0][1] ^ tag) | (v[1][1] ^ tag) | (v[n - 1][1] ^ tag)) == 0)) break;
        __builtin_amdgcn_s_sleep(1);
        ld();
      }
#pragma unroll
      for (int h = 0; h < n; ++h) {
        const float iv = __builtin_amdgcn_rcpf(bf_hi(v[h][0]));
        const int o = h < 2 ? 64 + 256 * h + lane * 4 : r * 4;
        *(float*)(smem + L_INV + o) = iv;
        *(float*)(smem + L_MNV + o) = n == 3 ? -bf_lo(v[h][0]) : -bf_lo(v[h][0]) * iv;
      }
      wait_lgkm0();
    };
    auto guard = [&](u32 f, int K, auto&& lim_, auto&& step) {
      int b = 0, lim = 0;
#pragma nounroll
      for (int spin = 0; spin < (1 << 22); ++spin) {
        const u64 v = ldf(f);
        if (b < lim) { bar(); step(b++); } else __builtin_amdgcn_s_sleep(1);
        if (b < lim) { bar(); step(b++); }
        lim = lim_((u32)__ballot(v == tok));
        if (lim >= 100) break;
        lim = min(K, lim);
      }
#pragma nounroll
      while (b < K) { bar(); step(b++); }
    };
    auto ahead = [](int msk, int spp, int D) {
      return [=](u32 m) { const int n = __builtin_ctz(~(m & (m >> 16))); return n >= 16 ? 100 : (n & msk) * spp - D; };
    };
    const __amdgpu_buffer_rsrc_t rx = rsrc(x, SEQ * DM * 2), rq = rsrc(wqkv, DM * 3 * DM * 2);
    u32 wx, ww;
    {
      const int n = idx * 64 + lane, m = idx * 48 + (lane < 48 ? lane : 47), k = (m * 5462) >> 16, p = m - 12 * k;
      wx = (n & 511) * 2048 + ((n >> 9) & 1) * 128 + ((idx >> 4) * 1024 + (xcd >> 2) * 512) * 2048;
      ww = k * 6144 + (p >> 2) * 2048 + (p & 3) * 128 + (xcd & 3) * 512;
    }
    {
      const u64 ep = ldf(OFF_EP);
      u16 bv[4];
#pragma unroll
      for (int h = 0; h < 4; ++h) {
        const int t = min(lane + 64 * h, 223), f = t >> 4, rho = t & 15, wn = f / 7, j = f % 7;
        bv[h] = b1[tnA * 224 + (j == 6 ? 96 + 16 * wn + rho : (4 * wn + (j >> 1)) * 32 + 8 * (rho >> 2) + 4 * (j & 1) + (rho & 3))];
      }
      DMA(rsrc(b2, DM * 2), (LDS_AS void*)(smem + L_B2), 4, lane * 4, (tnB & 14) * 128, 0, 0);
      pin2(wx, ww);
      __builtin_amdgcn_s_sleep(XW);
      bf16x8 xf[16];
#pragma unroll
      for (int e = 0; e < 16; ++e)
        xf[e] = __builtin_bit_cast(bf16x8, __builtin_amdgcn_raw_buffer_load_b128(rx, (r >> 1) * 2048 + (r & 1) * 64 + g * 16 + e * 128,
            (tmA * 128 + tnA * 8) * 2048, 0));
      auto probe = [&](int tn) { return flag(F_PR, tmA ^ (4 * (idx & 1)), tn); };
      const u32x4 one4 = {0x3f803f80u, 0x3f803f80u, 0x3f803f80u, 0x3f803f80u};
      const bf16x8 ones = __builtin_bit_cast(bf16x8, one4);
      f32x4 sm[2], sq[2];
      u32 tlb;
      u64 vt;
      for_n<LG0>([&](auto S) {
        constexpr int s = decltype(S)::value;
        bar();
        if constexpr (s == TOKB - 1) {
          __builtin_amdgcn_raw_buffer_store_b32(0, rs, blockIdx.x < NVOTE ? probe(tnA ^ 4) + (lane & 31) * 4 : 0x80000000u, 0, 16);
#pragma unroll
          for (int h = 0; h < 4; ++h) ((float*)(smem + L_BS))[lane + 64 * h] = bf_lo(bv[h]);
          tok = ((u64)(u32)__builtin_amdgcn_readfirstlane((u32)ep) | ((u64)(u32)__builtin_amdgcn_readfirstlane((u32)(ep >> 32)) << 32))
                * 6364136223846793005ull + 1442695040888963407ull;
          *(LDS_AS u32x4*)(smem + L_CNT) = (u32x4){0u, 0u, (u32)tok, (u32)(tok >> 32)};
          wait_lgkm0();
          asm volatile("s_load_dword %0, %1, 0x0" : "=s"(tlb) : "s"(ws + OFF_EP + 128) : "memory");
        }
        if constexpr (s >= PF0 && (s - PF0) % PFS == 0 && (s - PF0) / PFS < 8 - PFJ) {
          const int j = (PFJ + (s - PF0) / PFS + rot0) & 7;
          DMA(rx, (LDS_AS void*)(smem + L_PF), 4, wx, j * 256, 0, 0);
          DMA(rq, (LDS_AS void*)(smem + L_PF), 4, ww, j * (128 * 6144), 0, 0);
        }
        if constexpr (s >= XP && s < XP + 4) {
#pragma unroll
          for (int e = 4 * (s - XP); e < 4 * (s - XP) + 4; ++e) {
            const f32x4 z = {0.f, 0.f, 0.f, 0.f};
            asm volatile("" : "+v"(xf[e]));
            sm[e & 1] = __builtin_amdgcn_mfma_f32_16x16x32_bf16(ones, xf[e], e < 2 ? z : sm[e & 1], 0, 0, 0);
            sq[e & 1] = __builtin_amdgcn_mfma_f32_16x16x32_bf16(xf[e], xf[e], e < 2 ? z : sq[e & 1], 0, 0, 0);
          }
        }
        if constexpr (s >= XP && s <= XP + 4) asm volatile("" : "+v"(sm[0]), "+v"(sm[1]), "+v"(sq[0]), "+v"(sq[1]));
        if constexpr (s == XP + 4) {
          const f32x4 q4 = sq[0] + sq[1];
          float q = (r & 2) ? ((r & 1) ? q4[3] : q4[2]) : ((r & 1) ? q4[1] : q4[0]), t = sm[0][0] + sm[1][0];
          q += __uint_as_float(__builtin_amdgcn_mov_dpp(__float_as_uint(q), 0xb1, 0xf, 0xf, false));
          t += __uint_as_float(__builtin_amdgcn_mov_dpp(__float_as_uint(t), 0xb1, 0xf, 0xf, false));
          const float m = t * (1.f / DM);
          put(0, m, fmaxf(__builtin_fmaf(-t, m, q), 0.f), g == (r >> 2) && !(r & 1), r >> 1);
        }
        if constexpr (s == PRB) {
          wait_lgkm0();
          asm volatile("" :: "s"(tlb));
          if (blockIdx.x < NVOTE) {
            u64 t0, t1;
            u32 v;
            asm volatile("s_memtime %0\n\ts_waitcnt lgkmcnt(0)\n\ts_load_dword %2, %3, 0x0\n\ts_waitcnt lgkmcnt(0)\n\t"
                         "s_memtime %1\n\ts_waitcnt lgkmcnt(0)" : "=&s"(t0), "=&s"(t1), "=&s"(v) : "s"(ws + probe(tnA)) : "memory");
            const u32 t = min((u32)t1 - (u32)t0, 4095u);
            st8(tok ^ t, lane == 0, flag(F_XC, 0, blockIdx.x));
          }
        }
        if constexpr (s == LG0 - 4) vt = ldf(flag(F_XC, 0, r));
      });
      static_assert(XP >= TOKB && PRB > XP + 4 && LG0 - 4 > PRB && LG0 + 3 <= 31 && L_CNT % 16 == 0, "schedule");
#pragma nounroll
      for (int spin = 0; spin < (1 << 22); ++spin) {
        if (__all((vt ^ tok) < 4096)) break;
        __builtin_amdgcn_s_sleep(1);
        vt = ldf(flag(F_XC, 0, r));
      }
      const u32 t = (u32)(vt ^ tok);
      const u64 slower = __ballot(t > (u32)__builtin_amdgcn_mov_dpp(t, 0x128, 0xf, 0xf, false));
      const u32 c = __builtin_popcount((u32)slower & 255) > 4 ? 8192 : 0;
      *(volatile LDS_AS u32*)(smem + L_C13) = c;
      gather(IC<1>{}, 0);
      c13 = c;
      bars(34 - CLB - (LG0 + 3));
    }
#pragma nounroll
    for (int pair = 0; pair < 2; ++pair) {
#pragma unroll
      for (int e = 0; e < 16; ++e)
        st16(((u32x4){~0u, ~0u, ~0u, ~0u}), xbox(pair, 0) + (e + (e & 8)) * 1024 + lane * 16);
      bars(CLB);
      wait_vm<0>();
      if (ZO + ZG > 0) __builtin_amdgcn_s_sleep(ZO + ZG);
      guard(flag(4 * pair + 1, tmB, (rotB + r) & 15), 16, ahead(15, 1, DN), [](int) {});
      if (Z1 + ZG > 0) __builtin_amdgcn_s_sleep(Z1 + ZG);
      guard(flag(4 * pair + 2, tmA, (rotA + r) & 15), KW1, ahead(14, 2, DW), [](int) {});
      reduce(2 * pair + 1);
      bars(LG - (KW1 + 4));
      gather(IC<0>{}, 2 * pair + 1);
      bars(33 - (LG + 3));
      const u32 w3 = lane < 56 ? OFF_W2 + ((idx >> 1) * 112 + (tnB & 1) * 56 + (idx & 1) * 14) * 4096 + lane * 1024 + 384 : 0x80000000u;
      if (D2 & 2) {
        if (pair == 0) poll(flag(F_W2, 8 * (tnB & 1) + (r & 7), idx >> 1), tok);
        DMA(rs, (LDS_AS void*)(smem + L_PF), 4, w3, 0, 0, 16);
      }
      if (Z2 + ZG > 0) __builtin_amdgcn_s_sleep(Z2 + ZG);
      guard(flag(lane < 8 ? 4 * pair + 3 : lane < 16 ? F_2ND + pair : F_3RD + pair, tmB, 8 * (tnB & 1) + ((lane < 8 ? r ^ (rotA >> 1) : r) & 7)), 13,
            [](u32 m) { const int n = __builtin_ctz(~m | (1u << 24)); return n >= 24 ? 100 : min(n, 16) - 3; },
            [&](int b) {
              if (b == 0 || b == 3) st8(tok, lane == 0, flag(b ? F_3RD + pair : F_2ND + pair, tmA, tnA));
              if ((D2 & 2) && b == D2B) DMA(rs, (LDS_AS void*)(smem + L_PF), 4, w3, 28 * 4096, 0, 16);
            });
      if (pair) break;
      bars(WQ - 13);
      for_n<29 - WQ>([&](auto S) {
        constexpr int s = decltype(S)::value;
        if constexpr (s < 4) {
          DMA(rq, (LDS_AS void*)(smem + L_PF), 4, ww, ((2 * s + (rotA >> 1)) & 7) * (128 * 6144), 0, 0);
          DMA(rq, (LDS_AS void*)(smem + L_PF), 4, ww, ((2 * s + 1 + (rotA >> 1)) & 7) * (128 * 6144), 0, 0);
        }
        bar();
      });
      if (ZQ + ZG > 0) __builtin_amdgcn_s_sleep(ZQ + ZG);
      guard(flag(4, (g & 1) ? tmh : tmA, (rotA + r) & 15), KW1, ahead(14, 2, DW), [](int) {});
      st8(tok, lane == 0 && blockIdx.x == 0, OFF_EP);
      reduce(2);
      bars(LG - (KW1 + 4));
      gather(IC<1>{}, 2);
      bars(33 - CLB - (LG + 3));
      static_assert(WQ <= 25 && LG + 3 <= 31, "schedule");
    }
    QUIT();
  }
#endif
}

extern "C" void kernel_launch(void* const* d_in, const int* in_sizes, int n_in, void* d_out, int out_size,
                              void* d_ws, size_t ws_size, hipStream_t stream) {
  const u16* x0 = (const u16*)d_in[0];
  const u16* wqkv = (const u16*)d_in[1];
  const u16* wo = (const u16*)d_in[2];
  const u16* w1 = (const u16*)d_in[4];
  const u16* b1 = (const u16*)d_in[5];
  const u16* w2 = (const u16*)d_in[6];
  const u16* b2 = (const u16*)d_in[7];
  (void)hipFuncSetAttribute((const void*)mega_k, hipFuncAttributeMaxDynamicSharedMemorySize, LDS_BYTES);
  mega_k<<<256, 512, LDS_BYTES, stream>>>((char*)d_ws + (ws_size - WS_USED - WS_ALIGN), x0, wqkv, wo, w1, b1, w2, b2, (u16*)d_out);
}
```

```cpp
#include <hip/hip_runtime.h>
#include <stdint.h>
#include <type_traits>
#include <utility>

typedef unsigned short u16;
typedef unsigned int u32;
typedef unsigned long long u64;
typedef __bf16 bf16x8 __attribute__((ext_vector_type(8)));
typedef __bf16 bf16x2 __attribute__((ext_vector_type(2)));
typedef short s16x4 __attribute__((ext_vector_type(4)));
typedef float f32x4 __attribute__((ext_vector_type(4)));
typedef float f32x2 __attribute__((ext_vector_type(2)));
typedef u32 u32x4 __attribute__((ext_vector_type(4)));
typedef u32 u32x2 __attribute__((ext_vector_type(2)));
typedef u32 u32x10 __attribute__((ext_vector_type(10)));
typedef int i32x4 __attribute__((ext_vector_type(4)));
typedef int i32x2 __attribute__((ext_vector_type(2)));
#define LDS_AS __attribute__((address_space(3)))
#define GLB_AS __attribute__((address_space(1)))

constexpr int SEQ = 2048, DM = 1024, FF = 3584;
constexpr float LN_EPS = 1e-5f;

#define DN 4
#define DW 5
#define UAUX 2
#define PFW0 30
#define PF0 4
#define PFS 2
#define PFJ 2
#define CQP 5
#define HL 8
#define FLD 3
#define XW 30
#define TOKB 3
#define XP 8
#define LG0 24
#define LG 25
#define WQ 24
#define G0 27
#define G1 12
#define H2 4
#define PUBJ 3
#define CB 1
#define KW1 11
#define CLB 1
#define SND 12
#define RST 9
#define XB2 1
#define XA2 1
#define XBO 1
#define CW 1
#define PRB 13
#define CQG 22
#define R3W 1
#define HSL 10
#define HN 7
#define HB 3
#define LXA 1
#define LXB 1
#define ZO0 42
#define Z10 24
#define GSL 1
#define PNS 0
#define LVA 16
#define LVB 6
#define DB 3
#define SNK 31
#define D2 0
#define D2B 6
#define RL2 15
#define RL1 10
#define RLQ 16
#define ROT 7
#ifndef NVOTE
#define NVOTE 16
#endif
#define NP 1
#define ZO 25
#define Z1 25
#define Z2 0
#define ZQ 50
#define TW2 1
#define OA4 0
#define LK 42
#define ZG 0
#define PS 2
constexpr u32 MB = 1u << 20;
constexpr u32 QS = 5120, Q2 = D2 ? 4096 : QS;
constexpr u32 XS = 8 * MB, HS = 32 * MB;
constexpr u32 OFF_WO = 0;
constexpr u32 OFF_W1 = 3 * MB;
constexpr u32 OFF_W2 = 13 * MB;
constexpr u32 OFF_CS1 = 22 * MB;
constexpr u32 OFF_FL = 25 * MB;
constexpr u32 OFF_EP = OFF_FL + 26 * 32768;
constexpr u32 OFF_DBG = 26 * MB;
constexpr u32 OFF_XC = 28 * MB;
constexpr u32 OFF_X = 28 * MB;
constexpr u32 OFF_O = OFF_X + 4 * XS;
constexpr u32 OFF_H = OFF_O + 2 * XS;
constexpr u32 OFF_ST = OFF_H + 2 * HS;
constexpr u32 WS_USED = OFF_ST + 2 * MB, WS_ALIGN = 4 * MB;
#define HAS_ALIGN
constexpr int F_WO = 9, F_W1 = 10, F_W2 = 11, F_2ND = 12, F_3RD = 14, F_XC = 18, F_PR = 19, F_LN = 22;
__device__ __forceinline__ u32 qoff(u32 e, u32 slot) {
  return slot * 16 + (e * 1152 + (e == 3 ? 128 : 0)) + (slot >= (e == 3 ? 56 : 24 - 8 * e) ? 128 : 0);
}
__device__ __forceinline__ u32 hpos(u32 t, u32 j) {
  return (j & 2) == 0 ? 16 * (j & 1) + 2 * (t & 7) + (j >> 2) : 32 + 3 * (t & 7) + (j & 1) + (j >> 2) * 2;
}
__device__ __forceinline__ u32 aq(u32 kg, u32 half) { return kg * 32768 + (kg & 1) * 3072 + half * 16384; }
__device__ __forceinline__ u32 ppair(u32 tm) { return (tm & 3) + (tm >> 3) * 4; }
__device__ __forceinline__ u32 xpan(u32 tm, u32 c13) { return ppair(tm) * MB + (((((tm >> 2) ^ (tm >> 1)) & 1) << 13) ^ c13); }
__device__ __forceinline__ u32 hpan(u32 tm, u32 kh, u32 c13) {
  return ppair(tm) * (4 * MB) + (((kh ^ tm) & 1) << 21) + (((((tm >> 2) ^ kh ^ tm) & 1) << 13) ^ c13);
}
__device__ __forceinline__ u32 span(u32 tm, u32 c13) { return ppair(tm) * 65536 + (((tm & 4) << 11) ^ c13); }

constexpr int SLOT = 24576, NRING = 6, RING = SLOT * NRING;
constexpr int L_INV = RING, L_MNV = L_INV + 576, L_CNT = L_MNV + 576, L_TOK = L_CNT + 8, L_C13 = L_TOK + 8;
constexpr int L_CSQ = RING + 1280, L_CS1 = L_CSQ + 1024, L_BS = L_CS1 + 1024;
constexpr int L_XC = L_BS + 1024;
constexpr int L_IM = L_XC + 6144;
constexpr int L_B2 = L_IM + 4096, L_PF = L_B2 + 256;
constexpr int LDS_BYTES = L_PF + 256;

__device__ __forceinline__ float bf_lo(u32 w) { return __uint_as_float(w << 16); }
__device__ __forceinline__ float bf_hi(u32 w) { return __uint_as_float(w & 0xffff0000u); }
__device__ __forceinline__ u32 pack2(float lo, float hi) {
  f32x2 v = {lo, hi};
  bf16x2 r = __builtin_convertvector(v, bf16x2);
  return __builtin_bit_cast(u32, r);
}
__device__ __forceinline__ float bfr(float x) { return bf_lo(pack2(x, 0.f) & 0xffffu); }
__device__ __forceinline__ u32x4 gelu_tanh8(const f32x2 (&x)[4]) {
  const float c0 = -2.3022081983f, c1 = -0.1029432396f;
  f32x2 t[4];
#define STEP(e) do { _Pragma("unroll") for (int k = 0; k < 4; ++k) t[k] = e; __builtin_amdgcn_sched_barrier(0); } while (0)
  STEP(x[k] * x[k]);
  STEP(t[k] * c1 + c0);
  STEP(x[k] * t[k]);
  STEP(((f32x2){__builtin_amdgcn_exp2f(t[k][0]), __builtin_amdgcn_exp2f(t[k][1])}));
  STEP(t[k] + 1.f);
  STEP(((f32x2){__builtin_amdgcn_rcpf(t[k][0]), __builtin_amdgcn_rcpf(t[k][1])}));
  STEP(x[k] * t[k]);
#undef STEP
  return (u32x4){pack2(t[0][0], t[0][1]), pack2(t[1][0], t[1][1]), pack2(t[2][0], t[2][1]), pack2(t[3][0], t[3][1])};
}
template <int N_> __device__ __forceinline__ void wait_vm() { asm volatile("s_waitcnt vmcnt(%0)" ::"n"(N_) : "memory"); }
__device__ __forceinline__ void wait_lgkm0() { asm volatile("s_waitcnt lgkmcnt(0)" ::: "memory"); }
__device__ __forceinline__ void bar() {
  __builtin_amdgcn_sched_barrier(0);
  asm volatile("s_barrier" ::: "memory");
  __builtin_amdgcn_sched_barrier(0);
}
template <int V> using IC = std::integral_constant<int, V>;
template <typename F, int... I>
__device__ __forceinline__ void for_seq(F&& f, std::integer_sequence<int, I...>) { (f(IC<I>{}), ...); }
template <int N, typename F> __device__ __forceinline__ void for_n(F&& f) { for_seq(f, std::make_integer_sequence<int, N>{}); }

__device__ __forceinline__ float xsum(float x) {
#if defined(__HIP_DEVICE_COMPILE__)
  auto a = __builtin_amdgcn_permlane16_swap(__float_as_uint(x), __float_as_uint(x), false, false);
  x = __uint_as_float(a[0]) + __uint_as_float(a[1]);
  auto b = __builtin_amdgcn_permlane32_swap(__float_as_uint(x), __float_as_uint(x), false, false);
  x = __uint_as_float(b[0]) + __uint_as_float(b[1]);
#endif
  return x;
}
__device__ __forceinline__ float xmax(float x) {
#if defined(__HIP_DEVICE_COMPILE__)
  auto a = __builtin_amdgcn_permlane16_swap(__float_as_uint(x), __float_as_uint(x), false, false);
  x = fmaxf(__uint_as_float(a[0]), __uint_as_float(a[1]));
  auto b = __builtin_amdgcn_permlane32_swap(__float_as_uint(x), __float_as_uint(x), false, false);
  x = fmaxf(__uint_as_float(b[0]), __uint_as_float(b[1]));
#endif
  return x;
}
#define st16(v, off) __builtin_amdgcn_raw_buffer_store_b128(__builtin_bit_cast(i32x4, v), rs, (off), 0, 16)
__device__ __forceinline__ float sum8(const u32x4& f) {
  float s = bf_lo(f[0]) + bf_hi(f[0]);
#pragma unroll
  for (int k = 1; k < 4; ++k) { s += bf_lo(f[k]); s += bf_hi(f[k]); }
  return s;
}
__device__ __forceinline__ float fold32(float a, float b) {
#if defined(__HIP_DEVICE_COMPILE__)
  const auto x = __builtin_amdgcn_permlane32_swap(__float_as_uint(a), __float_as_uint(b), false, false);
  a = __uint_as_float(x[0]) + __uint_as_float(x[1]);
#endif
  return a;
}
__device__ __forceinline__ float fold16(float a, float b) {
#if defined(__HIP_DEVICE_COMPILE__)
  const auto x = __builtin_amdgcn_permlane16_swap(__float_as_uint(a), __float_as_uint(b), false, false);
  a = __uint_as_float(x[0]) + __uint_as_float(x[1]);
#endif
  return a;
}

__device__ __forceinline__ float row_parts(const u32x4 (&c)[2][2]) {
  float res = 0.f;
#if defined(__HIP_DEVICE_COMPILE__)
#define SB_() __builtin_amdgcn_sched_barrier(0)
  f32x2 v[2][8], t[2][4];
  float m[2], q[2];
#pragma unroll
  for (int k = 0; k < 16; ++k) v[k >> 3][k & 7] = (f32x2){bf_lo(c[k >> 3][(k >> 2) & 1][k & 3]), bf_hi(c[k >> 3][(k >> 2) & 1][k & 3])};
  SB_();
#pragma unroll
  for (int k = 0; k < 8; ++k) t[k >> 2][k & 3] = v[k >> 2][k & 3] + v[k >> 2][4 + (k & 3)];
  SB_();
#pragma unroll
  for (int k = 0; k < 4; ++k) t[k >> 1][k & 1] += t[k >> 1][2 + (k & 1)];
  SB_();
  t[0][0] += t[0][1]; t[1][0] += t[1][1];
  SB_();
  m[0] = t[0][0][0] + t[0][0][1]; m[1] = t[1][0][0] + t[1][0][1];
  SB_();
  const f32x2 mm = (f32x2){m[0], m[1]} * (1.f / 16.f);
  SB_();
#pragma unroll
  for (int k = 0; k < 16; ++k) v[k >> 3][k & 7] -= mm[k >> 3];
  const auto x = __builtin_amdgcn_permlane16_swap(__float_as_uint(mm[0]), __float_as_uint(mm[1]), false, false);
  SB_();
#pragma unroll
  for (int k = 0; k < 8; ++k) t[k >> 2][k & 3] = v[k >> 2][k & 3] * v[k >> 2][k & 3];
  const float d = __uint_as_float(x[0]) - __uint_as_float(x[1]);
  float h = __uint_as_float(x[0]) + __uint_as_float(x[1]), h2 = h;
  asm volatile("" : "+v"(h2));
  SB_();
#pragma unroll
  for (int k = 0; k < 8; ++k) t[k >> 2][k & 3] = v[k >> 2][4 + (k & 3)] * v[k >> 2][4 + (k & 3)] + t[k >> 2][k & 3];
  const auto y = __builtin_amdgcn_permlane32_swap(__float_as_uint(h), __float_as_uint(h2), false, false);
  SB_();
#pragma unroll
  for (int k = 0; k < 4; ++k) t[k >> 1][k & 1] += t[k >> 1][2 + (k & 1)];
  const float e = __uint_as_float(y[0]) - __uint_as_float(y[1]), d8 = 8.f * d;
  SB_();
  t[0][0] += t[0][1]; t[1][0] += t[1][1];
  const float mean = (__uint_as_float(y[0]) + __uint_as_float(y[1])) * 0.125f, e2 = e + e;
  SB_();
  q[0] = t[0][0][0] + t[0][0][1]; q[1] = t[1][0][0] + t[1][0][1];
  const float w = __builtin_fmaf(d8, d, e2 * e);
  SB_();
  const auto z = __builtin_amdgcn_permlane16_swap(__float_as_uint(q[0]), __float_as_uint(q[1]), false, false);
  res = fold32(mean, __uint_as_float(z[0]) + __uint_as_float(z[1]) + w);
#undef SB_
#endif
  return res;
}

__device__ __forceinline__ int fresh_lane(int tid) { int l = tid & 63; asm volatile("" : "+v"(l)); return l; }
__device__ __forceinline__ f32x4 zero4() {
  f32x4 z = {0.f, 0.f, 0.f, 0.f};
  asm volatile("" : "+v"(z));
  return z;
}
template <int BIG = 0> __device__ __forceinline__ void next_slot(int& so) {
  if constexpr (BIG) so = (so + 32768) & 131071; else { so += SLOT; if (so == RING) so = 0; }
}

#define DMA __builtin_amdgcn_raw_ptr_buffer_load_lds
#define SB() __builtin_amdgcn_sched_barrier(0)
__device__ __forceinline__ void mfma(f32x4& c, const bf16x8& a, const bf16x8& b) {
  asm volatile("v_mfma_f32_16x16x32_bf16 %0, %1, %2, %0" : "+v"(c) : "v"(a), "v"(b));
}
template <int OFF> __device__ __forceinline__ void dsrd(bf16x8& d, u32 addr) {
  asm volatile("ds_read_b128 %0, %1 offset:%2" : "=v"(d) : "v"(addr), "n"(OFF));
}
template <int OFF> __device__ __forceinline__ void dstr(bf16x8& d, u32 addr) {
  u32x2 lo, hi;
  asm volatile("ds_read_b64_tr_b16 %0, %2 offset:%3\n\tds_read_b64_tr_b16 %1, %2 offset:%4"
               : "=&v"(lo), "=&v"(hi) : "v"(addr), "n"(OFF), "n"(OFF + 512));
  const u32x4 v = {lo[0], lo[1], hi[0], hi[1]};
  d = __builtin_bit_cast(bf16x8, v);
}
template <int OFF> __device__ __forceinline__ void dstr1(u32x2& d, u32 addr) {
  asm volatile("ds_read_b64_tr_b16 %0, %1 offset:%2" : "=v"(d) : "v"(addr), "n"(OFF));
}
template <int OFF> __device__ __forceinline__ void dsrd1(float& d, u32 addr) {
  asm volatile("ds_read_b32 %0, %1 offset:%2" : "=v"(d) : "v"(addr), "n"(OFF));
}
template <int O0, int O1> __device__ __forceinline__ void dsrd2(f32x2& d, u32 addr) {
  asm volatile("ds_read2_b32 %0, %1 offset0:%2 offset1:%3" : "=v"(d) : "v"(addr), "n"(O0), "n"(O1));
}
template <int OFF> __device__ __forceinline__ void dsrd4(f32x4& d, u32 addr) {
  asm volatile("ds_read_b128 %0, %1 offset:%2" : "=v"(d) : "v"(addr), "n"(OFF));
}
template <class T> __device__ __forceinline__ void pin(T& a) { asm volatile("" : "+v"(a)); }
template <class T, class... R> __device__ __forceinline__ void pin(T& a, R&... r) { pin(a); pin(r...); }
template <class F> constexpr u64 lanes(F f) {
  u64 m = 0;
  for (int l = 0; l < 64; ++l) m |= (u64)(f(l & 15, l >> 4) ? 1 : 0) << l;
  return m;
}
constexpr u64 M_PAR = lanes([](int r, int) { return r & 1; });
constexpr u64 M_OWN0 = lanes([](int r, int g) { return 4 * g + (r & 1) <= r; });
constexpr u64 M_OWN1 = lanes([](int r, int g) { return 4 * g + (r & 1) + 2 <= r; });
#define LANE_IN(m) __builtin_amdgcn_inverse_ballot_w64(m)
__device__ __forceinline__ void pin1(u32& a) { asm volatile("" : "+v"(a)); }
__device__ __forceinline__ void pin2(u32& a, u32& b) { asm volatile("" : "+v"(a), "+v"(b)); }
constexpr int rd_at(int m, int n, int last) {
  for (int q = 0; q < n; ++q) if (q * last / (n - 1) == m) return q;
  return -1;
}
template <int NSL, int NM, int NR, int ENDB, class AD, class RD, class MM, class HK, class TL>
__device__ __forceinline__ void kloop(int& so, AD&& ad, RD&& rd, MM&& mm, HK&& hook, TL&& tail) {
  constexpr int BIG = NM == 32;
  ad(so, IC<0>{});
  bar();
  for_n<NR>([&](auto Q) { rd(IC<0>{}, Q); });
  next_slot<BIG>(so);
  ad(so, IC<1>{});
  hook(-1);
  auto body = [&](auto S) {
    constexpr int s = decltype(S)::value;
    wait_lgkm0();
    bar();
    for_n<NM>([&](auto M) {
      constexpr int m = decltype(M)::value;
      mm(S, M);
      constexpr int RL = BIG ? RL2 : NR - 1, q = rd_at(m, NR, RL);
      if constexpr (q >= 0) rd(IC<1 - s>{}, IC<q>{});
      if constexpr (m == RL + 1) { next_slot<BIG>(so); asm volatile("" : "+s"(so)); }
      if constexpr (m == RL + 2) ad(so, S);
      SB();
    });
  };
#pragma nounroll
  for (int u = NSL / 2 - 1; u > 0; --u) {
    body(IC<0>{});
    body(IC<1>{});
    hook(NSL / 2 - 1 - u);
  }
  if constexpr (BIG && TW2) {
    wait_lgkm0();
    bar();
    for_n<16>([&](auto M) {
      constexpr int m = decltype(M)::value;
      mm(IC<2>{}, M);
      rd(IC<1>{}, IC<(m < 6 ? m + 2 : m < 12 ? m + 4 : m < 14 ? m - 12 : m - 6)>{});
      SB();
    });
    asm volatile("s_waitcnt lgkmcnt(10)" ::: "memory");
    SB();
    for_n<48>([&](auto T) {
      constexpr int t = decltype(T)::value;
      if constexpr (t == 8) { wait_lgkm0(); bar(); so = 0; }
      mm(IC<(t >> 4 == 1 ? 2 : 3)>{}, IC<(t < 32 ? t : t - 16)>{});
      SB(); tail(T); SB();
    });
  } else {
    body(IC<0>{});
    wait_lgkm0();
    if constexpr (ENDB) { bar(); so = 0; }
    SB();
    for_n<NM>([&](auto M) { mm(IC<3>{}, M); SB(); tail(M); SB(); });
  }
  asm volatile("s_nop 15");
  SB();
}

template <int NM, int NR, int ENDB, class RD, class MM, class HK, class TL>
__device__ __forceinline__ void kloop6(RD&& rd, MM&& mm, HK&& hook, TL&& tail) {
  bar();
  for_n<NR>([&](auto Q) { rd(IC<0>{}, Q); });
  hook(-1);
  auto body = [&](auto S) {
    constexpr int s = decltype(S)::value;
    wait_lgkm0();
    bar();
    for_n<NM>([&](auto M) {
      constexpr int m = decltype(M)::value;
      mm(IC<s & 1>{}, M);
      constexpr int q = rd_at(m, NR, NR == 11 ? RL1 : RLQ);
      if constexpr (q >= 0) rd(IC<(s + 1) % NRING>{}, IC<q>{});
      SB();
    });
  };
#pragma nounroll
  for (int u = 5; u > 0; --u) for_n<NRING>(body);
  body(IC<0>{});
  wait_lgkm0();
  if constexpr (ENDB) bar();
  SB();
  for_n<NM>([&](auto M) { mm(IC<1>{}, M); SB(); tail(M); SB(); });
  asm volatile("s_nop 15");
  SB();
}

struct Slot { u32 k, a0, l0; };
template <int NSL, int P, int D, int X, class PR, class FI, class BR>
__device__ __forceinline__ void stream(int& so, int u0, PR&& prep, FI&& fire, BR&& bar) {
#pragma nounroll
  for (int u = u0; u < D + X; ++u) {
    const Slot a = prep(u);
    if constexpr ((D + X) * P > 63) wait_vm<63 - P>();
    for_n<P>([&](auto I) { fire(I, a); });
  }
  auto next = [&](int u) {
    Slot a = prep(u);
    asm volatile("" : "+s"(a.k), "+s"(a.a0), "+s"(a.l0), "+s"(so));
    return a;
  };
  auto head = [&](const Slot& a) {
    wait_vm<((D - 1) * P < 59 ? (D - 1) * P : 59)>();
    bar();
    for_n<4>([&](auto I) { fire(I, a); });
    SB();
  };
  auto rest = [&](const Slot& a) {
    if constexpr (D * P > 63) wait_vm<67 - P>();
    for_n<P - 4>([&](auto I) { fire(IC<4 + decltype(I)::value>{}, a); });
  };
  if constexpr ((ROT >> (NSL == 28 ? 0 : NSL == 16 ? 1 : 2)) & 1) {
    Slot a = next(X + D);
    if constexpr (X) { wait_vm<(D * P < 63 ? D * P : 63)>(); bar(); }
    head(a);
#pragma nounroll
    for (int u = X + D + 1; u < NSL; ++u) {
      const Slot b = next(u);
      rest(a);
      head(b);
      a = b;
    }
    rest(a);
  } else {
    if constexpr (X) { wait_vm<(D * P < 63 ? D * P : 63)>(); bar(); }
#pragma nounroll
    for (int u = X + D; u < NSL; ++u) {
      const Slot a = next(u);
      head(a);
      rest(a);
    }
  }
  for_n<D>([&](auto M) { constexpr int n = (D - 1 - decltype(M)::value) * P; wait_vm<(n < 63 ? n : 63)>(); bar(); });
}

#define STAMP(w, i)
#define TS(v)
#define TSW(v, i)
#define TSP 1

constexpr int w1_frag(int i, int e) {
  const int gam = 2 * (i - 2) + (e >> 1), gl = gam % 7;
  return (gam / 7) * 16 + (gl == 3 ? 6 + 7 * (e & 1) : (gl < 3 ? gl * 2 : 7 + (gl - 4) * 2) + (e & 1));
}
__device__ __forceinline__ void warm_code(u32 n, u32 lds) {
#if defined(__HIP_DEVICE_COMPILE__)
  u64 pc;
  u32 back, len;
  asm volatile("s_getpc_b64 %0\n.Lcw%=:\n\ts_mov_b32 %1, .Lcw%=-mega_k\n\ts_mov_b32 %2, .Lcode_end-mega_k" : "=s"(pc), "=s"(back), "=s"(len));
  pc -= back;
  const u32x4 rc = {(u32)pc, (u32)(pc >> 32), len, 0x00020000u};
  asm volatile("s_mov_b32 %0, m0\n\ts_mov_b32 m0, %1\n\ts_nop 0\n\tbuffer_load_dword %2, %3, 0 offen lds\n\ts_mov_b32 m0, %0"
               : "=&s"(back) : "s"(lds), "v"(n * 128), "s"(rc));
#endif
}

template <int N> __device__ __forceinline__ void announce(u32 fl, __amdgpu_buffer_rsrc_t rs) {
  u32x2 tk;
  u32 a, o, f, n;
  asm volatile("s_mov_b64 exec, 1\n\tv_mov_b32 %1, %7\n\tds_read_b64 %0, %1 offset:8\n\tv_mov_b32 %2, 1\n\tv_mov_b32 %3, %5\n\t"
               "s_waitcnt vmcnt(%8)\n\tds_add_rtn_u32 %1, %1, %2\n\ts_waitcnt lgkmcnt(0)\n\t"
               "v_readfirstlane_b32 %4, %1\n\ts_and_b32 %4, %4, 3\n\ts_cmp_eq_u32 %4, 3\n\ts_cbranch_scc0 .Lan%=\n\t"
               "buffer_store_dwordx2 %0, %3, %6, 0 offen sc1\n.Lan%=:\n\ts_mov_b64 exec, -1"
               : "=&v"(tk), "=&v"(a), "=&v"(o), "=&v"(f), "=&s"(n)
               : "s"(fl), "s"(rs), "n"(L_CNT), "n"(N) : "memory", "scc");
  static_assert(L_TOK == L_CNT + 8, "layout");
}

extern "C" __global__ __launch_bounds__(512, 2) void mega_k(char* __restrict__ ws0, const u16* __restrict__ x,
                                                 const u16* __restrict__ wqkv, const u16* __restrict__ wo,
                                                 const u16* __restrict__ w1, const u16* __restrict__ b1,
                                                 const u16* __restrict__ w2, const u16* __restrict__ b2,
                                                 u16* __restrict__ out) {
#if defined(__HIP_DEVICE_COMPILE__)
  extern __shared__ __attribute__((aligned(16))) char smem[];
  const int tid = threadIdx.x;
  int wid = __builtin_amdgcn_readfirstlane(tid >> 6);
#define FRESH_LANE() const int lane = fresh_lane(tid), r = lane & 15, g = lane >> 4; (void)r; (void)g
  const int xcd = blockIdx.x & 7, idx = blockIdx.x >> 3;
  asm volatile(".subsection 1\n.Lcode_end:\n\t.subsection 0");
  if (CW & 1) warm_code(((tid & 63) * 8 + wid) * 32 + idx, L_PF);
  SB();
  char* const ws = ws0 + (-(u32)(uintptr_t)ws0 & (WS_ALIGN - 1));
  const int tmA = (idx >> 4) * 8 + (xcd >> 2) * 4 + ((idx >> 2) & 3), tnA = (xcd & 3) * 4 + (idx & 3);
  const int tmB = (idx >> 4) * 8 + (xcd >> 1) * 2 + ((idx >> 3) & 1), tnB = (idx & 7) * 2 + (xcd & 1);
  const int rotA = 8 - 8 * (xcd & 1), rotB = 4 * ((xcd + 1) & 3), rot0 = xcd;
  const __amdgpu_buffer_rsrc_t rs = __builtin_amdgcn_make_buffer_rsrc((void*)ws, 0, WS_USED, 0x00020000);
  u32 c13 = 0;
  auto side = [&]() { c13 = __builtin_amdgcn_readfirstlane(*(const volatile LDS_AS u32*)(smem + L_C13)); };
  auto flag = [&](int set, int tm, int tn) { return OFF_FL + ((((set * 16 + tm) * 16 + tn) * 128) ^ c13); };
  auto xbox = [&](int pair, int o) { return OFF_XC + pair * (4 * MB) + ((idx * 4 + (xcd & 3)) ^ o) * 32768 + (((((tmB >> 2) ^ (idx >> 4)) & 1) << 13) ^ c13); };
  auto rsrc = [&](const u16* p, u32 bytes) { return __builtin_amdgcn_make_buffer_rsrc((void*)p, 0, bytes, 0x00020000); };
  auto token = [&]() {
    const u32x2 tk = *(const volatile LDS_AS u32x2*)(smem + L_TOK);
    return (u64)(u32)__builtin_amdgcn_readfirstlane(tk[0]) | ((u64)(u32)__builtin_amdgcn_readfirstlane(tk[1]) << 32);
  };
  auto ldf = [&](u32 f) {
    asm volatile("" ::: "memory");
    return __builtin_bit_cast(u64, __builtin_amdgcn_raw_buffer_load_b64(rs, f, 0, 16));
  };
  auto poll = [&](u32 f, u64 tok) {
#pragma nounroll
    for (int spin = 0; spin < (1 << 22); ++spin) {
      if (__all(ldf(f) == tok)) break;
      __builtin_amdgcn_s_sleep(1);
    }
    asm volatile("" ::: "memory");
  };
  auto poll_n = [&](u32 f, u64 tok, u64 (&v)[NP]) {
    auto look = [&](u64& d) { asm volatile("buffer_load_dwordx2 %0, %1, %2, 0 offen sc1" : "=v"(d) : "v"(f), "s"(rs) : "memory"); };
#pragma unroll
    for (int i = 0; i < NP; ++i) { if (i) __builtin_amdgcn_s_sleep(PS); look(v[i]); }
#pragma nounroll
    for (int spin = 0; spin < (1 << 21); ++spin) {
      bool up = false;
#pragma unroll
      for (int i = 0; i < NP; ++i) {
        asm volatile("s_waitcnt vmcnt(%1)" : "+v"(v[i]) : "n"(NP - 1) : "memory");
        up = __all(v[i] == tok);
        if (up) break;
        if (PNS) __builtin_amdgcn_s_sleep(PNS);
        look(v[i]);
      }
      if (up) break;
    }
    asm volatile("" ::: "memory");
  };
  auto st8 = [&](u64 v, bool on, u32 f) {
    __builtin_amdgcn_raw_buffer_store_b64(__builtin_bit_cast(i32x2, v), rs, on ? f : 0x80000000u, 0, 16);
  };
  int so = 0;
#define QUIT() __builtin_amdgcn_endpgm()

  auto x_lanes = [&](u32 (&vo)[4]) {
    FRESH_LANE();
#pragma unroll
    for (int e = 0; e < 4; ++e)
      vo[e] = (8 * e + (lane >> 3)) * 2048 + (((lane & 7) ^ (4 * (e & 1) + (lane >> 4))) * 16) - e * 1024;
  };
  auto x_half = [&](auto H, int U, const u32 (&vo)[4]) {
    constexpr int h = decltype(H)::value;
    const __amdgpu_buffer_rsrc_t rx = rsrc(x, SEQ * DM * 2);
    const u32 sx = tmA * 128 * 2048, sh = (tmA > 0 ? tmA * 128 - 16 : 0) * 2048;
    U = (U + 2 * rot0) & 15;
    for_n<8>([&](auto E) {
      constexpr int e = decltype(E)::value;
      DMA(rx, (LDS_AS void*)(smem + so + h * SLOT + (e >> 2) * 4096), 16, vo[e & 3],
          sx + (h * 64 + (e >> 2) * 32) * 2048 + U * 128, (e & 3) * 1024, 0);
    });
    DMA(rx, (LDS_AS void*)(smem + so + SLOT + 21504), 16, vo[h], sh + U * 128, h * 1024, 0);
    if constexpr (h == 1) { so += 2 * SLOT; if (so == RING) so = 0; }
  };
  auto q_lanes = [&](u32 (&vo)[4]) {
    FRESH_LANE();
#pragma unroll
    for (int e = 0; e < 4; ++e)
      vo[e] = (8 * e + (lane >> 3)) * 6144 + (((lane & 7) ^ (2 * ((lane >> 4) & 1) + 4 * (e & 1))) * 16) - e * 1024;
  };
  if (wid == 6) {
    FRESH_LANE();
    DMA(rsrc(wqkv, DM * 3 * DM * 2), (LDS_AS void*)(smem + L_PF), 4,
        lane < 24 && !(xcd & 1) ? ((rot0 * 128 + idx * 8 + (lane & 7)) & 1023) * 6144 + (lane >> 3) * 2048 : 0x80000000u,
        (xcd & 3) * 512 + 384, 0, 0);
  }
  asm volatile("" : "+s"(wid));
  if (wid == 5) {
    u32 vo[4];
    q_lanes(vo);
#pragma nounroll
    for (int u = 0; u < 5; ++u)
      for_n<12>([&](auto I) {
        constexpr int i = decltype(I)::value;
        DMA(rsrc(wqkv, DM * 3 * DM * 2), (LDS_AS void*)(smem + u * SLOT + 9216 + (i >> 2) * 4096), 16, vo[i & 3],
            ((u + 4 * rot0) & 31) * (32 * 6144) + (i >> 2) * 2048 + tnA * 128, (i & 3) * 1024, 0);
      });
  }
  asm volatile("" : "+s"(wid));
  if (wid == 4) {
    u32 vo[4];
    x_lanes(vo);
#pragma nounroll
    for (int U = 0; U < 3; ++U) { x_half(IC<0>{}, U, vo); x_half(IC<1>{}, U, vo); }
  }
  so = 0;
  asm volatile("" : "+s"(wid));

  if (wid < 4) {
    auto publish = [&](auto N, int ph, int tm, int tn) {
      SB();
      announce<decltype(N)::value>(flag(ph + 1, tm, tn), rs);
      SB();
    };
    const int wmi = wid >> 1, wni = wid & 1;

    const int w = 2 * (tnB & 1) + wmi, tn = (tnB & 14) + wni;
    u32x4 c[2][2];
    auto start = [&](f32x4 (&acc)[4][4], const u32x4 (&bb)[2], bool bias) {
#pragma unroll
      for (int i = 0; i < 2; ++i)
#pragma unroll
        for (int j = 0; j < 4; ++j) {
          const u32 c0 = c[i][j >> 1][2 * (j & 1)], c1 = c[i][j >> 1][2 * (j & 1) + 1];
          const u32 b0 = bb[j >> 1][2 * (j & 1)], b1 = bb[j >> 1][2 * (j & 1) + 1];
          f32x4 a = {bf_lo(c0), bf_hi(c0), bf_lo(c1), bf_hi(c1)};
          if (bias) a += (f32x4){bf_lo(b0), bf_hi(b0), bf_lo(b1), bf_hi(b1)};
          asm volatile("" : "+v"(a));
          acc[i][j] = a;
        }
    };
    u32 fo[4];
    auto dest = [&](auto E, int xo) {
      constexpr int e = decltype(E)::value, i = e >> 1, gl = e & 1;
      FRESH_LANE();
      const u32 a = OFF_X + xo * XS + xpan(tmB, c13) + tn * 65536 + aq(gl, w >> 1) + qoff((w & 1) * 2 + i, lane);
      const u32 b = (tmB * 128 + (2 * w + i) * 16 + r) * 2048 + tn * 128 + gl * 64 + g * 16;
      fo[e] = xo < 4 ? a : b;
      pin(fo[e]);
    };
    auto finish = [&](const f32x4 (&acc)[4][4], int xo, int ph) {
      FRESH_LANE();
      const __amdgpu_buffer_rsrc_t rd = __builtin_amdgcn_make_buffer_rsrc(xo < 4 ? (void*)ws : (void*)out, 0, xo < 4 ? WS_USED : SEQ * DM * 2, 0x00020000);
#pragma unroll
      for (int i = 0; i < 2; ++i)
#pragma unroll
        for (int gl = 0; gl < 2; ++gl) {
#pragma unroll
          for (int k = 0; k < 4; ++k) {
            const f32x4& a = acc[i][2 * gl + (k >> 1)];
            c[i][gl][k] = pack2(a[2 * (k & 1)], a[2 * (k & 1) + 1]);
          }
          __builtin_amdgcn_raw_buffer_store_b128(__builtin_bit_cast(i32x4, c[i][gl]), rd, fo[2 * i + gl], 0, 16);
        }
      if (xo < 4) {
        SB();
        TS(ta);
        const u32 tag = *(const volatile LDS_AS u32*)(smem + L_TOK);
        __builtin_amdgcn_raw_buffer_store_b64((i32x2){(int)__float_as_uint(row_parts(c)), (int)tag}, rs,
            OFF_ST + xo * 524288 + span(tmB, c13) + w * 16384 + tn * 128 + (lane & 24) * 256 + (lane & 7) * 16 + (g >> 1) * 8, 0, 16);
        SB();
        TS(tb);
        publish(IC<1>{}, ph, tmB, tnB);
        TS(tc);
      }
      STAMP(0, 4 * ph + 3);
    };

    auto level = [&](int n, auto STEP, auto MORE) {
      f32x4 d[8];
      bf16x8 a[2], b[4];
      u32 p = fresh_lane(tid) * 16, pg = L_CNT + 4;
      pin(p, pg);
      dsrd<0>(a[0], p); dsrd<1024>(a[1], p); dsrd<9216>(b[0], p); dsrd<10240>(b[1], p); dsrd<11264>(b[2], p); dsrd<12288>(b[3], p);
#pragma unroll
      for (int i = 0; i < 8; ++i) d[i] = zero4();
      wait_lgkm0();
#pragma nounroll
      for (; n > 0; --n) {
        u32 at, k;
        asm volatile("ds_read_b32 %0, %1" : "=v"(at) : "v"(pg) : "memory");
#pragma unroll
        for (int i = 0; i < 8; ++i) mfma(d[i], b[i & 3], a[i >> 2]);
        asm volatile("s_waitcnt lgkmcnt(0)\n\tv_readfirstlane_b32 %1, %2\n\ts_cmp_ge_u32 %1, %3\n\ts_cselect_b32 %1, %4, 64\n\ts_min_i32 %0, %0, %1"
                     : "+s"(n), "=&s"(k), "+v"(at) : "n"(decltype(STEP)::value), "n"(decltype(MORE)::value) : "memory", "scc");
      }
      asm volatile("s_nop 15\n\ts_nop 15" ::: "memory");
      pin(d[0], d[1], d[2], d[3], d[4], d[5], d[6], d[7]);
    };

#pragma nounroll
    for (int pair = 0; pair < 2; ++pair) {
      f32x4 aco[4][4];
      {
        f32x4 acc[4][6], acch[2];
        f32x4 muv[4], muh, cs[4];
        f32x2 mu2[2], ivk[2][2], fk[2][2], csv;
        u32 sel[4], oa[4];
        {
        FRESH_LANE();
        STAMP(0, 16 * pair);
        const u32 lB = 9216 + (8 * g + (r >> 2)) * 128 + (((2 * wni) ^ ((r >> 3) & 1) ^ ((g & 1) << 1)) * 32) + (r & 3) * 8;
        const u32 lA = r * 128 + ((g ^ (r >> 1)) * 16);
        auto loop = [&](auto WM, auto RAW) {
          constexpr int wm = decltype(WM)::value, raw = decltype(RAW)::value;
#pragma unroll
          for (int i = 0; i < 4; ++i)
#pragma unroll
            for (int j = 0; j < 6; ++j) acc[i][j] = zero4();
          acch[0] = zero4(); acch[1] = zero4();
          bf16x8 fA[2][4], fH[2];
          u32x2 hB[2][12];
          u32 pA[3][2], pB[3][2], pH[3][2];
          f32x4 accq;
          if (raw) { accq = zero4(); *(u32*)(smem + L_XC + lane * 4) = 0x3f803f80u; }
#pragma unroll
          for (int k = 0; k < 3; ++k) {
            pA[k][0] = (raw ? lA : lane * 16) + 2 * k * SLOT; pA[k][1] = (lA ^ 64) + 2 * k * SLOT;
            pB[k][0] = lB + 2 * k * SLOT; pB[k][1] = (lB ^ 32) + 2 * k * SLOT;
            pin1(pA[k][0]); pin2(pB[k][0], pB[k][1]);
            if (raw) {
              pH[k][0] = r ? pA[k][0] : L_XC - (SLOT + 21504) + lA; pH[k][1] = r ? pA[k][1] : L_XC - (SLOT + 21504) + (lA ^ 64);
              pin1(pA[k][1]); pin2(pH[k][0], pH[k][1]);
            }
          }
          int ln;
          u32 aR, aG, aK[2];
          f32x2 ivq[2];
          float mh, cv;
          float fq[2][4];
          auto fB = [&](int set, int j) {
            return __builtin_bit_cast(bf16x8, (u32x4){hB[set][2 * j][0], hB[set][2 * j][1], hB[set][2 * j + 1][0], hB[set][2 * j + 1][1]});
          };
          kloop6<26 + raw, 17, 0>(
            [&](auto SL, auto Q) {
              constexpr int set = decltype(SL)::value & 1, k = decltype(SL)::value >> 1, q = decltype(Q)::value, h = q - 5;
              if constexpr (q < 4) dsrd<raw ? wm * SLOT + q * 2048 : set * SLOT + wm * 4096 + q * 1024>(fA[set][q], pA[k][raw & set]);
              else if constexpr (q == 4) dsrd<raw ? SLOT + 21504 : set * SLOT + 8192>(fH[set], raw ? pH[k][set] : pA[k][0]);
              else dstr1<set * SLOT + (h >> 2) * 4096 + (h & 1) * 512>(hB[set][h], pB[k][(h >> 1) & 1]);
            },
            [&](auto SET, auto M) {
              constexpr int set = decltype(SET)::value, m = decltype(M)::value, j = m >> 2, i = (SNK & 2) && (j & 1) ? 3 - (m & 3) : m & 3;
              if constexpr (m < 16) mfma(acc[i][j], fB(set, j), fA[set][i]);
              else if constexpr (m < 24) mfma(acc[i][j], fA[set][i], fB(set, j));
              else if constexpr (m == 26) mfma(accq, fB(set, wm), fH[set]);
              else if constexpr (wm == 0) mfma(acch[i], fB(set, 2 + i), fH[set]);
              else mfma(acch[i], fH[set], fB(set, 4 + i));
            }, [&](int t) { if (t < 0) STAMP(0, 16 * pair + 1); },
            [&](auto M) {
              constexpr int m = decltype(M)::value;
              if constexpr (m == 0) {
                ln = fresh_lane(tid);
                aR = L_INV + wm * 256 + (ln & 15) * 4; aG = L_INV + wm * 256 + (ln & 48);
                pin(aR, aG);
              }
              if constexpr (m == 1) { dsrd2<16, 32>(ivq[0], aR); dsrd2<48, 64>(ivq[1], aR); dsrd4<640 + 192>(muv[3], aG); }
              if constexpr (m == 2) { aK[0] = aG + (ln & 1) * 4; aK[1] = aK[0] + 64; pin(aK[0], aK[1]); }
              if constexpr (m == 3) {
                const u32 a0 = LANE_IN(M_OWN0) ? aK[1] : aK[0], a1 = LANE_IN(M_OWN1) ? aK[1] : aK[0];
                aK[0] = a0; aK[1] = a1;
                pin(aK[0], aK[1]);
              }
              if constexpr (m == 4) { dsrd2<0, 16>(ivk[0][0], aK[0]); dsrd2<32, 48>(ivk[0][1], aK[0]); dsrd2<2, 18>(ivk[1][0], aK[1]); }
              if constexpr (m == 5) { dsrd2<34, 50>(ivk[1][1], aK[1]); dsrd2<160, 176>(mu2[0], aR); dsrd2<192, 208>(mu2[1], aR); }
              if constexpr (m == 6) { dsrd4<640>(muv[0], aG); dsrd4<640 + 64>(muv[1], aG); dsrd4<640 + 128>(muv[2], aG); }
              if constexpr (m == 7) {
                if constexpr (wm) dsrd4<576 - 256>(muh, aG); else dsrd1<576>(mh, aR);
                aK[0] = L_C13;
                pin(aK[0]);
                dsrd1<0>(cv, aK[0]);
              }
              if constexpr (m == 8) { aR += L_CSQ - L_INV - wm * 256 + wni * 384; aG += L_CSQ - L_INV - wm * 256 + wni * 384; pin(aR, aG); }
              if constexpr (!raw) {
                if constexpr (m == 9) { dsrd4<128>(cs[2], aG); dsrd4<192>(cs[3], aG); dsrd2<64, 80>(csv, aR); }
                if constexpr (m == 10) { dsrd4<0>(cs[0], aG); dsrd4<64>(cs[1], aG); }
              }
              if constexpr (m == 11) { sel[0] = LANE_IN(M_PAR) ? 0x01000c0cu : 0x0c0c0100u; pin(sel[0]); }
              if constexpr (m == 12) { sel[1] = LANE_IN(M_PAR) ? 0x03020c0cu : 0x0c0c0302u; pin(sel[1]); }
              if constexpr (m == 13) {
                sel[2] = LANE_IN(M_OWN0) ? sel[0] : 0x0c0c0c0cu; sel[3] = LANE_IN(M_OWN1) ? sel[1] : 0x0c0c0c0cu;
                pin(sel[2], sel[3]);
              }
              if constexpr (m == 14) {
                sel[0] = LANE_IN(M_OWN0) ? 0x0c0c0c0cu : sel[0]; sel[1] = LANE_IN(M_OWN1) ? 0x0c0c0c0cu : sel[1];
                pin(sel[0], sel[1]);
              }
              if constexpr (m == 16) {
                wait_lgkm0();
                pin(ivq[0], ivq[1], muv[0], muv[1], muv[2], muv[3], ivk[0][0], ivk[0][1], ivk[1][0], ivk[1][1]);
                pin(mu2[0], mu2[1], cv);
                if constexpr (wm) pin(muh); else { pin(mh); muh = (f32x4){mh, mh, mh, mh}; }
                if constexpr (!raw) pin(cs[0], cs[1], cs[2], cs[3], csv);
              }
              if constexpr (m >= 17 && m < 21) {
                constexpr int i = m - 17;
                float cq = ivq[i >> 1][i & 1] * (0.17677669529663687f * 1.4426950408889634f);
                pin(cq);
                fq[0][i] = ivk[0][i >> 1][i & 1] * cq; fq[1][i] = ivk[1][i >> 1][i & 1] * cq;
                pin(fq[0][i], fq[1][i]);
                if constexpr (i & 1) { fk[0][i >> 1] = (f32x2){fq[0][i - 1], fq[0][i]}; fk[1][i >> 1] = (f32x2){fq[1][i - 1], fq[1][i]}; }
              }
              if constexpr (m == 21) {
                oa[0] = ((OFF_O + pair * XS + xpan(tmA, 0) + tnA * 65536 + aq(wni, wm)) ^ __float_as_uint(cv)) + ln * 16;
                oa[1] = oa[0] + 128;
                pin(oa[0], oa[1]);
              }
              if constexpr (m == 23 && OA4) {
                oa[2] = LANE_IN(~0ull << 8) ? oa[1] : oa[0]; oa[3] = LANE_IN(~0ull << 56) ? oa[1] : oa[0];
                pin(oa[2], oa[3]);
              }
              if constexpr (m == 24 && OA4) {
                const u32 a0 = LANE_IN(~0ull << 24) ? oa[1] : oa[0], a1 = LANE_IN(~0ull << 16) ? oa[1] : oa[0];
                oa[0] = a0; oa[1] = a1;
                pin(oa[0], oa[1]);
              }
              if constexpr (m == 22) c13 = __builtin_amdgcn_readfirstlane(__float_as_uint(cv));
            });
          if constexpr (raw) {
            FRESH_LANE();
            if (LANE_IN(0x0001000100010001ull)) {
              *(f32x4*)(smem + aG + wm * 64) = accq;
              if (wm == 0) { *(f32x4*)(smem + aG + 128) = acch[0]; *(f32x4*)(smem + aG + 192) = acch[1]; }
            }
            if (wm == 1 && LANE_IN(0xffffull)) { *(float*)(smem + aR + 256) = acch[0][0]; *(float*)(smem + aR + 320) = acch[1][0]; }
            wait_lgkm0();
            bar();
            dsrd4<128>(cs[2], aG); dsrd4<192>(cs[3], aG); dsrd2<64, 80>(csv, aR); dsrd4<0>(cs[0], aG); dsrd4<64>(cs[1], aG);
#pragma unroll
            for (int i = 0; i < 2; ++i)
#pragma unroll
              for (int gl = 0; gl < 2; ++gl)
                c[i][gl] = __builtin_bit_cast(u32x4, __builtin_amdgcn_raw_buffer_load_b128(rsrc(x, SEQ * DM * 2),
                    (16 * i + r) * 2048 + g * 16, (tmB * 128 + w * 32) * 2048 + tn * 128 + gl * 64, 0));
            wait_lgkm0();
            pin(cs[0], cs[1], cs[2], cs[3], csv);
          }
        };
        if (pair == 0) { if (wmi == 0) loop(IC<0>{}, IC<1>{}); else loop(IC<1>{}, IC<1>{}); }
        else { if (wmi == 0) loop(IC<0>{}, IC<0>{}); else loop(IC<1>{}, IC<0>{}); }
        }
        FRESH_LANE();
        STAMP(0, 16 * pair + 2);
        auto fix_qk = [&](const f32x4& a0, const f32x4& a1, float mu, int f) {
          const f32x4 y0 = cs[f] * mu + a0, y1 = cs[f + 1] * mu + a1;
          u32x4 v = {pack2(y0[0], y0[1]), pack2(y0[2], y0[3]), pack2(y1[0], y1[1]), pack2(y1[2], y1[3])};
          pin(v);
          return v;
        };
        auto fix_v = [&](const f32x4& a, const f32x4& mu, float cw) {
          const f32x4 y = mu * cw + a;
          u32x2 v = {pack2(y[0], y[1]), pack2(y[2], y[3])};
          pin(v);
          return v;
        };
        u32x4 Qf[4], Kf[5];
        u32x2 Vf[5][2];
        char* xc = smem + L_XC + wni * 3072;
        Kf[4] = fix_qk(acc[3][2], acc[3][3], mu2[1][1], 2);
        Vf[4][0] = fix_v(acc[3][4], muv[3], csv[0]); Vf[4][1] = fix_v(acc[3][5], muv[3], csv[1]);
        if (wmi == 0) {
          *(u32x4*)(xc + lane * 16) = Kf[4];
          *(u32x2*)(xc + 1024 + lane * 8) = Vf[4][0]; *(u32x2*)(xc + 1536 + lane * 8) = Vf[4][1];
          Kf[0] = fix_qk(acch[0], acch[1], muh[0], 2);
        } else {
          asm volatile("" : "=v"(Kf[0]));
          f32x2 cl = csv;
          pin(cl);
          const f32x4 y0 = muh * cl[0] + acch[0], y1 = muh * cl[1] + acch[1];
          SB();
          *(u32x2*)(xc + 2048 + lane * 8) = (u32x2){pack2(y0[0], y0[1]), pack2(y0[2], y0[3])};
          *(u32x2*)(xc + 2560 + lane * 8) = (u32x2){pack2(y1[0], y1[1]), pack2(y1[2], y1[3])};
        }
        pin(mu2[0], mu2[1]);
#pragma unroll
        for (int i = 0; i < 3; ++i) Kf[i + 1] = fix_qk(acc[i][2], acc[i][3], mu2[i >> 1][i & 1], 2);
        wait_lgkm0();
        bar();
        TS(td);
        if (wmi == 1) Kf[0] = *(const u32x4*)(xc + lane * 16);
        Vf[0][0] = *(const u32x2*)(xc + (wmi == 0 ? 2048 : 1024) + lane * 8);
        Vf[0][1] = *(const u32x2*)(xc + (wmi == 0 ? 2560 : 1536) + lane * 8);
        const f32x4 z = {0.f, 0.f, 0.f, 0.f};
        f32x4 S[4][2];
        pin(mu2[0], mu2[1], csv);
        auto qk = [&](int i, int k) {
          SB();
          S[i][k] = __builtin_amdgcn_mfma_f32_16x16x32_bf16(__builtin_bit_cast(bf16x8, Kf[i + k]), __builtin_bit_cast(bf16x8, Qf[i]), z, 0, 0, 0);
          SB();
        };
#pragma unroll
        for (int i = 3; i >= 0; --i) {
          f32x4 y0 = cs[0] * mu2[i >> 1][i & 1] + acc[i][0], y1 = cs[1] * mu2[i >> 1][i & 1] + acc[i][1];
          SB();
          Qf[i] = (u32x4){pack2(y0[0], y0[1]), pack2(y0[2], y0[3]), pack2(y1[0], y1[1]), pack2(y1[2], y1[3])};
          pin(Qf[i]);
          SB();
        }
        f32x2 yv[4];
        auto fill = [&](auto K0, auto N) {
          for_n<decltype(N)::value>([&](auto K) {
            constexpr int k = decltype(K0)::value + decltype(K)::value, i = k >> 3, h = (k >> 1) & 1, c = k & 1;
            SB();
            if constexpr (k & 4) { Vf[i + 1][h][c] = pack2(yv[k & 3][0], yv[k & 3][1]); }
            else {
              yv[k & 3] = (f32x2){muv[i][2 * c], muv[i][2 * c + 1]} * csv[h] + (f32x2){acc[i][4 + h][2 * c], acc[i][4 + h][2 * c + 1]};
            }
            if constexpr (k > 8 && k < 12) { SB(); pin(yv[(k - 1) & 3]); }
          });
          SB();
        };
        const bool par = LANE_IN(M_PAR), own0 = LANE_IN(M_OWN0), own1 = LANE_IN(M_OWN1);
        f32x2 l[2][2];
        {
          auto pick = [](bool c, float a, float b) { return c ? a : b; };
          float sp[4][4];
          auto half = [&](int i) {
#pragma unroll
            for (int k = 0; k < 4; ++k) sp[i][k] = pick(par, S[i][k >> 1][2 * (k & 1) + 1], S[i][k >> 1][2 * (k & 1)]);
            SB();
          };
          auto tile = [&](int i) {
            l[0][i >> 1][i & 1] = pick(own0, sp[i][2], sp[i][0]);
            l[1][i >> 1][i & 1] = pick(own1, sp[i][3], sp[i][1]);
            SB();
          };
          qk(3, 1); qk(3, 0); qk(2, 1); qk(2, 0); qk(1, 1); qk(1, 0); qk(0, 1); qk(0, 0);
          half(3); half(2); half(1); tile(3); tile(2); half(0); tile(1); tile(0);
        }
#pragma unroll
        for (int i = 3; i >= 0; --i) { l[i & 1][i >> 1] *= fk[i & 1][i >> 1]; SB(); }
        f32x2 t[2];
        t[1] = __builtin_elementwise_maximum(l[0][1], l[1][1]);
        SB();
        {
          const u64 first = tmA == 0 && wmi == 0 ? ~0ull : 0ull;
          if (LANE_IN(first & ~M_OWN1)) l[1][0][0] = -1.4426950e9f;
          if (LANE_IN(first & ~M_OWN0)) l[0][0][0] = -1.4426950e9f;
        }
        SB();
        t[0][1] = __builtin_elementwise_maximum(l[0][0][1], l[1][0][1]);
        SB();
        t[0][0] = __builtin_elementwise_maximum(l[0][0][0], l[1][0][0]);
        auto all4 = [&](auto K0, f32x2 (&v)[2], auto f, auto mid, auto&& more) {
          constexpr int k0 = decltype(K0)::value;
          auto U = [](float a) { return __float_as_uint(a); };
          auto F = [](u32 a) { return __uint_as_float(a); };
          fill(IC<k0>{}, IC<1>{});
          const auto b = __builtin_amdgcn_permlane32_swap(U(v[0][1]), U(v[1][1]), false, false);
          SB();
          const auto a = __builtin_amdgcn_permlane32_swap(U(v[0][0]), U(v[1][0]), false, false);
          fill(IC<k0 + 1>{}, IC<1>{});
          const float fb = f(F(b[0]), F(b[1]));
          SB();
          const float fa = f(F(a[0]), F(a[1]));
          fill(IC<k0 + 2>{}, IC<2>{});
          const auto c = __builtin_amdgcn_permlane16_swap(U(fa), U(fb), false, false);
          fill(IC<k0 + 4>{}, IC<1>{});
          float t0 = f(F(c[0]), F(c[1])), t1 = f(F(c[1]), F(c[0]));
          pin(t0, t1);
          more(IC<0>{});
          t0 = mid(t0); t1 = mid(t1);
          fill(IC<k0 + 5>{}, IC<2>{});
          const auto d = __builtin_amdgcn_permlane16_swap(U(t0), U(t1), false, false);
          fill(IC<k0 + 7>{}, IC<1>{});
          u32 d0 = d[0], d1 = d[1], d2 = d[0], d3 = d[1];
          pin(d2, d3);
          fill(IC<k0 + 8>{}, IC<2>{});
          const auto e0 = __builtin_amdgcn_permlane32_swap(d0, d2, false, false);
          const auto e1 = __builtin_amdgcn_permlane32_swap(d1, d3, false, false);
          fill(IC<k0 + 10>{}, IC<2>{});
          v[0] = (f32x2){F(e0[0]), F(e1[0])}; v[1] = (f32x2){F(e0[1]), F(e1[1])};
        };
        all4(IC<0>{}, t, [](float a, float b) { return __builtin_elementwise_maximum(a, b); }, [](float a) { return a; }, [](auto) {});
#pragma unroll
        for (int i = 0; i < 4; ++i) { l[i & 1][i >> 1] -= t[i >> 1]; SB(); if (i == 0) pin(yv[3]); }
#pragma unroll
        for (int i = 0; i < 4; ++i) {
          l[i & 1][i >> 1] = (f32x2){__builtin_amdgcn_exp2f(l[i & 1][i >> 1][0]), __builtin_amdgcn_exp2f(l[i & 1][i >> 1][1])};
          SB();
        }
        t[0] = l[0][0] + l[1][0];
        l[0][0] *= ivk[0][0];
        SB();
        t[1] = l[0][1] + l[1][1];
        l[1][0] *= ivk[1][0];
        all4(IC<12>{}, t, [](float a, float b) { return a + b; }, [](float a) { return __builtin_amdgcn_rcpf(a); },
             [&](auto) { SB(); l[0][1] *= ivk[0][1]; l[1][1] *= ivk[1][1]; SB(); });
#pragma unroll
        for (int i = 0; i < 4; ++i) { l[i & 1][i >> 1] *= t[i >> 1]; SB(); }
        u32 pw[4];
#pragma unroll
        for (int i = 0; i < 4; ++i) pw[i] = pack2(l[0][i >> 1][i & 1], l[1][i >> 1][i & 1]);
        u32x10 V[2];
#pragma unroll
        for (int i = 0; i < 10; ++i) { V[0][i] = Vf[i >> 1][0][i & 1]; V[1][i] = Vf[i >> 1][1][i & 1]; }
        pin(V[0], V[1]);
        u32x4 P[4];
        f32x4 O[4][2];
        for_n<7>([&](auto I) {
          constexpr int i = decltype(I)::value, m = i - 1, e = i - 3;
          u32x4 o;
          auto pv = [&](auto H) {
            constexpr int h = decltype(H)::value;
            SB();
            if constexpr (i < 4) {
              P[i][2 * h] = __builtin_amdgcn_perm(pw[i], pw[i], sel[2 * h]);
              P[i][2 * h + 1] = __builtin_amdgcn_perm(pw[i], pw[i], sel[2 * h + 1]);
            }
            if constexpr (e >= 0) { o[2 * h] = pack2(O[e][h][0], O[e][h][1]); o[2 * h + 1] = pack2(O[e][h][2], O[e][h][3]); }
            SB();
            if constexpr (m >= 0 && m < 4)
              O[m][h] = __builtin_amdgcn_mfma_f32_16x16x32_bf16(__builtin_bit_cast(bf16x8, __builtin_shufflevector(V[h], V[h], 2 * m, 2 * m + 1, 2 * m + 2, 2 * m + 3)),
                                                                __builtin_bit_cast(bf16x8, P[m]), z, 0, 0, 0);
            SB();
          };
          pv(IC<0>{}); pv(IC<1>{});
          if constexpr (e >= 0) st16(o, (OA4 ? oa[e] : LANE_IN(~0ull << (e == 3 ? 56 : 24 - 8 * e)) ? oa[1] : oa[0]) + qoff(e, 0));
        });
        SB();
        TS(te);
        start(aco, c[0], false);
        TS(tf);
        publish(IC<0>{}, 4 * pair, tmA, tnA);
        TS(tg);
        if (pair == 0) level(LVA, IC<1>{}, IC<LXA>{});
        TS(th);
        TSW(td, 35); TSW(te, 34);
        STAMP(0, 16 * pair + 3);
      }
      {
        FRESH_LANE();
        bf16x8 fA[2][2][2], fB[2][2][4];
        u32 pA, pB;
        so = 2 * SLOT;
        for_n<4>([&](auto E) { dest(E, 2 * pair + 1); });
        kloop<16, 16, 12, 0>(so,
          [&](int s_, auto) { pA = lane * 16 + wmi * 2048 + s_; pB = lane * 16 + wni * 4096 + s_; pin2(pA, pB); },
          [&](auto SET, auto Q) {
            constexpr int set = decltype(SET)::value, q = decltype(Q)::value, s = q / 6, m = q % 6;
            if constexpr (m < 2) dsrd<s * 4096 + m * 1024>(fA[set][s][m], pA);
            else dsrd<8192 + s * 8192 + (m - 2) * 1024>(fB[set][s][m - 2], pB);
          },
          [&](auto SET, auto M) {
            constexpr int set = decltype(SET)::value & 1, m = decltype(M)::value;
            constexpr int s = (SNK & 4) ? (m ^ (m >> 1)) & 1 : m >> 3, j = (SNK & 4) ? m >> 2 : (m >> 1) & 3, i = (SNK & 4) ? ((m >> 1) ^ (m >> 2)) & 1 : m & 1;
            mfma(aco[i][j], fB[set][s][j], fA[set][s][i]);
          },
          [&](int t) { if (t < 0) STAMP(0, 16 * pair + 5); }, [](auto) {});
        STAMP(0, 16 * pair + 6);
        finish(aco, 2 * pair + 1, 4 * pair + 1);
        if (pair == 0) level(LVB, IC<2>{}, IC<LXB>{});
      }
      {
        f32x4 acc[4][7];
        f32x2 iv[2], mv[2];
        f32x4 tb[2][4];
        u32 aT, ho[4];
        auto tables = [&](auto J) {
          constexpr int j = decltype(J)::value;
          dsrd4<128 * j>(tb[j & 1][0], aT); dsrd4<128 * j + L_BS - L_CS1>(tb[j & 1][1], aT);
          if constexpr (j < 3) { dsrd4<128 * j + 64>(tb[j & 1][2], aT); dsrd4<128 * j + 64 + L_BS - L_CS1>(tb[j & 1][3], aT); }
        };
        {
        FRESH_LANE();
#pragma unroll
        for (int i = 0; i < 4; ++i)
#pragma unroll
          for (int j = 0; j < 7; ++j) acc[i][j] = zero4();
        bf16x8 fA[2][4], fB[2][7];
        u32 pA[3], pB[3];
#pragma unroll
        for (int k = 0; k < 3; ++k) {
          pA[k] = lane * 16 + wmi * 4096 + 2 * k * SLOT; pB[k] = lane * 16 + wni * 7168 + 2 * k * SLOT;
          pin2(pA[k], pB[k]);
        }
        kloop6<28, 11, 1>(
          [&](auto SL, auto Q) {
            constexpr int set = decltype(SL)::value & 1, k = decltype(SL)::value >> 1, q = decltype(Q)::value;
            if constexpr (q < 4) dsrd<set * SLOT + q * 1024>(fA[set][q], pA[k]);
            else dsrd<set * SLOT + 8192 + (q - 4) * 1024>(fB[set][q - 4], pB[k]);
          },
          [&](auto SET, auto M) {
            constexpr int set = decltype(SET)::value, m = decltype(M)::value, j0 = m >> 2, i0 = (SNK & 1) && (j0 & 1) ? 3 - (m & 3) : m & 3;
            constexpr int i = i0, j = j0;
            mfma(acc[i][j], fB[set][j], fA[set][i]);
          }, [&](int t) { if (t < 0) STAMP(0, 16 * pair + 9); },
          [&](auto M) {
            constexpr int m = decltype(M)::value;
            if constexpr (m == 0) { aT = L_INV + 64 + wmi * 256 + (fresh_lane(tid) & 15) * 4; pin(aT); }
            if constexpr (m == 1) { dsrd2<0, 16>(iv[0], aT); dsrd2<32, 48>(iv[1], aT); dsrd2<144, 160>(mv[0], aT); dsrd2<176, 192>(mv[1], aT); }
            if constexpr (m == 2) { aT = L_CS1 + wni * 448 + (fresh_lane(tid) & 48); pin(aT); }
            if constexpr (m == 3) tables(IC<0>{});
            if constexpr (m >= 4 && m < 12 && m % 2 == 0) { ho[m / 2 - 2] = qoff(m / 2 - 2, fresh_lane(tid)); pin(ho[m / 2 - 2]); }
          });
        }
        FRESH_LANE();
        STAMP(0, 16 * pair + 10);
        const u32 hbase = OFF_H + pair * HS + hpan(tmA, tnA >> 3, c13);
        auto act = [&](const f32x4& a0, const f32x4& a1, const f32x4& cs0, const f32x4& bs0, const f32x4& cs1, const f32x4& bs1, int i0, int i1) {
          f32x4 y0 = cs0 * mv[i0 >> 1][i0 & 1] + bs0, y1 = cs1 * mv[i1 >> 1][i1 & 1] + bs1;
          SB();
          y0 = a0 * iv[i0 >> 1][i0 & 1] + y0; y1 = a1 * iv[i1 >> 1][i1 & 1] + y1;
          SB();
          const f32x2 y[4] = {{y0[0], y0[1]}, {y0[2], y0[3]}, {y1[0], y1[1]}, {y1[2], y1[3]}};
          return gelu_tanh8(y);
        };
        for_n<3>([&](auto JP) {
          constexpr int jp = decltype(JP)::value;
          wait_lgkm0();
          f32x4 (&t)[4] = tb[jp & 1];
          pin(t[0], t[1], t[2], t[3]);
          if constexpr (jp == 0) pin(iv[0], iv[1], mv[0], mv[1]);
          tables(IC<jp + 1>{});
          u32 hb = hbase + aq(hpos(tnA, wni * 4 + jp), wmi);
          asm volatile("" : "+s"(hb));
#pragma unroll
          for (int i = 0; i < 4; ++i) {
            u32 a = hb + ho[i];
            pin(a);
            const u32x4 v = act(acc[i][2 * jp], acc[i][2 * jp + 1], t[0], t[1], t[2], t[3], i, i);
            st16(v, a);
            SB();
            if (4 * jp + i == 3 + PUBJ) publish(IC<PUBJ>{}, 4 * pair + 2, tmA, tnA);
          }
        });
        {
          wait_lgkm0();
          pin(tb[1][0], tb[1][1]);
#pragma unroll
          for (int i = 0; i < 4; i += 2) {
            u32 a = hbase + aq(hpos(tnA, 3), wmi) + qoff(i + (g & 1), (2 * wni + (g >> 1)) * 16 + r);
            pin(a);
            const u32x4 u = act(acc[i][6], acc[i + 1][6], tb[1][0], tb[1][1], tb[1][0], tb[1][1], i, i + 1);
            const auto s0 = __builtin_amdgcn_permlane16_swap(u[0], u[2], false, false);
            const auto s1 = __builtin_amdgcn_permlane16_swap(u[1], u[3], false, false);
            const u32x4 v = {s0[0], s1[0], s0[1], s1[1]};
            st16(v, a);
            SB();
          }
        }
        STAMP(0, 16 * pair + 11);
      }
      {
        FRESH_LANE();
        f32x4 acc[4][4];
        SB();
        const u32x4 bb[2] = {*(const u32x4*)(smem + L_B2 + wni * 128 + g * 16), *(const u32x4*)(smem + L_B2 + wni * 128 + 64 + g * 16)};
#pragma unroll
        for (int i = 2; i < 4; ++i)
#pragma unroll
          for (int j = 0; j < 4; ++j) acc[i][j] = zero4();
        start(acc, bb, true);
        bf16x8 fA[2][2][4], fB[2][2][4];
        u32 pA, pB;
        const u32 xc = wmi * 16384 + wni * 4096;
        wait_vm<6>();
        so = 0;
        u32x4 xv[4];
        auto look = [&]() {
          asm volatile("buffer_load_dwordx4 %0, %4, %5, %6 offen sc1\n\tbuffer_load_dwordx4 %1, %4, %5, %6 offen offset:1024 sc1\n\t"
                       "buffer_load_dwordx4 %2, %4, %5, %6 offen offset:2048 sc1\n\tbuffer_load_dwordx4 %3, %4, %5, %6 offen offset:3072 sc1"
                       : "=&v"(xv[0]), "=&v"(xv[1]), "=&v"(xv[2]), "=&v"(xv[3]) : "v"(lane * 16), "s"(rs), "s"(xbox(pair, 1) + xc) : "memory");
        };
        kloop<28, 32, 16, 1>(so,
          [&](int s_, auto) { pA = lane * 16 + wmi * 2048 + s_; pB = lane * 16 + wni * 4096 + s_; pin2(pA, pB); },
          [&](auto SET, auto Q) {
            constexpr int set = decltype(SET)::value, q = decltype(Q)::value, s = q >> 3, m = q & 7;
            if constexpr (m < 4) dsrd<s * 8192 + (m >> 1) * 4096 + (m & 1) * 1024>(fA[set][s][m], pA);
            else dsrd<16384 + s * 8192 + (m - 4) * 1024>(fB[set][s][m - 4], pB);
          },
          [&](auto SET, auto M) {
            constexpr int set = decltype(SET)::value & 1, m = decltype(M)::value, o = decltype(SET)::value > 1 ? 0 : SNK >> 3;
            constexpr int s = o == 0 ? (m >> 3) & 1 : o == 1 ? m >> 4 : o == 2 ? m & 1 : (m ^ (m >> 1)) & 1;
            constexpr int j = o == 0 ? (m >> 1) & 3 : o == 1 ? (m >> 2) & 3 : m >> 3;
            constexpr int i = o == 0 ? (m & 1) + (m < 16 ? 2 : 0) : o == 1 ? ((m & 4) ? 3 - (m & 3) : m & 3) : o == 3 && (m & 8) ? 3 - ((m >> 1) & 3) : (m >> 1) & 3;
            mfma(acc[i][j], fB[set][s][j], fA[set][s][i]);
          },
          [&](int t) { if (t < 0) { STAMP(0, 16 * pair + 13); } else wait_vm<0>(); },
          [&](auto M) {
            constexpr int k = decltype(M)::value - SND - 1;
            if constexpr (k >= 0 && k < 8 && k % 2 == 0) {
              const f32x4 a = acc[2][k >> 1], b = acc[3][k >> 1];
              const u32x4 v = {pack2(a[0], a[1]), pack2(a[2], a[3]), pack2(b[0], b[1]), pack2(b[2], b[3])};
              st16(__builtin_elementwise_min(v, (u32x4){~1u, ~1u, ~1u, ~1u}), xbox(pair, 0) + xc + lane * 16 + k * 512);
            }
            if constexpr (decltype(M)::value == (TW2 ? LK : 31)) look();
          });
        STAMP(0, 16 * pair + 14);
        for_n<4>([&](auto E) { dest(E, pair == 0 ? 2 : 4); });
#pragma nounroll
        for (int spin = 0; spin < (1 << 22); ++spin) {
          asm volatile("s_waitcnt vmcnt(0)" : "+v"(xv[0]), "+v"(xv[1]), "+v"(xv[2]), "+v"(xv[3]) :: "memory");
          const u32x4 o = __builtin_elementwise_max(__builtin_elementwise_max(xv[0], xv[1]), __builtin_elementwise_max(xv[2], xv[3]));
          if (!__any(max(max(o[0], o[1]), max(o[2], o[3])) == ~0u)) break;
          look();
        }
#pragma unroll
        for (int k = 0; k < 4; ++k) {
          acc[0][k] += (f32x4){bf_lo(xv[k][0]), bf_hi(xv[k][0]), bf_lo(xv[k][1]), bf_hi(xv[k][1])};
          acc[1][k] += (f32x4){bf_lo(xv[k][2]), bf_hi(xv[k][2]), bf_lo(xv[k][3]), bf_hi(xv[k][3])};
        }
        finish(acc, pair == 0 ? 2 : 4, 4 * pair + 3);
      }
    }
  } else if (wid == 4) {
    {
      u32 vo[4];
      x_lanes(vo);
      wait_vm<36>();
      bar();
      bar();
#pragma nounroll
      for (int U = 3; U < 16; ++U) {
        wait_vm<18>();
        bar();
        x_half(IC<0>{}, U, vo);
        bar();
        x_half(IC<1>{}, U, vo);
      }
      wait_vm<18>(); bar(); bar();
      wait_vm<0>(); bar(); bar(); bar();
    }
    auto phase = [&](auto NSL_, auto HALO_, u32 src, u32 hsrc, int ph  , int tm, int tmh) {
      constexpr int NSL = decltype(NSL_)::value, HALO = decltype(HALO_)::value;
      constexpr int BIG = NSL == 28, PA = BIG ? 16 : 8, PT = PA + HALO, D = BIG ? DB : NSL == 16 ? DN : DW, X = BIG ? XA2 : 0;
      FRESH_LANE();
      u32 vo[4];
#pragma unroll
      for (int e = 0; e < 4; ++e) vo[e] = qoff(e, lane) - e * 1024;
      u64 pv[NP];
      pin(vo[0], vo[1], vo[2], vo[3]);
      asm volatile("" : "+s"(src));
      { constexpr int z = BIG ? Z2 : NSL == 16 ? ZO : HALO ? ZQ : Z1; if constexpr (z > 0) __builtin_amdgcn_s_sleep(z); }
      if constexpr (NSL == 16 && ZO0 > 0) { if (ph == 0) __builtin_amdgcn_s_sleep(ZO0); }
      if constexpr (NSL == 32 && !HALO && Z10 > 0) { if (ph == 1) __builtin_amdgcn_s_sleep(Z10); }
      poll_n(flag(ph + 1, (lane & 16) ? tmh : tm, BIG ? 8 * (tnB & 1) + ((rotA >> 1) + (r < D + X ? r : 0))
                                                 : NSL == 16 ? (rotB + (r < DN ? r : 0)) & 15 : rotA + (r & (DW > 4 ? 3 : 1))), token(), pv);
      if constexpr (!BIG && !HALO) *(volatile LDS_AS u32*)(smem + L_CNT + 4) = ph + 1;
      STAMP(1, 4 * (ph + 1));
      const u32 flip = BIG ? (tnB & 1) * 16384 : 0;
      stream<NSL, PT, D, X>(so, 0,
        [&](int u) {
          u = BIG ? (u < 8 ? u ^ (rotA >> 1) : u) : NSL == 16 ? (u + rotB) & 15 : u ^ (2 * rotA);
          const u32 ko = aq(NSL == 32 ? u : 2 * u, 0), l = so;
          next_slot<BIG>(so);
          return Slot{ko, src + ko + flip, l};
        },
        [&](auto E, const Slot& a) {
          constexpr int e = decltype(E)::value;
          if constexpr (e == PA) DMA(rs, (LDS_AS void*)(a.l0 + 8192), 16, vo[3], hsrc + a.k, 0, 16);
          else DMA(rs, (LDS_AS void*)(a.l0 + (e >> 2) * 4096), 16, vo[e & 3],
                   e < 4 ? a.a0 : src + a.k + (NSL == 16 ? aq(e >> 2, 0) : aq(e >> 3, (e >> 2) & 1) + ((e & 4) ? -flip : flip)),
                   (e & 3) * 1024, 16);
        }, [&]() { bar(); });
#pragma unroll
      for (int i = 0; i < NP; ++i) asm volatile("" :: "v"(pv[i]));
    };
#pragma nounroll
    for (int pair = 0; pair < 2; ++pair) {
      const int tmh = tmA > 0 ? tmA - 1 : 0;
      const u32 xq = OFF_X + 2 * pair * XS;
      if (pair) phase(IC<32>{}, IC<1>{}, xq + xpan(tmA, c13), xq + xpan(tmh, c13) + 16384 + 3072, 3, tmA, tmh);
      side();
      bar();
      phase(IC<16>{}, IC<0>{}, OFF_O + pair * XS + xpan(tmB, c13) + aq(0, tnB & 1), 0, 4 * pair, tmB, tmB);
      phase(IC<32>{}, IC<0>{}, xq + XS + xpan(tmA, c13), 0, 4 * pair + 1, tmA, tmA);
      bar();
      so = 0;
      phase(IC<28>{}, IC<0>{}, OFF_H + pair * HS + hpan(tmB, tnB & 1, c13), 0, 4 * pair + 2, tmB, tmB);
      bar();
      so = 0;
    }
  } else if (wid == 5) {
    FRESH_LANE();
    auto phase = [&](auto NSL_, auto PB_, auto AUX_, int u0, int add, int rot, __amdgpu_buffer_rsrc_t rb, const u32 (&vo)[4], u32 boff,
                     u32 src, u32 step, auto&& q) {
      constexpr int NSL = decltype(NSL_)::value, PB = decltype(PB_)::value;
      stream<NSL, PB, (NSL == 28 ? DB : 5), (NSL == 28 ? XB2 : NSL == 16 ? XBO : 0)>(so, u0,
        [&](int u) {
          u = NSL == 28 ? (u < 8 ? u ^ rot : u) : ((u + add) & (NSL - 1)) ^ rot;
          const u32 l = so + boff;
          next_slot<NSL == 28>(so);
          return Slot{0, src + u * step, l};
        },
        [&](auto I, const Slot& a) {
          constexpr int i = decltype(I)::value;
          DMA(rb, (LDS_AS void*)(a.l0 + (i >> 2) * 4096), 16, vo[i & 3], a.a0 + q(i >> 2), (i & 3) * 1024, decltype(AUX_)::value);
        }, [&]() { bar(); });
    };
    auto tiles = [&](auto NSL_, auto PB_, u32 boff, u32 src) {
      constexpr int NSL = decltype(NSL_)::value, PB = decltype(PB_)::value;
      u32 vo[4];
#pragma unroll
      for (int e = 0; e < 4; ++e) vo[e] = NSL == 28 && D2 ? lane * 16 : qoff(e, lane) - e * 1024;
      constexpr u32 Q = NSL == 28 ? Q2 : QS;
      phase(NSL_, PB_, IC<16>{}, 0, NSL == 16 ? rotB : 0, NSL == 28 ? rotA >> 1 : NSL == 16 ? 0 : 2 * rotA, rs, vo, boff, src,
            PB == 16 ? 2 * Q : 4 * Q, [](int i) { return PB == 16 ? (i >> 1) * Q + (i & 1) * ((NSL == 28 ? 112 : 32) * Q) : i * Q; });
    };
    so = 5 * SLOT;
#pragma nounroll
    for (int pair = 0; pair < 2; ++pair) {
      {
        u32 vo[4];
        q_lanes(vo);
        phase(IC<32>{}, IC<12>{}, IC<0>{}, pair ? 0 : 5, pair ? 0 : 4 * rot0, pair ? 2 * rotA : 0, rsrc(wqkv, DM * 3 * DM * 2), vo, 9216,
              tnA * 128, 32 * 6144, [](int i) { return i * 2048; });
      }
      if (pair == 0) bar();
      bar();
      tiles(IC<16>{}, IC<16>{}, 8192, OFF_WO + (tnB & 14) * (32 * QS));
      if (pair == 0) poll(flag(F_W1, r, (tnA & 14) + (g & 1)), token());
      tiles(IC<32>{}, IC<14>{}, 8192, OFF_W1 + tnA * (32 * 4 * QS));
      bar();
      so = 0;
      if (pair == 0) poll(flag(F_W2, 8 * (tnB & 1) + (r & 7), (tnB & 14) + (r >> 3)), token());
      tiles(IC<28>{}, IC<16>{}, 16384, OFF_W2 + ((tnB & 14) * 112 + (tnB & 1) * 56) * Q2);
      bar();
      so = 0;
    }
  } else if (wid == 6) {
    FRESH_LANE();
    const int rr = lane >> 3, c16 = (lane & 7) * 16;
    u32x4 U[7][4], F[4];
    auto u_load = [&](auto I) {
      constexpr int i = decltype(I)::value;
#pragma unroll
      for (int e = 0; e < 4; ++e) {
        if constexpr (i < 2)
          U[(i + 5) % 7][e] = __builtin_bit_cast(u32x4, __builtin_amdgcn_raw_buffer_load_b128(rsrc(wo, DM * DM * 2),
              ((rr & 3) + 16 * (rr >> 2)) * 2048 + c16, ((2 * tmA + i) * 32 + 4 * e) * 2048 + (tnA ^ (4 * i)) * 128, UAUX));
        else if constexpr (i < 9)
          U[(i + 5) % 7][e] = __builtin_bit_cast(u32x4, __builtin_amdgcn_raw_buffer_load_b128(rsrc(w1, DM * FF * 2),
              rr * 7168 + c16, ((2 * tmA + (tnA & 1)) * 32 + 8 * e) * 7168 + (7 * (tnA >> 1) + i - 2) * 128, UAUX));
        else
          U[(i + 5) % 7][e] = __builtin_bit_cast(u32x4, __builtin_amdgcn_raw_buffer_load_b128(rsrc(w2, DM * FF * 2),
              rr * 2048 + c16, ((7 * tmA + i - 9) * 32 + 8 * e) * 2048 + tnA * 128, UAUX));
      }
    };
    auto u_write = [&](auto I) {
      constexpr int i = decltype(I)::value;
#pragma unroll
      for (int e = 0; e < 4; ++e) {
        const u32x4 v = U[(i + 5) % 7][e];
        *(u32x4*)(smem + L_IM + e * 1024 + rr * 128 + (c16 ^ ((rr & 2) << 5))) = (e & 1) ? (u32x4){v[2], v[3], v[0], v[1]} : v;
      }
    };
    auto u_store = [&](auto I) {
      constexpr int i = decltype(I)::value;
      const u32 sw = ((r & 8) << 3) | ((g & 1) << 3);
      const char* im = smem + L_IM + (8 * g + (r >> 2)) * 128;
      for_n<4>([&](auto E) {
        constexpr int e = decltype(E)::value;
        constexpr bool split = i >= 2 && i < 9 && (2 * (i - 2) + (e >> 1)) % 7 == 3;
        const char* p = im + (((e >> 1) * 64 + (split ? (e & 1) * 32 + (r & 3) * 8 : (r & 3) * 16 + (e & 1) * 8)) ^ sw);
        const u32x2 lo = __builtin_bit_cast(u32x2, __builtin_amdgcn_ds_read_tr16_b64_v4i16((LDS_AS s16x4*)p));
        const u32x2 hi = __builtin_bit_cast(u32x2, __builtin_amdgcn_ds_read_tr16_b64_v4i16((LDS_AS s16x4*)(p + 512)));
        F[e] = (u32x4){lo[0], lo[1], hi[0], hi[1]};
      });
      for_n<4>([&](auto E) {
        constexpr int e = decltype(E)::value;
        if constexpr (i < 2) st16(F[e], OFF_WO + ((tnA ^ (4 * i)) * 32 + 2 * tmA + i) * QS + qoff(e, lane));
        else if constexpr (i < 9) {
          constexpr int f = w1_frag(i, e);
          st16(F[e], OFF_W1 + ((((tnA & 14) + (f >> 4)) * 32 + 2 * tmA + (tnA & 1)) * 4 + ((f & 15) >> 2)) * QS + qoff(f & 3, lane));
        } else st16(F[e], OFF_W2 + (tnA * 112 + (tmA >> 3) * 56 + hpos(tmA, i - 9)) * Q2 + (D2 ? e * 1024 + lane * 16 : qoff(e, lane)));
      });
    };
    auto u_sums = [&](auto I) {
      constexpr int i = decltype(I)::value;
      constexpr u32 K = w1_frag(i, 0) | (w1_frag(i, 1) << 8) | (w1_frag(i, 2) << 16) | (w1_frag(i, 3) << 24);
      f32x4 cs = {0.f, 0.f, 0.f, 0.f};
#pragma unroll
      for (int e = 0; e < 4; ++e) {
        const u32 o = (r >> 2) == e ? 0x3f803f80u : 0u;
        cs = __builtin_amdgcn_mfma_f32_16x16x32_bf16(__builtin_bit_cast(bf16x8, (u32x4){o, o, o, o}), __builtin_bit_cast(bf16x8, F[e]), cs, 0, 0, 0);
      }
      const u32 f = (K >> (8 * g)) & 255;
      __builtin_amdgcn_raw_buffer_store_b32(__float_as_uint(cs[0]), rs,
          OFF_CS1 + ((((tnA & 14) + (f >> 4)) * 32 + 2 * tmA + (tnA & 1)) * 256 + (f & 15) * 16 + r) * 4, 0, 16);
    };
    u64 tok = 0;
    auto up = [&](int set) { st8(tok, lane == 0, flag(set, tmA, tnA)); };
    f32x4 cv[32], ca[2];
    auto cs_load = [&](u32 src, int p0, int p1) {
#pragma unroll
      for (int p = p0; p < p1; ++p)
        cv[p] = __builtin_bit_cast(f32x4, __builtin_amdgcn_raw_buffer_load_b128(rs, lane * 16 + (p & 3) * 1024, src + (p >> 2) * 4096, 16));
    };
    auto cs_add = [&](int p0, int p1) {
#pragma unroll
      for (int p = p0; p < p1; ++p) {
        asm volatile("" : "+v"(cv[p]));
        if (p < 2) ca[p] = cv[p];
        else {
#pragma unroll
          for (int k = 0; k < 4; ++k) {
            float t = ca[p & 1][k];
            const float u = cv[p][k];
            asm("v_add_f32 %0, %0, %1" : "+v"(t) : "v"(u));
            ca[p & 1][k] = t;
          }
        }
      }
      asm volatile("" : "+v"(ca[0]), "+v"(ca[1]));
    };
    u64 vf = 0;
    const u32 fo = flag(F_WO, r, ((tnB & 14) + (g & 1)) ^ (2 * (g & 2)));
    __builtin_amdgcn_s_sleep(PFW0);
    u_load(IC<0>{});
    u_load(IC<1>{});
    for_n<34>([&](auto S) {
      constexpr int s = decltype(S)::value;
      bar();
      if constexpr (s == TOKB) { tok = token(); asm volatile("" : "+s"(tok)); }
      if constexpr (s >= HL && s < HL + 4) {
        if constexpr ((s - HL) % 2 == 0) u_write(IC<(s - HL) / 2>{}); else u_store(IC<(s - HL) / 2>{});
      }
      if constexpr (s == HL + 3 + FLD) { wait_vm<0>(); up(F_WO); }
      if constexpr (s == G0 - 4) vf = ldf(fo);
      if constexpr (s == G0 - 1) { if (!__all(vf == tok)) poll(fo, tok); }
      if constexpr (s == G0) for_n<4>([&](auto I) { u_load(IC<2 + decltype(I)::value>{}); });
      if constexpr (s == G0 + 1) for_n<3>([&](auto I) { u_load(IC<6 + decltype(I)::value>{}); });
    });
    static_assert(TOKB < HL && HL + 3 + FLD < G0 - 4 && G0 + 1 < 33, "schedule");
    if (HSL) __builtin_amdgcn_s_sleep(HSL);
    for_n<7>([&](auto I) {
      constexpr int i = 2 + decltype(I)::value;
      if constexpr (i - 2 >= HN) bar();
      u_write(IC<i>{});
      u_store(IC<i>{});
      u_sums(IC<i>{});
    });
    for_n<HB>([&](auto) { bar(); });
    wait_vm<0>();
    up(F_W1);
    static_assert(7 - HN + HB <= G1 + 1, "schedule");
#pragma nounroll
    for (int u = 7 - HN + HB; u <= G1; ++u) bar();
    for_n<7>([&](auto I) {
      u_load(IC<9 + decltype(I)::value>{});
      if constexpr (decltype(I)::value == 1 || decltype(I)::value == 3) bar();
    });
#pragma nounroll
    for (int u = G1 + 3; u < 16 + H2; ++u) bar();
    for_n<14>([&](auto S) {
      constexpr int s = decltype(S)::value, i = 9 + s / 2;
      bar();
      if constexpr (s % 2 == 0) u_write(IC<i>{}); else u_store(IC<i>{});
    });
    for_n<12>([&](auto S) {
      constexpr int s = decltype(S)::value;
      bar();
      if constexpr (s == 3) { wait_vm<16>(); up(F_W2); }
      if constexpr (s < 6) cs_load(OFF_CS1 + tnA * 32768, s * 16 / 3, (s + 1) * 16 / 3);
      if constexpr (s >= 8) cs_add(8 * (s - 8), 8 * (s - 7));
    });
    static_assert(H2 + 14 + 12 <= 31, "schedule");
    *(f32x4*)(smem + L_CS1 + lane * 16) = ca[0] + ca[1];
    wait_lgkm0();
    QUIT();
  } else {
    FRESH_LANE();
    const int tmh = tmA > 0 ? tmA - 1 : 0;
    u64 tok;
    auto bars = [&](int n) {
#pragma nounroll
      for (int u = 0; u < n; ++u) bar();
    };
    auto ror8 = [](float v) { return __uint_as_float(__builtin_amdgcn_mov_dpp(__float_as_uint(v), 0x128, 0xf, 0xf, false)); };
    auto put = [&](int xi, float mean, float m2, bool on, int row) {
      const float sd = __builtin_amdgcn_sqrtf(bfr(bfr(m2 * (1.f / DM)) + LN_EPS));
      st8(pack2(mean, sd) | (tok << 32), on, flag(F_LN + xi, tmA, tnA) + row * 8);
    };
    auto reduce = [&](int xi) {
      const u32 src = OFF_ST + xi * 524288 + span(tmA, c13) + (tnA >> 2) * 16384 + (tnA & 3) * 2048, tag = (u32)tok;
      u32x4 a, b;
      auto ld = [&]() {
        asm volatile("" ::: "memory");
        a = __builtin_bit_cast(u32x4, __builtin_amdgcn_raw_buffer_load_b128(rs, lane * 16, src, 16));
        b = __builtin_bit_cast(u32x4, __builtin_amdgcn_raw_buffer_load_b128(rs, lane * 16 + 1024, src, 16));
      };
      ld();
      bar(); bar(); bar();
#pragma nounroll
      for (int spin = 0; spin < (1 << 22); ++spin) {
        if (__all((((a[1] ^ tag) | (a[3] ^ tag)) | ((b[1] ^ tag) | (b[3] ^ tag))) == 0)) break;
        __builtin_amdgcn_s_sleep(1);
        ld();
      }
      float m = __uint_as_float(a[0]) + __uint_as_float(b[0]);
      m = xsum(m + ror8(m)) * (1.f / 16.f);
      const float d0 = __uint_as_float(a[0]) - m, d1 = __uint_as_float(b[0]) - m;
      float q = __uint_as_float(a[2]) + __uint_as_float(b[2]) + 64.f * (d0 * d0 + d1 * d1);
      asm volatile("" : "+v"(m), "+v"(q));
      bar();
      asm volatile("" : "+v"(m), "+v"(q));
      put(xi, m, xsum(q + ror8(q)), lane < 8, lane);
    };
    auto gather = [&](auto H, int xi) {
      constexpr int n = 2 + decltype(H)::value;
      const u32 tag = (u32)tok, f0 = flag(F_LN + xi, tmA, lane >> 3) + (lane & 7) * 8;
      const u32 f[3] = {f0, f0 + 1024, flag(F_LN + xi, tmh, 14 + (r >> 3)) + (lane & 7) * 8};
      u32x2 v[3];
      auto ld = [&]() {
#pragma unroll
        for (int h = 0; h < n; ++h) v[h] = __builtin_bit_cast(u32x2, ldf(f[h]));
      };
      ld();
      bar(); bar(); bar();
#pragma nounroll
      for (int spin = 0; spin < (1 << 22); ++spin) {
        if (__all(((v[0][1] ^ tag) | (v[1][1] ^ tag) | (v[n - 1][1] ^ tag)) == 0)) break;
        __builtin_amdgcn_s_sleep(1);
        ld();
      }
#pragma unroll
      for (int h = 0; h < n; ++h) {
        const float iv = __builtin_amdgcn_rcpf(bf_hi(v[h][0]));
        const int o = h < 2 ? 64 + 256 * h + lane * 4 : r * 4;
        *(float*)(smem + L_INV + o) = iv;
        *(float*)(smem + L_MNV + o) = n == 3 ? -bf_lo(v[h][0]) : -bf_lo(v[h][0]) * iv;
      }
      wait_lgkm0();
    };
    auto guard = [&](u32 f, int K, auto&& lim_, auto&& step) {
      int b = 0, lim = 0;
#pragma nounroll
      for (int spin = 0; spin < (1 << 22); ++spin) {
        const u64 v = ldf(f);
        if (b < lim) { bar(); step(b++); } else __builtin_amdgcn_s_sleep(GSL);
        if (b < lim) { bar(); step(b++); }
        lim = lim_((u32)__ballot(v == tok));
        if (lim >= 100) break;
        lim = min(K, lim);
      }
#pragma nounroll
      while (b < K) { bar(); step(b++); }
    };
    auto ahead = [](int msk, int spp, int D) {
      return [=](u32 m) { const int n = __builtin_ctz(~(m & (m >> 16))); return n >= 16 ? 100 : (n & msk) * spp - D; };
    };
    const __amdgpu_buffer_rsrc_t rx = rsrc(x, SEQ * DM * 2), rq = rsrc(wqkv, DM * 3 * DM * 2);
    u32 wx, ww;
    {
      const int n = idx * 64 + lane, m = idx * 48 + (lane < 48 ? lane : 47), k = (m * 5462) >> 16, p = m - 12 * k;
      wx = (n & 511) * 2048 + ((n >> 9) & 1) * 128 + ((idx >> 4) * 1024 + (xcd >> 2) * 512) * 2048;
      ww = k * 6144 + (p >> 2) * 2048 + (p & 3) * 128 + (xcd & 3) * 512;
    }
    {
      const u64 ep = ldf(OFF_EP);
      u16 bv[4];
#pragma unroll
      for (int h = 0; h < 4; ++h) {
        const int t = min(lane + 64 * h, 223), f = t >> 4, rho = t & 15, wn = f / 7, j = f % 7;
        bv[h] = b1[tnA * 224 + (j == 6 ? 96 + 16 * wn + rho : (4 * wn + (j >> 1)) * 32 + 8 * (rho >> 2) + 4 * (j & 1) + (rho & 3))];
      }
      DMA(rsrc(b2, DM * 2), (LDS_AS void*)(smem + L_B2), 4, lane * 4, (tnB & 14) * 128, 0, 0);
      pin2(wx, ww);
      __builtin_amdgcn_s_sleep(XW);
      bf16x8 xf[16];
#pragma unroll
      for (int e = 0; e < 16; ++e)
        xf[e] = __builtin_bit_cast(bf16x8, __builtin_amdgcn_raw_buffer_load_b128(rx, (r >> 1) * 2048 + (r & 1) * 64 + g * 16 + e * 128,
            (tmA * 128 + tnA * 8) * 2048, 0));
      auto probe = [&](int tn) { return flag(F_PR, tmA ^ (4 * (idx & 1)), tn); };
      const u32x4 one4 = {0x3f803f80u, 0x3f803f80u, 0x3f803f80u, 0x3f803f80u};
      const bf16x8 ones = __builtin_bit_cast(bf16x8, one4);
      f32x4 sm[2], sq[2];
      u32 tlb;
      u64 vt;
      for_n<LG0>([&](auto S) {
        constexpr int s = decltype(S)::value;
        bar();
        if constexpr (s == TOKB - 1) {
          __builtin_amdgcn_raw_buffer_store_b32(0, rs, blockIdx.x < NVOTE ? probe(tnA ^ 4) + (lane & 31) * 4 : 0x80000000u, 0, 16);
#pragma unroll
          for (int h = 0; h < 4; ++h) ((float*)(smem + L_BS))[lane + 64 * h] = bf_lo(bv[h]);
          tok = ((u64)(u32)__builtin_amdgcn_readfirstlane((u32)ep) | ((u64)(u32)__builtin_amdgcn_readfirstlane((u32)(ep >> 32)) << 32))
                * 6364136223846793005ull + 1442695040888963407ull;
          *(LDS_AS u32x4*)(smem + L_CNT) = (u32x4){0u, 0u, (u32)tok, (u32)(tok >> 32)};
          wait_lgkm0();
          asm volatile("s_load_dword %0, %1, 0x0" : "=s"(tlb) : "s"(ws + OFF_EP + 128) : "memory");
        }
        if constexpr (s >= PF0 && (s - PF0) % PFS == 0 && (s - PF0) / PFS < 8 - PFJ) {
          const int j = (PFJ + (s - PF0) / PFS + rot0) & 7;
          DMA(rx, (LDS_AS void*)(smem + L_PF), 4, wx, j * 256, 0, 0);
          DMA(rq, (LDS_AS void*)(smem + L_PF), 4, ww, j * (128 * 6144), 0, 0);
        }
        if constexpr (s >= XP && s < XP + 4) {
#pragma unroll
          for (int e = 4 * (s - XP); e < 4 * (s - XP) + 4; ++e) {
            const f32x4 z = {0.f, 0.f, 0.f, 0.f};
            asm volatile("" : "+v"(xf[e]));
            sm[e & 1] = __builtin_amdgcn_mfma_f32_16x16x32_bf16(ones, xf[e], e < 2 ? z : sm[e & 1], 0, 0, 0);
            sq[e & 1] = __builtin_amdgcn_mfma_f32_16x16x32_bf16(xf[e], xf[e], e < 2 ? z : sq[e & 1], 0, 0, 0);
          }
        }
        if constexpr (s >= XP && s <= XP + 4) asm volatile("" : "+v"(sm[0]), "+v"(sm[1]), "+v"(sq[0]), "+v"(sq[1]));
        if constexpr (s == XP + 4) {
          const f32x4 q4 = sq[0] + sq[1];
          float q = (r & 2) ? ((r & 1) ? q4[3] : q4[2]) : ((r & 1) ? q4[1] : q4[0]), t = sm[0][0] + sm[1][0];
          q += __uint_as_float(__builtin_amdgcn_mov_dpp(__float_as_uint(q), 0xb1, 0xf, 0xf, false));
          t += __uint_as_float(__builtin_amdgcn_mov_dpp(__float_as_uint(t), 0xb1, 0xf, 0xf, false));
          const float m = t * (1.f / DM);
          put(0, m, fmaxf(__builtin_fmaf(-t, m, q), 0.f), g == (r >> 2) && !(r & 1), r >> 1);
        }
        if constexpr (s == PRB) {
          wait_lgkm0();
          asm volatile("" :: "s"(tlb));
          if (blockIdx.x < NVOTE) {
            u64 t0, t1;
            u32 v;
            asm volatile("s_memtime %0\n\ts_waitcnt lgkmcnt(0)\n\ts_load_dword %2, %3, 0x0\n\ts_waitcnt lgkmcnt(0)\n\t"
                         "s_memtime %1\n\ts_waitcnt lgkmcnt(0)" : "=&s"(t0), "=&s"(t1), "=&s"(v) : "s"(ws + probe(tnA)) : "memory");
            const u32 t = min((u32)t1 - (u32)t0, 4095u);
            st8(tok ^ t, lane == 0, flag(F_XC, 0, blockIdx.x));
          }
        }
        if constexpr (s == LG0 - 4) vt = ldf(flag(F_XC, 0, r));
      });
      static_assert(XP >= TOKB && PRB > XP + 4 && LG0 - 4 > PRB && LG0 + 3 <= 31 && L_CNT % 16 == 0, "schedule");
#pragma nounroll
      for (int spin = 0; spin < (1 << 22); ++spin) {
        if (__all((vt ^ tok) < 4096)) break;
        __builtin_amdgcn_s_sleep(1);
        vt = ldf(flag(F_XC, 0, r));
      }
      const u32 t = (u32)(vt ^ tok);
      const u64 slower = __ballot(t > (u32)__builtin_amdgcn_mov_dpp(t, 0x128, 0xf, 0xf, false));
      const u32 c = __builtin_popcount((u32)slower & 255) > 4 ? 8192 : 0;
      *(volatile LDS_AS u32*)(smem + L_C13) = c;
      gather(IC<1>{}, 0);
      c13 = c;
      bars(34 - CLB - (LG0 + 3));
    }
#pragma nounroll
    for (int pair = 0; pair < 2; ++pair) {
#pragma unroll
      for (int e = 0; e < 16; ++e)
        st16(((u32x4){~0u, ~0u, ~0u, ~0u}), xbox(pair, 0) + (e + (e & 8)) * 1024 + lane * 16);
      bars(CLB);
      wait_vm<0>();
      if (ZO + ZG > 0) __builtin_amdgcn_s_sleep(ZO + ZG);
      if (ZO0 > 0 && pair == 0) __builtin_amdgcn_s_sleep(ZO0);
      guard(flag(4 * pair + 1, tmB, (rotB + r) & 15), 16, ahead(15, 1, DN), [](int) {});
      if (Z1 + ZG > 0) __builtin_amdgcn_s_sleep(Z1 + ZG);
      if (Z10 > 0 && pair == 0) __builtin_amdgcn_s_sleep(Z10);
      guard(flag(4 * pair + 2, tmA, (rotA + r) & 15), KW1, ahead(14, 2, DW), [](int) {});
      reduce(2 * pair + 1);
      bars(LG - (KW1 + 4));
      gather(IC<0>{}, 2 * pair + 1);
      bars(33 - (LG + 3));
      const u32 w3 = lane < 56 ? OFF_W2 + ((idx >> 1) * 112 + (tnB & 1) * 56 + (idx & 1) * 14) * 4096 + lane * 1024 + 384 : 0x80000000u;
      if (D2 & 2) {
        if (pair == 0) poll(flag(F_W2, 8 * (tnB & 1) + (r & 7), idx >> 1), tok);
        DMA(rs, (LDS_AS void*)(smem + L_PF), 4, w3, 0, 0, 16);
      }
      if (Z2 + ZG > 0) __builtin_amdgcn_s_sleep(Z2 + ZG);
      guard(flag(lane < 8 ? 4 * pair + 3 : lane < 16 ? F_2ND + pair : F_3RD + pair, tmB, 8 * (tnB & 1) + ((lane < 8 ? r ^ (rotA >> 1) : r) & 7)), 13,
            [](u32 m) { const int n = __builtin_ctz(~m | (1u << 24)); return n >= 24 ? 100 : min(n, 16) - 3; },
            [&](int b) {
              if (b == 0 || b == 3) st8(tok, lane == 0, flag(b ? F_3RD + pair : F_2ND + pair, tmA, tnA));
              if ((D2 & 2) && b == D2B) DMA(rs, (LDS_AS void*)(smem + L_PF), 4, w3, 28 * 4096, 0, 16);
            });
      if (pair) break;
      bars(WQ - 13);
      for_n<29 - WQ>([&](auto S) {
        constexpr int s = decltype(S)::value;
        if constexpr (s < 4) {
          DMA(rq, (LDS_AS void*)(smem + L_PF), 4, ww, ((2 * s + (rotA >> 1)) & 7) * (128 * 6144), 0, 0);
          DMA(rq, (LDS_AS void*)(smem + L_PF), 4, ww, ((2 * s + 1 + (rotA >> 1)) & 7) * (128 * 6144), 0, 0);
        }
        bar();
      });
      if (ZQ + ZG > 0) __builtin_amdgcn_s_sleep(ZQ + ZG);
      guard(flag(4, (g & 1) ? tmh : tmA, (rotA + r) & 15), KW1, ahead(14, 2, DW), [](int) {});
      st8(tok, lane == 0 && blockIdx.x == 0, OFF_EP);
      reduce(2);
      bars(LG - (KW1 + 4));
      gather(IC<1>{}, 2);
      bars(33 - CLB - (LG + 3));
      static_assert(WQ <= 25 && LG + 3 <= 31, "schedule");
    }
    QUIT();
  }
#endif
}

extern "C" void kernel_launch(void* const* d_in, const int* in_sizes, int n_in, void* d_out, int out_size,
                              void* d_ws, size_t ws_size, hipStream_t stream) {
  const u16* x0 = (const u16*)d_in[0];
  const u16* wqkv = (const u16*)d_in[1];
  const u16* wo = (const u16*)d_in[2];
  const u16* w1 = (const u16*)d_in[4];
  const u16* b1 = (const u16*)d_in[5];
  const u16* w2 = (const u16*)d_in[6];
  const u16* b2 = (const u16*)d_in[7];
  (void)hipFuncSetAttribute((const void*)mega_k, hipFuncAttributeMaxDynamicSharedMemorySize, LDS_BYTES);
  mega_k<<<256, 512, LDS_BYTES, stream>>>((char*)d_ws + (ws_size - WS_USED - WS_ALIGN), x0, wqkv, wo, w1, b1, w2, b2, (u16*)d_out);
}
```

```cpp
#include <hip/hip_runtime.h>
#include <stdint.h>
#include <type_traits>
#include <utility>

typedef unsigned short u16;
typedef unsigned int u32;
typedef unsigned long long u64;
typedef __bf16 bf16x8 __attribute__((ext_vector_type(8)));
typedef __bf16 bf16x2 __attribute__((ext_vector_type(2)));
typedef short s16x4 __attribute__((ext_vector_type(4)));
typedef float f32x4 __attribute__((ext_vector_type(4)));
typedef float f32x2 __attribute__((ext_vector_type(2)));
typedef u32 u32x4 __attribute__((ext_vector_type(4)));
typedef u32 u32x2 __attribute__((ext_vector_type(2)));
typedef u32 u32x10 __attribute__((ext_vector_type(10)));
typedef int i32x4 __attribute__((ext_vector_type(4)));
typedef int i32x2 __attribute__((ext_vector_type(2)));
#define LDS_AS __attribute__((address_space(3)))
#define GLB_AS __attribute__((address_space(1)))

constexpr int SEQ = 2048, DM = 1024, FF = 3584;
constexpr float LN_EPS = 1e-5f;

#define DN 4
#define DW 5
#define UAUX 2
#define PFW0 30
#define PF0 4
#define PFS 2
#define PFJ 2
#define CQP 5
#define HL 8
#define FLD 3
#define XW 30
#define TOKB 3
#define XP 8
#define LG0 24
#define LG 25
#define WQ 24
#define G0 27
#define G1 12
#define H2 4
#define PUBJ 3
#define CB 1
#define KW1 11
#define CLB 1
#define SND 12
#define RST 9
#define XB2 1
#define XA2 1
#define XBO 1
#define CW 1
#define PRB 13
#define CQG 22
#define R3W 1
#define HSL 10
#define HN 7
#define HB 3
#define LXA 1
#define LXB 1
#define ZO0 42
#define Z10 24
#define GSL 1
#define PNS 0
#define NCP 4
#define RSEL 0
#define NC2 4
#define LVA 16
#define LVB 6
#define DB 3
#define SNK 31
#define D2 0
#define D2B 6
#define RL2 15
#define RL1 10
#define RLQ 16
#define ROT 7
#ifndef NVOTE
#define NVOTE 16
#endif
#define NP 1
#define ZO 25
#define Z1 25
#define Z2 55
#define ZQ 50
#define TW2 1
#define OA4 0
#define LK 42
#define ZG 0
#define PS 2
constexpr u32 MB = 1u << 20;
constexpr u32 QS = 5120, Q2 = D2 ? 4096 : QS;
constexpr u32 XS = 8 * MB, HS = 32 * MB;
constexpr u32 OFF_WO = 0;
constexpr u32 OFF_W1 = 3 * MB;
constexpr u32 OFF_W2 = 13 * MB;
constexpr u32 OFF_CS1 = 22 * MB;
constexpr u32 OFF_FL = 25 * MB;
constexpr u32 OFF_EP = OFF_FL + 26 * 32768;
constexpr u32 OFF_DBG = 26 * MB;
constexpr u32 OFF_XC = 28 * MB;
constexpr u32 OFF_X = 28 * MB;
constexpr u32 OFF_O = OFF_X + 4 * XS;
constexpr u32 OFF_H = OFF_O + 2 * XS;
constexpr u32 OFF_ST = OFF_H + 2 * HS;
constexpr u32 WS_USED = OFF_ST + 2 * MB, WS_ALIGN = 4 * MB;
#define HAS_ALIGN
constexpr int F_WO = 27, F_W1 = 28, F_W2 = 29, F_2ND = 0, F_3RD = 2, F_XC = 18, F_PR = 19, F_LN = 22;
__device__ __forceinline__ u32 qoff(u32 e, u32 slot) {
  return slot * 16 + (e * 1152 + (e == 3 ? 128 : 0)) + (slot >= (e == 3 ? 56 : 24 - 8 * e) ? 128 : 0);
}
__device__ __forceinline__ u32 hpos(u32 t, u32 j) {
  return (j & 2) == 0 ? 16 * (j & 1) + 2 * (t & 7) + (j >> 2) : 32 + 3 * (t & 7) + (j & 1) + (j >> 2) * 2;
}
__device__ __forceinline__ u32 aq(u32 kg, u32 half) { return kg * 32768 + (kg & 1) * 3072 + half * 16384; }
__device__ __forceinline__ u32 ppair(u32 tm) { return (tm & 3) + (tm >> 3) * 4; }
__device__ __forceinline__ u32 xpan(u32 tm, u32 c13) { return ppair(tm) * MB + (((((tm >> 2) ^ (tm >> 1)) & 1) << 13) ^ c13); }
__device__ __forceinline__ u32 hpan(u32 tm, u32 kh, u32 c13) {
  return ppair(tm) * (4 * MB) + (((kh ^ tm) & 1) << 21) + (((((tm >> 2) ^ kh ^ tm) & 1) << 13) ^ c13);
}
__device__ __forceinline__ u32 span(u32 tm, u32 c13) { return ppair(tm) * 65536 + (((tm & 4) << 11) ^ c13); }

constexpr int SLOT = 24576, NRING = 6, RING = SLOT * NRING;
constexpr int L_INV = RING, L_MNV = L_INV + 576, L_CNT = L_MNV + 576, L_TOK = L_CNT + 8, L_C13 = L_TOK + 8;
constexpr int L_CSQ = RING + 1280, L_CS1 = L_CSQ + 1024, L_BS = L_CS1 + 1024;
constexpr int L_XC = L_BS + 1024;
constexpr int L_IM = L_XC + 6144;
constexpr int L_B2 = L_IM + 4096, L_PF = L_B2 + 256;
constexpr int LDS_BYTES = L_PF + 256;

__device__ __forceinline__ float bf_lo(u32 w) { return __uint_as_float(w << 16); }
__device__ __forceinline__ float bf_hi(u32 w) { return __uint_as_float(w & 0xffff0000u); }
__device__ __forceinline__ u32 pack2(float lo, float hi) {
  f32x2 v = {lo, hi};
  bf16x2 r = __builtin_convertvector(v, bf16x2);
  return __builtin_bit_cast(u32, r);
}
__device__ __forceinline__ float bfr(float x) { return bf_lo(pack2(x, 0.f) & 0xffffu); }
__device__ __forceinline__ u32x4 gelu_tanh8(const f32x2 (&x)[4]) {
  const float c0 = -2.3022081983f, c1 = -0.1029432396f;
  f32x2 t[4];
#define STEP(e) do { _Pragma("unroll") for (int k = 0; k < 4; ++k) t[k] = e; __builtin_amdgcn_sched_barrier(0); } while (0)
  STEP(x[k] * x[k]);
  STEP(t[k] * c1 + c0);
  STEP(x[k] * t[k]);
  STEP(((f32x2){__builtin_amdgcn_exp2f(t[k][0]), __builtin_amdgcn_exp2f(t[k][1])}));
  STEP(t[k] + 1.f);
  STEP(((f32x2){__builtin_amdgcn_rcpf(t[k][0]), __builtin_amdgcn_rcpf(t[k][1])}));
  STEP(x[k] * t[k]);
#undef STEP
  return (u32x4){pack2(t[0][0], t[0][1]), pack2(t[1][0], t[1][1]), pack2(t[2][0], t[2][1]), pack2(t[3][0], t[3][1])};
}
template <int N_> __device__ __forceinline__ void wait_vm() { asm volatile("s_waitcnt vmcnt(%0)" ::"n"(N_) : "memory"); }
__device__ __forceinline__ void wait_lgkm0() { asm volatile("s_waitcnt lgkmcnt(0)" ::: "memory"); }
__device__ __forceinline__ void bar() {
  __builtin_amdgcn_sched_barrier(0);
  asm volatile("s_barrier" ::: "memory");
  __builtin_amdgcn_sched_barrier(0);
}
template <int V> using IC = std::integral_constant<int, V>;
template <typename F, int... I>
__device__ __forceinline__ void for_seq(F&& f, std::integer_sequence<int, I...>) { (f(IC<I>{}), ...); }
template <int N, typename F> __device__ __forceinline__ void for_n(F&& f) { for_seq(f, std::make_integer_sequence<int, N>{}); }

__device__ __forceinline__ float xsum(float x) {
#if defined(__HIP_DEVICE_COMPILE__)
  auto a = __builtin_amdgcn_permlane16_swap(__float_as_uint(x), __float_as_uint(x), false, false);
  x = __uint_as_float(a[0]) + __uint_as_float(a[1]);
  auto b = __builtin_amdgcn_permlane32_swap(__float_as_uint(x), __float_as_uint(x), false, false);
  x = __uint_as_float(b[0]) + __uint_as_float(b[1]);
#endif
  return x;
}
__device__ __forceinline__ float xmax(float x) {
#if defined(__HIP_DEVICE_COMPILE__)
  auto a = __builtin_amdgcn_permlane16_swap(__float_as_uint(x), __float_as_uint(x), false, false);
  x = fmaxf(__uint_as_float(a[0]), __uint_as_float(a[1]));
  auto b = __builtin_amdgcn_permlane32_swap(__float_as_uint(x), __float_as_uint(x), false, false);
  x = fmaxf(__uint_as_float(b[0]), __uint_as_float(b[1]));
#endif
  return x;
}
#define st16(v, off) __builtin_amdgcn_raw_buffer_store_b128(__builtin_bit_cast(i32x4, v), rs, (off), 0, 16)
__device__ __forceinline__ float sum8(const u32x4& f) {
  float s = bf_lo(f[0]) + bf_hi(f[0]);
#pragma unroll
  for (int k = 1; k < 4; ++k) { s += bf_lo(f[k]); s += bf_hi(f[k]); }
  return s;
}
__device__ __forceinline__ float fold32(float a, float b) {
#if defined(__HIP_DEVICE_COMPILE__)
  const auto x = __builtin_amdgcn_permlane32_swap(__float_as_uint(a), __float_as_uint(b), false, false);
  a = __uint_as_float(x[0]) + __uint_as_float(x[1]);
#endif
  return a;
}
__device__ __forceinline__ float fold16(float a, float b) {
#if defined(__HIP_DEVICE_COMPILE__)
  const auto x = __builtin_amdgcn_permlane16_swap(__float_as_uint(a), __float_as_uint(b), false, false);
  a = __uint_as_float(x[0]) + __uint_as_float(x[1]);
#endif
  return a;
}

__device__ __forceinline__ float row_parts(const u32x4 (&c)[2][2]) {
  float res = 0.f;
#if defined(__HIP_DEVICE_COMPILE__)
#define SB_() __builtin_amdgcn_sched_barrier(0)
  f32x2 v[2][8], t[2][4];
  float m[2], q[2];
#pragma unroll
  for (int k = 0; k < 16; ++k) v[k >> 3][k & 7] = (f32x2){bf_lo(c[k >> 3][(k >> 2) & 1][k & 3]), bf_hi(c[k >> 3][(k >> 2) & 1][k & 3])};
  SB_();
#pragma unroll
  for (int k = 0; k < 8; ++k) t[k >> 2][k & 3] = v[k >> 2][k & 3] + v[k >> 2][4 + (k & 3)];
  SB_();
#pragma unroll
  for (int k = 0; k < 4; ++k) t[k >> 1][k & 1] += t[k >> 1][2 + (k & 1)];
  SB_();
  t[0][0] += t[0][1]; t[1][0] += t[1][1];
  SB_();
  m[0] = t[0][0][0] + t[0][0][1]; m[1] = t[1][0][0] + t[1][0][1];
  SB_();
  const f32x2 mm = (f32x2){m[0], m[1]} * (1.f / 16.f);
  SB_();
#pragma unroll
  for (int k = 0; k < 16; ++k) v[k >> 3][k & 7] -= mm[k >> 3];
  const auto x = __builtin_amdgcn_permlane16_swap(__float_as_uint(mm[0]), __float_as_uint(mm[1]), false, false);
  SB_();
#pragma unroll
  for (int k = 0; k < 8; ++k) t[k >> 2][k & 3] = v[k >> 2][k & 3] * v[k >> 2][k & 3];
  const float d = __uint_as_float(x[0]) - __uint_as_float(x[1]);
  float h = __uint_as_float(x[0]) + __uint_as_float(x[1]), h2 = h;
  asm volatile("" : "+v"(h2));
  SB_();
#pragma unroll
  for (int k = 0; k < 8; ++k) t[k >> 2][k & 3] = v[k >> 2][4 + (k & 3)] * v[k >> 2][4 + (k & 3)] + t[k >> 2][k & 3];
  const auto y = __builtin_amdgcn_permlane32_swap(__float_as_uint(h), __float_as_uint(h2), false, false);
  SB_();
#pragma unroll
  for (int k = 0; k < 4; ++k) t[k >> 1][k & 1] += t[k >> 1][2 + (k & 1)];
  const float e = __uint_as_float(y[0]) - __uint_as_float(y[1]), d8 = 8.f * d;
  SB_();
  t[0][0] += t[0][1]; t[1][0] += t[1][1];
  const float mean = (__uint_as_float(y[0]) + __uint_as_float(y[1])) * 0.125f, e2 = e + e;
  SB_();
  q[0] = t[0][0][0] + t[0][0][1]; q[1] = t[1][0][0] + t[1][0][1];
  const float w = __builtin_fmaf(d8, d, e2 * e);
  SB_();
  const auto z = __builtin_amdgcn_permlane16_swap(__float_as_uint(q[0]), __float_as_uint(q[1]), false, false);
  res = fold32(mean, __uint_as_float(z[0]) + __uint_as_float(z[1]) + w);
#undef SB_
#endif
  return res;
}

__device__ __forceinline__ int fresh_lane(int tid) { int l = tid & 63; asm volatile("" : "+v"(l)); return l; }
__device__ __forceinline__ f32x4 zero4() {
  f32x4 z = {0.f, 0.f, 0.f, 0.f};
  asm volatile("" : "+v"(z));
  return z;
}
template <int BIG = 0> __device__ __forceinline__ void next_slot(int& so) {
  if constexpr (BIG) so = (so + 32768) & 131071; else { so += SLOT; if (so == RING) so = 0; }
}

#define DMA __builtin_amdgcn_raw_ptr_buffer_load_lds
#define SB() __builtin_amdgcn_sched_barrier(0)
__device__ __forceinline__ void mfma(f32x4& c, const bf16x8& a, const bf16x8& b) {
  asm volatile("v_mfma_f32_16x16x32_bf16 %0, %1, %2, %0" : "+v"(c) : "v"(a), "v"(b));
}
template <int OFF> __device__ __forceinline__ void dsrd(bf16x8& d, u32 addr) {
  asm volatile("ds_read_b128 %0, %1 offset:%2" : "=v"(d) : "v"(addr), "n"(OFF));
}
template <int OFF> __device__ __forceinline__ void dstr(bf16x8& d, u32 addr) {
  u32x2 lo, hi;
  asm volatile("ds_read_b64_tr_b16 %0, %2 offset:%3\n\tds_read_b64_tr_b16 %1, %2 offset:%4"
               : "=&v"(lo), "=&v"(hi) : "v"(addr), "n"(OFF), "n"(OFF + 512));
  const u32x4 v = {lo[0], lo[1], hi[0], hi[1]};
  d = __builtin_bit_cast(bf16x8, v);
}
template <int OFF> __device__ __forceinline__ void dstr1(u32x2& d, u32 addr) {
  asm volatile("ds_read_b64_tr_b16 %0, %1 offset:%2" : "=v"(d) : "v"(addr), "n"(OFF));
}
template <int OFF> __device__ __forceinline__ void dsrd1(float& d, u32 addr) {
  asm volatile("ds_read_b32 %0, %1 offset:%2" : "=v"(d) : "v"(addr), "n"(OFF));
}
template <int O0, int O1> __device__ __forceinline__ void dsrd2(f32x2& d, u32 addr) {
  asm volatile("ds_read2_b32 %0, %1 offset0:%2 offset1:%3" : "=v"(d) : "v"(addr), "n"(O0), "n"(O1));
}
template <int OFF> __device__ __forceinline__ void dsrd4(f32x4& d, u32 addr) {
  asm volatile("ds_read_b128 %0, %1 offset:%2" : "=v"(d) : "v"(addr), "n"(OFF));
}
template <class T> __device__ __forceinline__ void pin(T& a) { asm volatile("" : "+v"(a)); }
template <class T, class... R> __device__ __forceinline__ void pin(T& a, R&... r) { pin(a); pin(r...); }
template <class F> constexpr u64 lanes(F f) {
  u64 m = 0;
  for (int l = 0; l < 64; ++l) m |= (u64)(f(l & 15, l >> 4) ? 1 : 0) << l;
  return m;
}
constexpr u64 M_PAR = lanes([](int r, int) { return r & 1; });
constexpr u64 M_OWN0 = lanes([](int r, int g) { return 4 * g + (r & 1) <= r; });
constexpr u64 M_OWN1 = lanes([](int r, int g) { return 4 * g + (r & 1) + 2 <= r; });
#define LANE_IN(m) __builtin_amdgcn_inverse_ballot_w64(m)
__device__ __forceinline__ void pin1(u32& a) { asm volatile("" : "+v"(a)); }
__device__ __forceinline__ void pin2(u32& a, u32& b) { asm volatile("" : "+v"(a), "+v"(b)); }
constexpr int rd_at(int m, int n, int last) {
  for (int q = 0; q < n; ++q) if (q * last / (n - 1) == m) return q;
  return -1;
}
template <int NSL, int NM, int NR, int ENDB, class AD, class RD, class MM, class HK, class TL>
__device__ __forceinline__ void kloop(int& so, AD&& ad, RD&& rd, MM&& mm, HK&& hook, TL&& tail) {
  constexpr int BIG = NM == 32;
  ad(so, IC<0>{});
  bar();
  for_n<NR>([&](auto Q) { rd(IC<0>{}, Q); });
  next_slot<BIG>(so);
  ad(so, IC<1>{});
  hook(-1);
  auto body = [&](auto S) {
    constexpr int s = decltype(S)::value;
    wait_lgkm0();
    bar();
    for_n<NM>([&](auto M) {
      constexpr int m = decltype(M)::value;
      mm(S, M);
      constexpr int RL = BIG ? RL2 : NR - 1, q = rd_at(m, NR, RL);
      if constexpr (q >= 0) rd(IC<1 - s>{}, IC<q>{});
      if constexpr (m == RL + 1) { next_slot<BIG>(so); asm volatile("" : "+s"(so)); }
      if constexpr (m == RL + 2) ad(so, S);
      SB();
    });
  };
#pragma nounroll
  for (int u = NSL / 2 - 1; u > 0; --u) {
    body(IC<0>{});
    body(IC<1>{});
    hook(NSL / 2 - 1 - u);
  }
  if constexpr (BIG && TW2) {
    wait_lgkm0();
    bar();
    for_n<16>([&](auto M) {
      constexpr int m = decltype(M)::value;
      mm(IC<2>{}, M);
      rd(IC<1>{}, IC<(m < 6 ? m + 2 : m < 12 ? m + 4 : m < 14 ? m - 12 : m - 6)>{});
      SB();
    });
    asm volatile("s_waitcnt lgkmcnt(10)" ::: "memory");
    SB();
    for_n<48>([&](auto T) {
      constexpr int t = decltype(T)::value;
      if constexpr (t == 8) { wait_lgkm0(); bar(); so = 0; }
      mm(IC<(t >> 4 == 1 ? 2 : 3)>{}, IC<(t < 32 ? t : t - 16)>{});
      SB(); tail(T); SB();
    });
  } else {
    body(IC<0>{});
    wait_lgkm0();
    if constexpr (ENDB) { bar(); so = 0; }
    SB();
    for_n<NM>([&](auto M) { mm(IC<3>{}, M); SB(); tail(M); SB(); });
  }
  asm volatile("s_nop 15");
  SB();
}

template <int NM, int NR, int ENDB, class RD, class MM, class HK, class TL>
__device__ __forceinline__ void kloop6(RD&& rd, MM&& mm, HK&& hook, TL&& tail) {
  bar();
  for_n<NR>([&](auto Q) { rd(IC<0>{}, Q); });
  hook(-1);
  auto body = [&](auto S) {
    constexpr int s = decltype(S)::value;
    wait_lgkm0();
    bar();
    for_n<NM>([&](auto M) {
      constexpr int m = decltype(M)::value;
      mm(IC<s & 1>{}, M);
      constexpr int q = rd_at(m, NR, NR == 11 ? RL1 : RLQ);
      if constexpr (q >= 0) rd(IC<(s + 1) % NRING>{}, IC<q>{});
      SB();
    });
  };
#pragma nounroll
  for (int u = 5; u > 0; --u) for_n<NRING>(body);
  body(IC<0>{});
  wait_lgkm0();
  if constexpr (ENDB) bar();
  SB();
  for_n<NM>([&](auto M) { mm(IC<1>{}, M); SB(); tail(M); SB(); });
  asm volatile("s_nop 15");
  SB();
}

struct Slot { u32 k, a0, l0; };
template <int NSL, int P, int D, int X, class PR, class FI, class BR>
__device__ __forceinline__ void stream(int& so, int u0, PR&& prep, FI&& fire, BR&& bar) {
#pragma nounroll
  for (int u = u0; u < D + X; ++u) {
    const Slot a = prep(u);
    if constexpr ((D + X) * P > 63) wait_vm<63 - P>();
    for_n<P>([&](auto I) { fire(I, a); });
  }
  auto next = [&](int u) {
    Slot a = prep(u);
    asm volatile("" : "+s"(a.k), "+s"(a.a0), "+s"(a.l0), "+s"(so));
    return a;
  };
  auto head = [&](const Slot& a) {
    wait_vm<((D - 1) * P < 59 ? (D - 1) * P : 59)>();
    bar();
    for_n<4>([&](auto I) { fire(I, a); });
    SB();
  };
  auto rest = [&](const Slot& a) {
    if constexpr (D * P > 63) wait_vm<67 - P>();
    for_n<P - 4>([&](auto I) { fire(IC<4 + decltype(I)::value>{}, a); });
  };
  if constexpr ((ROT >> (NSL == 28 ? 0 : NSL == 16 ? 1 : 2)) & 1) {
    Slot a = next(X + D);
    if constexpr (X) { wait_vm<(D * P < 63 ? D * P : 63)>(); bar(); }
    head(a);
#pragma nounroll
    for (int u = X + D + 1; u < NSL; ++u) {
      const Slot b = next(u);
      rest(a);
      head(b);
      a = b;
    }
    rest(a);
  } else {
    if constexpr (X) { wait_vm<(D * P < 63 ? D * P : 63)>(); bar(); }
#pragma nounroll
    for (int u = X + D; u < NSL; ++u) {
      const Slot a = next(u);
      head(a);
      rest(a);
    }
  }
  for_n<D>([&](auto M) { constexpr int n = (D - 1 - decltype(M)::value) * P; wait_vm<(n < 63 ? n : 63)>(); bar(); });
}

#define STAMP(w, i)
#define TS(v)
#define TSW(v, i)
#define TSP 1

constexpr int w1_frag(int i, int e) {
  const int gam = 2 * (i - 2) + (e >> 1), gl = gam % 7;
  return (gam / 7) * 16 + (gl == 3 ? 6 + 7 * (e & 1) : (gl < 3 ? gl * 2 : 7 + (gl - 4) * 2) + (e & 1));
}
__device__ __forceinline__ void warm_code(u32 n, u32 lds) {
#if defined(__HIP_DEVICE_COMPILE__)
  u64 pc;
  u32 back, len;
  asm volatile("s_getpc_b64 %0\n.Lcw%=:\n\ts_mov_b32 %1, .Lcw%=-mega_k\n\ts_mov_b32 %2, .Lcode_end-mega_k" : "=s"(pc), "=s"(back), "=s"(len));
  pc -= back;
  const u32x4 rc = {(u32)pc, (u32)(pc >> 32), len, 0x00020000u};
  asm volatile("s_mov_b32 %0, m0\n\ts_mov_b32 m0, %1\n\ts_nop 0\n\tbuffer_load_dword %2, %3, 0 offen lds\n\ts_mov_b32 m0, %0"
               : "=&s"(back) : "s"(lds), "v"(n * 128), "s"(rc));
#endif
}

template <int N> __device__ __forceinline__ void announce(u32 fl, __amdgpu_buffer_rsrc_t rs) {
  u32x2 tk;
  u32 a, o, f, n;
  asm volatile("s_mov_b64 exec, %9\n\tv_mov_b32 %1, %7\n\tds_read_b64 %0, %1 offset:8\n\tv_mbcnt_lo_u32_b32 %3, -1, 0\n\tv_mov_b32 %2, 1\n\t"
               "v_lshlrev_b32 %3, 18, %3\n\tv_add_u32 %3, %5, %3\n\ts_mov_b64 exec, 1\n\t"
               "s_waitcnt vmcnt(%8)\n\tds_add_rtn_u32 %1, %1, %2\n\ts_waitcnt lgkmcnt(0)\n\t"
               "v_readfirstlane_b32 %4, %1\n\ts_and_b32 %4, %4, 3\n\ts_cmp_eq_u32 %4, 3\n\ts_cbranch_scc0 .Lan%=\n\t"
               "s_mov_b64 exec, %9\n\tbuffer_store_dwordx2 %0, %3, %6, 0 offen sc1\n.Lan%=:\n\ts_mov_b64 exec, -1"
               : "=&v"(tk), "=&v"(a), "=&v"(o), "=&v"(f), "=&s"(n)
               : "s"(fl), "s"(rs), "n"(L_CNT), "n"(N), "n"((1 << NCP) - 1) : "memory", "scc");
  static_assert(L_TOK == L_CNT + 8, "layout");
}

extern "C" __global__ __launch_bounds__(512, 2) void mega_k(char* __restrict__ ws0, const u16* __restrict__ x,
                                                 const u16* __restrict__ wqkv, const u16* __restrict__ wo,
                                                 const u16* __restrict__ w1, const u16* __restrict__ b1,
                                                 const u16* __restrict__ w2, const u16* __restrict__ b2,
                                                 u16* __restrict__ out) {
#if defined(__HIP_DEVICE_COMPILE__)
  extern __shared__ __attribute__((aligned(16))) char smem[];
  const int tid = threadIdx.x;
  int wid = __builtin_amdgcn_readfirstlane(tid >> 6);
#define FRESH_LANE() const int lane = fresh_lane(tid), r = lane & 15, g = lane >> 4; (void)r; (void)g
  const int xcd = blockIdx.x & 7, idx = blockIdx.x >> 3;
  asm volatile(".subsection 1\n.Lcode_end:\n\t.subsection 0");
  if (CW & 1) warm_code(((tid & 63) * 8 + wid) * 32 + idx, L_PF);
  SB();
  char* const ws = ws0 + (-(u32)(uintptr_t)ws0 & (WS_ALIGN - 1));
  const int tmA = (idx >> 4) * 8 + (xcd >> 2) * 4 + ((idx >> 2) & 3), tnA = (xcd & 3) * 4 + (idx & 3);
  const int tmB = (idx >> 4) * 8 + (xcd >> 1) * 2 + ((idx >> 3) & 1), tnB = (idx & 7) * 2 + (xcd & 1);
  const int rotA = 8 - 8 * (xcd & 1), rotB = 4 * ((xcd + 1) & 3), rot0 = xcd;
  const __amdgpu_buffer_rsrc_t rs = __builtin_amdgcn_make_buffer_rsrc((void*)ws, 0, WS_USED, 0x00020000);
  u32 c13 = 0;
  auto side = [&]() { c13 = __builtin_amdgcn_readfirstlane(*(const volatile LDS_AS u32*)(smem + L_C13)); };
  auto flag = [&](int set, int tm, int tn) { return OFF_FL + ((((set * 16 + tm) * 16 + tn) * 128) ^ c13); };
  const u32 cpy = ((RSEL ? xcd : idx) & (NCP - 1)) * 262144 - MB;
  auto xbox = [&](int pair, int o) { return OFF_XC + pair * (4 * MB) + ((idx * 4 + (xcd & 3)) ^ o) * 32768 + (((((tmB >> 2) ^ (idx >> 4)) & 1) << 13) ^ c13); };
  auto rsrc = [&](const u16* p, u32 bytes) { return __builtin_amdgcn_make_buffer_rsrc((void*)p, 0, bytes, 0x00020000); };
  auto token = [&]() {
    const u32x2 tk = *(const volatile LDS_AS u32x2*)(smem + L_TOK);
    return (u64)(u32)__builtin_amdgcn_readfirstlane(tk[0]) | ((u64)(u32)__builtin_amdgcn_readfirstlane(tk[1]) << 32);
  };
  auto ldf = [&](u32 f) {
    asm volatile("" ::: "memory");
    return __builtin_bit_cast(u64, __builtin_amdgcn_raw_buffer_load_b64(rs, f, 0, 16));
  };
  auto poll = [&](u32 f, u64 tok) {
#pragma nounroll
    for (int spin = 0; spin < (1 << 22); ++spin) {
      if (__all(ldf(f) == tok)) break;
      __builtin_amdgcn_s_sleep(1);
    }
    asm volatile("" ::: "memory");
  };
  auto poll_n = [&](u32 f, u64 tok, u64 (&v)[NP]) {
    auto look = [&](u64& d) { asm volatile("buffer_load_dwordx2 %0, %1, %2, 0 offen sc1" : "=v"(d) : "v"(f), "s"(rs) : "memory"); };
#pragma unroll
    for (int i = 0; i < NP; ++i) { if (i) __builtin_amdgcn_s_sleep(PS); look(v[i]); }
#pragma nounroll
    for (int spin = 0; spin < (1 << 21); ++spin) {
      bool up = false;
#pragma unroll
      for (int i = 0; i < NP; ++i) {
        asm volatile("s_waitcnt vmcnt(%1)" : "+v"(v[i]) : "n"(NP - 1) : "memory");
        up = __all(v[i] == tok);
        if (up) break;
        if (PNS) __builtin_amdgcn_s_sleep(PNS);
        look(v[i]);
      }
      if (up) break;
    }
    asm volatile("" ::: "memory");
  };
  auto st8 = [&](u64 v, bool on, u32 f) {
    __builtin_amdgcn_raw_buffer_store_b64(__builtin_bit_cast(i32x2, v), rs, on ? f : 0x80000000u, 0, 16);
  };
  int so = 0;
#define QUIT() __builtin_amdgcn_endpgm()

  auto x_lanes = [&](u32 (&vo)[4]) {
    FRESH_LANE();
#pragma unroll
    for (int e = 0; e < 4; ++e)
      vo[e] = (8 * e + (lane >> 3)) * 2048 + (((lane & 7) ^ (4 * (e & 1) + (lane >> 4))) * 16) - e * 1024;
  };
  auto x_half = [&](auto H, int U, const u32 (&vo)[4]) {
    constexpr int h = decltype(H)::value;
    const __amdgpu_buffer_rsrc_t rx = rsrc(x, SEQ * DM * 2);
    const u32 sx = tmA * 128 * 2048, sh = (tmA > 0 ? tmA * 128 - 16 : 0) * 2048;
    U = (U + 2 * rot0) & 15;
    for_n<8>([&](auto E) {
      constexpr int e = decltype(E)::value;
      DMA(rx, (LDS_AS void*)(smem + so + h * SLOT + (e >> 2) * 4096), 16, vo[e & 3],
          sx + (h * 64 + (e >> 2) * 32) * 2048 + U * 128, (e & 3) * 1024, 0);
    });
    DMA(rx, (LDS_AS void*)(smem + so + SLOT + 21504), 16, vo[h], sh + U * 128, h * 1024, 0);
    if constexpr (h == 1) { so += 2 * SLOT; if (so == RING) so = 0; }
  };
  auto q_lanes = [&](u32 (&vo)[4]) {
    FRESH_LANE();
#pragma unroll
    for (int e = 0; e < 4; ++e)
      vo[e] = (8 * e + (lane >> 3)) * 6144 + (((lane & 7) ^ (2 * ((lane >> 4) & 1) + 4 * (e & 1))) * 16) - e * 1024;
  };
  if (wid == 6) {
    FRESH_LANE();
    DMA(rsrc(wqkv, DM * 3 * DM * 2), (LDS_AS void*)(smem + L_PF), 4,
        lane < 24 && !(xcd & 1) ? ((rot0 * 128 + idx * 8 + (lane & 7)) & 1023) * 6144 + (lane >> 3) * 2048 : 0x80000000u,
        (xcd & 3) * 512 + 384, 0, 0);
  }
  asm volatile("" : "+s"(wid));
  if (wid == 5) {
    u32 vo[4];
    q_lanes(vo);
#pragma nounroll
    for (int u = 0; u < 5; ++u)
      for_n<12>([&](auto I) {
        constexpr int i = decltype(I)::value;
        DMA(rsrc(wqkv, DM * 3 * DM * 2), (LDS_AS void*)(smem + u * SLOT + 9216 + (i >> 2) * 4096), 16, vo[i & 3],
            ((u + 4 * rot0) & 31) * (32 * 6144) + (i >> 2) * 2048 + tnA * 128, (i & 3) * 1024, 0);
      });
  }
  asm volatile("" : "+s"(wid));
  if (wid == 4) {
    u32 vo[4];
    x_lanes(vo);
#pragma nounroll
    for (int U = 0; U < 3; ++U) { x_half(IC<0>{}, U, vo); x_half(IC<1>{}, U, vo); }
  }
  so = 0;
  asm volatile("" : "+s"(wid));

  if (wid < 4) {
    auto publish = [&](auto N, int ph, int tm, int tn) {
      SB();
      announce<decltype(N)::value>(flag(ph + 1, tm, tn) - MB, rs);
      SB();
    };
    const int wmi = wid >> 1, wni = wid & 1;

    const int w = 2 * (tnB & 1) + wmi, tn = (tnB & 14) + wni;
    u32x4 c[2][2];
    auto start = [&](f32x4 (&acc)[4][4], const u32x4 (&bb)[2], bool bias) {
#pragma unroll
      for (int i = 0; i < 2; ++i)
#pragma unroll
        for (int j = 0; j < 4; ++j) {
          const u32 c0 = c[i][j >> 1][2 * (j & 1)], c1 = c[i][j >> 1][2 * (j & 1) + 1];
          const u32 b0 = bb[j >> 1][2 * (j & 1)], b1 = bb[j >> 1][2 * (j & 1) + 1];
          f32x4 a = {bf_lo(c0), bf_hi(c0), bf_lo(c1), bf_hi(c1)};
          if (bias) a += (f32x4){bf_lo(b0), bf_hi(b0), bf_lo(b1), bf_hi(b1)};
          asm volatile("" : "+v"(a));
          acc[i][j] = a;
        }
    };
    u32 fo[4];
    auto dest = [&](auto E, int xo) {
      constexpr int e = decltype(E)::value, i = e >> 1, gl = e & 1;
      FRESH_LANE();
      const u32 a = OFF_X + xo * XS + xpan(tmB, c13) + tn * 65536 + aq(gl, w >> 1) + qoff((w & 1) * 2 + i, lane);
      const u32 b = (tmB * 128 + (2 * w + i) * 16 + r) * 2048 + tn * 128 + gl * 64 + g * 16;
      fo[e] = xo < 4 ? a : b;
      pin(fo[e]);
    };
    auto finish = [&](const f32x4 (&acc)[4][4], int xo, int ph) {
      FRESH_LANE();
      const __amdgpu_buffer_rsrc_t rd = __builtin_amdgcn_make_buffer_rsrc(xo < 4 ? (void*)ws : (void*)out, 0, xo < 4 ? WS_USED : SEQ * DM * 2, 0x00020000);
#pragma unroll
      for (int i = 0; i < 2; ++i)
#pragma unroll
        for (int gl = 0; gl < 2; ++gl) {
#pragma unroll
          for (int k = 0; k < 4; ++k) {
            const f32x4& a = acc[i][2 * gl + (k >> 1)];
            c[i][gl][k] = pack2(a[2 * (k & 1)], a[2 * (k & 1) + 1]);
          }
          __builtin_amdgcn_raw_buffer_store_b128(__builtin_bit_cast(i32x4, c[i][gl]), rd, fo[2 * i + gl], 0, 16);
        }
      if (xo < 4) {
        SB();
        TS(ta);
        const u32 tag = *(const volatile LDS_AS u32*)(smem + L_TOK);
        __builtin_amdgcn_raw_buffer_store_b64((i32x2){(int)__float_as_uint(row_parts(c)), (int)tag}, rs,
            OFF_ST + xo * 524288 + span(tmB, c13) + w * 16384 + tn * 128 + (lane & 24) * 256 + (lane & 7) * 16 + (g >> 1) * 8, 0, 16);
        SB();
        TS(tb);
        publish(IC<1>{}, ph, tmB, tnB);
        TS(tc);
      }
      STAMP(0, 4 * ph + 3);
    };

    auto level = [&](int n, auto STEP, auto MORE) {
      f32x4 d[8];
      bf16x8 a[2], b[4];
      u32 p = fresh_lane(tid) * 16, pg = L_CNT + 4;
      pin(p, pg);
      dsrd<0>(a[0], p); dsrd<1024>(a[1], p); dsrd<9216>(b[0], p); dsrd<10240>(b[1], p); dsrd<11264>(b[2], p); dsrd<12288>(b[3], p);
#pragma unroll
      for (int i = 0; i < 8; ++i) d[i] = zero4();
      wait_lgkm0();
#pragma nounroll
      for (; n > 0; --n) {
        u32 at, k;
        asm volatile("ds_read_b32 %0, %1" : "=v"(at) : "v"(pg) : "memory");
#pragma unroll
        for (int i = 0; i < 8; ++i) mfma(d[i], b[i & 3], a[i >> 2]);
        asm volatile("s_waitcnt lgkmcnt(0)\n\tv_readfirstlane_b32 %1, %2\n\ts_cmp_ge_u32 %1, %3\n\ts_cselect_b32 %1, %4, 64\n\ts_min_i32 %0, %0, %1"
                     : "+s"(n), "=&s"(k), "+v"(at) : "n"(decltype(STEP)::value), "n"(decltype(MORE)::value) : "memory", "scc");
      }
      asm volatile("s_nop 15\n\ts_nop 15" ::: "memory");
      pin(d[0], d[1], d[2], d[3], d[4], d[5], d[6], d[7]);
    };

#pragma nounroll
    for (int pair = 0; pair < 2; ++pair) {
      f32x4 aco[4][4];
      {
        f32x4 acc[4][6], acch[2];
        f32x4 muv[4], muh, cs[4];
        f32x2 mu2[2], ivk[2][2], fk[2][2], csv;
        u32 sel[4], oa[4];
        {
        FRESH_LANE();
        STAMP(0, 16 * pair);
        const u32 lB = 9216 + (8 * g + (r >> 2)) * 128 + (((2 * wni) ^ ((r >> 3) & 1) ^ ((g & 1) << 1)) * 32) + (r & 3) * 8;
        const u32 lA = r * 128 + ((g ^ (r >> 1)) * 16);
        auto loop = [&](auto WM, auto RAW) {
          constexpr int wm = decltype(WM)::value, raw = decltype(RAW)::value;
#pragma unroll
          for (int i = 0; i < 4; ++i)
#pragma unroll
            for (int j = 0; j < 6; ++j) acc[i][j] = zero4();
          acch[0] = zero4(); acch[1] = zero4();
          bf16x8 fA[2][4], fH[2];
          u32x2 hB[2][12];
          u32 pA[3][2], pB[3][2], pH[3][2];
          f32x4 accq;
          if (raw) { accq = zero4(); *(u32*)(smem + L_XC + lane * 4) = 0x3f803f80u; }
#pragma unroll
          for (int k = 0; k < 3; ++k) {
            pA[k][0] = (raw ? lA : lane * 16) + 2 * k * SLOT; pA[k][1] = (lA ^ 64) + 2 * k * SLOT;
            pB[k][0] = lB + 2 * k * SLOT; pB[k][1] = (lB ^ 32) + 2 * k * SLOT;
            pin1(pA[k][0]); pin2(pB[k][0], pB[k][1]);
            if (raw) {
              pH[k][0] = r ? pA[k][0] : L_XC - (SLOT + 21504) + lA; pH[k][1] = r ? pA[k][1] : L_XC - (SLOT + 21504) + (lA ^ 64);
              pin1(pA[k][1]); pin2(pH[k][0], pH[k][1]);
            }
          }
          int ln;
          u32 aR, aG, aK[2];
          f32x2 ivq[2];
          float mh, cv;
          float fq[2][4];
          auto fB = [&](int set, int j) {
            return __builtin_bit_cast(bf16x8, (u32x4){hB[set][2 * j][0], hB[set][2 * j][1], hB[set][2 * j + 1][0], hB[set][2 * j + 1][1]});
          };
          kloop6<26 + raw, 17, 0>(
            [&](auto SL, auto Q) {
              constexpr int set = decltype(SL)::value & 1, k = decltype(SL)::value >> 1, q = decltype(Q)::value, h = q - 5;
              if constexpr (q < 4) dsrd<raw ? wm * SLOT + q * 2048 : set * SLOT + wm * 4096 + q * 1024>(fA[set][q], pA[k][raw & set]);
              else if constexpr (q == 4) dsrd<raw ? SLOT + 21504 : set * SLOT + 8192>(fH[set], raw ? pH[k][set] : pA[k][0]);
              else dstr1<set * SLOT + (h >> 2) * 4096 + (h & 1) * 512>(hB[set][h], pB[k][(h >> 1) & 1]);
            },
            [&](auto SET, auto M) {
              constexpr int set = decltype(SET)::value, m = decltype(M)::value, j = m >> 2, i = (SNK & 2) && (j & 1) ? 3 - (m & 3) : m & 3;
              if constexpr (m < 16) mfma(acc[i][j], fB(set, j), fA[set][i]);
              else if constexpr (m < 24) mfma(acc[i][j], fA[set][i], fB(set, j));
              else if constexpr (m == 26) mfma(accq, fB(set, wm), fH[set]);
              else if constexpr (wm == 0) mfma(acch[i], fB(set, 2 + i), fH[set]);
              else mfma(acch[i], fH[set], fB(set, 4 + i));
            }, [&](int t) { if (t < 0) STAMP(0, 16 * pair + 1); },
            [&](auto M) {
              constexpr int m = decltype(M)::value;
              if constexpr (m == 0) {
                ln = fresh_lane(tid);
                aR = L_INV + wm * 256 + (ln & 15) * 4; aG = L_INV + wm * 256 + (ln & 48);
                pin(aR, aG);
              }
              if constexpr (m == 1) { dsrd2<16, 32>(ivq[0], aR); dsrd2<48, 64>(ivq[1], aR); dsrd4<640 + 192>(muv[3], aG); }
              if constexpr (m == 2) { aK[0] = aG + (ln & 1) * 4; aK[1] = aK[0] + 64; pin(aK[0], aK[1]); }
              if constexpr (m == 3) {
                const u32 a0 = LANE_IN(M_OWN0) ? aK[1] : aK[0], a1 = LANE_IN(M_OWN1) ? aK[1] : aK[0];
                aK[0] = a0; aK[1] = a1;
                pin(aK[0], aK[1]);
              }
              if constexpr (m == 4) { dsrd2<0, 16>(ivk[0][0], aK[0]); dsrd2<32, 48>(ivk[0][1], aK[0]); dsrd2<2, 18>(ivk[1][0], aK[1]); }
              if constexpr (m == 5) { dsrd2<34, 50>(ivk[1][1], aK[1]); dsrd2<160, 176>(mu2[0], aR); dsrd2<192, 208>(mu2[1], aR); }
              if constexpr (m == 6) { dsrd4<640>(muv[0], aG); dsrd4<640 + 64>(muv[1], aG); dsrd4<640 + 128>(muv[2], aG); }
              if constexpr (m == 7) {
                if constexpr (wm) dsrd4<576 - 256>(muh, aG); else dsrd1<576>(mh, aR);
                aK[0] = L_C13;
                pin(aK[0]);
                dsrd1<0>(cv, aK[0]);
              }
              if constexpr (m == 8) { aR += L_CSQ - L_INV - wm * 256 + wni * 384; aG += L_CSQ - L_INV - wm * 256 + wni * 384; pin(aR, aG); }
              if constexpr (!raw) {
                if constexpr (m == 9) { dsrd4<128>(cs[2], aG); dsrd4<192>(cs[3], aG); dsrd2<64, 80>(csv, aR); }
                if constexpr (m == 10) { dsrd4<0>(cs[0], aG); dsrd4<64>(cs[1], aG); }
              }
              if constexpr (m == 11) { sel[0] = LANE_IN(M_PAR) ? 0x01000c0cu : 0x0c0c0100u; pin(sel[0]); }
              if constexpr (m == 12) { sel[1] = LANE_IN(M_PAR) ? 0x03020c0cu : 0x0c0c0302u; pin(sel[1]); }
              if constexpr (m == 13) {
                sel[2] = LANE_IN(M_OWN0) ? sel[0] : 0x0c0c0c0cu; sel[3] = LANE_IN(M_OWN1) ? sel[1] : 0x0c0c0c0cu;
                pin(sel[2], sel[3]);
              }
              if constexpr (m == 14) {
                sel[0] = LANE_IN(M_OWN0) ? 0x0c0c0c0cu : sel[0]; sel[1] = LANE_IN(M_OWN1) ? 0x0c0c0c0cu : sel[1];
                pin(sel[0], sel[1]);
              }
              if constexpr (m == 16) {
                wait_lgkm0();
                pin(ivq[0], ivq[1], muv[0], muv[1], muv[2], muv[3], ivk[0][0], ivk[0][1], ivk[1][0], ivk[1][1]);
                pin(mu2[0], mu2[1], cv);
                if constexpr (wm) pin(muh); else { pin(mh); muh = (f32x4){mh, mh, mh, mh}; }
                if constexpr (!raw) pin(cs[0], cs[1], cs[2], cs[3], csv);
              }
              if constexpr (m >= 17 && m < 21) {
                constexpr int i = m - 17;
                float cq = ivq[i >> 1][i & 1] * (0.17677669529663687f * 1.4426950408889634f);
                pin(cq);
                fq[0][i] = ivk[0][i >> 1][i & 1] * cq; fq[1][i] = ivk[1][i >> 1][i & 1] * cq;
                pin(fq[0][i], fq[1][i]);
                if constexpr (i & 1) { fk[0][i >> 1] = (f32x2){fq[0][i - 1], fq[0][i]}; fk[1][i >> 1] = (f32x2){fq[1][i - 1], fq[1][i]}; }
              }
              if constexpr (m == 21) {
                oa[0] = ((OFF_O + pair * XS + xpan(tmA, 0) + tnA * 65536 + aq(wni, wm)) ^ __float_as_uint(cv)) + ln * 16;
                oa[1] = oa[0] + 128;
                pin(oa[0], oa[1]);
              }
              if constexpr (m == 23 && OA4) {
                oa[2] = LANE_IN(~0ull << 8) ? oa[1] : oa[0]; oa[3] = LANE_IN(~0ull << 56) ? oa[1] : oa[0];
                pin(oa[2], oa[3]);
              }
              if constexpr (m == 24 && OA4) {
                const u32 a0 = LANE_IN(~0ull << 24) ? oa[1] : oa[0], a1 = LANE_IN(~0ull << 16) ? oa[1] : oa[0];
                oa[0] = a0; oa[1] = a1;
                pin(oa[0], oa[1]);
              }
              if constexpr (m == 22) c13 = __builtin_amdgcn_readfirstlane(__float_as_uint(cv));
            });
          if constexpr (raw) {
            FRESH_LANE();
            if (LANE_IN(0x0001000100010001ull)) {
              *(f32x4*)(smem + aG + wm * 64) = accq;
              if (wm == 0) { *(f32x4*)(smem + aG + 128) = acch[0]; *(f32x4*)(smem + aG + 192) = acch[1]; }
            }
            if (wm == 1 && LANE_IN(0xffffull)) { *(float*)(smem + aR + 256) = acch[0][0]; *(float*)(smem + aR + 320) = acch[1][0]; }
            wait_lgkm0();
            bar();
            dsrd4<128>(cs[2], aG); dsrd4<192>(cs[3], aG); dsrd2<64, 80>(csv, aR); dsrd4<0>(cs[0], aG); dsrd4<64>(cs[1], aG);
#pragma unroll
            for (int i = 0; i < 2; ++i)
#pragma unroll
              for (int gl = 0; gl < 2; ++gl)
                c[i][gl] = __builtin_bit_cast(u32x4, __builtin_amdgcn_raw_buffer_load_b128(rsrc(x, SEQ * DM * 2),
                    (16 * i + r) * 2048 + g * 16, (tmB * 128 + w * 32) * 2048 + tn * 128 + gl * 64, 0));
            wait_lgkm0();
            pin(cs[0], cs[1], cs[2], cs[3], csv);
          }
        };
        if (pair == 0) { if (wmi == 0) loop(IC<0>{}, IC<1>{}); else loop(IC<1>{}, IC<1>{}); }
        else { if (wmi == 0) loop(IC<0>{}, IC<0>{}); else loop(IC<1>{}, IC<0>{}); }
        }
        FRESH_LANE();
        STAMP(0, 16 * pair + 2);
        auto fix_qk = [&](const f32x4& a0, const f32x4& a1, float mu, int f) {
          const f32x4 y0 = cs[f] * mu + a0, y1 = cs[f + 1] * mu + a1;
          u32x4 v = {pack2(y0[0], y0[1]), pack2(y0[2], y0[3]), pack2(y1[0], y1[1]), pack2(y1[2], y1[3])};
          pin(v);
          return v;
        };
        auto fix_v = [&](const f32x4& a, const f32x4& mu, float cw) {
          const f32x4 y = mu * cw + a;
          u32x2 v = {pack2(y[0], y[1]), pack2(y[2], y[3])};
          pin(v);
          return v;
        };
        u32x4 Qf[4], Kf[5];
        u32x2 Vf[5][2];
        char* xc = smem + L_XC + wni * 3072;
        Kf[4] = fix_qk(acc[3][2], acc[3][3], mu2[1][1], 2);
        Vf[4][0] = fix_v(acc[3][4], muv[3], csv[0]); Vf[4][1] = fix_v(acc[3][5], muv[3], csv[1]);
        if (wmi == 0) {
          *(u32x4*)(xc + lane * 16) = Kf[4];
          *(u32x2*)(xc + 1024 + lane * 8) = Vf[4][0]; *(u32x2*)(xc + 1536 + lane * 8) = Vf[4][1];
          Kf[0] = fix_qk(acch[0], acch[1], muh[0], 2);
        } else {
          asm volatile("" : "=v"(Kf[0]));
          f32x2 cl = csv;
          pin(cl);
          const f32x4 y0 = muh * cl[0] + acch[0], y1 = muh * cl[1] + acch[1];
          SB();
          *(u32x2*)(xc + 2048 + lane * 8) = (u32x2){pack2(y0[0], y0[1]), pack2(y0[2], y0[3])};
          *(u32x2*)(xc + 2560 + lane * 8) = (u32x2){pack2(y1[0], y1[1]), pack2(y1[2], y1[3])};
        }
        pin(mu2[0], mu2[1]);
#pragma unroll
        for (int i = 0; i < 3; ++i) Kf[i + 1] = fix_qk(acc[i][2], acc[i][3], mu2[i >> 1][i & 1], 2);
        wait_lgkm0();
        bar();
        TS(td);
        if (wmi == 1) Kf[0] = *(const u32x4*)(xc + lane * 16);
        Vf[0][0] = *(const u32x2*)(xc + (wmi == 0 ? 2048 : 1024) + lane * 8);
        Vf[0][1] = *(const u32x2*)(xc + (wmi == 0 ? 2560 : 1536) + lane * 8);
        const f32x4 z = {0.f, 0.f, 0.f, 0.f};
        f32x4 S[4][2];
        pin(mu2[0], mu2[1], csv);
        auto qk = [&](int i, int k) {
          SB();
          S[i][k] = __builtin_amdgcn_mfma_f32_16x16x32_bf16(__builtin_bit_cast(bf16x8, Kf[i + k]), __builtin_bit_cast(bf16x8, Qf[i]), z, 0, 0, 0);
          SB();
        };
#pragma unroll
        for (int i = 3; i >= 0; --i) {
          f32x4 y0 = cs[0] * mu2[i >> 1][i & 1] + acc[i][0], y1 = cs[1] * mu2[i >> 1][i & 1] + acc[i][1];
          SB();
          Qf[i] = (u32x4){pack2(y0[0], y0[1]), pack2(y0[2], y0[3]), pack2(y1[0], y1[1]), pack2(y1[2], y1[3])};
          pin(Qf[i]);
          SB();
        }
        f32x2 yv[4];
        auto fill = [&](auto K0, auto N) {
          for_n<decltype(N)::value>([&](auto K) {
            constexpr int k = decltype(K0)::value + decltype(K)::value, i = k >> 3, h = (k >> 1) & 1, c = k & 1;
            SB();
            if constexpr (k & 4) { Vf[i + 1][h][c] = pack2(yv[k & 3][0], yv[k & 3][1]); }
            else {
              yv[k & 3] = (f32x2){muv[i][2 * c], muv[i][2 * c + 1]} * csv[h] + (f32x2){acc[i][4 + h][2 * c], acc[i][4 + h][2 * c + 1]};
            }
            if constexpr (k > 8 && k < 12) { SB(); pin(yv[(k - 1) & 3]); }
          });
          SB();
        };
        const bool par = LANE_IN(M_PAR), own0 = LANE_IN(M_OWN0), own1 = LANE_IN(M_OWN1);
        f32x2 l[2][2];
        {
          auto pick = [](bool c, float a, float b) { return c ? a : b; };
          float sp[4][4];
          auto half = [&](int i) {
#pragma unroll
            for (int k = 0; k < 4; ++k) sp[i][k] = pick(par, S[i][k >> 1][2 * (k & 1) + 1], S[i][k >> 1][2 * (k & 1)]);
            SB();
          };
          auto tile = [&](int i) {
            l[0][i >> 1][i & 1] = pick(own0, sp[i][2], sp[i][0]);
            l[1][i >> 1][i & 1] = pick(own1, sp[i][3], sp[i][1]);
            SB();
          };
          qk(3, 1); qk(3, 0); qk(2, 1); qk(2, 0); qk(1, 1); qk(1, 0); qk(0, 1); qk(0, 0);
          half(3); half(2); half(1); tile(3); tile(2); half(0); tile(1); tile(0);
        }
#pragma unroll
        for (int i = 3; i >= 0; --i) { l[i & 1][i >> 1] *= fk[i & 1][i >> 1]; SB(); }
        f32x2 t[2];
        t[1] = __builtin_elementwise_maximum(l[0][1], l[1][1]);
        SB();
        {
          const u64 first = tmA == 0 && wmi == 0 ? ~0ull : 0ull;
          if (LANE_IN(first & ~M_OWN1)) l[1][0][0] = -1.4426950e9f;
          if (LANE_IN(first & ~M_OWN0)) l[0][0][0] = -1.4426950e9f;
        }
        SB();
        t[0][1] = __builtin_elementwise_maximum(l[0][0][1], l[1][0][1]);
        SB();
        t[0][0] = __builtin_elementwise_maximum(l[0][0][0], l[1][0][0]);
        auto all4 = [&](auto K0, f32x2 (&v)[2], auto f, auto mid, auto&& more) {
          constexpr int k0 = decltype(K0)::value;
          auto U = [](float a) { return __float_as_uint(a); };
          auto F = [](u32 a) { return __uint_as_float(a); };
          fill(IC<k0>{}, IC<1>{});
          const auto b = __builtin_amdgcn_permlane32_swap(U(v[0][1]), U(v[1][1]), false, false);
          SB();
          const auto a = __builtin_amdgcn_permlane32_swap(U(v[0][0]), U(v[1][0]), false, false);
          fill(IC<k0 + 1>{}, IC<1>{});
          const float fb = f(F(b[0]), F(b[1]));
          SB();
          const float fa = f(F(a[0]), F(a[1]));
          fill(IC<k0 + 2>{}, IC<2>{});
          const auto c = __builtin_amdgcn_permlane16_swap(U(fa), U(fb), false, false);
          fill(IC<k0 + 4>{}, IC<1>{});
          float t0 = f(F(c[0]), F(c[1])), t1 = f(F(c[1]), F(c[0]));
          pin(t0, t1);
          more(IC<0>{});
          t0 = mid(t0); t1 = mid(t1);
          fill(IC<k0 + 5>{}, IC<2>{});
          const auto d = __builtin_amdgcn_permlane16_swap(U(t0), U(t1), false, false);
          fill(IC<k0 + 7>{}, IC<1>{});
          u32 d0 = d[0], d1 = d[1], d2 = d[0], d3 = d[1];
          pin(d2, d3);
          fill(IC<k0 + 8>{}, IC<2>{});
          const auto e0 = __builtin_amdgcn_permlane32_swap(d0, d2, false, false);
          const auto e1 = __builtin_amdgcn_permlane32_swap(d1, d3, false, false);
          fill(IC<k0 + 10>{}, IC<2>{});
          v[0] = (f32x2){F(e0[0]), F(e1[0])}; v[1] = (f32x2){F(e0[1]), F(e1[1])};
        };
        all4(IC<0>{}, t, [](float a, float b) { return __builtin_elementwise_maximum(a, b); }, [](float a) { return a; }, [](auto) {});
#pragma unroll
        for (int i = 0; i < 4; ++i) { l[i & 1][i >> 1] -= t[i >> 1]; SB(); if (i == 0) pin(yv[3]); }
#pragma unroll
        for (int i = 0; i < 4; ++i) {
          l[i & 1][i >> 1] = (f32x2){__builtin_amdgcn_exp2f(l[i & 1][i >> 1][0]), __builtin_amdgcn_exp2f(l[i & 1][i >> 1][1])};
          SB();
        }
        t[0] = l[0][0] + l[1][0];
        l[0][0] *= ivk[0][0];
        SB();
        t[1] = l[0][1] + l[1][1];
        l[1][0] *= ivk[1][0];
        all4(IC<12>{}, t, [](float a, float b) { return a + b; }, [](float a) { return __builtin_amdgcn_rcpf(a); },
             [&](auto) { SB(); l[0][1] *= ivk[0][1]; l[1][1] *= ivk[1][1]; SB(); });
#pragma unroll
        for (int i = 0; i < 4; ++i) { l[i & 1][i >> 1] *= t[i >> 1]; SB(); }
        u32 pw[4];
#pragma unroll
        for (int i = 0; i < 4; ++i) pw[i] = pack2(l[0][i >> 1][i & 1], l[1][i >> 1][i & 1]);
        u32x10 V[2];
#pragma unroll
        for (int i = 0; i < 10; ++i) { V[0][i] = Vf[i >> 1][0][i & 1]; V[1][i] = Vf[i >> 1][1][i & 1]; }
        pin(V[0], V[1]);
        u32x4 P[4];
        f32x4 O[4][2];
        for_n<7>([&](auto I) {
          constexpr int i = decltype(I)::value, m = i - 1, e = i - 3;
          u32x4 o;
          auto pv = [&](auto H) {
            constexpr int h = decltype(H)::value;
            SB();
            if constexpr (i < 4) {
              P[i][2 * h] = __builtin_amdgcn_perm(pw[i], pw[i], sel[2 * h]);
              P[i][2 * h + 1] = __builtin_amdgcn_perm(pw[i], pw[i], sel[2 * h + 1]);
            }
            if constexpr (e >= 0) { o[2 * h] = pack2(O[e][h][0], O[e][h][1]); o[2 * h + 1] = pack2(O[e][h][2], O[e][h][3]); }
            SB();
            if constexpr (m >= 0 && m < 4)
              O[m][h] = __builtin_amdgcn_mfma_f32_16x16x32_bf16(__builtin_bit_cast(bf16x8, __builtin_shufflevector(V[h], V[h], 2 * m, 2 * m + 1, 2 * m + 2, 2 * m + 3)),
                                                                __builtin_bit_cast(bf16x8, P[m]), z, 0, 0, 0);
            SB();
          };
          pv(IC<0>{}); pv(IC<1>{});
          if constexpr (e >= 0) st16(o, (OA4 ? oa[e] : LANE_IN(~0ull << (e == 3 ? 56 : 24 - 8 * e)) ? oa[1] : oa[0]) + qoff(e, 0));
        });
        SB();
        TS(te);
        start(aco, c[0], false);
        TS(tf);
        publish(IC<0>{}, 4 * pair, tmA, tnA);
        TS(tg);
        if (pair == 0) level(LVA, IC<1>{}, IC<LXA>{});
        TS(th);
        TSW(td, 35); TSW(te, 34);
        STAMP(0, 16 * pair + 3);
      }
      {
        FRESH_LANE();
        bf16x8 fA[2][2][2], fB[2][2][4];
        u32 pA, pB;
        so = 2 * SLOT;
        for_n<4>([&](auto E) { dest(E, 2 * pair + 1); });
        kloop<16, 16, 12, 0>(so,
          [&](int s_, auto) { pA = lane * 16 + wmi * 2048 + s_; pB = lane * 16 + wni * 4096 + s_; pin2(pA, pB); },
          [&](auto SET, auto Q) {
            constexpr int set = decltype(SET)::value, q = decltype(Q)::value, s = q / 6, m = q % 6;
            if constexpr (m < 2) dsrd<s * 4096 + m * 1024>(fA[set][s][m], pA);
            else dsrd<8192 + s * 8192 + (m - 2) * 1024>(fB[set][s][m - 2], pB);
          },
          [&](auto SET, auto M) {
            constexpr int set = decltype(SET)::value & 1, m = decltype(M)::value;
            constexpr int s = (SNK & 4) ? (m ^ (m >> 1)) & 1 : m >> 3, j = (SNK & 4) ? m >> 2 : (m >> 1) & 3, i = (SNK & 4) ? ((m >> 1) ^ (m >> 2)) & 1 : m & 1;
            mfma(aco[i][j], fB[set][s][j], fA[set][s][i]);
          },
          [&](int t) { if (t < 0) STAMP(0, 16 * pair + 5); }, [](auto) {});
        STAMP(0, 16 * pair + 6);
        finish(aco, 2 * pair + 1, 4 * pair + 1);
        if (pair == 0) level(LVB, IC<2>{}, IC<LXB>{});
      }
      {
        f32x4 acc[4][7];
        f32x2 iv[2], mv[2];
        f32x4 tb[2][4];
        u32 aT, ho[4];
        auto tables = [&](auto J) {
          constexpr int j = decltype(J)::value;
          dsrd4<128 * j>(tb[j & 1][0], aT); dsrd4<128 * j + L_BS - L_CS1>(tb[j & 1][1], aT);
          if constexpr (j < 3) { dsrd4<128 * j + 64>(tb[j & 1][2], aT); dsrd4<128 * j + 64 + L_BS - L_CS1>(tb[j & 1][3], aT); }
        };
        {
        FRESH_LANE();
#pragma unroll
        for (int i = 0; i < 4; ++i)
#pragma unroll
          for (int j = 0; j < 7; ++j) acc[i][j] = zero4();
        bf16x8 fA[2][4], fB[2][7];
        u32 pA[3], pB[3];
#pragma unroll
        for (int k = 0; k < 3; ++k) {
          pA[k] = lane * 16 + wmi * 4096 + 2 * k * SLOT; pB[k] = lane * 16 + wni * 7168 + 2 * k * SLOT;
          pin2(pA[k], pB[k]);
        }
        kloop6<28, 11, 1>(
          [&](auto SL, auto Q) {
            constexpr int set = decltype(SL)::value & 1, k = decltype(SL)::value >> 1, q = decltype(Q)::value;
            if constexpr (q < 4) dsrd<set * SLOT + q * 1024>(fA[set][q], pA[k]);
            else dsrd<set * SLOT + 8192 + (q - 4) * 1024>(fB[set][q - 4], pB[k]);
          },
          [&](auto SET, auto M) {
            constexpr int set = decltype(SET)::value, m = decltype(M)::value, j0 = m >> 2, i0 = (SNK & 1) && (j0 & 1) ? 3 - (m & 3) : m & 3;
            constexpr int i = i0, j = j0;
            mfma(acc[i][j], fB[set][j], fA[set][i]);
          }, [&](int t) { if (t < 0) STAMP(0, 16 * pair + 9); },
          [&](auto M) {
            constexpr int m = decltype(M)::value;
            if constexpr (m == 0) { aT = L_INV + 64 + wmi * 256 + (fresh_lane(tid) & 15) * 4; pin(aT); }
            if constexpr (m == 1) { dsrd2<0, 16>(iv[0], aT); dsrd2<32, 48>(iv[1], aT); dsrd2<144, 160>(mv[0], aT); dsrd2<176, 192>(mv[1], aT); }
            if constexpr (m == 2) { aT = L_CS1 + wni * 448 + (fresh_lane(tid) & 48); pin(aT); }
            if constexpr (m == 3) tables(IC<0>{});
            if constexpr (m >= 4 && m < 12 && m % 2 == 0) { ho[m / 2 - 2] = qoff(m / 2 - 2, fresh_lane(tid)); pin(ho[m / 2 - 2]); }
          });
        }
        FRESH_LANE();
        STAMP(0, 16 * pair + 10);
        const u32 hbase = OFF_H + pair * HS + hpan(tmA, tnA >> 3, c13);
        auto act = [&](const f32x4& a0, const f32x4& a1, const f32x4& cs0, const f32x4& bs0, const f32x4& cs1, const f32x4& bs1, int i0, int i1) {
          f32x4 y0 = cs0 * mv[i0 >> 1][i0 & 1] + bs0, y1 = cs1 * mv[i1 >> 1][i1 & 1] + bs1;
          SB();
          y0 = a0 * iv[i0 >> 1][i0 & 1] + y0; y1 = a1 * iv[i1 >> 1][i1 & 1] + y1;
          SB();
          const f32x2 y[4] = {{y0[0], y0[1]}, {y0[2], y0[3]}, {y1[0], y1[1]}, {y1[2], y1[3]}};
          return gelu_tanh8(y);
        };
        for_n<3>([&](auto JP) {
          constexpr int jp = decltype(JP)::value;
          wait_lgkm0();
          f32x4 (&t)[4] = tb[jp & 1];
          pin(t[0], t[1], t[2], t[3]);
          if constexpr (jp == 0) pin(iv[0], iv[1], mv[0], mv[1]);
          tables(IC<jp + 1>{});
          u32 hb = hbase + aq(hpos(tnA, wni * 4 + jp), wmi);
          asm volatile("" : "+s"(hb));
#pragma unroll
          for (int i = 0; i < 4; ++i) {
            u32 a = hb + ho[i];
            pin(a);
            const u32x4 v = act(acc[i][2 * jp], acc[i][2 * jp + 1], t[0], t[1], t[2], t[3], i, i);
            st16(v, a);
            SB();
            if (4 * jp + i == 3 + PUBJ) publish(IC<PUBJ>{}, 4 * pair + 2, tmA, tnA);
          }
        });
        {
          wait_lgkm0();
          pin(tb[1][0], tb[1][1]);
#pragma unroll
          for (int i = 0; i < 4; i += 2) {
            u32 a = hbase + aq(hpos(tnA, 3), wmi) + qoff(i + (g & 1), (2 * wni + (g >> 1)) * 16 + r);
            pin(a);
            const u32x4 u = act(acc[i][6], acc[i + 1][6], tb[1][0], tb[1][1], tb[1][0], tb[1][1], i, i + 1);
            const auto s0 = __builtin_amdgcn_permlane16_swap(u[0], u[2], false, false);
            const auto s1 = __builtin_amdgcn_permlane16_swap(u[1], u[3], false, false);
            const u32x4 v = {s0[0], s1[0], s0[1], s1[1]};
            st16(v, a);
            SB();
          }
        }
        STAMP(0, 16 * pair + 11);
      }
      {
        FRESH_LANE();
        f32x4 acc[4][4];
        SB();
        const u32x4 bb[2] = {*(const u32x4*)(smem + L_B2 + wni * 128 + g * 16), *(const u32x4*)(smem + L_B2 + wni * 128 + 64 + g * 16)};
#pragma unroll
        for (int i = 2; i < 4; ++i)
#pragma unroll
          for (int j = 0; j < 4; ++j) acc[i][j] = zero4();
        start(acc, bb, true);
        bf16x8 fA[2][2][4], fB[2][2][4];
        u32 pA, pB;
        const u32 xc = wmi * 16384 + wni * 4096;
        wait_vm<6>();
        so = 0;
        u32x4 xv[4];
        auto look = [&]() {
          asm volatile("buffer_load_dwordx4 %0, %4, %5, %6 offen sc1\n\tbuffer_load_dwordx4 %1, %4, %5, %6 offen offset:1024 sc1\n\t"
                       "buffer_load_dwordx4 %2, %4, %5, %6 offen offset:2048 sc1\n\tbuffer_load_dwordx4 %3, %4, %5, %6 offen offset:3072 sc1"
                       : "=&v"(xv[0]), "=&v"(xv[1]), "=&v"(xv[2]), "=&v"(xv[3]) : "v"(lane * 16), "s"(rs), "s"(xbox(pair, 1) + xc) : "memory");
        };
        kloop<28, 32, 16, 1>(so,
          [&](int s_, auto) { pA = lane * 16 + wmi * 2048 + s_; pB = lane * 16 + wni * 4096 + s_; pin2(pA, pB); },
          [&](auto SET, auto Q) {
            constexpr int set = decltype(SET)::value, q = decltype(Q)::value, s = q >> 3, m = q & 7;
            if constexpr (m < 4) dsrd<s * 8192 + (m >> 1) * 4096 + (m & 1) * 1024>(fA[set][s][m], pA);
            else dsrd<16384 + s * 8192 + (m - 4) * 1024>(fB[set][s][m - 4], pB);
          },
          [&](auto SET, auto M) {
            constexpr int set = decltype(SET)::value & 1, m = decltype(M)::value, o = decltype(SET)::value > 1 ? 0 : SNK >> 3;
            constexpr int s = o == 0 ? (m >> 3) & 1 : o == 1 ? m >> 4 : o == 2 ? m & 1 : (m ^ (m >> 1)) & 1;
            constexpr int j = o == 0 ? (m >> 1) & 3 : o == 1 ? (m >> 2) & 3 : m >> 3;
            constexpr int i = o == 0 ? (m & 1) + (m < 16 ? 2 : 0) : o == 1 ? ((m & 4) ? 3 - (m & 3) : m & 3) : o == 3 && (m & 8) ? 3 - ((m >> 1) & 3) : (m >> 1) & 3;
            mfma(acc[i][j], fB[set][s][j], fA[set][s][i]);
          },
          [&](int t) { if (t < 0) { STAMP(0, 16 * pair + 13); } else wait_vm<0>(); },
          [&](auto M) {
            constexpr int k = decltype(M)::value - SND - 1;
            if constexpr (k >= 0 && k < 8 && k % 2 == 0) {
              const f32x4 a = acc[2][k >> 1], b = acc[3][k >> 1];
              const u32x4 v = {pack2(a[0], a[1]), pack2(a[2], a[3]), pack2(b[0], b[1]), pack2(b[2], b[3])};
              st16(__builtin_elementwise_min(v, (u32x4){~1u, ~1u, ~1u, ~1u}), xbox(pair, 0) + xc + lane * 16 + k * 512);
            }
            if constexpr (decltype(M)::value == (TW2 ? LK : 31)) look();
          });
        STAMP(0, 16 * pair + 14);
        for_n<4>([&](auto E) { dest(E, pair == 0 ? 2 : 4); });
#pragma nounroll
        for (int spin = 0; spin < (1 << 22); ++spin) {
          asm volatile("s_waitcnt vmcnt(0)" : "+v"(xv[0]), "+v"(xv[1]), "+v"(xv[2]), "+v"(xv[3]) :: "memory");
          const u32x4 o = __builtin_elementwise_max(__builtin_elementwise_max(xv[0], xv[1]), __builtin_elementwise_max(xv[2], xv[3]));
          if (!__any(max(max(o[0], o[1]), max(o[2], o[3])) == ~0u)) break;
          look();
        }
#pragma unroll
        for (int k = 0; k < 4; ++k) {
          acc[0][k] += (f32x4){bf_lo(xv[k][0]), bf_hi(xv[k][0]), bf_lo(xv[k][1]), bf_hi(xv[k][1])};
          acc[1][k] += (f32x4){bf_lo(xv[k][2]), bf_hi(xv[k][2]), bf_lo(xv[k][3]), bf_hi(xv[k][3])};
        }
        finish(acc, pair == 0 ? 2 : 4, 4 * pair + 3);
      }
    }
  } else if (wid == 4) {
    {
      u32 vo[4];
      x_lanes(vo);
      wait_vm<36>();
      bar();
      bar();
#pragma nounroll
      for (int U = 3; U < 16; ++U) {
        wait_vm<18>();
        bar();
        x_half(IC<0>{}, U, vo);
        bar();
        x_half(IC<1>{}, U, vo);
      }
      wait_vm<18>(); bar(); bar();
      wait_vm<0>(); bar(); bar(); bar();
    }
    auto phase = [&](auto NSL_, auto HALO_, u32 src, u32 hsrc, int ph  , int tm, int tmh) {
      constexpr int NSL = decltype(NSL_)::value, HALO = decltype(HALO_)::value;
      constexpr int BIG = NSL == 28, PA = BIG ? 16 : 8, PT = PA + HALO, D = BIG ? DB : NSL == 16 ? DN : DW, X = BIG ? XA2 : 0;
      FRESH_LANE();
      u32 vo[4];
#pragma unroll
      for (int e = 0; e < 4; ++e) vo[e] = qoff(e, lane) - e * 1024;
      u64 pv[NP];
      pin(vo[0], vo[1], vo[2], vo[3]);
      asm volatile("" : "+s"(src));
      { constexpr int z = BIG ? Z2 : NSL == 16 ? ZO : HALO ? ZQ : Z1; if constexpr (z > 0) __builtin_amdgcn_s_sleep(z); }
      if constexpr (NSL == 16 && ZO0 > 0) { if (ph == 0) __builtin_amdgcn_s_sleep(ZO0); }
      if constexpr (NSL == 32 && !HALO && Z10 > 0) { if (ph == 1) __builtin_amdgcn_s_sleep(Z10); }
      poll_n(cpy + flag(ph + 1, (lane & 16) ? tmh : tm, BIG ? 8 * (tnB & 1) + ((rotA >> 1) + (r < D + X ? r : 0))
                                                 : NSL == 16 ? (rotB + (r < DN ? r : 0)) & 15 : rotA + (r & (DW > 4 ? 3 : 1))), token(), pv);
      if constexpr (!BIG && !HALO) *(volatile LDS_AS u32*)(smem + L_CNT + 4) = ph + 1;
      STAMP(1, 4 * (ph + 1));
      const u32 flip = BIG ? (tnB & 1) * 16384 : 0;
      stream<NSL, PT, D, X>(so, 0,
        [&](int u) {
          u = BIG ? (u < 8 ? u ^ (rotA >> 1) : u) : NSL == 16 ? (u + rotB) & 15 : u ^ (2 * rotA);
          const u32 ko = aq(NSL == 32 ? u : 2 * u, 0), l = so;
          next_slot<BIG>(so);
          return Slot{ko, src + ko + flip, l};
        },
        [&](auto E, const Slot& a) {
          constexpr int e = decltype(E)::value;
          if constexpr (e == PA) DMA(rs, (LDS_AS void*)(a.l0 + 8192), 16, vo[3], hsrc + a.k, 0, 16);
          else DMA(rs, (LDS_AS void*)(a.l0 + (e >> 2) * 4096), 16, vo[e & 3],
                   e < 4 ? a.a0 : src + a.k + (NSL == 16 ? aq(e >> 2, 0) : aq(e >> 3, (e >> 2) & 1) + ((e & 4) ? -flip : flip)),
                   (e & 3) * 1024, 16);
        }, [&]() { bar(); });
#pragma unroll
      for (int i = 0; i < NP; ++i) asm volatile("" :: "v"(pv[i]));
    };
#pragma nounroll
    for (int pair = 0; pair < 2; ++pair) {
      const int tmh = tmA > 0 ? tmA - 1 : 0;
      const u32 xq = OFF_X + 2 * pair * XS;
      if (pair) phase(IC<32>{}, IC<1>{}, xq + xpan(tmA, c13), xq + xpan(tmh, c13) + 16384 + 3072, 3, tmA, tmh);
      side();
      bar();
      phase(IC<16>{}, IC<0>{}, OFF_O + pair * XS + xpan(tmB, c13) + aq(0, tnB & 1), 0, 4 * pair, tmB, tmB);
      phase(IC<32>{}, IC<0>{}, xq + XS + xpan(tmA, c13), 0, 4 * pair + 1, tmA, tmA);
      bar();
      so = 0;
      phase(IC<28>{}, IC<0>{}, OFF_H + pair * HS + hpan(tmB, tnB & 1, c13), 0, 4 * pair + 2, tmB, tmB);
      bar();
      so = 0;
    }
  } else if (wid == 5) {
    FRESH_LANE();
    auto phase = [&](auto NSL_, auto PB_, auto AUX_, int u0, int add, int rot, __amdgpu_buffer_rsrc_t rb, const u32 (&vo)[4], u32 boff,
                     u32 src, u32 step, auto&& q) {
      constexpr int NSL = decltype(NSL_)::value, PB = decltype(PB_)::value;
      stream<NSL, PB, (NSL == 28 ? DB : 5), (NSL == 28 ? XB2 : NSL == 16 ? XBO : 0)>(so, u0,
        [&](int u) {
          u = NSL == 28 ? (u < 8 ? u ^ rot : u) : ((u + add) & (NSL - 1)) ^ rot;
          const u32 l = so + boff;
          next_slot<NSL == 28>(so);
          return Slot{0, src + u * step, l};
        },
        [&](auto I, const Slot& a) {
          constexpr int i = decltype(I)::value;
          DMA(rb, (LDS_AS void*)(a.l0 + (i >> 2) * 4096), 16, vo[i & 3], a.a0 + q(i >> 2), (i & 3) * 1024, decltype(AUX_)::value);
        }, [&]() { bar(); });
    };
    auto tiles = [&](auto NSL_, auto PB_, u32 boff, u32 src) {
      constexpr int NSL = decltype(NSL_)::value, PB = decltype(PB_)::value;
      u32 vo[4];
#pragma unroll
      for (int e = 0; e < 4; ++e) vo[e] = NSL == 28 && D2 ? lane * 16 : qoff(e, lane) - e * 1024;
      constexpr u32 Q = NSL == 28 ? Q2 : QS;
      phase(NSL_, PB_, IC<16>{}, 0, NSL == 16 ? rotB : 0, NSL == 28 ? rotA >> 1 : NSL == 16 ? 0 : 2 * rotA, rs, vo, boff, src,
            PB == 16 ? 2 * Q : 4 * Q, [](int i) { return PB == 16 ? (i >> 1) * Q + (i & 1) * ((NSL == 28 ? 112 : 32) * Q) : i * Q; });
    };
    so = 5 * SLOT;
#pragma nounroll
    for (int pair = 0; pair < 2; ++pair) {
      {
        u32 vo[4];
        q_lanes(vo);
        phase(IC<32>{}, IC<12>{}, IC<0>{}, pair ? 0 : 5, pair ? 0 : 4 * rot0, pair ? 2 * rotA : 0, rsrc(wqkv, DM * 3 * DM * 2), vo, 9216,
              tnA * 128, 32 * 6144, [](int i) { return i * 2048; });
      }
      if (pair == 0) bar();
      bar();
      tiles(IC<16>{}, IC<16>{}, 8192, OFF_WO + (tnB & 14) * (32 * QS));
      if (pair == 0) poll(flag(F_W1, r, (tnA & 14) + (g & 1)), token());
      tiles(IC<32>{}, IC<14>{}, 8192, OFF_W1 + tnA * (32 * 4 * QS));
      bar();
      so = 0;
      if (pair == 0) poll(flag(F_W2, 8 * (tnB & 1) + (r & 7), (tnB & 14) + (r >> 3)), token());
      tiles(IC<28>{}, IC<16>{}, 16384, OFF_W2 + ((tnB & 14) * 112 + (tnB & 1) * 56) * Q2);
      bar();
      so = 0;
    }
  } else if (wid == 6) {
    FRESH_LANE();
    const int rr = lane >> 3, c16 = (lane & 7) * 16;
    u32x4 U[7][4], F[4];
    auto u_load = [&](auto I) {
      constexpr int i = decltype(I)::value;
#pragma unroll
      for (int e = 0; e < 4; ++e) {
        if constexpr (i < 2)
          U[(i + 5) % 7][e] = __builtin_bit_cast(u32x4, __builtin_amdgcn_raw_buffer_load_b128(rsrc(wo, DM * DM * 2),
              ((rr & 3) + 16 * (rr >> 2)) * 2048 + c16, ((2 * tmA + i) * 32 + 4 * e) * 2048 + (tnA ^ (4 * i)) * 128, UAUX));
        else if constexpr (i < 9)
          U[(i + 5) % 7][e] = __builtin_bit_cast(u32x4, __builtin_amdgcn_raw_buffer_load_b128(rsrc(w1, DM * FF * 2),
              rr * 7168 + c16, ((2 * tmA + (tnA & 1)) * 32 + 8 * e) * 7168 + (7 * (tnA >> 1) + i - 2) * 128, UAUX));
        else
          U[(i + 5) % 7][e] = __builtin_bit_cast(u32x4, __builtin_amdgcn_raw_buffer_load_b128(rsrc(w2, DM * FF * 2),
              rr * 2048 + c16, ((7 * tmA + i - 9) * 32 + 8 * e) * 2048 + tnA * 128, UAUX));
      }
    };
    auto u_write = [&](auto I) {
      constexpr int i = decltype(I)::value;
#pragma unroll
      for (int e = 0; e < 4; ++e) {
        const u32x4 v = U[(i + 5) % 7][e];
        *(u32x4*)(smem + L_IM + e * 1024 + rr * 128 + (c16 ^ ((rr & 2) << 5))) = (e & 1) ? (u32x4){v[2], v[3], v[0], v[1]} : v;
      }
    };
    auto u_store = [&](auto I) {
      constexpr int i = decltype(I)::value;
      const u32 sw = ((r & 8) << 3) | ((g & 1) << 3);
      const char* im = smem + L_IM + (8 * g + (r >> 2)) * 128;
      for_n<4>([&](auto E) {
        constexpr int e = decltype(E)::value;
        constexpr bool split = i >= 2 && i < 9 && (2 * (i - 2) + (e >> 1)) % 7 == 3;
        const char* p = im + (((e >> 1) * 64 + (split ? (e & 1) * 32 + (r & 3) * 8 : (r & 3) * 16 + (e & 1) * 8)) ^ sw);
        const u32x2 lo = __builtin_bit_cast(u32x2, __builtin_amdgcn_ds_read_tr16_b64_v4i16((LDS_AS s16x4*)p));
        const u32x2 hi = __builtin_bit_cast(u32x2, __builtin_amdgcn_ds_read_tr16_b64_v4i16((LDS_AS s16x4*)(p + 512)));
        F[e] = (u32x4){lo[0], lo[1], hi[0], hi[1]};
      });
      for_n<4>([&](auto E) {
        constexpr int e = decltype(E)::value;
        if constexpr (i < 2) st16(F[e], OFF_WO + ((tnA ^ (4 * i)) * 32 + 2 * tmA + i) * QS + qoff(e, lane));
        else if constexpr (i < 9) {
          constexpr int f = w1_frag(i, e);
          st16(F[e], OFF_W1 + ((((tnA & 14) + (f >> 4)) * 32 + 2 * tmA + (tnA & 1)) * 4 + ((f & 15) >> 2)) * QS + qoff(f & 3, lane));
        } else st16(F[e], OFF_W2 + (tnA * 112 + (tmA >> 3) * 56 + hpos(tmA, i - 9)) * Q2 + (D2 ? e * 1024 + lane * 16 : qoff(e, lane)));
      });
    };
    auto u_sums = [&](auto I) {
      constexpr int i = decltype(I)::value;
      constexpr u32 K = w1_frag(i, 0) | (w1_frag(i, 1) << 8) | (w1_frag(i, 2) << 16) | (w1_frag(i, 3) << 24);
      f32x4 cs = {0.f, 0.f, 0.f, 0.f};
#pragma unroll
      for (int e = 0; e < 4; ++e) {
        const u32 o = (r >> 2) == e ? 0x3f803f80u : 0u;
        cs = __builtin_amdgcn_mfma_f32_16x16x32_bf16(__builtin_bit_cast(bf16x8, (u32x4){o, o, o, o}), __builtin_bit_cast(bf16x8, F[e]), cs, 0, 0, 0);
      }
      const u32 f = (K >> (8 * g)) & 255;
      __builtin_amdgcn_raw_buffer_store_b32(__float_as_uint(cs[0]), rs,
          OFF_CS1 + ((((tnA & 14) + (f >> 4)) * 32 + 2 * tmA + (tnA & 1)) * 256 + (f & 15) * 16 + r) * 4, 0, 16);
    };
    u64 tok = 0;
    auto up = [&](int set) { st8(tok, lane == 0, flag(set, tmA, tnA)); };
    f32x4 cv[32], ca[2];
    auto cs_load = [&](u32 src, int p0, int p1) {
#pragma unroll
      for (int p = p0; p < p1; ++p)
        cv[p] = __builtin_bit_cast(f32x4, __builtin_amdgcn_raw_buffer_load_b128(rs, lane * 16 + (p & 3) * 1024, src + (p >> 2) * 4096, 16));
    };
    auto cs_add = [&](int p0, int p1) {
#pragma unroll
      for (int p = p0; p < p1; ++p) {
        asm volatile("" : "+v"(cv[p]));
        if (p < 2) ca[p] = cv[p];
        else {
#pragma unroll
          for (int k = 0; k < 4; ++k) {
            float t = ca[p & 1][k];
            const float u = cv[p][k];
            asm("v_add_f32 %0, %0, %1" : "+v"(t) : "v"(u));
            ca[p & 1][k] = t;
          }
        }
      }
      asm volatile("" : "+v"(ca[0]), "+v"(ca[1]));
    };
    u64 vf = 0;
    const u32 fo = flag(F_WO, r, ((tnB & 14) + (g & 1)) ^ (2 * (g & 2)));
    __builtin_amdgcn_s_sleep(PFW0);
    const u64 wf = ldf(lane < 32 ? flag(lane & 7, tmA, tnA) + (lane >> 3) * 262144 - MB : flag(lane < 48 ? lane - 32 : F_LN + (lane & 7), tmA, tnA));
    u_load(IC<0>{});
    u_load(IC<1>{});
    for_n<34>([&](auto S) {
      constexpr int s = decltype(S)::value;
      bar();
      if constexpr (s == TOKB) { tok = token(); asm volatile("" : "+s"(tok)); }
      if constexpr (s >= HL && s < HL + 4) {
        if constexpr ((s - HL) % 2 == 0) u_write(IC<(s - HL) / 2>{}); else u_store(IC<(s - HL) / 2>{});
      }
      if constexpr (s == HL + 3 + FLD) { wait_vm<0>(); asm volatile("" :: "v"(wf)); up(F_WO); }
      if constexpr (s == G0 - 4) vf = ldf(fo);
      if constexpr (s == G0 - 1) { if (!__all(vf == tok)) poll(fo, tok); }
      if constexpr (s == G0) for_n<4>([&](auto I) { u_load(IC<2 + decltype(I)::value>{}); });
      if constexpr (s == G0 + 1) for_n<3>([&](auto I) { u_load(IC<6 + decltype(I)::value>{}); });
    });
    static_assert(TOKB < HL && HL + 3 + FLD < G0 - 4 && G0 + 1 < 33, "schedule");
    if (HSL) __builtin_amdgcn_s_sleep(HSL);
    for_n<7>([&](auto I) {
      constexpr int i = 2 + decltype(I)::value;
      if constexpr (i - 2 >= HN) bar();
      u_write(IC<i>{});
      u_store(IC<i>{});
      u_sums(IC<i>{});
    });
    for_n<HB>([&](auto) { bar(); });
    wait_vm<0>();
    up(F_W1);
    static_assert(7 - HN + HB <= G1 + 1, "schedule");
#pragma nounroll
    for (int u = 7 - HN + HB; u <= G1; ++u) bar();
    for_n<7>([&](auto I) {
      u_load(IC<9 + decltype(I)::value>{});
      if constexpr (decltype(I)::value == 1 || decltype(I)::value == 3) bar();
    });
#pragma nounroll
    for (int u = G1 + 3; u < 16 + H2; ++u) bar();
    for_n<14>([&](auto S) {
      constexpr int s = decltype(S)::value, i = 9 + s / 2;
      bar();
      if constexpr (s % 2 == 0) u_write(IC<i>{}); else u_store(IC<i>{});
    });
    for_n<12>([&](auto S) {
      constexpr int s = decltype(S)::value;
      bar();
      if constexpr (s == 3) { wait_vm<16>(); up(F_W2); }
      if constexpr (s < 6) cs_load(OFF_CS1 + tnA * 32768, s * 16 / 3, (s + 1) * 16 / 3);
      if constexpr (s >= 8) cs_add(8 * (s - 8), 8 * (s - 7));
    });
    static_assert(H2 + 14 + 12 <= 31, "schedule");
    *(f32x4*)(smem + L_CS1 + lane * 16) = ca[0] + ca[1];
    wait_lgkm0();
    QUIT();
  } else {
    FRESH_LANE();
    const int tmh = tmA > 0 ? tmA - 1 : 0;
    u64 tok;
    auto bars = [&](int n) {
#pragma nounroll
      for (int u = 0; u < n; ++u) bar();
    };
    auto ror8 = [](float v) { return __uint_as_float(__builtin_amdgcn_mov_dpp(__float_as_uint(v), 0x128, 0xf, 0xf, false)); };
    auto put = [&](int xi, float mean, float m2, bool on, int row) {
      const float sd = __builtin_amdgcn_sqrtf(bfr(bfr(m2 * (1.f / DM)) + LN_EPS));
      st8(pack2(mean, sd) | (tok << 32), on, flag(F_LN + xi, tmA, tnA) + row * 8);
    };
    auto reduce = [&](int xi) {
      const u32 src = OFF_ST + xi * 524288 + span(tmA, c13) + (tnA >> 2) * 16384 + (tnA & 3) * 2048, tag = (u32)tok;
      u32x4 a, b;
      auto ld = [&]() {
        asm volatile("" ::: "memory");
        a = __builtin_bit_cast(u32x4, __builtin_amdgcn_raw_buffer_load_b128(rs, lane * 16, src, 16));
        b = __builtin_bit_cast(u32x4, __builtin_amdgcn_raw_buffer_load_b128(rs, lane * 16 + 1024, src, 16));
      };
      ld();
      bar(); bar(); bar();
#pragma nounroll
      for (int spin = 0; spin < (1 << 22); ++spin) {
        if (__all((((a[1] ^ tag) | (a[3] ^ tag)) | ((b[1] ^ tag) | (b[3] ^ tag))) == 0)) break;
        __builtin_amdgcn_s_sleep(1);
        ld();
      }
      float m = __uint_as_float(a[0]) + __uint_as_float(b[0]);
      m = xsum(m + ror8(m)) * (1.f / 16.f);
      const float d0 = __uint_as_float(a[0]) - m, d1 = __uint_as_float(b[0]) - m;
      float q = __uint_as_float(a[2]) + __uint_as_float(b[2]) + 64.f * (d0 * d0 + d1 * d1);
      asm volatile("" : "+v"(m), "+v"(q));
      bar();
      asm volatile("" : "+v"(m), "+v"(q));
      put(xi, m, xsum(q + ror8(q)), lane < 8, lane);
    };
    auto gather = [&](auto H, int xi) {
      constexpr int n = 2 + decltype(H)::value;
      const u32 tag = (u32)tok, f0 = flag(F_LN + xi, tmA, lane >> 3) + (lane & 7) * 8;
      const u32 f[3] = {f0, f0 + 1024, flag(F_LN + xi, tmh, 14 + (r >> 3)) + (lane & 7) * 8};
      u32x2 v[3];
      auto ld = [&]() {
#pragma unroll
        for (int h = 0; h < n; ++h) v[h] = __builtin_bit_cast(u32x2, ldf(f[h]));
      };
      ld();
      bar(); bar(); bar();
#pragma nounroll
      for (int spin = 0; spin < (1 << 22); ++spin) {
        if (__all(((v[0][1] ^ tag) | (v[1][1] ^ tag) | (v[n - 1][1] ^ tag)) == 0)) break;
        __builtin_amdgcn_s_sleep(1);
        ld();
      }
#pragma unroll
      for (int h = 0; h < n; ++h) {
        const float iv = __builtin_amdgcn_rcpf(bf_hi(v[h][0]));
        const int o = h < 2 ? 64 + 256 * h + lane * 4 : r * 4;
        *(float*)(smem + L_INV + o) = iv;
        *(float*)(smem + L_MNV + o) = n == 3 ? -bf_lo(v[h][0]) : -bf_lo(v[h][0]) * iv;
      }
      wait_lgkm0();
    };
    auto guard = [&](u32 f, int K, auto&& lim_, auto&& step) {
      int b = 0, lim = 0;
#pragma nounroll
      for (int spin = 0; spin < (1 << 22); ++spin) {
        const u64 v = ldf(f);
        if (b < lim) { bar(); step(b++); } else __builtin_amdgcn_s_sleep(GSL);
        if (b < lim) { bar(); step(b++); }
        lim = lim_((u32)__ballot(v == tok));
        if (lim >= 100) break;
        lim = min(K, lim);
      }
#pragma nounroll
      while (b < K) { bar(); step(b++); }
    };
    auto ahead = [](int msk, int spp, int D) {
      return [=](u32 m) { const int n = __builtin_ctz(~(m & (m >> 16))); return n >= 16 ? 100 : (n & msk) * spp - D; };
    };
    const __amdgpu_buffer_rsrc_t rx = rsrc(x, SEQ * DM * 2), rq = rsrc(wqkv, DM * 3 * DM * 2);
    u32 wx, ww;
    {
      const int n = idx * 64 + lane, m = idx * 48 + (lane < 48 ? lane : 47), k = (m * 5462) >> 16, p = m - 12 * k;
      wx = (n & 511) * 2048 + ((n >> 9) & 1) * 128 + ((idx >> 4) * 1024 + (xcd >> 2) * 512) * 2048;
      ww = k * 6144 + (p >> 2) * 2048 + (p & 3) * 128 + (xcd & 3) * 512;
    }
    {
      const u64 ep = ldf(OFF_EP);
      u16 bv[4];
#pragma unroll
      for (int h = 0; h < 4; ++h) {
        const int t = min(lane + 64 * h, 223), f = t >> 4, rho = t & 15, wn = f / 7, j = f % 7;
        bv[h] = b1[tnA * 224 + (j == 6 ? 96 + 16 * wn + rho : (4 * wn + (j >> 1)) * 32 + 8 * (rho >> 2) + 4 * (j & 1) + (rho & 3))];
      }
      DMA(rsrc(b2, DM * 2), (LDS_AS void*)(smem + L_B2), 4, lane * 4, (tnB & 14) * 128, 0, 0);
      pin2(wx, ww);
      __builtin_amdgcn_s_sleep(XW);
      bf16x8 xf[16];
#pragma unroll
      for (int e = 0; e < 16; ++e)
        xf[e] = __builtin_bit_cast(bf16x8, __builtin_amdgcn_raw_buffer_load_b128(rx, (r >> 1) * 2048 + (r & 1) * 64 + g * 16 + e * 128,
            (tmA * 128 + tnA * 8) * 2048, 0));
      auto probe = [&](int tn) { return flag(F_PR, tmA ^ (4 * (idx & 1)), tn); };
      const u32x4 one4 = {0x3f803f80u, 0x3f803f80u, 0x3f803f80u, 0x3f803f80u};
      const bf16x8 ones = __builtin_bit_cast(bf16x8, one4);
      f32x4 sm[2], sq[2];
      u32 tlb;
      u64 vt;
      for_n<LG0>([&](auto S) {
        constexpr int s = decltype(S)::value;
        bar();
        if constexpr (s == TOKB - 1) {
          __builtin_amdgcn_raw_buffer_store_b32(0, rs, blockIdx.x < NVOTE ? probe(tnA ^ 4) + (lane & 31) * 4 : 0x80000000u, 0, 16);
#pragma unroll
          for (int h = 0; h < 4; ++h) ((float*)(smem + L_BS))[lane + 64 * h] = bf_lo(bv[h]);
          tok = ((u64)(u32)__builtin_amdgcn_readfirstlane((u32)ep) | ((u64)(u32)__builtin_amdgcn_readfirstlane((u32)(ep >> 32)) << 32))
                * 6364136223846793005ull + 1442695040888963407ull;
          *(LDS_AS u32x4*)(smem + L_CNT) = (u32x4){0u, 0u, (u32)tok, (u32)(tok >> 32)};
          wait_lgkm0();
          asm volatile("s_load_dword %0, %1, 0x0" : "=s"(tlb) : "s"(ws + OFF_EP + 128) : "memory");
        }
        if constexpr (s >= PF0 && (s - PF0) % PFS == 0 && (s - PF0) / PFS < 8 - PFJ) {
          const int j = (PFJ + (s - PF0) / PFS + rot0) & 7;
          DMA(rx, (LDS_AS void*)(smem + L_PF), 4, wx, j * 256, 0, 0);
          DMA(rq, (LDS_AS void*)(smem + L_PF), 4, ww, j * (128 * 6144), 0, 0);
        }
        if constexpr (s >= XP && s < XP + 4) {
#pragma unroll
          for (int e = 4 * (s - XP); e < 4 * (s - XP) + 4; ++e) {
            const f32x4 z = {0.f, 0.f, 0.f, 0.f};
            asm volatile("" : "+v"(xf[e]));
            sm[e & 1] = __builtin_amdgcn_mfma_f32_16x16x32_bf16(ones, xf[e], e < 2 ? z : sm[e & 1], 0, 0, 0);
            sq[e & 1] = __builtin_amdgcn_mfma_f32_16x16x32_bf16(xf[e], xf[e], e < 2 ? z : sq[e & 1], 0, 0, 0);
          }
        }
        if constexpr (s >= XP && s <= XP + 4) asm volatile("" : "+v"(sm[0]), "+v"(sm[1]), "+v"(sq[0]), "+v"(sq[1]));
        if constexpr (s == XP + 4) {
          const f32x4 q4 = sq[0] + sq[1];
          float q = (r & 2) ? ((r & 1) ? q4[3] : q4[2]) : ((r & 1) ? q4[1] : q4[0]), t = sm[0][0] + sm[1][0];
          q += __uint_as_float(__builtin_amdgcn_mov_dpp(__float_as_uint(q), 0xb1, 0xf, 0xf, false));
          t += __uint_as_float(__builtin_amdgcn_mov_dpp(__float_as_uint(t), 0xb1, 0xf, 0xf, false));
          const float m = t * (1.f / DM);
          put(0, m, fmaxf(__builtin_fmaf(-t, m, q), 0.f), g == (r >> 2) && !(r & 1), r >> 1);
        }
        if constexpr (s == PRB) {
          wait_lgkm0();
          asm volatile("" :: "s"(tlb));
          if (blockIdx.x < NVOTE) {
            u64 t0, t1;
            u32 v;
            asm volatile("s_memtime %0\n\ts_waitcnt lgkmcnt(0)\n\ts_load_dword %2, %3, 0x0\n\ts_waitcnt lgkmcnt(0)\n\t"
                         "s_memtime %1\n\ts_waitcnt lgkmcnt(0)" : "=&s"(t0), "=&s"(t1), "=&s"(v) : "s"(ws + probe(tnA)) : "memory");
            const u32 t = min((u32)t1 - (u32)t0, 4095u);
            st8(tok ^ t, lane == 0, flag(F_XC, 0, blockIdx.x));
          }
        }
        if constexpr (s == LG0 - 4) vt = ldf(flag(F_XC, 0, r));
      });
      static_assert(XP >= TOKB && PRB > XP + 4 && LG0 - 4 > PRB && LG0 + 3 <= 31 && L_CNT % 16 == 0, "schedule");
#pragma nounroll
      for (int spin = 0; spin < (1 << 22); ++spin) {
        if (__all((vt ^ tok) < 4096)) break;
        __builtin_amdgcn_s_sleep(1);
        vt = ldf(flag(F_XC, 0, r));
      }
      const u32 t = (u32)(vt ^ tok);
      const u64 slower = __ballot(t > (u32)__builtin_amdgcn_mov_dpp(t, 0x128, 0xf, 0xf, false));
      const u32 c = __builtin_popcount((u32)slower & 255) > 4 ? 8192 : 0;
      *(volatile LDS_AS u32*)(smem + L_C13) = c;
      gather(IC<1>{}, 0);
      c13 = c;
      bars(34 - CLB - (LG0 + 3));
    }
#pragma nounroll
    for (int pair = 0; pair < 2; ++pair) {
#pragma unroll
      for (int e = 0; e < 16; ++e)
        st16(((u32x4){~0u, ~0u, ~0u, ~0u}), xbox(pair, 0) + (e + (e & 8)) * 1024 + lane * 16);
      bars(CLB);
      wait_vm<0>();
      if (ZO + ZG > 0) __builtin_amdgcn_s_sleep(ZO + ZG);
      if (ZO0 > 0 && pair == 0) __builtin_amdgcn_s_sleep(ZO0);
      guard(cpy + flag(4 * pair + 1, tmB, (rotB + r) & 15), 16, ahead(15, 1, DN), [](int) {});
      if (Z1 + ZG > 0) __builtin_amdgcn_s_sleep(Z1 + ZG);
      if (Z10 > 0 && pair == 0) __builtin_amdgcn_s_sleep(Z10);
      guard(cpy + flag(4 * pair + 2, tmA, (rotA + r) & 15), KW1, ahead(14, 2, DW), [](int) {});
      reduce(2 * pair + 1);
      bars(LG - (KW1 + 4));
      gather(IC<0>{}, 2 * pair + 1);
      bars(33 - (LG + 3));
      const u32 w3 = lane < 56 ? OFF_W2 + ((idx >> 1) * 112 + (tnB & 1) * 56 + (idx & 1) * 14) * 4096 + lane * 1024 + 384 : 0x80000000u;
      if (D2 & 2) {
        if (pair == 0) poll(flag(F_W2, 8 * (tnB & 1) + (r & 7), idx >> 1), tok);
        DMA(rs, (LDS_AS void*)(smem + L_PF), 4, w3, 0, 0, 16);
      }
      if (Z2 + ZG > 0) __builtin_amdgcn_s_sleep(Z2 + ZG);
      guard((lane < 8 ? cpy : (idx & (NC2 - 1)) * 131072) + flag(lane < 8 ? 4 * pair + 3 : lane < 16 ? F_2ND + pair : F_3RD + pair, tmB, 8 * (tnB & 1) + ((lane < 8 ? r ^ (rotA >> 1) : r) & 7)), 13,
            [](u32 m) { const int n = __builtin_ctz(~m | (1u << 24)); return n >= 24 ? 100 : min(n, 16) - 3; },
            [&](int b) {
              if (b == 0 || b == 3) st8(tok, lane < NC2, flag(b ? F_3RD + pair : F_2ND + pair, tmA, tnA) + lane * 131072);
              if ((D2 & 2) && b == D2B) DMA(rs, (LDS_AS void*)(smem + L_PF), 4, w3, 28 * 4096, 0, 16);
            });
      if (pair) break;
      bars(WQ - 13);
      for_n<29 - WQ>([&](auto S) {
        constexpr int s = decltype(S)::value;
        if constexpr (s < 4) {
          DMA(rq, (LDS_AS void*)(smem + L_PF), 4, ww, ((2 * s + (rotA >> 1)) & 7) * (128 * 6144), 0, 0);
          DMA(rq, (LDS_AS void*)(smem + L_PF), 4, ww, ((2 * s + 1 + (rotA >> 1)) & 7) * (128 * 6144), 0, 0);
        }
        bar();
      });
      if (ZQ + ZG > 0) __builtin_amdgcn_s_sleep(ZQ + ZG);
      guard(cpy + flag(4, (g & 1) ? tmh : tmA, (rotA + r) & 15), KW1, ahead(14, 2, DW), [](int) {});
      st8(tok, lane == 0 && blockIdx.x == 0, OFF_EP);
      reduce(2);
      bars(LG - (KW1 + 4));
      gather(IC<1>{}, 2);
      bars(33 - CLB - (LG + 3));
      static_assert(WQ <= 25 && LG + 3 <= 31, "schedule");
    }
    QUIT();
  }
#endif
}

extern "C" void kernel_launch(void* const* d_in, const int* in_sizes, int n_in, void* d_out, int out_size,
                              void* d_ws, size_t ws_size, hipStream_t stream) {
  const u16* x0 = (const u16*)d_in[0];
  const u16* wqkv = (const u16*)d_in[1];
  const u16* wo = (const u16*)d_in[2];
  const u16* w1 = (const u16*)d_in[4];
  const u16* b1 = (const u16*)d_in[5];
  const u16* w2 = (const u16*)d_in[6];
  const u16* b2 = (const u16*)d_in[7];
  (void)hipFuncSetAttribute((const void*)mega_k, hipFuncAttributeMaxDynamicSharedMemorySize, LDS_BYTES);
  mega_k<<<256, 512, LDS_BYTES, stream>>>((char*)d_ws + (ws_size - WS_USED - WS_ALIGN), x0, wqkv, wo, w1, b1, w2, b2, (u16*)d_out);
}
```
